```python
import torch

def _softmax(x, axis=-1):
    return torch.softmax(x, dim=axis)

def _gelu(x):
    return torch.nn.functional.gelu(x, approximate='tanh')

def _silu(x):
    return torch.nn.functional.silu(x)

def _sigmoid(x):
    return torch.sigmoid(x)

def _layernorm(x, axis=-1, eps=1e-05):
    m = x.mean(dim=axis, keepdim=True)
    v = x.var(dim=axis, keepdim=True, unbiased=False)
    return (x - m) / torch.sqrt(v + eps)

def _scatter_add(tgt, idx, src, axis=0):
    return tgt.clone().index_add_(axis, idx, src)

def _segment_sum(src, seg, n):
    out = torch.zeros((n,) + src.shape[1:], dtype=src.dtype)
    return out.index_add_(0, seg, src)

def _pad(x, pw):
    flat = tuple((v for pair in reversed(pw) for v in pair))
    return torch.nn.functional.pad(x, flat)
kernel_name = 'adaLN_cond__mha_block__glu_gated_bilinear__prompt'

def setup_inputs():

    _w = lambda a: (lambda t: t.to(torch.bfloat16) if t.is_floating_point() else t)(torch.as_tensor(a))

    def _A_setup():
        B, S, D, H = (1, 2048, 1024, 32)
        return {'x': _w((lambda *s: torch.randn(*s, dtype=torch.bfloat16) * 0.05)(B, S, D)), 'wqkv': _w((lambda *s: torch.randn(*s, dtype=torch.bfloat16) * 0.05)(D, 3 * D)), 'wo': _w((lambda *s: torch.randn(*s, dtype=torch.bfloat16) * 0.05)(D, D))}
    _A_in = _A_setup()

    def _B_setup():
        B, S, D, F = (1, 2048, 1024, 3584)
        return {'x': _w((lambda *s: torch.randn(*s, dtype=torch.bfloat16) * 0.05)(B, S, D)), 'w_gate': _w((lambda *s: torch.randn(*s, dtype=torch.bfloat16) * 1.6)(D, F) * D ** (-0.5)), 'w_up': _w((lambda *s: torch.randn(*s, dtype=torch.bfloat16) * 1.6)(D, F) * D ** (-0.5)), 'w_down': _w((lambda *s: torch.randn(*s, dtype=torch.bfloat16) * 1.6)(F, D) * F ** (-0.5))}
    _B_in = _B_setup()
    return {'A_x': _A_in['x'], 'A_wqkv': _A_in['wqkv'], 'A_wo': _A_in['wo'], 'B_x': _B_in['x'], 'B_w_gate': _B_in['w_gate'], 'B_w_up': _B_in['w_up'], 'B_w_down': _B_in['w_down'], 'c': _w((lambda *s: torch.randn(*s, dtype=torch.bfloat16) * 0.2)(1, 1024) * 8.0), 'w_mod': _w((lambda *s: torch.randn(*s, dtype=torch.bfloat16) * 0.2)(1024, 6 * 1024) * 1024 ** (-0.5) * 8.0)}

def reference(A_x, A_wqkv, A_wo, B_x, B_w_gate, B_w_up, B_w_down, c, w_mod):

    def _A_ref(x, wqkv, wo):
        B, S, D = x.shape
        H = 32
        Dh = D // H
        qkv = (lambda x, *d: x.permute(*d))((x @ wqkv).reshape(B, S, 3, H, Dh), 2, 0, 3, 1, 4)
        q, k, v = (qkv[0], qkv[1], qkv[2])
        scores = torch.einsum('bhqd,bhkd->bhqk', q, k) * Dh ** (-0.5)
        p = _softmax(scores, axis=-1)
        o = torch.einsum('bhqk,bhkd->bhqd', p, v)
        return (lambda x, *d: x.permute(*d))(o, 0, 2, 1, 3).reshape(B, S, D) @ wo

    def _B_ref(x, w_gate, w_up, w_down):
        g = torch.einsum('bsd,df->bsf', x, w_gate)
        u = torch.einsum('bsd,df->bsf', x, w_up)
        h = g * u
        return torch.einsum('bsf,fd->bsd', h, w_down)
    x = A_x
    mod = (_silu(c) @ w_mod).reshape(c.shape[0], 6, 1, -1)
    sa, ca, ga, sb, cb, gb = (mod[:, 0].clone(), mod[:, 1].clone(), mod[:, 2].clone(), mod[:, 3].clone(), mod[:, 4].clone(), mod[:, 5].clone())
    h = _layernorm(x, axis=-1) * (1 + ca) + sa
    x = x + ga * _A_ref(h, A_wqkv, A_wo)
    h = _layernorm(x, axis=-1) * (1 + cb) + sb
    x = x + gb * _B_ref(h, B_w_gate, B_w_up, B_w_down)
    return x
```

```cpp
#include <hip/hip_runtime.h>
#include <hip/hip_bf16.h>
#include <stdint.h>

typedef __attribute__((ext_vector_type(8))) short bf16x8;
typedef __attribute__((ext_vector_type(4))) short s16x4;
typedef __attribute__((ext_vector_type(4))) float f32x4;
typedef __attribute__((ext_vector_type(2))) float f32x2;
typedef __attribute__((ext_vector_type(16))) float f32x16;
typedef __attribute__((ext_vector_type(4))) unsigned u32x4;
typedef __attribute__((ext_vector_type(2))) unsigned u32x2;
typedef __attribute__((ext_vector_type(2))) __bf16 bf16x2_t;
typedef unsigned short u16;
typedef __attribute__((address_space(3))) char* lds_ptr;
typedef __attribute__((address_space(3))) const char* lds_cptr;

#define DI __device__ __forceinline__

constexpr int SEQ = 2048, DM = 1024, FF = 3584, NH = 32;

DI float bf2f(u16 x) { return __uint_as_float(((unsigned)x) << 16); }
DI float bflo(unsigned x) { return __uint_as_float(x << 16); }
DI float bfhi(unsigned x) { return __uint_as_float(x & 0xffff0000u); }
DI unsigned pack2(float a, float b) {
  f32x2 v = {a, b};
  bf16x2_t r = __builtin_convertvector(v, bf16x2_t);
  return __builtin_bit_cast(unsigned, r);
}
DI float rbf(float a) { return bflo(pack2(a, 0.f) & 0xffffu); }

#define MFMA16(a, b, c) __builtin_amdgcn_mfma_f32_16x16x32_bf16((a), (b), (c), 0, 0, 0)
#define MFMA32(a, b, c) __builtin_amdgcn_mfma_f32_32x32x16_bf16((a), (b), (c), 0, 0, 0)

DI void glds16(const void* g, lds_ptr l) {
  __builtin_amdgcn_global_load_lds((const __attribute__((address_space(1))) unsigned*)g,
                                   (__attribute__((address_space(3))) unsigned*)l, 16, 0, 0);
}
DI bf16x8 lds_read128(lds_cptr p) { return *(const __attribute__((address_space(3))) bf16x8*)p; }
DI s16x4 lds_tr(lds_cptr p) {
  typedef short v4i16_t __attribute__((ext_vector_type(4)));
  return __builtin_bit_cast(s16x4, __builtin_amdgcn_ds_read_tr16_b64_v4i16((__attribute__((address_space(3))) v4i16_t*)p));
}
DI bf16x8 lds_tr8(lds_cptr p0, lds_cptr p1) {
  s16x4 a = lds_tr(p0), b = lds_tr(p1);
  return __builtin_shufflevector(a, b, 0, 1, 2, 3, 4, 5, 6, 7);
}
template <int N> DI void wait_vm() { asm volatile("s_waitcnt vmcnt(%0)" ::"n"(N) : "memory"); }
DI void bar() { __builtin_amdgcn_s_barrier(); }

__global__ __launch_bounds__(1024) void k_mod(const u16* __restrict__ c, const u16* __restrict__ wmod,
                                               float* __restrict__ mod) {
  __shared__ float s_s[1024];
  __shared__ float s_red[16][64];
  const int t = threadIdx.x;
  {
    float cv = bf2f(c[t]);
    float sg = 1.f / (1.f + __expf(-cv));
    s_s[t] = rbf(cv * sg);
  }
  const int cc = t & 7, rg = t >> 3;
  const u16* wp = wmod + (size_t)rg * (6 * DM) + blockIdx.x * 64 + cc * 8;
  u32x4 w[8];
#pragma unroll
  for (int i = 0; i < 8; ++i) w[i] = *(const u32x4*)(wp + (size_t)i * 128 * (6 * DM));
  __syncthreads();
  float acc[8];
#pragma unroll
  for (int j = 0; j < 8; ++j) acc[j] = 0.f;
#pragma unroll
  for (int i = 0; i < 8; ++i) {
    float s = s_s[rg + 128 * i];
#pragma unroll
    for (int j = 0; j < 4; ++j) {
      acc[2 * j] += s * bflo(w[i][j]);
      acc[2 * j + 1] += s * bfhi(w[i][j]);
    }
  }
#pragma unroll
  for (int j = 0; j < 8; ++j) {
    float v = acc[j];
    v += __shfl_xor(v, 8);
    v += __shfl_xor(v, 16);
    v += __shfl_xor(v, 32);
    acc[j] = v;
  }
  const int lane = t & 63, wv = t >> 6;
  if (lane < 8) {
#pragma unroll
    for (int j = 0; j < 8; ++j) s_red[wv][lane * 8 + j] = acc[j];
  }
  __syncthreads();
  if (t < 64) {
    float v = 0.f;
#pragma unroll
    for (int i = 0; i < 16; ++i) v += s_red[i][t];
    mod[blockIdx.x * 64 + t] = rbf(v);
  }
}

__global__ __launch_bounds__(512) void k_ln(const u16* __restrict__ x, const float* __restrict__ shift,
                                             const float* __restrict__ scale, u16* __restrict__ hblk) {
  const int lane = threadIdx.x & 63, wv = threadIdx.x >> 6;
  const int row = blockIdx.x * 8 + wv;
  const u16* xr = x + (size_t)row * DM;
  u32x4 v0 = *(const u32x4*)(xr + lane * 8);
  u32x4 v1 = *(const u32x4*)(xr + 512 + lane * 8);
  float f[16];
#pragma unroll
  for (int j = 0; j < 4; ++j) {
    f[2 * j] = bflo(v0[j]); f[2 * j + 1] = bfhi(v0[j]);
    f[8 + 2 * j] = bflo(v1[j]); f[8 + 2 * j + 1] = bfhi(v1[j]);
  }
  float s = 0.f;
#pragma unroll
  for (int j = 0; j < 16; ++j) s += f[j];
#pragma unroll
  for (int o = 1; o < 64; o <<= 1) s += __shfl_xor(s, o);
  const float mean = s * (1.f / DM);
  float q = 0.f;
#pragma unroll
  for (int j = 0; j < 16; ++j) { float d = f[j] - mean; q += d * d; }
#pragma unroll
  for (int o = 1; o < 64; o <<= 1) q += __shfl_xor(q, o);
  const float mb = rbf(mean);
  const float vb = rbf(q * (1.f / DM));
  const float den = rbf(sqrtf(rbf(vb + 1e-5f)));
#pragma unroll
  for (int half = 0; half < 2; ++half) {
    const int k0 = half * 512 + lane * 8;
    f32x4 sc0 = *(const f32x4*)(scale + k0), sc1 = *(const f32x4*)(scale + k0 + 4);
    f32x4 sh0 = *(const f32x4*)(shift + k0), sh1 = *(const f32x4*)(shift + k0 + 4);
    float y[8];
#pragma unroll
    for (int j = 0; j < 8; ++j) {
      float sc = j < 4 ? sc0[j] : sc1[j - 4];
      float sh = j < 4 ? sh0[j] : sh1[j - 4];
      float g = rbf(1.f + sc);
      float d = rbf(f[half * 8 + j] - mb);
      float n = rbf(d / den);
      y[j] = rbf(n * g) + sh;
    }
    u32x4 o;
#pragma unroll
    for (int j = 0; j < 4; ++j) o[j] = pack2(y[2 * j], y[2 * j + 1]);
    *(u32x4*)(hblk + ((size_t)(k0 >> 5) * SEQ + row) * 32 + (k0 & 31)) = o;
  }
}

DI int hA(int r) { return (-r) & 3; }

template <int BM, int NGRP, int KS, int NST>
struct GCfg {
  static constexpr int A_SUB = BM * 64;
  static constexpr int W_SUB = NGRP * 4096;
  static constexpr int STAGE = KS * (A_SUB + W_SUB);
  static constexpr int APW = KS * BM / 128;
  static constexpr int WPW = KS * NGRP / 2;
  static constexpr int PPW = APW + WPW;
  static constexpr int LDS = STAGE * NST;
};

template <class C>
struct Loader {
  const u16* ap[C::APW];
  const u16* wp[C::WPW];
  int aoff[C::APW];
  int woff[C::WPW];
};

template <class C, int BM, int NGRP, int KS, int NST, int MT, int NT, int KSPLIT, int NKT>
struct GemmCore {
  static constexpr int SPW = KS / KSPLIT;

  static DI void issue(Loader<C>& ld, lds_ptr stage_base, long a_adv, long w_adv) {
#pragma unroll
    for (int i = 0; i < C::APW; ++i) { glds16(ld.ap[i], stage_base + ld.aoff[i]); ld.ap[i] += a_adv; }
#pragma unroll
    for (int i = 0; i < C::WPW; ++i) { glds16(ld.wp[i], stage_base + ld.woff[i]); ld.wp[i] += w_adv; }
  }
};

template <int MT, int NT, bool PF>
DI void mma_step(f32x4 (&acc)[NT][MT], bf16x8 (&wf)[NT], bf16x8 (&af)[MT], bf16x8 (&an)[MT],
                 lds_cptr abase_next, const int (&waddr)[NT], lds_cptr wbase_next) {
#pragma unroll
  for (int n = 0; n < NT; ++n) {
#pragma unroll
    for (int m = 0; m < MT; ++m) acc[n][m] = MFMA16(wf[n], af[m], acc[n][m]);
    if (PF) {
      wf[n] = lds_tr8(wbase_next + waddr[n], wbase_next + waddr[n] + 512);
      if (n < MT) an[n] = lds_read128(abase_next + n * 1024);
    }
    __builtin_amdgcn_sched_barrier(0);
  }
  if (PF) {
#pragma unroll
    for (int m = NT; m < MT; ++m) an[m] = lds_read128(abase_next + m * 1024);
#pragma unroll
    for (int m = 0; m < MT; ++m) af[m] = an[m];
  }
}

template <int BM, int NGRP, int KS, int NST, int MT, int NT, int KSPLIT, int NKT>
DI void gemm_mainloop(f32x4 (&acc)[NT][MT], lds_ptr smem, Loader<GCfg<BM, NGRP, KS, NST>>& ld, long a_adv, long w_adv,
                      int aaddr, const int (&waddr)[NT], int kg) {
  typedef GCfg<BM, NGRP, KS, NST> C;
  typedef GemmCore<C, BM, NGRP, KS, NST, MT, NT, KSPLIT, NKT> G;
  constexpr int SPW = KS / KSPLIT;
  constexpr int P = C::PPW;
  static_assert(NKT >= NST, "too few k tiles");
#pragma unroll
  for (int s = 0; s < NST - 1; ++s) G::issue(ld, smem + s * C::STAGE, a_adv, w_adv);
  wait_vm<P*(NST - 2)>();
  bar();
  bf16x8 wf[NT], af[MT], an[MT];
  const int a_sub0 = kg * C::A_SUB + aaddr;
  const int w_sub0 = KS * C::A_SUB + kg * C::W_SUB;
  {
    lds_cptr ab = smem + a_sub0;
    lds_cptr wb = smem + w_sub0;
#pragma unroll
    for (int m = 0; m < MT; ++m) af[m] = lds_read128(ab + m * 1024);
#pragma unroll
    for (int n = 0; n < NT; ++n) wf[n] = lds_tr8(wb + waddr[n], wb + waddr[n] + 512);
  }
  int cur = 0;
  int nxt = C::STAGE;
  int fre = (NST - 1) * C::STAGE;
  auto body = [&](auto issue_tag, auto last_tag) {
    constexpr bool DOISSUE = decltype(issue_tag)::value;
    constexpr bool LAST = decltype(last_tag)::value;
    if (DOISSUE) G::issue(ld, smem + fre, a_adv, w_adv);
#pragma unroll
    for (int s = 0; s < SPW; ++s) {
      if (s + 1 < SPW) {
        mma_step<MT, NT, true>(acc, wf, af, an, smem + cur + a_sub0 + (s + 1) * KSPLIT * C::A_SUB, waddr,
                               smem + cur + w_sub0 + (s + 1) * KSPLIT * C::W_SUB);
      } else if (!LAST) {
        mma_step<MT, NT, true>(acc, wf, af, an, smem + nxt + a_sub0, waddr, smem + nxt + w_sub0);
      } else {
        mma_step<MT, NT, false>(acc, wf, af, an, smem, waddr, smem);
      }
    }
    fre = cur; cur = nxt; nxt = (nxt + C::STAGE == NST * C::STAGE) ? 0 : nxt + C::STAGE;
  };
  typedef std::integral_constant<bool, true> T_;
  typedef std::integral_constant<bool, false> F_;
  for (int kt = 0; kt <= NKT - NST; ++kt) {
    wait_vm<P*(NST - 3)>();
    bar();
    body(T_{}, F_{});
  }
#pragma unroll
  for (int r = NST - 2; r >= 0; --r) {
    if (r >= 1) {
      if (r - 1 == 0) wait_vm<0>();
      else if (r - 1 == 1) wait_vm<P>();
      else if (r - 1 == 2) wait_vm<2 * P>();
      else if (r - 1 == 3) wait_vm<3 * P>();
      else wait_vm<0>();
      bar();
      body(F_{}, F_{});
    } else {
      body(F_{}, T_{});
    }
  }
}

template <class C, int BM, int NGRP, int KS>
DI void setup_loader_A(Loader<C>& ld, const u16* Ablk, int m0, int w, int lane) {
#pragma unroll
  for (int i = 0; i < C::APW; ++i) {
    int a = w + 8 * i;
    int sub = a / (BM / 16), j = a % (BM / 16);
    ld.aoff[i] = sub * C::A_SUB + j * 1024;
    int row = j * 16 + (lane >> 2);
    int c = (lane & 3) ^ hA((lane >> 4) & 3);
    ld.ap[i] = Ablk + ((size_t)sub * SEQ + m0 + row) * 32 + c * 8;
  }
}

DI int wfrag_addr(int t, int lane) {
  int g = lane >> 4, q = (lane & 15) >> 2, p = lane & 3;
  int f = ((q >> 1) & 1) | ((g & 1) << 1);
  return (t >> 2) * 4096 + (8 * g + q) * 128 + (((t & 3) ^ f) * 32) + p * 8;
}
DI int afrag_addr(int mrow0, int lane) {
  int r = lane & 15, g = lane >> 4;
  return (mrow0 + r) * 64 + ((g ^ hA((r >> 2) & 3)) * 16);
}

constexpr float QSCALE = 0.17677669529663687f * 1.4426950408889634f;
typedef GCfg<128, 3, 2, 3> CQ;
__global__ __launch_bounds__(512, 2) void k_qkv(const u16* __restrict__ Ablk, const u16* __restrict__ W,
                                                 u16* __restrict__ out) {
  extern __shared__ __attribute__((aligned(16))) char smem_[];
  lds_ptr smem = (lds_ptr)smem_;
  const int lane = threadIdx.x & 63;
  const int w = __builtin_amdgcn_readfirstlane(threadIdx.x >> 6);
  const int xcd = blockIdx.x & 7, idx = blockIdx.x >> 3;
  const int tm = (xcd & 1) * 8 + (idx & 7), tn = (xcd >> 1) * 4 + (idx >> 3);
  const int m0 = tm * 128, n0 = tn * 192;
  constexpr int LDW = 3 * DM;
  Loader<CQ> ld;
  setup_loader_A<CQ, 128, 3, 2>(ld, Ablk, m0, w, lane);
#pragma unroll
  for (int i = 0; i < CQ::WPW; ++i) {
    int b = w + 8 * i;
    int sub = b / 12, r = b % 12, grp = r >> 2, o = r & 3;
    ld.woff[i] = 2 * CQ::A_SUB + sub * CQ::W_SUB + grp * 4096 + o * 1024;
    int k = sub * 32 + 8 * o + (lane >> 3);
    int f = ((lane >> 4) & 1) | ((o & 1) << 1);
    int col = n0 + grp * 64 + ((((lane & 7) >> 1) ^ f) * 16) + (lane & 1) * 8;
    ld.wp[i] = W + (size_t)k * LDW + col;
  }
  const int wm = w & 1, wn = w >> 1;
  int waddr[3];
#pragma unroll
  for (int n = 0; n < 3; ++n) waddr[n] = wfrag_addr(wn * 3 + n, lane);
  const int aaddr = afrag_addr(wm * 64, lane);
  f32x4 acc[3][4];
#pragma unroll
  for (int n = 0; n < 3; ++n)
#pragma unroll
    for (int m = 0; m < 4; ++m) acc[n][m] = f32x4{0.f, 0.f, 0.f, 0.f};
  gemm_mainloop<128, 3, 2, 3, 4, 3, 1, DM / 64>(acc, smem, ld, (long)2 * SEQ * 32, (long)64 * LDW, aaddr, waddr, 0);
  const int g = lane >> 4;
#pragma unroll
  for (int n = 0; n < 3; ++n) {
    int col = n0 + (wn * 3 + n) * 16 + 4 * g;
    float sc = col < DM ? QSCALE : 1.f;
#pragma unroll
    for (int m = 0; m < 4; ++m) {
      int row = m0 + wm * 64 + m * 16 + (lane & 15);
      u32x2 o;
      o[0] = pack2(acc[n][m][0] * sc, acc[n][m][1] * sc);
      o[1] = pack2(acc[n][m][2] * sc, acc[n][m][3] * sc);
      *(u32x2*)(out + ((size_t)(col >> 5) * SEQ + row) * 32 + (col & 31)) = o;
    }
  }
}

typedef GCfg<128, 1, 2, 6> CP;
template <int KDIM>
__global__ __launch_bounds__(512, 2) void k_proj(const u16* __restrict__ Ablk, const u16* __restrict__ W,
                                                  const u16* __restrict__ res, const float* __restrict__ gate,
                                                  u16* __restrict__ out) {
  extern __shared__ __attribute__((aligned(16))) char smem_[];
  lds_ptr smem = (lds_ptr)smem_;
  const int lane = threadIdx.x & 63;
  const int w = __builtin_amdgcn_readfirstlane(threadIdx.x >> 6);
  const int xcd = blockIdx.x & 7, idx = blockIdx.x >> 3;
  const int tm = (xcd & 3) * 4 + (idx & 3), tn = (xcd >> 2) * 8 + (idx >> 2);
  const int m0 = tm * 128, n0 = tn * 64;
  constexpr int LDW = DM;
  Loader<CP> ld;
  setup_loader_A<CP, 128, 1, 2>(ld, Ablk, m0, w, lane);
  {
    int b = w;
    int sub = b >> 2, o = b & 3;
    ld.woff[0] = 2 * CP::A_SUB + sub * CP::W_SUB + o * 1024;
    int k = sub * 32 + 8 * o + (lane >> 3);
    int f = ((lane >> 4) & 1) | ((o & 1) << 1);
    int col = n0 + ((((lane & 7) >> 1) ^ f) * 16) + (lane & 1) * 8;
    ld.wp[0] = W + (size_t)k * LDW + col;
  }
  const int wm = w & 1, wn = (w >> 1) & 1, kg = w >> 2;
  int waddr[2];
#pragma unroll
  for (int n = 0; n < 2; ++n) waddr[n] = wfrag_addr(wn * 2 + n, lane);
  const int aaddr = afrag_addr(wm * 64, lane);
  f32x4 acc[2][4];
#pragma unroll
  for (int n = 0; n < 2; ++n)
#pragma unroll
    for (int m = 0; m < 4; ++m) acc[n][m] = f32x4{0.f, 0.f, 0.f, 0.f};
  gemm_mainloop<128, 1, 2, 6, 4, 2, 2, KDIM / 64>(acc, smem, ld, (long)2 * SEQ * 32, (long)64 * LDW, aaddr, waddr, kg);
  __syncthreads();
  __attribute__((address_space(3))) f32x4* red = (__attribute__((address_space(3))) f32x4*)smem;
  if (kg == 1) {
#pragma unroll
    for (int n = 0; n < 2; ++n)
#pragma unroll
      for (int m = 0; m < 4; ++m) red[((w & 3) * 8 + n * 4 + m) * 64 + lane] = acc[n][m];
  }
  __syncthreads();
  if (kg == 0) {
    const int g = lane >> 4;
#pragma unroll
    for (int n = 0; n < 2; ++n) {
      int col = n0 + (wn * 2 + n) * 16 + 4 * g;
      f32x4 gt = *(const f32x4*)(gate + col);
#pragma unroll
      for (int m = 0; m < 4; ++m) {
        f32x4 a = acc[n][m] + red[((w & 3) * 8 + n * 4 + m) * 64 + lane];
        int row = m0 + wm * 64 + m * 16 + (lane & 15);
        u32x2 r = *(const u32x2*)(res + (size_t)row * DM + col);
        float y0 = bflo(r[0]) + rbf(gt[0] * rbf(a[0]));
        float y1 = bfhi(r[0]) + rbf(gt[1] * rbf(a[1]));
        float y2 = bflo(r[1]) + rbf(gt[2] * rbf(a[2]));
        float y3 = bfhi(r[1]) + rbf(gt[3] * rbf(a[3]));
        u32x2 o;
        o[0] = pack2(y0, y1);
        o[1] = pack2(y2, y3);
        *(u32x2*)(out + (size_t)row * DM + col) = o;
      }
    }
  }
}

typedef GCfg<256, 4, 1, 4> CG;
template <int NH_>
DI void glu_wave(lds_ptr smem, Loader<CG>& ld, int lane, int wm, int t0, int m0, int f0, u16* __restrict__ out) {
  constexpr int NT = 2 * NH_;
  int waddr[NT];
#pragma unroll
  for (int n = 0; n < NH_; ++n) {
    waddr[n] = wfrag_addr(t0 + n, lane);
    waddr[NH_ + n] = waddr[n] + 8192;
  }
  const int aaddr = afrag_addr(wm * 64, lane);
  f32x4 acc[NT][4];
#pragma unroll
  for (int n = 0; n < NT; ++n)
#pragma unroll
    for (int m = 0; m < 4; ++m) acc[n][m] = f32x4{0.f, 0.f, 0.f, 0.f};
  gemm_mainloop<256, 4, 1, 4, 4, NT, 1, DM / 32>(acc, smem, ld, (long)SEQ * 32, (long)32 * FF, aaddr, waddr, 0);
  const int g = lane >> 4;
#pragma unroll
  for (int n = 0; n < NH_; ++n) {
    int col = f0 + (t0 + n) * 16 + 4 * g;
#pragma unroll
    for (int m = 0; m < 4; ++m) {
      int row = m0 + wm * 64 + m * 16 + (lane & 15);
      f32x4 a = acc[n][m], b = acc[NH_ + n][m];
      u32x2 o;
      o[0] = pack2(rbf(a[0]) * rbf(b[0]), rbf(a[1]) * rbf(b[1]));
      o[1] = pack2(rbf(a[2]) * rbf(b[2]), rbf(a[3]) * rbf(b[3]));
      *(u32x2*)(out + ((size_t)(col >> 5) * SEQ + row) * 32 + (col & 31)) = o;
    }
  }
}

__global__ __launch_bounds__(512, 2) void k_glu(const u16* __restrict__ Ablk, const u16* __restrict__ Wg,
                                                 const u16* __restrict__ Wu, u16* __restrict__ out) {
  extern __shared__ __attribute__((aligned(16))) char smem_[];
  lds_ptr smem = (lds_ptr)smem_;
  const int lane = threadIdx.x & 63;
  const int w = __builtin_amdgcn_readfirstlane(threadIdx.x >> 6);
  const int xcd = blockIdx.x & 7, idx = blockIdx.x >> 3;
  const int tm = idx & 7, tn = xcd * 4 + (idx >> 3);
  const int m0 = tm * 256, f0 = tn * 112;
  Loader<CG> ld;
  setup_loader_A<CG, 256, 4, 1>(ld, Ablk, m0, w, lane);
#pragma unroll
  for (int i = 0; i < CG::WPW; ++i) {
    int b = w + 8 * i;
    int grp = b >> 2, o = b & 3;
    ld.woff[i] = CG::A_SUB + grp * 4096 + o * 1024;
    int k = 8 * o + (lane >> 3);
    int f = ((lane >> 4) & 1) | ((o & 1) << 1);
    int pr = ((lane & 7) >> 1) ^ f;
    if ((grp & 1) && pr == 3) pr = 0;
    int col = f0 + (grp & 1) * 64 + pr * 16 + (lane & 1) * 8;
    ld.wp[i] = (grp >> 1 ? Wu : Wg) + (size_t)k * FF + col;
  }
  const int wm = w & 3;
  if (w < 4) glu_wave<4>(smem, ld, lane, wm, 0, m0, f0, out);
  else glu_wave<3>(smem, ld, lane, wm, 4, m0, f0, out);
}

constexpr int ACH = 128, ANST = 4, ASTAGE = ACH * 64 * 2;
__global__ __launch_bounds__(512, 2) void k_attn(const u16* __restrict__ qkv, u16* __restrict__ oblk) {
  extern __shared__ __attribute__((aligned(16))) char smem_[];
  lds_ptr smem = (lds_ptr)smem_;
  const int lane = threadIdx.x & 63;
  const int w = __builtin_amdgcn_readfirstlane(threadIdx.x >> 6);
  const int xcd = blockIdx.x & 7, idx = blockIdx.x >> 3;
  const int head = xcd * 4 + (idx >> 3), qb = idx & 7;
  const int r32 = lane & 31, hh = lane >> 5;
  const u16* Qp = qkv + (size_t)head * SEQ * 32;
  const u16* Kp = qkv + (size_t)(32 + head) * SEQ * 32;
  const u16* Vp = qkv + (size_t)(64 + head) * SEQ * 32;
  const int q0 = qb * 256 + w * 32;
  const u16* ksrc = Kp + (16 * w + (lane >> 2)) * 32 + (((lane & 3) ^ ((lane >> 4) & 3)) * 8);
  const u16* vsrc = Vp + (16 * w) * 32 + lane * 8;
  auto issue = [&](int buf) {
    glds16(ksrc, smem + buf * ASTAGE + w * 1024);
    glds16(vsrc, smem + buf * ASTAGE + 8192 + w * 1024);
    ksrc += ACH * 32; vsrc += ACH * 32;
  };
  issue(0); issue(1); issue(2);
  bf16x8 qf0 = *(const bf16x8*)(Qp + (q0 + r32) * 32 + hh * 8);
  bf16x8 qf1 = *(const bf16x8*)(Qp + (q0 + r32) * 32 + 16 + hh * 8);
  const int kaddr0 = r32 * 64 + (((0 + hh) ^ ((r32 >> 2) & 3)) * 16);
  const int kaddr1 = r32 * 64 + (((2 + hh) ^ ((r32 >> 2) & 3)) * 16);
  const int vaddr = 8192 + (4 * hh + ((lane & 15) >> 2)) * 64 + ((lane >> 4) & 1) * 32 + (lane & 3) * 8;
  f32x16 o, negm;
#pragma unroll
  for (int r = 0; r < 16; ++r) { o[r] = 0.f; negm[r] = 0.f; }
  float l = 0.f;
  bool first = true;
  constexpr int NCH = SEQ / ACH;
  for (int ch = 0; ch < NCH; ++ch) {
    if (ch + 2 < NCH) wait_vm<4>(); else if (ch + 1 < NCH) wait_vm<2>(); else wait_vm<0>();
    bar();
    if (ch + 3 < NCH) issue((ch + 3) & 3);
    lds_cptr sb = smem + (ch & 3) * ASTAGE;
#pragma unroll
    for (int t = 0; t < 4; ++t) {
      bf16x8 kf0 = lds_read128(sb + t * 2048 + kaddr0);
      bf16x8 kf1 = lds_read128(sb + t * 2048 + kaddr1);
      f32x16 s = MFMA32(kf0, qf0, negm);
      s = MFMA32(kf1, qf1, s);
      f32x16 p;
      float ts = 0.f;
#pragma unroll
      for (int r = 0; r < 16; ++r) { p[r] = __builtin_amdgcn_exp2f(s[r]); ts += p[r]; }
      if (__builtin_expect(first || __any(!(ts <= 16777216.f)), 0)) {
        float mx = s[0];
#pragma unroll
        for (int r = 1; r < 16; ++r) mx = fmaxf(mx, s[r]);
        auto sw = __builtin_amdgcn_permlane32_swap(__float_as_uint(mx), __float_as_uint(mx), false, false);
        mx = fmaxf(__uint_as_float(sw[0]), __uint_as_float(sw[1]));
        float dl = first ? mx : fmaxf(mx, 0.f);
        float sc = first ? 0.f : __builtin_amdgcn_exp2f(-dl);
        ts = 0.f;
#pragma unroll
        for (int r = 0; r < 16; ++r) {
          negm[r] -= dl; o[r] *= sc;
          p[r] = __builtin_amdgcn_exp2f(s[r] - dl); ts += p[r];
        }
        l *= sc;
        first = false;
      }
      l += ts;
      u32x4 pa, pb;
#pragma unroll
      for (int j = 0; j < 4; ++j) { pa[j] = pack2(p[2 * j], p[2 * j + 1]); pb[j] = pack2(p[8 + 2 * j], p[8 + 2 * j + 1]); }
      lds_cptr vb = sb + t * 2048 + vaddr;
      bf16x8 vf0 = lds_tr8(vb, vb + 512);
      bf16x8 vf1 = lds_tr8(vb + 1024, vb + 1536);
      o = MFMA32(vf0, __builtin_bit_cast(bf16x8, pa), o);
      o = MFMA32(vf1, __builtin_bit_cast(bf16x8, pb), o);
    }
  }
  auto sw = __builtin_amdgcn_permlane32_swap(__float_as_uint(l), __float_as_uint(l), false, false);
  const float rl = 1.f / (__uint_as_float(sw[0]) + __uint_as_float(sw[1]));
  u16* op = oblk + ((size_t)head * SEQ + q0 + r32) * 32 + 4 * hh;
#pragma unroll
  for (int g = 0; g < 4; ++g) {
    u32x2 v;
    v[0] = pack2(o[4 * g] * rl, o[4 * g + 1] * rl);
    v[1] = pack2(o[4 * g + 2] * rl, o[4 * g + 3] * rl);
    *(u32x2*)(op + 8 * g) = v;
  }
}

extern "C" void kernel_launch(void* const* d_in, const int* in_sizes, int n_in, void* d_out, int out_size,
                              void* d_ws, size_t ws_size, hipStream_t stream) {
  const u16* x = (const u16*)d_in[0];
  const u16* wqkv = (const u16*)d_in[1];
  const u16* wo = (const u16*)d_in[2];
  const u16* wg = (const u16*)d_in[4];
  const u16* wu = (const u16*)d_in[5];
  const u16* wd = (const u16*)d_in[6];
  const u16* c = (const u16*)d_in[7];
  const u16* wmod = (const u16*)d_in[8];
  u16* out = (u16*)d_out;
  char* ws = (char*)d_ws;
  float* mod = (float*)ws;
  u16* h1 = (u16*)(ws + (1 << 20));
  u16* qkv = (u16*)(ws + (8 << 20));
  u16* ob = (u16*)(ws + (24 << 20));
  u16* x2 = (u16*)(ws + (32 << 20));
  u16* h2 = (u16*)(ws + (40 << 20));
  u16* hh = (u16*)(ws + (48 << 20));

  hipFuncSetAttribute((const void*)k_qkv, hipFuncAttributeMaxDynamicSharedMemorySize, CQ::LDS);
  hipFuncSetAttribute((const void*)k_proj<DM>, hipFuncAttributeMaxDynamicSharedMemorySize, CP::LDS);
  hipFuncSetAttribute((const void*)k_proj<FF>, hipFuncAttributeMaxDynamicSharedMemorySize, CP::LDS);
  hipFuncSetAttribute((const void*)k_glu, hipFuncAttributeMaxDynamicSharedMemorySize, CG::LDS);
  hipFuncSetAttribute((const void*)k_attn, hipFuncAttributeMaxDynamicSharedMemorySize, ANST * ASTAGE);

  k_mod<<<96, 1024, 0, stream>>>(c, wmod, mod);
  k_ln<<<256, 512, 0, stream>>>(x, mod + 0 * DM, mod + 1 * DM, h1);
  k_qkv<<<256, 512, CQ::LDS, stream>>>(h1, wqkv, qkv);
  k_attn<<<256, 512, ANST * ASTAGE, stream>>>(qkv, ob);
  k_proj<DM><<<256, 512, CP::LDS, stream>>>(ob, wo, x, mod + 2 * DM, x2);
  k_ln<<<256, 512, 0, stream>>>(x2, mod + 3 * DM, mod + 4 * DM, h2);
  k_glu<<<256, 512, CG::LDS, stream>>>(h2, wg, wu, hh);
  k_proj<FF><<<256, 512, CP::LDS, stream>>>(hh, wd, x2, mod + 5 * DM, out);
}
```

```cpp
#include <hip/hip_runtime.h>
#include <hip/hip_bf16.h>
#include <stdint.h>

typedef __attribute__((ext_vector_type(8))) short bf16x8;
typedef __attribute__((ext_vector_type(4))) short s16x4;
typedef __attribute__((ext_vector_type(4))) float f32x4;
typedef __attribute__((ext_vector_type(2))) float f32x2;
typedef __attribute__((ext_vector_type(16))) float f32x16;
typedef __attribute__((ext_vector_type(4))) unsigned u32x4;
typedef __attribute__((ext_vector_type(2))) unsigned u32x2;
typedef __attribute__((ext_vector_type(2))) __bf16 bf16x2_t;
typedef unsigned short u16;
typedef __attribute__((address_space(3))) char* lds_ptr;
typedef __attribute__((address_space(3))) const char* lds_cptr;

#define DI __device__ __forceinline__

constexpr int SEQ = 2048, DM = 1024, FF = 3584, NH = 32;

DI float bf2f(u16 x) { return __uint_as_float(((unsigned)x) << 16); }
DI float bflo(unsigned x) { return __uint_as_float(x << 16); }
DI float bfhi(unsigned x) { return __uint_as_float(x & 0xffff0000u); }
DI unsigned pack2(float a, float b) {
  f32x2 v = {a, b};
  bf16x2_t r = __builtin_convertvector(v, bf16x2_t);
  return __builtin_bit_cast(unsigned, r);
}
DI float rbf(float a) { return bflo(pack2(a, 0.f) & 0xffffu); }

#define MFMA16(a, b, c) __builtin_amdgcn_mfma_f32_16x16x32_bf16((a), (b), (c), 0, 0, 0)
#define MFMA32(a, b, c) __builtin_amdgcn_mfma_f32_32x32x16_bf16((a), (b), (c), 0, 0, 0)

DI void glds16(const void* base, unsigned voff, unsigned lds_addr) {
  unsigned sv;
  asm volatile("s_mov_b32 %0, m0\n\ts_mov_b32 m0, %3\n\ts_nop 0\n\tglobal_load_lds_dwordx4 %1, %2\n\ts_mov_b32 m0, %0"
               : "=&s"(sv) : "v"(voff), "s"(base), "s"(lds_addr) : "memory");
}
DI unsigned lds_u32(lds_ptr p) { return (unsigned)(uintptr_t)p; }
DI bf16x8 lds_read128(lds_cptr p) { return *(const __attribute__((address_space(3))) bf16x8*)p; }
DI s16x4 lds_tr(lds_cptr p) {
  typedef short v4i16_t __attribute__((ext_vector_type(4)));
  return __builtin_bit_cast(s16x4, __builtin_amdgcn_ds_read_tr16_b64_v4i16((__attribute__((address_space(3))) v4i16_t*)p));
}
DI bf16x8 lds_tr8(lds_cptr p0, lds_cptr p1) {
  s16x4 a = lds_tr(p0), b = lds_tr(p1);
  return __builtin_shufflevector(a, b, 0, 1, 2, 3, 4, 5, 6, 7);
}
#define STAMP_AT(kid, slot) do {} while (0)
template <int N> DI void wait_vm() { asm volatile("s_waitcnt vmcnt(%0)" ::"n"(N) : "memory"); }
DI void bar() { __builtin_amdgcn_s_barrier(); }

__global__ __launch_bounds__(1024) void k_mod(const u16* __restrict__ c, const u16* __restrict__ wmod,
                                               float* __restrict__ mod) {
  __shared__ float s_s[1024];
  __shared__ float s_red[16][64];
  const int t = threadIdx.x;
  {
    float cv = bf2f(c[t]);
    float sg = 1.f / (1.f + __expf(-cv));
    s_s[t] = rbf(cv * sg);
  }
  const int cc = t & 7, rg = t >> 3;
  const u16* wp = wmod + (size_t)rg * (6 * DM) + blockIdx.x * 64 + cc * 8;
  u32x4 w[8];
#pragma unroll
  for (int i = 0; i < 8; ++i) w[i] = *(const u32x4*)(wp + (size_t)i * 128 * (6 * DM));
  __syncthreads();
  float acc[8];
#pragma unroll
  for (int j = 0; j < 8; ++j) acc[j] = 0.f;
#pragma unroll
  for (int i = 0; i < 8; ++i) {
    float s = s_s[rg + 128 * i];
#pragma unroll
    for (int j = 0; j < 4; ++j) {
      acc[2 * j] += s * bflo(w[i][j]);
      acc[2 * j + 1] += s * bfhi(w[i][j]);
    }
  }
#pragma unroll
  for (int j = 0; j < 8; ++j) {
    float v = acc[j];
    v += __shfl_xor(v, 8);
    v += __shfl_xor(v, 16);
    v += __shfl_xor(v, 32);
    acc[j] = v;
  }
  const int lane = t & 63, wv = t >> 6;
  if (lane < 8) {
#pragma unroll
    for (int j = 0; j < 8; ++j) s_red[wv][lane * 8 + j] = acc[j];
  }
  __syncthreads();
  if (t < 64) {
    float v = 0.f;
#pragma unroll
    for (int i = 0; i < 16; ++i) v += s_red[i][t];
    mod[blockIdx.x * 64 + t] = rbf(v);
  }
}

__global__ __launch_bounds__(512) void k_ln(const u16* __restrict__ x, const float* __restrict__ shift,
                                             const float* __restrict__ scale, u16* __restrict__ hblk) {
  const int lane = threadIdx.x & 63, wv = threadIdx.x >> 6;
  const int row = blockIdx.x * 8 + wv;
  const u16* xr = x + (size_t)row * DM;
  u32x4 v0 = *(const u32x4*)(xr + lane * 8);
  u32x4 v1 = *(const u32x4*)(xr + 512 + lane * 8);
  float f[16];
#pragma unroll
  for (int j = 0; j < 4; ++j) {
    f[2 * j] = bflo(v0[j]); f[2 * j + 1] = bfhi(v0[j]);
    f[8 + 2 * j] = bflo(v1[j]); f[8 + 2 * j + 1] = bfhi(v1[j]);
  }
  float s = 0.f;
#pragma unroll
  for (int j = 0; j < 16; ++j) s += f[j];
#pragma unroll
  for (int o = 1; o < 64; o <<= 1) s += __shfl_xor(s, o);
  const float mean = s * (1.f / DM);
  float q = 0.f;
#pragma unroll
  for (int j = 0; j < 16; ++j) { float d = f[j] - mean; q += d * d; }
#pragma unroll
  for (int o = 1; o < 64; o <<= 1) q += __shfl_xor(q, o);
  const float mb = rbf(mean);
  const float vb = rbf(q * (1.f / DM));
  const float den = rbf(sqrtf(rbf(vb + 1e-5f)));
#pragma unroll
  for (int half = 0; half < 2; ++half) {
    const int k0 = half * 512 + lane * 8;
    f32x4 sc0 = *(const f32x4*)(scale + k0), sc1 = *(const f32x4*)(scale + k0 + 4);
    f32x4 sh0 = *(const f32x4*)(shift + k0), sh1 = *(const f32x4*)(shift + k0 + 4);
    float y[8];
#pragma unroll
    for (int j = 0; j < 8; ++j) {
      float sc = j < 4 ? sc0[j] : sc1[j - 4];
      float sh = j < 4 ? sh0[j] : sh1[j - 4];
      float g = rbf(1.f + sc);
      float d = rbf(f[half * 8 + j] - mb);
      float n = rbf(d / den);
      y[j] = rbf(n * g) + sh;
    }
    u32x4 o;
#pragma unroll
    for (int j = 0; j < 4; ++j) o[j] = pack2(y[2 * j], y[2 * j + 1]);
    *(u32x4*)(hblk + ((size_t)(k0 >> 5) * SEQ + row) * 32 + (k0 & 31)) = o;
  }
}

DI int hA(int r) { return (-r) & 3; }

template <int BM, int NGRP, int KS, int NST>
struct GCfg {
  static constexpr int A_SUB = BM * 64;
  static constexpr int W_SUB = NGRP * 4096;
  static constexpr int STAGE = KS * (A_SUB + W_SUB);
  static constexpr int APW = KS * BM / 128;
  static constexpr int WPW = KS * NGRP / 2;
  static constexpr int PPW = APW + WPW;
  static constexpr int LDS = STAGE * NST;
};

template <class C>
struct Loader {
  const char* abase;
  const char* wbase[C::WPW];
  unsigned av[C::APW];
  unsigned wv[C::WPW];
  int aoff[C::APW];
  int woff[C::WPW];
};

template <int BM, int NGRP, int KS, int NST, int MT, int NT, int KSPLIT, int NKT, int KID, int IOFS>
struct Engine {
  typedef GCfg<BM, NGRP, KS, NST> C;
  static constexpr int SPW = KS / KSPLIT;
  static constexpr int P = C::PPW;
  static constexpr int G = NT * SPW;
  f32x4 (&acc)[NT][MT];
  lds_ptr smem;
  Loader<C>& ld;
  long a_adv, w_adv;
  const int (&waddr)[NT];
  int a_sub0, w_sub0;
  int cur, nxt, fre;
  bf16x8 wf[NT], af[2][MT];

  DI Engine(f32x4 (&acc_)[NT][MT], lds_ptr smem_, Loader<C>& ld_, long a_adv_, long w_adv_, int aaddr, const int (&waddr_)[NT], int kg)
      : acc(acc_), smem(smem_), ld(ld_), a_adv(a_adv_), w_adv(w_adv_), waddr(waddr_) {
    a_sub0 = kg * C::A_SUB + aaddr;
    w_sub0 = KS * C::A_SUB + kg * C::W_SUB;
  }
  template <int I> DI void piece(int stage_off) {
    if constexpr (I < C::APW) {
      glds16(ld.abase, ld.av[I], lds_u32(smem) + stage_off + ld.aoff[I]);
      if constexpr (I == C::APW - 1) ld.abase += a_adv * 2;
    } else {
      constexpr int J = I - C::APW;
      glds16(ld.wbase[J], ld.wv[J], lds_u32(smem) + stage_off + ld.woff[J]);
      ld.wbase[J] += w_adv * 2;
    }
  }
  template <int I> DI void issue_all(int stage_off) {
    if constexpr (I < P) { piece<I>(stage_off); issue_all<I + 1>(stage_off); }
  }
  template <int g, int I> DI void issue_at(int stage_off) {
    if constexpr (I < P) {
      constexpr int span = (G - IOFS) > P ? (G - IOFS) : P;
      constexpr int pos = IOFS + (I * span) / P < G ? IOFS + (I * span) / P : G - 1;
      if constexpr (pos == g) piece<I>(stage_off);
      issue_at<g, I + 1>(stage_off);
    }
  }
  template <int PAR, int S, bool PF, bool DOISSUE, int n>
  DI void groups(lds_cptr ab, lds_cptr wb) {
    if constexpr (n < NT) {
#pragma unroll
      for (int m = 0; m < MT; ++m) acc[n][m] = MFMA16(wf[n], af[PAR][m], acc[n][m]);
      if constexpr (PF) {
        wf[n] = lds_tr8(wb + waddr[n], wb + waddr[n] + 512);
#pragma unroll
        for (int m = 0; m < MT; ++m)
          if ((m * NT) / MT == n) af[PAR ^ 1][m] = lds_read128(ab + m * 1024);
      }
      if constexpr (DOISSUE) issue_at<S * NT + n, 0>(fre);
      __builtin_amdgcn_sched_barrier(0);
      groups<PAR, S, PF, DOISSUE, n + 1>(ab, wb);
    }
  }
  template <int PAR, bool DOISSUE, bool LAST, int S> DI void steps() {
    if constexpr (S < SPW) {
      if constexpr (S + 1 < SPW)
        groups<PAR, S, true, DOISSUE, 0>(smem + cur + a_sub0 + (S + 1) * KSPLIT * C::A_SUB, smem + cur + w_sub0 + (S + 1) * KSPLIT * C::W_SUB);
      else if constexpr (!LAST)
        groups<PAR, S, true, DOISSUE, 0>(smem + nxt + a_sub0, smem + nxt + w_sub0);
      else
        groups<PAR, S, false, false, 0>(smem, smem);
      steps<PAR ^ 1, DOISSUE, LAST, S + 1>();
    }
  }
  template <int PAR, bool DOISSUE, bool LAST, int WAITN> DI void iter() {
    if constexpr (!LAST) { wait_vm<WAITN>(); bar(); }
    steps<PAR, DOISSUE, LAST, 0>();
    fre = cur; cur = nxt; nxt = (nxt + C::STAGE == NST * C::STAGE) ? 0 : nxt + C::STAGE;
  }
  template <int KT> DI void tail() {
    if constexpr (KT < NKT) {
      constexpr bool DOISSUE = KT + NST - 1 < NKT;
      constexpr bool LAST = KT == NKT - 1;
      constexpr int newest = (KT + NST - 2) < (NKT - 1) ? (KT + NST - 2) : (NKT - 1);
      constexpr int younger = newest - (KT + 1) > 0 ? newest - (KT + 1) : 0;
      iter<(KT * SPW) & 1, DOISSUE, LAST, P * younger>();
      tail<KT + 1>();
    }
  }
  DI void run() {
    static_assert(NKT >= NST + 1, "too few k tiles");
    STAMP_AT(KID, 0);
#pragma unroll
    for (int s = 0; s < NST - 1; ++s) issue_all<0>(s * C::STAGE);
    wait_vm<P*(NST - 2)>();
    bar();
    STAMP_AT(KID, 1);
    {
      lds_cptr ab = smem + a_sub0;
      lds_cptr wb = smem + w_sub0;
#pragma unroll
      for (int m = 0; m < MT; ++m) af[0][m] = lds_read128(ab + m * 1024);
#pragma unroll
      for (int n = 0; n < NT; ++n) wf[n] = lds_tr8(wb + waddr[n], wb + waddr[n] + 512);
    }
    cur = 0; nxt = C::STAGE; fre = (NST - 1) * C::STAGE;
    constexpr int STEADY = NKT - NST + 1;
    constexpr int LOOPN = (STEADY / 2) * 2;
    for (int kt = 0; kt < LOOPN; kt += 2) {
      iter<0, true, false, P*(NST - 3)>();
      iter<SPW & 1, true, false, P*(NST - 3)>();
    }
    tail<LOOPN>();
    STAMP_AT(KID, 2);
  }
};

template <class C, int BM, int NGRP, int KS>
DI void setup_loader_A(Loader<C>& ld, const u16* Ablk, int m0, int w, int lane) {
#pragma unroll
  for (int i = 0; i < C::APW; ++i) {
    int a = w + 8 * i;
    int sub = a / (BM / 16), j = a % (BM / 16);
    ld.aoff[i] = sub * C::A_SUB + j * 1024;
    int row = j * 16 + (lane >> 2);
    int c = (lane & 3) ^ hA((lane >> 4) & 3);
    ld.av[i] = ((sub * SEQ + m0 + row) * 32 + c * 8) * 2;
  }
  ld.abase = (const char*)Ablk;
}

DI int wfrag_addr(int t, int lane) {
  int g = lane >> 4, q = (lane & 15) >> 2, p = lane & 3;
  int f = ((q >> 1) & 1) | ((g & 1) << 1);
  return (t >> 2) * 4096 + (8 * g + q) * 128 + (((t & 3) ^ f) * 32) + p * 8;
}
DI int afrag_addr(int mrow0, int lane) {
  int r = lane & 15, g = lane >> 4;
  return (mrow0 + r) * 64 + ((g ^ hA((r >> 2) & 3)) * 16);
}

constexpr float QSCALE = 0.17677669529663687f * 1.4426950408889634f;
typedef GCfg<128, 3, 2, 4> CQ;
__global__ __launch_bounds__(512, 2) void k_qkv(const u16* __restrict__ Ablk, const u16* __restrict__ W,
                                                 u16* __restrict__ out) {
  extern __shared__ __attribute__((aligned(16))) char smem_[];
  lds_ptr smem = (lds_ptr)smem_;
  const int lane = threadIdx.x & 63;
  const int w = __builtin_amdgcn_readfirstlane(threadIdx.x >> 6);
  const int xcd = blockIdx.x & 7, idx = blockIdx.x >> 3;
  const int tm = (xcd & 1) * 8 + (idx & 7), tn = (xcd >> 1) * 4 + (idx >> 3);
  const int m0 = tm * 128, n0 = tn * 192;
  constexpr int LDW = 3 * DM;
  Loader<CQ> ld;
  setup_loader_A<CQ, 128, 3, 2>(ld, Ablk, m0, w, lane);
#pragma unroll
  for (int i = 0; i < CQ::WPW; ++i) {
    int b = w + 8 * i;
    int sub = b / 12, r = b % 12, grp = r >> 2, o = r & 3;
    ld.woff[i] = 2 * CQ::A_SUB + sub * CQ::W_SUB + grp * 4096 + o * 1024;
    int k = sub * 32 + 8 * o + (lane >> 3);
    int f = ((lane >> 4) & 1) | ((o & 1) << 1);
    int col = n0 + grp * 64 + ((((lane & 7) >> 1) ^ f) * 16) + (lane & 1) * 8;
    ld.wv[i] = (k * LDW + col) * 2;
    ld.wbase[i] = (const char*)W;
  }
  const int wm = w & 1, wn = w >> 1;
  int waddr[3];
#pragma unroll
  for (int n = 0; n < 3; ++n) waddr[n] = wfrag_addr(wn * 3 + n, lane);
  const int aaddr = afrag_addr(wm * 64, lane);
  f32x4 acc[3][4];
#pragma unroll
  for (int n = 0; n < 3; ++n)
#pragma unroll
    for (int m = 0; m < 4; ++m) acc[n][m] = f32x4{0.f, 0.f, 0.f, 0.f};
  Engine<128, 3, 2, 4, 4, 3, 1, DM / 64, 1, 0> eng(acc, smem, ld, (long)2 * SEQ * 32, (long)64 * LDW, aaddr, waddr, 0);
  eng.run();
  const int g = lane >> 4;
#pragma unroll
  for (int n = 0; n < 3; ++n) {
    int col = n0 + (wn * 3 + n) * 16 + 4 * g;
    float sc = col < DM ? QSCALE : 1.f;
#pragma unroll
    for (int m = 0; m < 4; ++m) {
      int row = m0 + wm * 64 + m * 16 + (lane & 15);
      u32x2 o;
      o[0] = pack2(acc[n][m][0] * sc, acc[n][m][1] * sc);
      o[1] = pack2(acc[n][m][2] * sc, acc[n][m][3] * sc);
      *(u32x2*)(out + ((size_t)(col >> 5) * SEQ + row) * 32 + (col & 31)) = o;
    }
  }
  STAMP_AT(1, 3);
}

typedef GCfg<128, 1, 2, 6> CP;
template <int KDIM>
__global__ __launch_bounds__(512, 2) void k_proj(const u16* __restrict__ Ablk, const u16* __restrict__ W,
                                                  const u16* __restrict__ res, const float* __restrict__ gate,
                                                  u16* __restrict__ out) {
  extern __shared__ __attribute__((aligned(16))) char smem_[];
  lds_ptr smem = (lds_ptr)smem_;
  const int lane = threadIdx.x & 63;
  const int w = __builtin_amdgcn_readfirstlane(threadIdx.x >> 6);
  const int xcd = blockIdx.x & 7, idx = blockIdx.x >> 3;
  const int tm = (xcd & 3) * 4 + (idx & 3), tn = (xcd >> 2) * 8 + (idx >> 2);
  const int m0 = tm * 128, n0 = tn * 64;
  constexpr int LDW = DM;
  Loader<CP> ld;
  setup_loader_A<CP, 128, 1, 2>(ld, Ablk, m0, w, lane);
  {
    int b = w;
    int sub = b >> 2, o = b & 3;
    ld.woff[0] = 2 * CP::A_SUB + sub * CP::W_SUB + o * 1024;
    int k = sub * 32 + 8 * o + (lane >> 3);
    int f = ((lane >> 4) & 1) | ((o & 1) << 1);
    int col = n0 + ((((lane & 7) >> 1) ^ f) * 16) + (lane & 1) * 8;
    ld.wv[0] = (k * LDW + col) * 2;
    ld.wbase[0] = (const char*)W;
  }
  const int wm = w & 1, wn = (w >> 1) & 1, kg = w >> 2;
  int waddr[2];
#pragma unroll
  for (int n = 0; n < 2; ++n) waddr[n] = wfrag_addr(wn * 2 + n, lane);
  const int aaddr = afrag_addr(wm * 64, lane);
  f32x4 acc[2][4];
#pragma unroll
  for (int n = 0; n < 2; ++n)
#pragma unroll
    for (int m = 0; m < 4; ++m) acc[n][m] = f32x4{0.f, 0.f, 0.f, 0.f};
  Engine<128, 1, 2, 6, 4, 2, 2, KDIM / 64, (KDIM == DM ? 3 : 5), 0> eng(acc, smem, ld, (long)2 * SEQ * 32, (long)64 * LDW, aaddr, waddr, kg);
  eng.run();
  __syncthreads();
  __attribute__((address_space(3))) f32x4* red = (__attribute__((address_space(3))) f32x4*)smem;
  if (kg == 1) {
#pragma unroll
    for (int n = 0; n < 2; ++n)
#pragma unroll
      for (int m = 0; m < 4; ++m) red[((w & 3) * 8 + n * 4 + m) * 64 + lane] = acc[n][m];
  }
  __syncthreads();
  if (kg == 0) {
    const int g = lane >> 4;
#pragma unroll
    for (int n = 0; n < 2; ++n) {
      int col = n0 + (wn * 2 + n) * 16 + 4 * g;
      f32x4 gt = *(const f32x4*)(gate + col);
#pragma unroll
      for (int m = 0; m < 4; ++m) {
        f32x4 a = acc[n][m] + red[((w & 3) * 8 + n * 4 + m) * 64 + lane];
        int row = m0 + wm * 64 + m * 16 + (lane & 15);
        u32x2 r = *(const u32x2*)(res + (size_t)row * DM + col);
        float y0 = bflo(r[0]) + rbf(gt[0] * rbf(a[0]));
        float y1 = bfhi(r[0]) + rbf(gt[1] * rbf(a[1]));
        float y2 = bflo(r[1]) + rbf(gt[2] * rbf(a[2]));
        float y3 = bfhi(r[1]) + rbf(gt[3] * rbf(a[3]));
        u32x2 o;
        o[0] = pack2(y0, y1);
        o[1] = pack2(y2, y3);
        *(u32x2*)(out + (size_t)row * DM + col) = o;
      }
    }
  }
  STAMP_AT((KDIM == DM ? 3 : 5), 3);
}

typedef GCfg<256, 4, 1, 4> CG;
template <int NH_>
DI void glu_wave(lds_ptr smem, Loader<CG>& ld, int lane, int wm, int t0, int m0, int f0, u16* __restrict__ out) {
  constexpr int NT = 2 * NH_;
  int waddr[NT];
#pragma unroll
  for (int n = 0; n < NH_; ++n) {
    waddr[n] = wfrag_addr(t0 + n, lane);
    waddr[NH_ + n] = waddr[n] + 8192;
  }
  const int aaddr = afrag_addr(wm * 64, lane);
  f32x4 acc[NT][4];
#pragma unroll
  for (int n = 0; n < NT; ++n)
#pragma unroll
    for (int m = 0; m < 4; ++m) acc[n][m] = f32x4{0.f, 0.f, 0.f, 0.f};
  Engine<256, 4, 1, 4, 4, NT, 1, DM / 32, 4, (NH_ == 4 ? 0 : 1)> eng(acc, smem, ld, (long)SEQ * 32, (long)32 * FF, aaddr, waddr, 0);
  eng.run();
  const int g = lane >> 4;
#pragma unroll
  for (int n = 0; n < NH_; ++n) {
    int col = f0 + (t0 + n) * 16 + 4 * g;
#pragma unroll
    for (int m = 0; m < 4; ++m) {
      int row = m0 + wm * 64 + m * 16 + (lane & 15);
      f32x4 a = acc[n][m], b = acc[NH_ + n][m];
      u32x2 o;
      o[0] = pack2(rbf(a[0]) * rbf(b[0]), rbf(a[1]) * rbf(b[1]));
      o[1] = pack2(rbf(a[2]) * rbf(b[2]), rbf(a[3]) * rbf(b[3]));
      *(u32x2*)(out + ((size_t)(col >> 5) * SEQ + row) * 32 + (col & 31)) = o;
    }
  }
  STAMP_AT(4, 3);
}

__global__ __launch_bounds__(512, 2) void k_glu(const u16* __restrict__ Ablk, const u16* __restrict__ Wg,
                                                 const u16* __restrict__ Wu, u16* __restrict__ out) {
  extern __shared__ __attribute__((aligned(16))) char smem_[];
  lds_ptr smem = (lds_ptr)smem_;
  const int lane = threadIdx.x & 63;
  const int w = __builtin_amdgcn_readfirstlane(threadIdx.x >> 6);
  const int xcd = blockIdx.x & 7, idx = blockIdx.x >> 3;
  const int tm = idx & 7, tn = xcd * 4 + (idx >> 3);
  const int m0 = tm * 256, f0 = tn * 112;
  Loader<CG> ld;
  setup_loader_A<CG, 256, 4, 1>(ld, Ablk, m0, w, lane);
#pragma unroll
  for (int i = 0; i < CG::WPW; ++i) {
    int b = w + 8 * i;
    int grp = b >> 2, o = b & 3;
    ld.woff[i] = CG::A_SUB + grp * 4096 + o * 1024;
    int k = 8 * o + (lane >> 3);
    int f = ((lane >> 4) & 1) | ((o & 1) << 1);
    int pr = ((lane & 7) >> 1) ^ f;
    if ((grp & 1) && pr == 3) pr = 0;
    int col = f0 + (grp & 1) * 64 + pr * 16 + (lane & 1) * 8;
    ld.wv[i] = (k * FF + col) * 2;
    ld.wbase[i] = (const char*)(grp >> 1 ? Wu : Wg);
  }
  const int wm = w & 3;
  if (w < 4) glu_wave<4>(smem, ld, lane, wm, 0, m0, f0, out);
  else glu_wave<3>(smem, ld, lane, wm, 4, m0, f0, out);
}

constexpr int ACH = 128, ANST = 4, ASTAGE = ACH * 64 * 2;
__global__ __launch_bounds__(512, 2) void k_attn(const u16* __restrict__ qkv, u16* __restrict__ oblk) {
  extern __shared__ __attribute__((aligned(16))) char smem_[];
  lds_ptr smem = (lds_ptr)smem_;
  const int lane = threadIdx.x & 63;
  const int w = __builtin_amdgcn_readfirstlane(threadIdx.x >> 6);
  const int xcd = blockIdx.x & 7, idx = blockIdx.x >> 3;
  const int head = xcd * 4 + (idx >> 3), qb = idx & 7;
  const int r32 = lane & 31, hh = lane >> 5;
  const u16* Qp = qkv + (size_t)head * SEQ * 32;
  const u16* Kp = qkv + (size_t)(32 + head) * SEQ * 32;
  const u16* Vp = qkv + (size_t)(64 + head) * SEQ * 32;
  const int q0 = qb * 256 + w * 32;
  const unsigned kvo = ((16 * w + (lane >> 2)) * 32 + (((lane & 3) ^ ((lane >> 4) & 3)) * 8)) * 2;
  const unsigned vvo = ((16 * w) * 32 + lane * 8) * 2;
  const char* ksrc = (const char*)Kp;
  const char* vsrc = (const char*)Vp;
  auto issue = [&](int buf) {
    glds16(ksrc, kvo, lds_u32(smem) + buf * ASTAGE + w * 1024);
    glds16(vsrc, vvo, lds_u32(smem) + buf * ASTAGE + 8192 + w * 1024);
    ksrc += ACH * 64; vsrc += ACH * 64;
  };
  STAMP_AT(2, 0);
  issue(0); issue(1); issue(2);
  bf16x8 qf0 = *(const bf16x8*)(Qp + (q0 + r32) * 32 + hh * 8);
  bf16x8 qf1 = *(const bf16x8*)(Qp + (q0 + r32) * 32 + 16 + hh * 8);
  const int kaddr0 = r32 * 64 + (((0 + hh) ^ ((r32 >> 2) & 3)) * 16);
  const int kaddr1 = r32 * 64 + (((2 + hh) ^ ((r32 >> 2) & 3)) * 16);
  const int vaddr = 8192 + (4 * hh + ((lane & 15) >> 2)) * 64 + ((lane >> 4) & 1) * 32 + (lane & 3) * 8;
  f32x16 o, negm;
#pragma unroll
  for (int r = 0; r < 16; ++r) { o[r] = 0.f; negm[r] = 0.f; }
  f32x2 l2 = {0.f, 0.f};
  constexpr int NCH = SEQ / ACH;
  auto rowmax = [&](const f32x16& v) {
    float mx = fmaxf(fmaxf(v[0], v[1]), v[2]);
#pragma unroll
    for (int r = 3; r < 15; r += 2) mx = fmaxf(fmaxf(mx, v[r]), v[r + 1]);
    mx = fmaxf(mx, v[15]);
    auto sw = __builtin_amdgcn_permlane32_swap(__float_as_uint(mx), __float_as_uint(mx), false, false);
    return fmaxf(__uint_as_float(sw[0]), __uint_as_float(sw[1]));
  };
  wait_vm<4>();
  bar();
  f32x16 sc;
  {
    bf16x8 kf0 = lds_read128(smem + kaddr0);
    bf16x8 kf1 = lds_read128(smem + kaddr1);
    f32x16 s = MFMA32(kf0, qf0, negm);
    s = MFMA32(kf1, qf1, s);
    const float mx = rowmax(s);
#pragma unroll
    for (int r = 0; r < 16; ++r) { negm[r] = -mx; sc[r] = s[r] - mx; }
  }
  auto tile = [&](lds_cptr vb, lds_cptr knext, bool has_next) {
    f32x16 sn;
    if (has_next) {
      bf16x8 kf0 = lds_read128(knext + kaddr0);
      bf16x8 kf1 = lds_read128(knext + kaddr1);
      sn = MFMA32(kf0, qf0, negm);
      sn = MFMA32(kf1, qf1, sn);
    }
    bf16x8 vf0 = lds_tr8(vb, vb + 512);
    bf16x8 vf1 = lds_tr8(vb + 1024, vb + 1536);
    f32x16 p;
#pragma unroll
    for (int r = 0; r < 16; ++r) p[r] = __builtin_amdgcn_exp2f(sc[r]);
    f32x2 t2 = f32x2{p[0], p[1]} + f32x2{p[2], p[3]};
#pragma unroll
    for (int r = 4; r < 16; r += 2) t2 += f32x2{p[r], p[r + 1]};
    if (__builtin_expect(__any(!(t2[0] + t2[1] <= 16777216.f)), 0)) {
      const float dl = fmaxf(rowmax(sc), 0.f);
      const float f = __builtin_amdgcn_exp2f(-dl);
#pragma unroll
      for (int r = 0; r < 16; ++r) {
        negm[r] -= dl; o[r] *= f; sn[r] -= dl;
        p[r] = __builtin_amdgcn_exp2f(sc[r] - dl);
      }
      l2 *= f;
      t2 = f32x2{p[0], p[1]} + f32x2{p[2], p[3]};
#pragma unroll
      for (int r = 4; r < 16; r += 2) t2 += f32x2{p[r], p[r + 1]};
    }
    l2 += t2;
    u32x4 pa, pb;
#pragma unroll
    for (int j = 0; j < 4; ++j) { pa[j] = pack2(p[2 * j], p[2 * j + 1]); pb[j] = pack2(p[8 + 2 * j], p[8 + 2 * j + 1]); }
    o = MFMA32(vf0, __builtin_bit_cast(bf16x8, pa), o);
    o = MFMA32(vf1, __builtin_bit_cast(bf16x8, pb), o);
    sc = sn;
  };
  for (int ch = 0; ch < NCH; ++ch) {
    if (ch + 2 < NCH) wait_vm<2>(); else wait_vm<0>();
    bar();
    if (ch + 3 < NCH) issue((ch + 3) & 3);
    lds_cptr sb = smem + (ch & 3) * ASTAGE;
    lds_cptr nb = smem + ((ch + 1) & 3) * ASTAGE;
    tile(sb + vaddr, sb + 2048, true);
    tile(sb + 2048 + vaddr, sb + 4096, true);
    tile(sb + 4096 + vaddr, sb + 6144, true);
    tile(sb + 6144 + vaddr, nb, ch + 1 < NCH);
  }
  float l = l2[0] + l2[1];
  STAMP_AT(2, 2);
  auto sw = __builtin_amdgcn_permlane32_swap(__float_as_uint(l), __float_as_uint(l), false, false);
  const float rl = 1.f / (__uint_as_float(sw[0]) + __uint_as_float(sw[1]));
  u16* op = oblk + ((size_t)head * SEQ + q0 + r32) * 32 + 4 * hh;
#pragma unroll
  for (int g = 0; g < 4; ++g) {
    u32x2 v;
    v[0] = pack2(o[4 * g] * rl, o[4 * g + 1] * rl);
    v[1] = pack2(o[4 * g + 2] * rl, o[4 * g + 3] * rl);
    *(u32x2*)(op + 8 * g) = v;
  }
  STAMP_AT(2, 3);
}

extern "C" void kernel_launch(void* const* d_in, const int* in_sizes, int n_in, void* d_out, int out_size,
                              void* d_ws, size_t ws_size, hipStream_t stream) {
  const u16* x = (const u16*)d_in[0];
  const u16* wqkv = (const u16*)d_in[1];
  const u16* wo = (const u16*)d_in[2];
  const u16* wg = (const u16*)d_in[4];
  const u16* wu = (const u16*)d_in[5];
  const u16* wd = (const u16*)d_in[6];
  const u16* c = (const u16*)d_in[7];
  const u16* wmod = (const u16*)d_in[8];
  u16* out = (u16*)d_out;
  char* ws = (char*)d_ws;
  float* mod = (float*)ws;
  u16* h1 = (u16*)(ws + (1 << 20));
  u16* qkv = (u16*)(ws + (8 << 20));
  u16* ob = (u16*)(ws + (24 << 20));
  u16* x2 = (u16*)(ws + (32 << 20));
  u16* h2 = (u16*)(ws + (40 << 20));
  u16* hh = (u16*)(ws + (48 << 20));

  hipFuncSetAttribute((const void*)k_qkv, hipFuncAttributeMaxDynamicSharedMemorySize, CQ::LDS);
  hipFuncSetAttribute((const void*)k_proj<DM>, hipFuncAttributeMaxDynamicSharedMemorySize, CP::LDS);
  hipFuncSetAttribute((const void*)k_proj<FF>, hipFuncAttributeMaxDynamicSharedMemorySize, CP::LDS);
  hipFuncSetAttribute((const void*)k_glu, hipFuncAttributeMaxDynamicSharedMemorySize, CG::LDS);
  hipFuncSetAttribute((const void*)k_attn, hipFuncAttributeMaxDynamicSharedMemorySize, ANST * ASTAGE);

  k_mod<<<96, 1024, 0, stream>>>(c, wmod, mod);
  k_ln<<<256, 512, 0, stream>>>(x, mod + 0 * DM, mod + 1 * DM, h1);
  k_qkv<<<256, 512, CQ::LDS, stream>>>(h1, wqkv, qkv);
  k_attn<<<256, 512, ANST * ASTAGE, stream>>>(qkv, ob);
  k_proj<DM><<<256, 512, CP::LDS, stream>>>(ob, wo, x, mod + 2 * DM, x2);
  k_ln<<<256, 512, 0, stream>>>(x2, mod + 3 * DM, mod + 4 * DM, h2);
  k_glu<<<256, 512, CG::LDS, stream>>>(h2, wg, wu, hh);
  k_proj<FF><<<256, 512, CP::LDS, stream>>>(hh, wd, x2, mod + 5 * DM, out);
}
```

```cpp
#include <hip/hip_runtime.h>
#include <hip/hip_bf16.h>
#include <stdint.h>

typedef __attribute__((ext_vector_type(8))) short bf16x8;
typedef __attribute__((ext_vector_type(4))) short s16x4;
typedef __attribute__((ext_vector_type(4))) float f32x4;
typedef __attribute__((ext_vector_type(2))) float f32x2;
typedef __attribute__((ext_vector_type(16))) float f32x16;
typedef __attribute__((ext_vector_type(4))) unsigned u32x4;
typedef __attribute__((ext_vector_type(2))) unsigned u32x2;
typedef __attribute__((ext_vector_type(2))) __bf16 bf16x2_t;
typedef unsigned short u16;
typedef __attribute__((address_space(3))) char* lds_ptr;
typedef __attribute__((address_space(3))) const char* lds_cptr;

#define DI __device__ __forceinline__

constexpr int SEQ = 2048, DM = 1024, FF = 3584, NH = 32;

DI float bf2f(u16 x) { return __uint_as_float(((unsigned)x) << 16); }
DI float bflo(unsigned x) { return __uint_as_float(x << 16); }
DI float bfhi(unsigned x) { return __uint_as_float(x & 0xffff0000u); }
DI unsigned pack2(float a, float b) {
  f32x2 v = {a, b};
  bf16x2_t r = __builtin_convertvector(v, bf16x2_t);
  return __builtin_bit_cast(unsigned, r);
}
DI float rbf(float a) { return bflo(pack2(a, 0.f) & 0xffffu); }

#define MFMA16(a, b, c) __builtin_amdgcn_mfma_f32_16x16x32_bf16((a), (b), (c), 0, 0, 0)
#define MFMA32(a, b, c) __builtin_amdgcn_mfma_f32_32x32x16_bf16((a), (b), (c), 0, 0, 0)

DI void glds16(const void* base, unsigned voff, unsigned lds_addr) {
  unsigned sv;
  asm volatile("s_mov_b32 %0, m0\n\ts_mov_b32 m0, %3\n\ts_nop 0\n\tglobal_load_lds_dwordx4 %1, %2\n\ts_mov_b32 m0, %0"
               : "=&s"(sv) : "v"(voff), "s"(base), "s"(lds_addr) : "memory");
}
DI unsigned lds_u32(lds_ptr p) { return (unsigned)(uintptr_t)p; }
DI bf16x8 lds_read128(lds_cptr p) { return *(const __attribute__((address_space(3))) bf16x8*)p; }
DI s16x4 lds_tr(lds_cptr p) {
  typedef short v4i16_t __attribute__((ext_vector_type(4)));
  return __builtin_bit_cast(s16x4, __builtin_amdgcn_ds_read_tr16_b64_v4i16((__attribute__((address_space(3))) v4i16_t*)p));
}
DI bf16x8 lds_tr8(lds_cptr p0, lds_cptr p1) {
  s16x4 a = lds_tr(p0), b = lds_tr(p1);
  return __builtin_shufflevector(a, b, 0, 1, 2, 3, 4, 5, 6, 7);
}
#define STAMP_AT(kid, slot) do {} while (0)
template <int N> DI void wait_vm() { asm volatile("s_waitcnt vmcnt(%0)" ::"n"(N) : "memory"); }
DI void bar() { __builtin_amdgcn_s_barrier(); }

__global__ __launch_bounds__(1024) void k_mod(const u16* __restrict__ c, const u16* __restrict__ wmod,
                                               float* __restrict__ mod) {
  __shared__ float s_s[1024];
  __shared__ float s_red[16][64];
  const int t = threadIdx.x;
  {
    float cv = bf2f(c[t]);
    float sg = 1.f / (1.f + __expf(-cv));
    s_s[t] = rbf(cv * sg);
  }
  const int cc = t & 7, rg = t >> 3;
  const u16* wp = wmod + (size_t)rg * (6 * DM) + blockIdx.x * 64 + cc * 8;
  u32x4 w[8];
#pragma unroll
  for (int i = 0; i < 8; ++i) w[i] = *(const u32x4*)(wp + (size_t)i * 128 * (6 * DM));
  __syncthreads();
  float acc[8];
#pragma unroll
  for (int j = 0; j < 8; ++j) acc[j] = 0.f;
#pragma unroll
  for (int i = 0; i < 8; ++i) {
    float s = s_s[rg + 128 * i];
#pragma unroll
    for (int j = 0; j < 4; ++j) {
      acc[2 * j] += s * bflo(w[i][j]);
      acc[2 * j + 1] += s * bfhi(w[i][j]);
    }
  }
#pragma unroll
  for (int j = 0; j < 8; ++j) {
    float v = acc[j];
    v += __shfl_xor(v, 8);
    v += __shfl_xor(v, 16);
    v += __shfl_xor(v, 32);
    acc[j] = v;
  }
  const int lane = t & 63, wv = t >> 6;
  if (lane < 8) {
#pragma unroll
    for (int j = 0; j < 8; ++j) s_red[wv][lane * 8 + j] = acc[j];
  }
  __syncthreads();
  if (t < 64) {
    float v = 0.f;
#pragma unroll
    for (int i = 0; i < 16; ++i) v += s_red[i][t];
    mod[blockIdx.x * 64 + t] = rbf(v);
  }
}

__global__ __launch_bounds__(512) void k_ln(const u16* __restrict__ x, const float* __restrict__ shift,
                                             const float* __restrict__ scale, u16* __restrict__ hblk) {
  const int lane = threadIdx.x & 63, wv = threadIdx.x >> 6;
  const int row = blockIdx.x * 8 + wv;
  const u16* xr = x + (size_t)row * DM;
  u32x4 v0 = *(const u32x4*)(xr + lane * 8);
  u32x4 v1 = *(const u32x4*)(xr + 512 + lane * 8);
  float f[16];
#pragma unroll
  for (int j = 0; j < 4; ++j) {
    f[2 * j] = bflo(v0[j]); f[2 * j + 1] = bfhi(v0[j]);
    f[8 + 2 * j] = bflo(v1[j]); f[8 + 2 * j + 1] = bfhi(v1[j]);
  }
  float s = 0.f;
#pragma unroll
  for (int j = 0; j < 16; ++j) s += f[j];
#pragma unroll
  for (int o = 1; o < 64; o <<= 1) s += __shfl_xor(s, o);
  const float mean = s * (1.f / DM);
  float q = 0.f;
#pragma unroll
  for (int j = 0; j < 16; ++j) { float d = f[j] - mean; q += d * d; }
#pragma unroll
  for (int o = 1; o < 64; o <<= 1) q += __shfl_xor(q, o);
  const float mb = rbf(mean);
  const float vb = rbf(q * (1.f / DM));
  const float den = rbf(sqrtf(rbf(vb + 1e-5f)));
#pragma unroll
  for (int half = 0; half < 2; ++half) {
    const int k0 = half * 512 + lane * 8;
    f32x4 sc0 = *(const f32x4*)(scale + k0), sc1 = *(const f32x4*)(scale + k0 + 4);
    f32x4 sh0 = *(const f32x4*)(shift + k0), sh1 = *(const f32x4*)(shift + k0 + 4);
    float y[8];
#pragma unroll
    for (int j = 0; j < 8; ++j) {
      float sc = j < 4 ? sc0[j] : sc1[j - 4];
      float sh = j < 4 ? sh0[j] : sh1[j - 4];
      float g = rbf(1.f + sc);
      float d = rbf(f[half * 8 + j] - mb);
      float n = rbf(d / den);
      y[j] = rbf(n * g) + sh;
    }
    u32x4 o;
#pragma unroll
    for (int j = 0; j < 4; ++j) o[j] = pack2(y[2 * j], y[2 * j + 1]);
    *(u32x4*)(hblk + ((size_t)(k0 >> 5) * SEQ + row) * 32 + (k0 & 31)) = o;
  }
}

DI int hA(int r) { return (-r) & 3; }

template <int BM, int NGRP, int KS, int NST>
struct GCfg {
  static constexpr int A_SUB = BM * 64;
  static constexpr int W_SUB = NGRP * 4096;
  static constexpr int STAGE = KS * (A_SUB + W_SUB);
  static constexpr int APW = KS * BM / 128;
  static constexpr int WPW = KS * NGRP / 2;
  static constexpr int PPW = APW + WPW;
  static constexpr int LDS = STAGE * NST;
};

template <class C>
struct Loader {
  const char* abase;
  const char* wbase[C::WPW];
  unsigned av[C::APW];
  unsigned wv[C::WPW];
  int aoff[C::APW];
  int woff[C::WPW];
};

template <int BM, int NGRP, int KS, int NST, int MT, int NT, int KSPLIT, int NKT, int KID, int IOFS>
struct Engine {
  typedef GCfg<BM, NGRP, KS, NST> C;
  static constexpr int SPW = KS / KSPLIT;
  static constexpr int P = C::PPW;
  static constexpr int G = NT * SPW;
  f32x4 (&acc)[NT][MT];
  lds_ptr smem;
  Loader<C>& ld;
  long a_adv, w_adv;
  const int (&waddr)[NT];
  int a_sub0, w_sub0;
  int cur, nxt, fre;
  bf16x8 wf[NT], af[2][MT];

  DI Engine(f32x4 (&acc_)[NT][MT], lds_ptr smem_, Loader<C>& ld_, long a_adv_, long w_adv_, int aaddr, const int (&waddr_)[NT], int kg)
      : acc(acc_), smem(smem_), ld(ld_), a_adv(a_adv_), w_adv(w_adv_), waddr(waddr_) {
    a_sub0 = kg * C::A_SUB + aaddr;
    w_sub0 = KS * C::A_SUB + kg * C::W_SUB;
  }
  template <int I> DI void piece(int stage_off) {
    if constexpr (I < C::APW) {
      glds16(ld.abase, ld.av[I], lds_u32(smem) + stage_off + ld.aoff[I]);
      if constexpr (I == C::APW - 1) ld.abase += a_adv * 2;
    } else {
      constexpr int J = I - C::APW;
      glds16(ld.wbase[J], ld.wv[J], lds_u32(smem) + stage_off + ld.woff[J]);
      ld.wbase[J] += w_adv * 2;
    }
  }
  template <int I> DI void issue_all(int stage_off) {
    if constexpr (I < P) { piece<I>(stage_off); issue_all<I + 1>(stage_off); }
  }
  template <int g, int I> DI void issue_at(int stage_off) {
    if constexpr (I < P) {
      constexpr int span = (G - IOFS) > P ? (G - IOFS) : P;
      constexpr int pos = IOFS + (I * span) / P < G ? IOFS + (I * span) / P : G - 1;
      if constexpr (pos == g) piece<I>(stage_off);
      issue_at<g, I + 1>(stage_off);
    }
  }
  template <int PAR, int S, bool PF, bool DOISSUE, int n>
  DI void groups(lds_cptr ab, lds_cptr wb) {
    if constexpr (n < NT) {
#pragma unroll
      for (int m = 0; m < MT; ++m) acc[n][m] = MFMA16(wf[n], af[PAR][m], acc[n][m]);
      if constexpr (PF) {
        wf[n] = lds_tr8(wb + waddr[n], wb + waddr[n] + 512);
#pragma unroll
        for (int m = 0; m < MT; ++m)
          if ((m * NT) / MT == n) af[PAR ^ 1][m] = lds_read128(ab + m * 1024);
      }
      if constexpr (DOISSUE) issue_at<S * NT + n, 0>(fre);
      __builtin_amdgcn_sched_barrier(0);
      groups<PAR, S, PF, DOISSUE, n + 1>(ab, wb);
    }
  }
  template <int PAR, bool DOISSUE, bool LAST, int S> DI void steps() {
    if constexpr (S < SPW) {
      if constexpr (S + 1 < SPW)
        groups<PAR, S, true, DOISSUE, 0>(smem + cur + a_sub0 + (S + 1) * KSPLIT * C::A_SUB, smem + cur + w_sub0 + (S + 1) * KSPLIT * C::W_SUB);
      else if constexpr (!LAST)
        groups<PAR, S, true, DOISSUE, 0>(smem + nxt + a_sub0, smem + nxt + w_sub0);
      else
        groups<PAR, S, false, false, 0>(smem, smem);
      steps<PAR ^ 1, DOISSUE, LAST, S + 1>();
    }
  }
  template <int PAR, bool DOISSUE, bool LAST, int WAITN> DI void iter() {
    if constexpr (!LAST) { wait_vm<WAITN>(); bar(); }
    steps<PAR, DOISSUE, LAST, 0>();
    fre = cur; cur = nxt; nxt = (nxt + C::STAGE == NST * C::STAGE) ? 0 : nxt + C::STAGE;
  }
  template <int KT> DI void tail() {
    if constexpr (KT < NKT) {
      constexpr bool DOISSUE = KT + NST - 1 < NKT;
      constexpr bool LAST = KT == NKT - 1;
      constexpr int newest = (KT + NST - 2) < (NKT - 1) ? (KT + NST - 2) : (NKT - 1);
      constexpr int younger = newest - (KT + 1) > 0 ? newest - (KT + 1) : 0;
      iter<(KT * SPW) & 1, DOISSUE, LAST, P * younger>();
      tail<KT + 1>();
    }
  }
  DI void run() {
    static_assert(NKT >= NST + 1, "too few k tiles");
    STAMP_AT(KID, 0);
#pragma unroll
    for (int s = 0; s < NST - 1; ++s) issue_all<0>(s * C::STAGE);
    wait_vm<P*(NST - 2)>();
    bar();
    STAMP_AT(KID, 1);
    {
      lds_cptr ab = smem + a_sub0;
      lds_cptr wb = smem + w_sub0;
#pragma unroll
      for (int m = 0; m < MT; ++m) af[0][m] = lds_read128(ab + m * 1024);
#pragma unroll
      for (int n = 0; n < NT; ++n) wf[n] = lds_tr8(wb + waddr[n], wb + waddr[n] + 512);
    }
    cur = 0; nxt = C::STAGE; fre = (NST - 1) * C::STAGE;
    constexpr int STEADY = NKT - NST + 1;
    constexpr int LOOPN = (STEADY / 2) * 2;
    for (int kt = 0; kt < LOOPN; kt += 2) {
      iter<0, true, false, P*(NST - 3)>();
      iter<SPW & 1, true, false, P*(NST - 3)>();
    }
    tail<LOOPN>();
    STAMP_AT(KID, 2);
  }
};

template <class C, int BM, int NGRP, int KS>
DI void setup_loader_A(Loader<C>& ld, const u16* Ablk, int m0, int w, int lane) {
#pragma unroll
  for (int i = 0; i < C::APW; ++i) {
    int a = w + 8 * i;
    int sub = a / (BM / 16), j = a % (BM / 16);
    ld.aoff[i] = sub * C::A_SUB + j * 1024;
    int row = j * 16 + (lane >> 2);
    int c = (lane & 3) ^ hA((lane >> 4) & 3);
    ld.av[i] = ((sub * SEQ + m0 + row) * 32 + c * 8) * 2;
  }
  ld.abase = (const char*)Ablk;
}

DI int wfrag_addr(int t, int lane) {
  int g = lane >> 4, q = (lane & 15) >> 2, p = lane & 3;
  int f = ((q >> 1) & 1) | ((g & 1) << 1);
  return (t >> 2) * 4096 + (8 * g + q) * 128 + (((t & 3) ^ f) * 32) + p * 8;
}
DI int afrag_addr(int mrow0, int lane) {
  int r = lane & 15, g = lane >> 4;
  return (mrow0 + r) * 64 + ((g ^ hA((r >> 2) & 3)) * 16);
}

constexpr float QSCALE = 0.17677669529663687f * 1.4426950408889634f;
typedef GCfg<128, 3, 2, 4> CQ;
__global__ __launch_bounds__(512, 2) void k_qkv(const u16* __restrict__ Ablk, const u16* __restrict__ W,
                                                 u16* __restrict__ out) {
  extern __shared__ __attribute__((aligned(16))) char smem_[];
  lds_ptr smem = (lds_ptr)smem_;
  const int lane = threadIdx.x & 63;
  const int w = __builtin_amdgcn_readfirstlane(threadIdx.x >> 6);
  const int xcd = blockIdx.x & 7, idx = blockIdx.x >> 3;
  const int tm = (xcd & 1) * 8 + (idx & 7), tn = (xcd >> 1) * 4 + (idx >> 3);
  const int m0 = tm * 128, n0 = tn * 192;
  constexpr int LDW = 3 * DM;
  Loader<CQ> ld;
  setup_loader_A<CQ, 128, 3, 2>(ld, Ablk, m0, w, lane);
#pragma unroll
  for (int i = 0; i < CQ::WPW; ++i) {
    int b = w + 8 * i;
    int sub = b / 12, r = b % 12, grp = r >> 2, o = r & 3;
    ld.woff[i] = 2 * CQ::A_SUB + sub * CQ::W_SUB + grp * 4096 + o * 1024;
    int k = sub * 32 + 8 * o + (lane >> 3);
    int f = ((lane >> 4) & 1) | ((o & 1) << 1);
    int col = n0 + grp * 64 + ((((lane & 7) >> 1) ^ f) * 16) + (lane & 1) * 8;
    ld.wv[i] = (k * LDW + col) * 2;
    ld.wbase[i] = (const char*)W;
  }
  const int wm = w & 1, wn = w >> 1;
  int waddr[3];
#pragma unroll
  for (int n = 0; n < 3; ++n) waddr[n] = wfrag_addr(wn * 3 + n, lane);
  const int aaddr = afrag_addr(wm * 64, lane);
  f32x4 acc[3][4];
#pragma unroll
  for (int n = 0; n < 3; ++n)
#pragma unroll
    for (int m = 0; m < 4; ++m) acc[n][m] = f32x4{0.f, 0.f, 0.f, 0.f};
  Engine<128, 3, 2, 4, 4, 3, 1, DM / 64, 1, 0> eng(acc, smem, ld, (long)2 * SEQ * 32, (long)64 * LDW, aaddr, waddr, 0);
  eng.run();
  const int g = lane >> 4;
#pragma unroll
  for (int n = 0; n < 3; ++n) {
    int col = n0 + (wn * 3 + n) * 16 + 4 * g;
    float sc = col < DM ? QSCALE : 1.f;
#pragma unroll
    for (int m = 0; m < 4; ++m) {
      int row = m0 + wm * 64 + m * 16 + (lane & 15);
      u32x2 o;
      o[0] = pack2(acc[n][m][0] * sc, acc[n][m][1] * sc);
      o[1] = pack2(acc[n][m][2] * sc, acc[n][m][3] * sc);
      *(u32x2*)(out + ((size_t)(col >> 5) * SEQ + row) * 32 + (col & 31)) = o;
    }
  }
  STAMP_AT(1, 3);
}

typedef GCfg<128, 1, 2, 6> CP;
template <int KDIM>
__global__ __launch_bounds__(512, 2) void k_proj(const u16* __restrict__ Ablk, const u16* __restrict__ W,
                                                  const u16* __restrict__ res, const float* __restrict__ gate,
                                                  u16* __restrict__ out) {
  extern __shared__ __attribute__((aligned(16))) char smem_[];
  lds_ptr smem = (lds_ptr)smem_;
  const int lane = threadIdx.x & 63;
  const int w = __builtin_amdgcn_readfirstlane(threadIdx.x >> 6);
  const int xcd = blockIdx.x & 7, idx = blockIdx.x >> 3;
  const int tm = (xcd & 3) * 4 + (idx & 3), tn = (xcd >> 2) * 8 + (idx >> 2);
  const int m0 = tm * 128, n0 = tn * 64;
  constexpr int LDW = DM;
  Loader<CP> ld;
  setup_loader_A<CP, 128, 1, 2>(ld, Ablk, m0, w, lane);
  {
    int b = w;
    int sub = b >> 2, o = b & 3;
    ld.woff[0] = 2 * CP::A_SUB + sub * CP::W_SUB + o * 1024;
    int k = sub * 32 + 8 * o + (lane >> 3);
    int f = ((lane >> 4) & 1) | ((o & 1) << 1);
    int col = n0 + ((((lane & 7) >> 1) ^ f) * 16) + (lane & 1) * 8;
    ld.wv[0] = (k * LDW + col) * 2;
    ld.wbase[0] = (const char*)W;
  }
  const int wm = w & 1, wn = (w >> 1) & 1, kg = w >> 2;
  int waddr[2];
#pragma unroll
  for (int n = 0; n < 2; ++n) waddr[n] = wfrag_addr(wn * 2 + n, lane);
  const int aaddr = afrag_addr(wm * 64, lane);
  f32x4 acc[2][4];
#pragma unroll
  for (int n = 0; n < 2; ++n)
#pragma unroll
    for (int m = 0; m < 4; ++m) acc[n][m] = f32x4{0.f, 0.f, 0.f, 0.f};
  Engine<128, 1, 2, 6, 4, 2, 2, KDIM / 64, (KDIM == DM ? 3 : 5), 0> eng(acc, smem, ld, (long)2 * SEQ * 32, (long)64 * LDW, aaddr, waddr, kg);
  eng.run();
  __syncthreads();
  __attribute__((address_space(3))) f32x4* red = (__attribute__((address_space(3))) f32x4*)smem;
  if (kg == 1) {
#pragma unroll
    for (int n = 0; n < 2; ++n)
#pragma unroll
      for (int m = 0; m < 4; ++m) red[((w & 3) * 8 + n * 4 + m) * 64 + lane] = acc[n][m];
  }
  __syncthreads();
  if (kg == 0) {
    const int g = lane >> 4;
#pragma unroll
    for (int n = 0; n < 2; ++n) {
      int col = n0 + (wn * 2 + n) * 16 + 4 * g;
      f32x4 gt = *(const f32x4*)(gate + col);
#pragma unroll
      for (int m = 0; m < 4; ++m) {
        f32x4 a = acc[n][m] + red[((w & 3) * 8 + n * 4 + m) * 64 + lane];
        int row = m0 + wm * 64 + m * 16 + (lane & 15);
        u32x2 r = *(const u32x2*)(res + (size_t)row * DM + col);
        float y0 = bflo(r[0]) + rbf(gt[0] * rbf(a[0]));
        float y1 = bfhi(r[0]) + rbf(gt[1] * rbf(a[1]));
        float y2 = bflo(r[1]) + rbf(gt[2] * rbf(a[2]));
        float y3 = bfhi(r[1]) + rbf(gt[3] * rbf(a[3]));
        u32x2 o;
        o[0] = pack2(y0, y1);
        o[1] = pack2(y2, y3);
        *(u32x2*)(out + (size_t)row * DM + col) = o;
      }
    }
  }
  STAMP_AT((KDIM == DM ? 3 : 5), 3);
}

typedef GCfg<256, 4, 1, 4> CG;
template <int NH_>
DI void glu_wave(lds_ptr smem, Loader<CG>& ld, int lane, int wm, int t0, int m0, int f0, u16* __restrict__ out) {
  constexpr int NT = 2 * NH_;
  int waddr[NT];
#pragma unroll
  for (int n = 0; n < NH_; ++n) {
    waddr[n] = wfrag_addr(t0 + n, lane);
    waddr[NH_ + n] = waddr[n] + 8192;
  }
  const int aaddr = afrag_addr(wm * 64, lane);
  f32x4 acc[NT][4];
#pragma unroll
  for (int n = 0; n < NT; ++n)
#pragma unroll
    for (int m = 0; m < 4; ++m) acc[n][m] = f32x4{0.f, 0.f, 0.f, 0.f};
  Engine<256, 4, 1, 4, 4, NT, 1, DM / 32, 4, (NH_ == 4 ? 0 : 1)> eng(acc, smem, ld, (long)SEQ * 32, (long)32 * FF, aaddr, waddr, 0);
  eng.run();
  const int g = lane >> 4;
#pragma unroll
  for (int n = 0; n < NH_; ++n) {
    int col = f0 + (t0 + n) * 16 + 4 * g;
#pragma unroll
    for (int m = 0; m < 4; ++m) {
      int row = m0 + wm * 64 + m * 16 + (lane & 15);
      f32x4 a = acc[n][m], b = acc[NH_ + n][m];
      u32x2 o;
      o[0] = pack2(rbf(a[0]) * rbf(b[0]), rbf(a[1]) * rbf(b[1]));
      o[1] = pack2(rbf(a[2]) * rbf(b[2]), rbf(a[3]) * rbf(b[3]));
      *(u32x2*)(out + ((size_t)(col >> 5) * SEQ + row) * 32 + (col & 31)) = o;
    }
  }
  STAMP_AT(4, 3);
}

__global__ __launch_bounds__(512, 2) void k_glu(const u16* __restrict__ Ablk, const u16* __restrict__ Wg,
                                                 const u16* __restrict__ Wu, u16* __restrict__ out) {
  extern __shared__ __attribute__((aligned(16))) char smem_[];
  lds_ptr smem = (lds_ptr)smem_;
  const int lane = threadIdx.x & 63;
  const int w = __builtin_amdgcn_readfirstlane(threadIdx.x >> 6);
  const int xcd = blockIdx.x & 7, idx = blockIdx.x >> 3;
  const int tm = idx & 7, tn = xcd * 4 + (idx >> 3);
  const int m0 = tm * 256, f0 = tn * 112;
  Loader<CG> ld;
  setup_loader_A<CG, 256, 4, 1>(ld, Ablk, m0, w, lane);
#pragma unroll
  for (int i = 0; i < CG::WPW; ++i) {
    int b = w + 8 * i;
    int grp = b >> 2, o = b & 3;
    ld.woff[i] = CG::A_SUB + grp * 4096 + o * 1024;
    int k = 8 * o + (lane >> 3);
    int f = ((lane >> 4) & 1) | ((o & 1) << 1);
    int pr = ((lane & 7) >> 1) ^ f;
    if ((grp & 1) && pr == 3) pr = 0;
    int col = f0 + (grp & 1) * 64 + pr * 16 + (lane & 1) * 8;
    ld.wv[i] = (k * FF + col) * 2;
    ld.wbase[i] = (const char*)(grp >> 1 ? Wu : Wg);
  }
  const int wm = w & 3;
  if (w < 4) glu_wave<4>(smem, ld, lane, wm, 0, m0, f0, out);
  else glu_wave<3>(smem, ld, lane, wm, 4, m0, f0, out);
}

constexpr int ACH = 128, ANST = 4, ASTAGE = ACH * 64 * 2;
constexpr int ALDS = ANST * ASTAGE + 1024;
struct PfArgs { const char* p0; const char* p1; const char* p2; const char* p3; };

DI float rowmax16(const f32x16& v) {
  float mx = fmaxf(fmaxf(v[0], v[1]), v[2]);
#pragma unroll
  for (int r = 3; r < 15; r += 2) mx = fmaxf(fmaxf(mx, v[r]), v[r + 1]);
  mx = fmaxf(mx, v[15]);
  auto sw = __builtin_amdgcn_permlane32_swap(__float_as_uint(mx), __float_as_uint(mx), false, false);
  return fmaxf(__uint_as_float(sw[0]), __uint_as_float(sw[1]));
}

__global__ __launch_bounds__(512, 2) void k_attn(const u16* __restrict__ qkv, u16* __restrict__ oblk,
                                                  const char* __restrict__ pf_wo, const char* __restrict__ pf_wg,
                                                  const char* __restrict__ pf_wu, const char* __restrict__ pf_wd) {
  extern __shared__ __attribute__((aligned(16))) char smem_[];
  lds_ptr smem = (lds_ptr)smem_;
  const int lane = threadIdx.x & 63;
  const int w = __builtin_amdgcn_readfirstlane(threadIdx.x >> 6);
  const int xcd = blockIdx.x & 7, idx = blockIdx.x >> 3;
  const int head = xcd * 4 + (idx >> 3), qb = idx & 7;
  const int r32 = lane & 31, hh = lane >> 5;
  const int qg = w & 3, kh = w >> 2;
  const u16* Qp = qkv + (size_t)head * SEQ * 32;
  const u16* Kp = qkv + (size_t)(32 + head) * SEQ * 32;
  const u16* Vp = qkv + (size_t)(64 + head) * SEQ * 32;
  const int q0 = qb * 256 + qg * 64;
  const unsigned kvo = ((16 * w + (lane >> 2)) * 32 + (((lane & 3) ^ ((lane >> 4) & 3)) * 8)) * 2;
  const unsigned vvo = ((16 * w) * 32 + lane * 8) * 2;
  const char* ksrc = (const char*)Kp;
  const char* vsrc = (const char*)Vp;
  auto issue = [&](int buf) {
    glds16(ksrc, kvo, lds_u32(smem) + buf * ASTAGE + w * 1024);
    glds16(vsrc, vvo, lds_u32(smem) + buf * ASTAGE + 8192 + w * 1024);
    ksrc += ACH * 64; vsrc += ACH * 64;
  };
  const unsigned pfo = lane * 16;
  auto prefetch = [&](int ch) {
    int pc = ch * 8 + w;
    const char* base;
    if (pc < 8) base = pf_wo + (size_t)blockIdx.x * 8192 + pc * 1024;
    else if (pc < 36) base = pf_wg + (size_t)blockIdx.x * 28672 + (pc - 8) * 1024;
    else if (pc < 64) base = pf_wu + (size_t)blockIdx.x * 28672 + (pc - 36) * 1024;
    else if (pc < 92) base = pf_wd + (size_t)blockIdx.x * 28672 + (pc - 64) * 1024;
    else base = pf_wo + (size_t)blockIdx.x * 8192;
    glds16(base, pfo, lds_u32(smem) + ANST * ASTAGE);
  };
  STAMP_AT(2, 0);
  issue(0); issue(1); issue(2);
  bf16x8 qa0 = *(const bf16x8*)(Qp + (q0 + r32) * 32 + hh * 8);
  bf16x8 qa1 = *(const bf16x8*)(Qp + (q0 + r32) * 32 + 16 + hh * 8);
  bf16x8 qb0 = *(const bf16x8*)(Qp + (q0 + 32 + r32) * 32 + hh * 8);
  bf16x8 qb1 = *(const bf16x8*)(Qp + (q0 + 32 + r32) * 32 + 16 + hh * 8);
  const int kaddr0 = kh * 4096 + r32 * 64 + (((0 + hh) ^ ((r32 >> 2) & 3)) * 16);
  const int kaddr1 = kh * 4096 + r32 * 64 + (((2 + hh) ^ ((r32 >> 2) & 3)) * 16);
  const int vaddr = 8192 + kh * 4096 + (4 * hh + ((lane & 15) >> 2)) * 64 + ((lane >> 4) & 1) * 32 + (lane & 3) * 8;
  f32x16 oa, ob, nma, nmb, sa, sb;
#pragma unroll
  for (int r = 0; r < 16; ++r) { oa[r] = 0.f; ob[r] = 0.f; nma[r] = 0.f; nmb[r] = 0.f; }
  f32x2 la = {0.f, 0.f}, lb = {0.f, 0.f};
  constexpr int NCH = SEQ / ACH;
  wait_vm<4>();
  bar();
  {
    bf16x8 kf0 = lds_read128(smem + kaddr0);
    bf16x8 kf1 = lds_read128(smem + kaddr1);
    sa = MFMA32(kf0, qa0, nma); sa = MFMA32(kf1, qa1, sa);
    sb = MFMA32(kf0, qb0, nmb); sb = MFMA32(kf1, qb1, sb);
    const float ma = rowmax16(sa), mb = rowmax16(sb);
#pragma unroll
    for (int r = 0; r < 16; ++r) { nma[r] = -ma; sa[r] -= ma; nmb[r] = -mb; sb[r] -= mb; }
  }
  auto half = [&](f32x16& s, f32x16& o, f32x16& nm, f32x2& l, const bf16x8& q0f, const bf16x8& q1f,
                  const bf16x8& kn0, const bf16x8& kn1, const bf16x8& vf0, const bf16x8& vf1, bool has_next) {
    f32x16 p;
#pragma unroll
    for (int r = 0; r < 16; ++r) p[r] = __builtin_amdgcn_exp2f(s[r]);
    f32x2 ta = f32x2{p[0], p[1]} + f32x2{p[2], p[3]};
    f32x2 tb = f32x2{p[4], p[5]} + f32x2{p[6], p[7]};
    ta += f32x2{p[8], p[9]}; tb += f32x2{p[10], p[11]};
    ta += f32x2{p[12], p[13]}; tb += f32x2{p[14], p[15]};
    ta += tb;
    if (__builtin_expect(__any(!(ta[0] + ta[1] <= 16777216.f)), 0)) {
      const float dl = fmaxf(rowmax16(s), 0.f);
      const float f = __builtin_amdgcn_exp2f(-dl);
#pragma unroll
      for (int r = 0; r < 16; ++r) { nm[r] -= dl; o[r] *= f; p[r] = __builtin_amdgcn_exp2f(s[r] - dl); }
      l *= f;
      ta = f32x2{p[0], p[1]} + f32x2{p[2], p[3]};
#pragma unroll
      for (int r = 4; r < 16; r += 2) ta += f32x2{p[r], p[r + 1]};
    }
    l += ta;
    if (has_next) { s = MFMA32(kn0, q0f, nm); s = MFMA32(kn1, q1f, s); }
    u32x4 pa, pb;
#pragma unroll
    for (int j = 0; j < 4; ++j) { pa[j] = pack2(p[2 * j], p[2 * j + 1]); pb[j] = pack2(p[8 + 2 * j], p[8 + 2 * j + 1]); }
    o = MFMA32(vf0, __builtin_bit_cast(bf16x8, pa), o);
    o = MFMA32(vf1, __builtin_bit_cast(bf16x8, pb), o);
  };
  auto step = [&](lds_cptr vb, lds_cptr knext, bool has_next) {
    bf16x8 kn0, kn1;
    if (has_next) { kn0 = lds_read128(knext + kaddr0); kn1 = lds_read128(knext + kaddr1); }
    bf16x8 vf0 = lds_tr8(vb, vb + 512);
    bf16x8 vf1 = lds_tr8(vb + 1024, vb + 1536);
    half(sa, oa, nma, la, qa0, qa1, kn0, kn1, vf0, vf1, has_next);
    half(sb, ob, nmb, lb, qb0, qb1, kn0, kn1, vf0, vf1, has_next);
  };
  for (int ch = 0; ch < NCH; ++ch) {
    if (ch + 2 < NCH) wait_vm<4>(); else wait_vm<0>();
    bar();
    if (ch + 3 < NCH) { issue((ch + 3) & 3); prefetch(ch); }
    lds_cptr sbuf = smem + (ch & 3) * ASTAGE;
    lds_cptr nbuf = smem + ((ch + 1) & 3) * ASTAGE;
    step(sbuf + vaddr, sbuf + 2048, true);
    step(sbuf + 2048 + vaddr, nbuf, ch + 1 < NCH);
  }
  STAMP_AT(2, 2);
  __syncthreads();
  {
    __attribute__((address_space(3))) float* xb = (__attribute__((address_space(3))) float*)smem;
    const f32x16& og = kh == 0 ? ob : oa;
    const float lg = kh == 0 ? lb[0] + lb[1] : la[0] + la[1];
    const float mg = kh == 0 ? nmb[0] : nma[0];
    __attribute__((address_space(3))) float* dst = xb + w * (18 * 64) + lane;
#pragma unroll
    for (int r = 0; r < 16; ++r) dst[r * 64] = og[r];
    dst[16 * 64] = lg; dst[17 * 64] = mg;
    __syncthreads();
    const __attribute__((address_space(3))) float* src = xb + (w ^ 4) * (18 * 64) + lane;
    f32x16 om = kh == 0 ? oa : ob;
    float lm = kh == 0 ? la[0] + la[1] : lb[0] + lb[1];
    const float nm_me = kh == 0 ? nma[0] : nmb[0];
    const float nm_ot = src[17 * 64];
    const float nmin = fminf(nm_me, nm_ot);
    const float f_me = __builtin_amdgcn_exp2f(nmin - nm_me), f_ot = __builtin_amdgcn_exp2f(nmin - nm_ot);
    lm = lm * f_me + src[16 * 64] * f_ot;
    auto sw = __builtin_amdgcn_permlane32_swap(__float_as_uint(lm), __float_as_uint(lm), false, false);
    const float rl = 1.f / (__uint_as_float(sw[0]) + __uint_as_float(sw[1]));
    const float g_me = f_me * rl, g_ot = f_ot * rl;
    u16* op = oblk + ((size_t)head * SEQ + q0 + kh * 32 + r32) * 32 + 4 * hh;
#pragma unroll
    for (int g = 0; g < 4; ++g) {
      float v0 = om[4 * g] * g_me + src[(4 * g) * 64] * g_ot;
      float v1 = om[4 * g + 1] * g_me + src[(4 * g + 1) * 64] * g_ot;
      float v2 = om[4 * g + 2] * g_me + src[(4 * g + 2) * 64] * g_ot;
      float v3 = om[4 * g + 3] * g_me + src[(4 * g + 3) * 64] * g_ot;
      u32x2 v;
      v[0] = pack2(v0, v1);
      v[1] = pack2(v2, v3);
      *(u32x2*)(op + 8 * g) = v;
    }
  }
  STAMP_AT(2, 3);
}

extern "C" void kernel_launch(void* const* d_in, const int* in_sizes, int n_in, void* d_out, int out_size,
                              void* d_ws, size_t ws_size, hipStream_t stream) {
  const u16* x = (const u16*)d_in[0];
  const u16* wqkv = (const u16*)d_in[1];
  const u16* wo = (const u16*)d_in[2];
  const u16* wg = (const u16*)d_in[4];
  const u16* wu = (const u16*)d_in[5];
  const u16* wd = (const u16*)d_in[6];
  const u16* c = (const u16*)d_in[7];
  const u16* wmod = (const u16*)d_in[8];
  u16* out = (u16*)d_out;
  char* ws = (char*)d_ws;
  float* mod = (float*)ws;
  u16* h1 = (u16*)(ws + (1 << 20));
  u16* qkv = (u16*)(ws + (8 << 20));
  u16* ob = (u16*)(ws + (24 << 20));
  u16* x2 = (u16*)(ws + (32 << 20));
  u16* h2 = (u16*)(ws + (40 << 20));
  u16* hh = (u16*)(ws + (48 << 20));

  hipFuncSetAttribute((const void*)k_qkv, hipFuncAttributeMaxDynamicSharedMemorySize, CQ::LDS);
  hipFuncSetAttribute((const void*)k_proj<DM>, hipFuncAttributeMaxDynamicSharedMemorySize, CP::LDS);
  hipFuncSetAttribute((const void*)k_proj<FF>, hipFuncAttributeMaxDynamicSharedMemorySize, CP::LDS);
  hipFuncSetAttribute((const void*)k_glu, hipFuncAttributeMaxDynamicSharedMemorySize, CG::LDS);
  hipFuncSetAttribute((const void*)k_attn, hipFuncAttributeMaxDynamicSharedMemorySize, ALDS);

  k_mod<<<96, 1024, 0, stream>>>(c, wmod, mod);
  k_ln<<<256, 512, 0, stream>>>(x, mod + 0 * DM, mod + 1 * DM, h1);
  k_qkv<<<256, 512, CQ::LDS, stream>>>(h1, wqkv, qkv);
  k_attn<<<256, 512, ALDS, stream>>>(qkv, ob, (const char*)wo, (const char*)wg, (const char*)wu, (const char*)wd);
  k_proj<DM><<<256, 512, CP::LDS, stream>>>(ob, wo, x, mod + 2 * DM, x2);
  k_ln<<<256, 512, 0, stream>>>(x2, mod + 3 * DM, mod + 4 * DM, h2);
  k_glu<<<256, 512, CG::LDS, stream>>>(h2, wg, wu, hh);
  k_proj<FF><<<256, 512, CP::LDS, stream>>>(hh, wd, x2, mod + 5 * DM, out);
}
```

```cpp
#include <hip/hip_runtime.h>
#include <hip/hip_bf16.h>
#include <stdint.h>

typedef __attribute__((ext_vector_type(8))) short bf16x8;
typedef __attribute__((ext_vector_type(4))) short s16x4;
typedef __attribute__((ext_vector_type(4))) float f32x4;
typedef __attribute__((ext_vector_type(2))) float f32x2;
typedef __attribute__((ext_vector_type(16))) float f32x16;
typedef __attribute__((ext_vector_type(4))) unsigned u32x4;
typedef __attribute__((ext_vector_type(2))) unsigned u32x2;
typedef __attribute__((ext_vector_type(2))) __bf16 bf16x2_t;
typedef unsigned short u16;
typedef __attribute__((address_space(3))) char* lds_ptr;
typedef __attribute__((address_space(3))) const char* lds_cptr;

#define DI __device__ __forceinline__

constexpr int SEQ = 2048, DM = 1024, FF = 3584, NH = 32;

DI float bf2f(u16 x) { return __uint_as_float(((unsigned)x) << 16); }
DI float bflo(unsigned x) { return __uint_as_float(x << 16); }
DI float bfhi(unsigned x) { return __uint_as_float(x & 0xffff0000u); }
DI unsigned pack2(float a, float b) {
  f32x2 v = {a, b};
  bf16x2_t r = __builtin_convertvector(v, bf16x2_t);
  return __builtin_bit_cast(unsigned, r);
}
DI float rbf(float a) { return bflo(pack2(a, 0.f) & 0xffffu); }

#define MFMA16(a, b, c) __builtin_amdgcn_mfma_f32_16x16x32_bf16((a), (b), (c), 0, 0, 0)
#define MFMA32(a, b, c) __builtin_amdgcn_mfma_f32_32x32x16_bf16((a), (b), (c), 0, 0, 0)

DI void glds16(const void* base, unsigned voff, unsigned lds_addr) {
  unsigned sv;
  asm volatile("s_mov_b32 %0, m0\n\ts_mov_b32 m0, %3\n\ts_nop 0\n\tglobal_load_lds_dwordx4 %1, %2\n\ts_mov_b32 m0, %0"
               : "=&s"(sv) : "v"(voff), "s"(base), "s"(lds_addr) : "memory");
}
DI unsigned lds_u32(lds_ptr p) { return (unsigned)(uintptr_t)p; }
DI bf16x8 lds_read128(lds_cptr p) { return *(const __attribute__((address_space(3))) bf16x8*)p; }
DI s16x4 lds_tr(lds_cptr p) {
  typedef short v4i16_t __attribute__((ext_vector_type(4)));
  return __builtin_bit_cast(s16x4, __builtin_amdgcn_ds_read_tr16_b64_v4i16((__attribute__((address_space(3))) v4i16_t*)p));
}
DI bf16x8 lds_tr8(lds_cptr p0, lds_cptr p1) {
  s16x4 a = lds_tr(p0), b = lds_tr(p1);
  return __builtin_shufflevector(a, b, 0, 1, 2, 3, 4, 5, 6, 7);
}
#define STAMP_AT(kid, slot) do {} while (0)
template <int N> DI void wait_vm() { asm volatile("s_waitcnt vmcnt(%0)" ::"n"(N) : "memory"); }
DI void bar() { __builtin_amdgcn_s_barrier(); }

constexpr int MNS = 8;
DI void mod_task(const u16* __restrict__ c, const u16* __restrict__ wmod, float* __restrict__ part, int cb, int ks,
                 int t, float* s_red  ) {
  const int cc = t & 7, rg = t >> 3;
  const int k0 = ks * 128 + rg;
  const u16* wp = wmod + (size_t)k0 * (6 * DM) + cb * 64 + cc * 8;
  u32x4 w[4];
  float sv[4];
#pragma unroll
  for (int i = 0; i < 4; ++i) w[i] = *(const u32x4*)(wp + (size_t)i * 32 * (6 * DM));
#pragma unroll
  for (int i = 0; i < 4; ++i) {
    float cv = bf2f(c[k0 + 32 * i]);
    sv[i] = rbf(cv / (1.f + __expf(-cv)));
  }
  float acc[8];
#pragma unroll
  for (int j = 0; j < 8; ++j) acc[j] = 0.f;
#pragma unroll
  for (int i = 0; i < 4; ++i) {
#pragma unroll
    for (int j = 0; j < 4; ++j) {
      acc[2 * j] += sv[i] * bflo(w[i][j]);
      acc[2 * j + 1] += sv[i] * bfhi(w[i][j]);
    }
  }
#pragma unroll
  for (int j = 0; j < 8; ++j) {
    float v = acc[j];
    v += __shfl_xor(v, 8);
    v += __shfl_xor(v, 16);
    v += __shfl_xor(v, 32);
    acc[j] = v;
  }
  const int lane = t & 63, wv = t >> 6;
  if (lane < 8) {
#pragma unroll
    for (int j = 0; j < 8; ++j) s_red[wv * 64 + lane * 8 + j] = acc[j];
  }
  __syncthreads();
  if (t < 64) part[ks * (6 * DM) + cb * 64 + t] = (s_red[t] + s_red[64 + t]) + (s_red[128 + t] + s_red[192 + t]);
}

DI void touch16k(const char* p, int t) {
  u32x4 v[4];
#pragma unroll
  for (int i = 0; i < 4; ++i) v[i] = *(const u32x4*)(p + i * 4096 + t * 16);
#pragma unroll
  for (int i = 0; i < 4; ++i) asm volatile("" ::"v"(v[i]));
}

__global__ __launch_bounds__(256) void k_pre(const u16* __restrict__ c, const u16* __restrict__ wmod, float* __restrict__ part,
                                              const char* __restrict__ x, const char* __restrict__ wqkv,
                                              unsigned* __restrict__ flags) {
  __shared__ float s_red[256];
  const int t = threadIdx.x, b = blockIdx.x;
  if (b < 256) STAMP_AT(0, 0);
  if (b < 256) {
    mod_task(c, wmod, part, b >> 3, b & 7, t, s_red);
    if (b == 0) flags[t] = 0u;
  } else if (b < 512) {
    touch16k(x + (size_t)(b - 256) * 16384, t);
  } else {
    touch16k(wqkv + (size_t)(b - 512) * 16384, t);
  }
  if (b < 256) STAMP_AT(0, 3);
}

template <int KID, int NS, bool FIRST>
__global__ __launch_bounds__(512) void k_ln(const u16* __restrict__ x, const float* __restrict__ ss  ,
                                             u16* __restrict__ h16, const u16* __restrict__ c,
                                             const u16* __restrict__ wmod, float* __restrict__ part) {
  __shared__ __attribute__((aligned(16))) float sm[2048];
  const int t = threadIdx.x;
  if (FIRST && blockIdx.x >= 256) {
    const int task = (blockIdx.x - 256) * 2 + (t >> 8);
    mod_task(c, wmod, part, 32 + (task >> 3), task & 7, t & 255, sm + (t >> 8) * 256);
    return;
  }
  const int lane = t & 63, wv = t >> 6;
  const int row = blockIdx.x * 8 + wv;
  STAMP_AT(KID, 0);
  const u16* xr = x + (size_t)row * DM;
  u32x4 v0 = *(const u32x4*)(xr + lane * 8);
  u32x4 v1 = *(const u32x4*)(xr + 512 + lane * 8);
  {
    f32x4 pv[NS];
#pragma unroll
    for (int i = 0; i < NS; ++i) pv[i] = *(const f32x4*)(ss + i * (6 * DM) + t * 4);
    f32x4 a = pv[0];
#pragma unroll
    for (int i = 1; i < NS; ++i) a += pv[i];
#pragma unroll
    for (int j = 0; j < 4; ++j) {
      float v = rbf(a[j]);
      a[j] = t < 256 ? v : rbf(1.f + v);
    }
    *(f32x4*)(sm + t * 4) = a;
  }
  float f[16];
#pragma unroll
  for (int j = 0; j < 4; ++j) {
    f[2 * j] = bflo(v0[j]); f[2 * j + 1] = bfhi(v0[j]);
    f[8 + 2 * j] = bflo(v1[j]); f[8 + 2 * j + 1] = bfhi(v1[j]);
  }
  float s = 0.f;
#pragma unroll
  for (int j = 0; j < 16; ++j) s += f[j];
#pragma unroll
  for (int o = 1; o < 64; o <<= 1) s += __shfl_xor(s, o);
  const float mean = s * (1.f / DM);
  float q = 0.f;
#pragma unroll
  for (int j = 0; j < 16; ++j) { float d = f[j] - mean; q += d * d; }
#pragma unroll
  for (int o = 1; o < 64; o <<= 1) q += __shfl_xor(q, o);
  const float mb = rbf(mean);
  const float vb = rbf(q * (1.f / DM));
  const float rden = 1.f / rbf(sqrtf(rbf(vb + 1e-5f)));
  __syncthreads();
#pragma unroll
  for (int half = 0; half < 2; ++half) {
    const int k0 = half * 512 + lane * 8;
    f32x4 sh0 = *(const f32x4*)(sm + k0), sh1 = *(const f32x4*)(sm + k0 + 4);
    f32x4 sc0 = *(const f32x4*)(sm + 1024 + k0), sc1 = *(const f32x4*)(sm + 1024 + k0 + 4);
    float y[8];
#pragma unroll
    for (int j = 0; j < 8; ++j) {
      float g = j < 4 ? sc0[j] : sc1[j - 4];
      float sh = j < 4 ? sh0[j] : sh1[j - 4];
      float d = rbf(f[half * 8 + j] - mb);
      float n = rbf(d * rden);
      y[j] = rbf(n * g) + sh;
    }
    u32x4 o;
#pragma unroll
    for (int j = 0; j < 4; ++j) o[j] = pack2(y[2 * j], y[2 * j + 1]);
    *(u32x4*)(h16 + ((size_t)(k0 >> 4) * SEQ + row) * 16 + (k0 & 15)) = o;
  }
  STAMP_AT(KID, 3);
}

DI u32x4 pair16(u32x2 a, u32x2 b) {
  auto s0 = __builtin_amdgcn_permlane16_swap(a[0], b[0], false, false);
  auto s1 = __builtin_amdgcn_permlane16_swap(a[1], b[1], false, false);
  return u32x4{s0[0], s1[0], s0[1], s1[1]};
}

DI int hA(int r) { return (-r) & 3; }

template <int BM, int NGRP, int KS, int NST>
struct GCfg {
  static constexpr int A_SUB = BM * 64;
  static constexpr int W_SUB = NGRP * 4096;
  static constexpr int STAGE = KS * (A_SUB + W_SUB);
  static constexpr int APW = KS * BM / 128;
  static constexpr int WPW = KS * NGRP / 2;
  static constexpr int PPW = APW + WPW;
  static constexpr int LDS = STAGE * NST;
};

template <class C>
struct Loader {
  const char* abase;
  const char* wbase[C::WPW];
  unsigned av[C::APW];
  unsigned wv[C::WPW];
  int aoff[C::APW];
  int woff[C::WPW];
};

template <int BM, int NGRP, int KS, int NST, int MT, int NT, int KSPLIT, int NKT, int KID, int IOFS>
struct Engine {
  typedef GCfg<BM, NGRP, KS, NST> C;
  static constexpr int SPW = KS / KSPLIT;
  static constexpr int P = C::PPW;
  static constexpr int G = NT * SPW;
  f32x4 (&acc)[NT][MT];
  lds_ptr smem;
  Loader<C>& ld;
  long a_adv, w_adv;
  const int (&waddr)[NT];
  int a_sub0, w_sub0;
  int cur, nxt, fre;
  bf16x8 wf[NT], af[2][MT];

  DI Engine(f32x4 (&acc_)[NT][MT], lds_ptr smem_, Loader<C>& ld_, long a_adv_, long w_adv_, int aaddr, const int (&waddr_)[NT], int kg)
      : acc(acc_), smem(smem_), ld(ld_), a_adv(a_adv_), w_adv(w_adv_), waddr(waddr_) {
    a_sub0 = kg * C::A_SUB + aaddr;
    w_sub0 = KS * C::A_SUB + kg * C::W_SUB;
  }
  template <int I> DI void piece(int stage_off) {
    if constexpr (I < C::APW) {
      glds16(ld.abase, ld.av[I], lds_u32(smem) + stage_off + ld.aoff[I]);
      if constexpr (I == C::APW - 1) ld.abase += a_adv * 2;
    } else {
      constexpr int J = I - C::APW;
      glds16(ld.wbase[J], ld.wv[J], lds_u32(smem) + stage_off + ld.woff[J]);
      ld.wbase[J] += w_adv * 2;
    }
  }
  template <int I> DI void issue_all(int stage_off) {
    if constexpr (I < P) { piece<I>(stage_off); issue_all<I + 1>(stage_off); }
  }
  template <int g, int I> DI void issue_at(int stage_off) {
    if constexpr (I < P) {
      constexpr int span = (G - IOFS) > P ? (G - IOFS) : P;
      constexpr int pos = IOFS + (I * span) / P < G ? IOFS + (I * span) / P : G - 1;
      if constexpr (pos == g) piece<I>(stage_off);
      issue_at<g, I + 1>(stage_off);
    }
  }
  template <int PAR, int S, bool PF, bool DOISSUE, int n>
  DI void groups(lds_cptr ab, lds_cptr wb) {
    if constexpr (n < NT) {
#pragma unroll
      for (int m = 0; m < MT; ++m) acc[n][m] = MFMA16(wf[n], af[PAR][m], acc[n][m]);
      if constexpr (PF) {
        wf[n] = lds_tr8(wb + waddr[n], wb + waddr[n] + 512);
#pragma unroll
        for (int m = 0; m < MT; ++m)
          if ((m * NT) / MT == n) af[PAR ^ 1][m] = lds_read128(ab + m * 1024);
      }
      if constexpr (DOISSUE) issue_at<S * NT + n, 0>(fre);
      __builtin_amdgcn_sched_barrier(0);
      groups<PAR, S, PF, DOISSUE, n + 1>(ab, wb);
    }
  }
  template <int PAR, bool DOISSUE, bool LAST, int S> DI void steps() {
    if constexpr (S < SPW) {
      if constexpr (S + 1 < SPW)
        groups<PAR, S, true, DOISSUE, 0>(smem + cur + a_sub0 + (S + 1) * KSPLIT * C::A_SUB, smem + cur + w_sub0 + (S + 1) * KSPLIT * C::W_SUB);
      else if constexpr (!LAST)
        groups<PAR, S, true, DOISSUE, 0>(smem + nxt + a_sub0, smem + nxt + w_sub0);
      else
        groups<PAR, S, false, false, 0>(smem, smem);
      steps<PAR ^ 1, DOISSUE, LAST, S + 1>();
    }
  }
  template <int PAR, bool DOISSUE, bool LAST, int WAITN> DI void iter() {
    if constexpr (!LAST) { wait_vm<WAITN>(); bar(); }
    steps<PAR, DOISSUE, LAST, 0>();
    fre = cur; cur = nxt; nxt = (nxt + C::STAGE == NST * C::STAGE) ? 0 : nxt + C::STAGE;
  }
  template <int KT> DI void tail() {
    if constexpr (KT < NKT) {
      constexpr bool DOISSUE = KT + NST - 1 < NKT;
      constexpr bool LAST = KT == NKT - 1;
      constexpr int newest = (KT + NST - 2) < (NKT - 1) ? (KT + NST - 2) : (NKT - 1);
      constexpr int younger = newest - (KT + 1) > 0 ? newest - (KT + 1) : 0;
      iter<(KT * SPW) & 1, DOISSUE, LAST, P * younger>();
      tail<KT + 1>();
    }
  }
  DI void run() {
    static_assert(NKT >= NST + 1, "too few k tiles");
    STAMP_AT(KID, 0);
#pragma unroll
    for (int s = 0; s < NST - 1; ++s) issue_all<0>(s * C::STAGE);
    wait_vm<P*(NST - 2)>();
    bar();
    STAMP_AT(KID, 1);
    {
      lds_cptr ab = smem + a_sub0;
      lds_cptr wb = smem + w_sub0;
#pragma unroll
      for (int m = 0; m < MT; ++m) af[0][m] = lds_read128(ab + m * 1024);
#pragma unroll
      for (int n = 0; n < NT; ++n) wf[n] = lds_tr8(wb + waddr[n], wb + waddr[n] + 512);
    }
    cur = 0; nxt = C::STAGE; fre = (NST - 1) * C::STAGE;
    constexpr int STEADY = NKT - NST + 1;
    constexpr int LOOPN = (STEADY / 2) * 2;
    for (int kt = 0; kt < LOOPN; kt += 2) {
      iter<0, true, false, P*(NST - 3)>();
      iter<SPW & 1, true, false, P*(NST - 3)>();
    }
    tail<LOOPN>();
    STAMP_AT(KID, 2);
  }
};

template <class C, int BM, int NGRP, int KS>
DI void setup_loader_A(Loader<C>& ld, const u16* Ablk, int m0, int w, int lane) {
#pragma unroll
  for (int i = 0; i < C::APW; ++i) {
    int a = w + 8 * i;
    int sub = a / (BM / 16), j = a % (BM / 16);
    ld.aoff[i] = sub * C::A_SUB + j * 1024;
    int row = j * 16 + (lane >> 2);
    int c = (lane & 3) ^ hA((lane >> 4) & 3);
    ld.av[i] = (((2 * sub + (c >> 1)) * SEQ + m0 + row) * 16 + (c & 1) * 8) * 2;
  }
  ld.abase = (const char*)Ablk;
}

DI int wfrag_addr(int t, int lane) {
  int g = lane >> 4, q = (lane & 15) >> 2, p = lane & 3;
  int f = ((q >> 1) & 1) | ((g & 1) << 1);
  return (t >> 2) * 4096 + (8 * g + q) * 128 + (((t & 3) ^ f) * 32) + p * 8;
}
DI int afrag_addr(int mrow0, int lane) {
  int r = lane & 15, g = lane >> 4;
  return (mrow0 + r) * 64 + ((g ^ hA((r >> 2) & 3)) * 16);
}

constexpr float QSCALE = 0.17677669529663687f * 1.4426950408889634f;
typedef GCfg<128, 3, 2, 4> CQ;
__global__ __launch_bounds__(512, 2) void k_qkv(const u16* __restrict__ Ablk, const u16* __restrict__ W,
                                                 u16* __restrict__ out) {
  extern __shared__ __attribute__((aligned(16))) char smem_[];
  lds_ptr smem = (lds_ptr)smem_;
  const int lane = threadIdx.x & 63;
  const int w = __builtin_amdgcn_readfirstlane(threadIdx.x >> 6);
  const int xcd = blockIdx.x & 7, idx = blockIdx.x >> 3;
  const int tm = (xcd & 1) * 8 + (idx & 7), tn = (xcd >> 1) * 4 + (idx >> 3);
  const int m0 = tm * 128, n0 = tn * 192;
  constexpr int LDW = 3 * DM;
  Loader<CQ> ld;
  setup_loader_A<CQ, 128, 3, 2>(ld, Ablk, m0, w, lane);
#pragma unroll
  for (int i = 0; i < CQ::WPW; ++i) {
    int b = w + 8 * i;
    int sub = b / 12, r = b % 12, grp = r >> 2, o = r & 3;
    ld.woff[i] = 2 * CQ::A_SUB + sub * CQ::W_SUB + grp * 4096 + o * 1024;
    int k = sub * 32 + 8 * o + (lane >> 3);
    int f = ((lane >> 4) & 1) | ((o & 1) << 1);
    int col = n0 + grp * 64 + ((((lane & 7) >> 1) ^ f) * 16) + (lane & 1) * 8;
    ld.wv[i] = (k * LDW + col) * 2;
    ld.wbase[i] = (const char*)W;
  }
  const int wm = w & 1, wn = w >> 1;
  int waddr[3];
#pragma unroll
  for (int n = 0; n < 3; ++n) waddr[n] = wfrag_addr(wn * 3 + n, lane);
  const int aaddr = afrag_addr(wm * 64, lane);
  f32x4 acc[3][4];
#pragma unroll
  for (int n = 0; n < 3; ++n)
#pragma unroll
    for (int m = 0; m < 4; ++m) acc[n][m] = f32x4{0.f, 0.f, 0.f, 0.f};
  Engine<128, 3, 2, 4, 4, 3, 1, DM / 64, 1, 0> eng(acc, smem, ld, (long)2 * SEQ * 32, (long)64 * LDW, aaddr, waddr, 0);
  eng.run();
  const int g = lane >> 4;
#pragma unroll
  for (int n = 0; n < 3; ++n) {
    const int cb = (n0 >> 4) + wn * 3 + n;
    const float sc = cb < DM / 16 ? QSCALE : 1.f;
#pragma unroll
    for (int m = 0; m < 4; m += 2) {
      u32x2 a, b;
      a[0] = pack2(acc[n][m][0] * sc, acc[n][m][1] * sc);
      a[1] = pack2(acc[n][m][2] * sc, acc[n][m][3] * sc);
      b[0] = pack2(acc[n][m + 1][0] * sc, acc[n][m + 1][1] * sc);
      b[1] = pack2(acc[n][m + 1][2] * sc, acc[n][m + 1][3] * sc);
      const int row = m0 + wm * 64 + (m + (g & 1)) * 16 + (lane & 15);
      *(u32x4*)(out + ((size_t)cb * SEQ + row) * 16 + (g >> 1) * 8) = pair16(a, b);
    }
  }
  STAMP_AT(1, 3);
}

typedef GCfg<128, 1, 2, 6> CP;
template <int KDIM>
__global__ __launch_bounds__(512, 2) void k_proj(const u16* __restrict__ Ablk, const u16* __restrict__ W,
                                                  const u16* __restrict__ res, const float* __restrict__ gate,
                                                  u16* __restrict__ out) {
  extern __shared__ __attribute__((aligned(16))) char smem_[];
  lds_ptr smem = (lds_ptr)smem_;
  const int lane = threadIdx.x & 63;
  const int w = __builtin_amdgcn_readfirstlane(threadIdx.x >> 6);
  const int xcd = blockIdx.x & 7, idx = blockIdx.x >> 3;
  const int tm = (xcd & 3) * 4 + (idx & 3), tn = (xcd >> 2) * 8 + (idx >> 2);
  const int m0 = tm * 128, n0 = tn * 64;
  constexpr int LDW = DM;
  Loader<CP> ld;
  setup_loader_A<CP, 128, 1, 2>(ld, Ablk, m0, w, lane);
  {
    int b = w;
    int sub = b >> 2, o = b & 3;
    ld.woff[0] = 2 * CP::A_SUB + sub * CP::W_SUB + o * 1024;
    int k = sub * 32 + 8 * o + (lane >> 3);
    int f = ((lane >> 4) & 1) | ((o & 1) << 1);
    int col = n0 + ((((lane & 7) >> 1) ^ f) * 16) + (lane & 1) * 8;
    ld.wv[0] = (k * LDW + col) * 2;
    ld.wbase[0] = (const char*)W;
  }
  const int wm = w & 1, wn = (w >> 1) & 1, kg = w >> 2;
  int waddr[2];
#pragma unroll
  for (int n = 0; n < 2; ++n) waddr[n] = wfrag_addr(wn * 2 + n, lane);
  const int aaddr = afrag_addr(wm * 64, lane);
  f32x4 acc[2][4];
#pragma unroll
  for (int n = 0; n < 2; ++n)
#pragma unroll
    for (int m = 0; m < 4; ++m) acc[n][m] = f32x4{0.f, 0.f, 0.f, 0.f};
  Engine<128, 1, 2, 6, 4, 2, 2, KDIM / 64, (KDIM == DM ? 3 : 5), 0> eng(acc, smem, ld, (long)2 * SEQ * 32, (long)64 * LDW, aaddr, waddr, kg);
  eng.run();
  __syncthreads();
  __attribute__((address_space(3))) f32x4* red = (__attribute__((address_space(3))) f32x4*)smem;
  if (kg == 1) {
#pragma unroll
    for (int n = 0; n < 2; ++n)
#pragma unroll
      for (int m = 0; m < 4; ++m) red[((w & 3) * 8 + n * 4 + m) * 64 + lane] = acc[n][m];
  }
  __syncthreads();
  if (kg == 0) {
    const int g = lane >> 4;
#pragma unroll
    for (int n = 0; n < 2; ++n) {
      int col = n0 + (wn * 2 + n) * 16 + 4 * g;
      f32x4 gt = *(const f32x4*)(gate + col);
#pragma unroll
      for (int m = 0; m < 4; ++m) {
        f32x4 a = acc[n][m] + red[((w & 3) * 8 + n * 4 + m) * 64 + lane];
        int row = m0 + wm * 64 + m * 16 + (lane & 15);
        u32x2 r = *(const u32x2*)(res + (size_t)row * DM + col);
        float y0 = bflo(r[0]) + rbf(gt[0] * rbf(a[0]));
        float y1 = bfhi(r[0]) + rbf(gt[1] * rbf(a[1]));
        float y2 = bflo(r[1]) + rbf(gt[2] * rbf(a[2]));
        float y3 = bfhi(r[1]) + rbf(gt[3] * rbf(a[3]));
        u32x2 o;
        o[0] = pack2(y0, y1);
        o[1] = pack2(y2, y3);
        *(u32x2*)(out + (size_t)row * DM + col) = o;
      }
    }
  }
  STAMP_AT((KDIM == DM ? 3 : 5), 3);
}

typedef GCfg<256, 4, 1, 4> CG;
template <int NH_>
DI void glu_wave(lds_ptr smem, Loader<CG>& ld, int lane, int wm, int t0, int m0, int f0, u16* __restrict__ out) {
  constexpr int NT = 2 * NH_;
  int waddr[NT];
#pragma unroll
  for (int n = 0; n < NH_; ++n) {
    waddr[n] = wfrag_addr(t0 + n, lane);
    waddr[NH_ + n] = waddr[n] + 8192;
  }
  const int aaddr = afrag_addr(wm * 64, lane);
  f32x4 acc[NT][4];
#pragma unroll
  for (int n = 0; n < NT; ++n)
#pragma unroll
    for (int m = 0; m < 4; ++m) acc[n][m] = f32x4{0.f, 0.f, 0.f, 0.f};
  Engine<256, 4, 1, 4, 4, NT, 1, DM / 32, 4, (NH_ == 4 ? 0 : 1)> eng(acc, smem, ld, (long)SEQ * 32, (long)32 * FF, aaddr, waddr, 0);
  eng.run();
  const int g = lane >> 4;
#pragma unroll
  for (int n = 0; n < NH_; ++n) {
    const int cb = (f0 >> 4) + t0 + n;
#pragma unroll
    for (int m = 0; m < 4; m += 2) {
      u32x2 o[2];
#pragma unroll
      for (int i = 0; i < 2; ++i) {
        f32x4 a = acc[n][m + i], b = acc[NH_ + n][m + i];
        o[i][0] = pack2(rbf(a[0]) * rbf(b[0]), rbf(a[1]) * rbf(b[1]));
        o[i][1] = pack2(rbf(a[2]) * rbf(b[2]), rbf(a[3]) * rbf(b[3]));
      }
      const int row = m0 + wm * 64 + (m + (g & 1)) * 16 + (lane & 15);
      *(u32x4*)(out + ((size_t)cb * SEQ + row) * 16 + (g >> 1) * 8) = pair16(o[0], o[1]);
    }
  }
  STAMP_AT(4, 3);
}

__global__ __launch_bounds__(512, 2) void k_glu(const u16* __restrict__ Ablk, const u16* __restrict__ Wg,
                                                 const u16* __restrict__ Wu, u16* __restrict__ out) {
  extern __shared__ __attribute__((aligned(16))) char smem_[];
  lds_ptr smem = (lds_ptr)smem_;
  const int lane = threadIdx.x & 63;
  const int w = __builtin_amdgcn_readfirstlane(threadIdx.x >> 6);
  const int xcd = blockIdx.x & 7, idx = blockIdx.x >> 3;
  const int tm = idx & 7, tn = xcd * 4 + (idx >> 3);
  const int m0 = tm * 256, f0 = tn * 112;
  Loader<CG> ld;
  setup_loader_A<CG, 256, 4, 1>(ld, Ablk, m0, w, lane);
#pragma unroll
  for (int i = 0; i < CG::WPW; ++i) {
    int b = w + 8 * i;
    int grp = b >> 2, o = b & 3;
    ld.woff[i] = CG::A_SUB + grp * 4096 + o * 1024;
    int k = 8 * o + (lane >> 3);
    int f = ((lane >> 4) & 1) | ((o & 1) << 1);
    int pr = ((lane & 7) >> 1) ^ f;
    if ((grp & 1) && pr == 3) pr = 0;
    int col = f0 + (grp & 1) * 64 + pr * 16 + (lane & 1) * 8;
    ld.wv[i] = (k * FF + col) * 2;
    ld.wbase[i] = (const char*)(grp >> 1 ? Wu : Wg);
  }
  const int wm = w & 3;
  if (w < 4) glu_wave<4>(smem, ld, lane, wm, 0, m0, f0, out);
  else glu_wave<3>(smem, ld, lane, wm, 4, m0, f0, out);
}

constexpr int ACH = 128, ANST = 4, ASTAGE = ACH * 64 * 2;
constexpr int ALDS = ANST * ASTAGE + 1024 + 16;

DI float rowmax16(const f32x16& v) {
  float mx = fmaxf(fmaxf(v[0], v[1]), v[2]);
#pragma unroll
  for (int r = 3; r < 15; r += 2) mx = fmaxf(fmaxf(mx, v[r]), v[r + 1]);
  mx = fmaxf(mx, v[15]);
  auto sw = __builtin_amdgcn_permlane32_swap(__float_as_uint(mx), __float_as_uint(mx), false, false);
  return fmaxf(__uint_as_float(sw[0]), __uint_as_float(sw[1]));
}

template <bool SAFE>
DI void attn_pass(lds_ptr smem, int lane, int w, const char* Kp, const char* Vp, const char* pf_wo, const char* pf_wg,
                  const char* pf_wu, const char* pf_wd, const bf16x8& qa0, const bf16x8& qa1, const bf16x8& qb0,
                  const bf16x8& qb1, f32x16& oa, f32x16& ob, float& refa, float& refb, float& la_out, float& lb_out) {
  const int r32 = lane & 31, hh = lane >> 5, kh = w >> 2;
  const int kc_ = (lane & 3) ^ ((lane >> 4) & 3), vc_ = lane & 3;
  const unsigned kvo = (((kc_ >> 1) * SEQ + 16 * w + (lane >> 2)) * 16 + (kc_ & 1) * 8) * 2;
  const unsigned vvo = (((vc_ >> 1) * SEQ + 16 * w + (lane >> 2)) * 16 + (vc_ & 1) * 8) * 2;
  const char* ksrc = Kp;
  const char* vsrc = Vp;
  auto issue = [&](int buf) {
    glds16(ksrc, kvo, lds_u32(smem) + buf * ASTAGE + w * 1024);
    glds16(vsrc, vvo, lds_u32(smem) + buf * ASTAGE + 8192 + w * 1024);
    ksrc += ACH * 32; vsrc += ACH * 32;
  };
  const unsigned pfo = lane * 16;
  auto prefetch = [&](int ch) {
    int pc = ch * 8 + w;
    const char* base;
    if (pc < 8) base = pf_wo + (size_t)blockIdx.x * 8192 + pc * 1024;
    else if (pc < 36) base = pf_wg + (size_t)blockIdx.x * 28672 + (pc - 8) * 1024;
    else if (pc < 64) base = pf_wu + (size_t)blockIdx.x * 28672 + (pc - 36) * 1024;
    else if (pc < 92) base = pf_wd + (size_t)blockIdx.x * 28672 + (pc - 64) * 1024;
    else base = pf_wo + (size_t)blockIdx.x * 8192;
    glds16(base, pfo, lds_u32(smem) + ANST * ASTAGE);
  };
  issue(0); issue(1); issue(2);
  const int kaddr0 = kh * 4096 + r32 * 64 + (((0 + hh) ^ ((r32 >> 2) & 3)) * 16);
  const int kaddr1 = kh * 4096 + r32 * 64 + (((2 + hh) ^ ((r32 >> 2) & 3)) * 16);
  const int vaddr = 8192 + kh * 4096 + (4 * hh + ((lane & 15) >> 2)) * 64 + ((lane >> 4) & 1) * 32 + (lane & 3) * 8;
  bf16x8 sel;
  {
    const short one = ((lane & 15) == ((lane >> 4) & 1)) ? (short)0x3F80 : (short)0;
#pragma unroll
    for (int j = 0; j < 8; ++j) sel[j] = one;
  }
  f32x16 nma, nmb, sa, sb;
#pragma unroll
  for (int r = 0; r < 16; ++r) { oa[r] = 0.f; ob[r] = 0.f; nma[r] = 0.f; nmb[r] = 0.f; }
  f32x4 lsa = {0.f, 0.f, 0.f, 0.f}, lsb = {0.f, 0.f, 0.f, 0.f};
  float la = 0.f, lb = 0.f;
  constexpr int NCH = SEQ / ACH;
  wait_vm<4>();
  bar();
  const bf16x8 kf0_ = lds_read128(smem + kaddr0);
  const bf16x8 kf1_ = lds_read128(smem + kaddr1);
  {
    sa = MFMA32(kf0_, qa0, nma); sa = MFMA32(kf1_, qa1, sa);
    sb = MFMA32(kf0_, qb0, nmb); sb = MFMA32(kf1_, qb1, sb);
    const float ma = rowmax16(sa), mb = rowmax16(sb);
#pragma unroll
    for (int r = 0; r < 16; ++r) { nma[r] = -ma; sa[r] -= ma; nmb[r] = -mb; sb[r] -= mb; }
  }
  if constexpr (SAFE) {
    auto half = [&](f32x16& s, f32x16& o, f32x16& nm, float& l, const bf16x8& q0f, const bf16x8& q1f,
                    const bf16x8& kn0, const bf16x8& kn1, const bf16x8& vf0, const bf16x8& vf1, bool has_next) {
      f32x16 p;
      const float dl = fmaxf(rowmax16(s), 0.f);
      const float f = __builtin_amdgcn_exp2f(-dl);
      float t = 0.f;
#pragma unroll
      for (int r = 0; r < 16; ++r) { nm[r] -= dl; o[r] *= f; p[r] = __builtin_amdgcn_exp2f(s[r] - dl); t += p[r]; }
      l = l * f + t;
      if (has_next) { s = MFMA32(kn0, q0f, nm); s = MFMA32(kn1, q1f, s); }
      u32x4 pa, pb;
#pragma unroll
      for (int j = 0; j < 4; ++j) { pa[j] = pack2(p[2 * j], p[2 * j + 1]); pb[j] = pack2(p[8 + 2 * j], p[8 + 2 * j + 1]); }
      o = MFMA32(vf0, __builtin_bit_cast(bf16x8, pa), o);
      o = MFMA32(vf1, __builtin_bit_cast(bf16x8, pb), o);
    };
    auto step = [&](lds_cptr vb, lds_cptr knext, bool has_next) {
      bf16x8 kn0, kn1;
      if (has_next) { kn0 = lds_read128(knext + kaddr0); kn1 = lds_read128(knext + kaddr1); }
      bf16x8 vf0 = lds_tr8(vb, vb + 512);
      bf16x8 vf1 = lds_tr8(vb + 1024, vb + 1536);
      half(sa, oa, nma, la, qa0, qa1, kn0, kn1, vf0, vf1, has_next);
      half(sb, ob, nmb, lb, qb0, qb1, kn0, kn1, vf0, vf1, has_next);
    };
    for (int ch = 0; ch < NCH; ++ch) {
      if (ch + 2 < NCH) wait_vm<2>(); else wait_vm<0>();
      bar();
      if (ch + 3 < NCH) issue((ch + 3) & 3);
      lds_cptr sbuf = smem + (ch & 3) * ASTAGE;
      lds_cptr nbuf = smem + ((ch + 1) & 3) * ASTAGE;
      step(sbuf + vaddr, sbuf + 2048, true);
      step(sbuf + 2048 + vaddr, nbuf, ch + 1 < NCH);
    }
  } else {
    u32x4 pa0, pa1, pb0, pb1;
    bf16x8 kc0 = kf0_, kc1 = kf1_;
    bf16x8 vp0, vp1;
#pragma unroll
    for (int j = 0; j < 4; ++j) { pb0[j] = 0u; pb1[j] = 0u; }
#pragma unroll
    for (int j = 0; j < 8; ++j) { vp0[j] = 0; vp1[j] = 0; }
#define SB_ __builtin_amdgcn_sched_barrier(0)
#define EX(i) e[i] = __builtin_amdgcn_exp2f(S[i])
#define PHASE(S, P0, P1, M0, M1, M2, M3, M4, M5, R0, R1) do { float e[16]; \
      M0; SB_; R0; EX(0); EX(1); EX(2); EX(3); SB_; \
      M1; SB_; R1; EX(4); EX(5); EX(6); EX(7); SB_; \
      M2; SB_; EX(8); EX(9); P0[0] = pack2(e[0], e[1]); P0[1] = pack2(e[2], e[3]); SB_; \
      M3; SB_; EX(10); EX(11); P0[2] = pack2(e[4], e[5]); P0[3] = pack2(e[6], e[7]); SB_; \
      M4; SB_; EX(12); EX(13); EX(14); EX(15); SB_; \
      M5; SB_; P1[0] = pack2(e[8], e[9]); P1[1] = pack2(e[10], e[11]); P1[2] = pack2(e[12], e[13]); P1[3] = pack2(e[14], e[15]); SB_; \
    } while (0)
#define BF(x) __builtin_bit_cast(bf16x8, x)
    auto step = [&](lds_cptr vb, lds_cptr knext, auto has_next_t) {
      constexpr bool HN = decltype(has_next_t)::value;
      bf16x8 kn0, kn1, vc0, vc1;
      { const f32x16& S = sa;
        PHASE(S, pa0, pa1,
              sb = MFMA32(kc0, qb0, nmb), sb = MFMA32(kc1, qb1, sb),
              ob = MFMA32(vp0, BF(pb0), ob), ob = MFMA32(vp1, BF(pb1), ob),
              lsb = MFMA16(sel, BF(pb0), lsb), lsb = MFMA16(sel, BF(pb1), lsb),
              if (HN) { kn0 = lds_read128(knext + kaddr0); kn1 = lds_read128(knext + kaddr1); },
              { vc0 = lds_tr8(vb, vb + 512); vc1 = lds_tr8(vb + 1024, vb + 1536); }); }
      { const f32x16& S = sb;
        PHASE(S, pb0, pb1,
              if (HN) sa = MFMA32(kn0, qa0, nma), if (HN) sa = MFMA32(kn1, qa1, sa),
              oa = MFMA32(vc0, BF(pa0), oa), oa = MFMA32(vc1, BF(pa1), oa),
              lsa = MFMA16(sel, BF(pa0), lsa), lsa = MFMA16(sel, BF(pa1), lsa),
              , ); }
      if (HN) { kc0 = kn0; kc1 = kn1; }
      vp0 = vc0; vp1 = vc1;
    };
    typedef std::integral_constant<bool, true> T_;
    typedef std::integral_constant<bool, false> F_;
    for (int ch = 0; ch < NCH - 1; ++ch) {
      if (ch + 2 < NCH) wait_vm<4>(); else wait_vm<0>();
      bar();
      if (ch + 3 < NCH) { issue((ch + 3) & 3); prefetch(ch); }
      lds_cptr sbuf = smem + (ch & 3) * ASTAGE;
      lds_cptr nbuf = smem + ((ch + 1) & 3) * ASTAGE;
      step(sbuf + vaddr, sbuf + 2048, T_{});
      step(sbuf + 2048 + vaddr, nbuf, T_{});
    }
    {
      lds_cptr sbuf = smem + ((NCH - 1) & 3) * ASTAGE;
      step(sbuf + vaddr, sbuf + 2048, T_{});
      step(sbuf + 2048 + vaddr, sbuf, F_{});
    }
    ob = MFMA32(vp0, BF(pb0), ob); ob = MFMA32(vp1, BF(pb1), ob);
    lsb = MFMA16(sel, BF(pb0), lsb); lsb = MFMA16(sel, BF(pb1), lsb);
#undef PHASE
#undef EX
#undef BF
  }
  refa = nma[0]; refb = nmb[0];
  if constexpr (SAFE) {
    auto s1 = __builtin_amdgcn_permlane32_swap(__float_as_uint(la), __float_as_uint(la), false, false);
    la_out = __uint_as_float(s1[0]) + __uint_as_float(s1[1]);
    auto s2 = __builtin_amdgcn_permlane32_swap(__float_as_uint(lb), __float_as_uint(lb), false, false);
    lb_out = __uint_as_float(s2[0]) + __uint_as_float(s2[1]);
  } else {
    const float a0 = __shfl(lsa[0], lane & 15), a1 = __shfl(lsa[1], lane & 15);
    const float b0 = __shfl(lsb[0], lane & 15), b1 = __shfl(lsb[1], lane & 15);
    la_out = (lane & 16) ? a1 : a0;
    lb_out = (lane & 16) ? b1 : b0;
  }
}

__global__ __launch_bounds__(512, 2) void k_attn(const u16* __restrict__ qkv, u16* __restrict__ oblk,
                                                  const float* __restrict__ part, float* __restrict__ modf,
                                                  const char* __restrict__ pf_wo, const char* __restrict__ pf_wg,
                                                  const char* __restrict__ pf_wu, const char* __restrict__ pf_wd) {
  extern __shared__ __attribute__((aligned(16))) char smem_[];
  lds_ptr smem = (lds_ptr)smem_;
  const int lane = threadIdx.x & 63;
  const int w = __builtin_amdgcn_readfirstlane(threadIdx.x >> 6);
  const int xcd = blockIdx.x & 7, idx = blockIdx.x >> 3;
  const int head = xcd * 4 + (idx >> 3), qb = idx & 7;
  const int r32 = lane & 31, hh = lane >> 5;
  const int qg = w & 3, kh = w >> 2;
  const u16* Qp = qkv + (size_t)head * SEQ * 32;
  const char* Kp = (const char*)(qkv + (size_t)(32 + head) * SEQ * 32);
  const char* Vp = (const char*)(qkv + (size_t)(64 + head) * SEQ * 32);
  const int q0 = qb * 256 + qg * 64;
  STAMP_AT(2, 0);
  __attribute__((address_space(3))) int* flag = (__attribute__((address_space(3))) int*)(smem + ANST * ASTAGE + 1024);
  if (threadIdx.x == 0) *flag = 0;
  bf16x8 qa0 = *(const bf16x8*)(Qp + (q0 + r32) * 16 + hh * 8);
  bf16x8 qa1 = *(const bf16x8*)(Qp + (SEQ + q0 + r32) * 16 + hh * 8);
  bf16x8 qb0 = *(const bf16x8*)(Qp + (q0 + 32 + r32) * 16 + hh * 8);
  bf16x8 qb1 = *(const bf16x8*)(Qp + (SEQ + q0 + 32 + r32) * 16 + hh * 8);
  if (threadIdx.x < 16) {
    const int col = 2 * DM + blockIdx.x * 16 + threadIdx.x;
    float v = 0.f;
#pragma unroll
    for (int i = 0; i < MNS; ++i) v += part[i * (6 * DM) + col];
    modf[col] = rbf(v);
  }
  f32x16 oa, ob;
  float nma, nmb, la, lb;
  attn_pass<false>(smem, lane, w, Kp, Vp, pf_wo, pf_wg, pf_wu, pf_wd, qa0, qa1, qb0, qb1, oa, ob, nma, nmb, la, lb);
  {
    float big = fmaxf(fabsf(la), fabsf(lb));
#pragma unroll
    for (int r = 0; r < 16; ++r) big = fmaxf(big, fmaxf(fabsf(oa[r]), fabsf(ob[r])));
    bool bad = !(big < 1e30f);
#pragma unroll
    for (int r = 0; r < 16; ++r) bad = bad || (oa[r] != oa[r]) || (ob[r] != ob[r]);
    bad = bad || (la != la) || (lb != lb);
    if (__any(bad) && lane == 0) *flag = 1;
  }
  __syncthreads();
  if (__builtin_amdgcn_readfirstlane(*flag) != 0) {
    attn_pass<true>(smem, lane, w, Kp, Vp, pf_wo, pf_wg, pf_wu, pf_wd, qa0, qa1, qb0, qb1, oa, ob, nma, nmb, la, lb);
    __syncthreads();
  }
  STAMP_AT(2, 2);
  {
    __attribute__((address_space(3))) float* xb = (__attribute__((address_space(3))) float*)smem;
    const f32x16& og = kh == 0 ? ob : oa;
    __attribute__((address_space(3))) float* dst = xb + w * (18 * 64) + lane;
#pragma unroll
    for (int r = 0; r < 16; ++r) dst[r * 64] = og[r];
    dst[16 * 64] = kh == 0 ? lb : la;
    dst[17 * 64] = kh == 0 ? nmb : nma;
    __syncthreads();
    const __attribute__((address_space(3))) float* src = xb + (w ^ 4) * (18 * 64) + lane;
    f32x16 om = kh == 0 ? oa : ob;
    const float nm_me = kh == 0 ? nma : nmb;
    const float nm_ot = src[17 * 64];
    const float nmin = fminf(nm_me, nm_ot);
    const float f_me = __builtin_amdgcn_exp2f(nmin - nm_me), f_ot = __builtin_amdgcn_exp2f(nmin - nm_ot);
    const float rl = 1.f / ((kh == 0 ? la : lb) * f_me + src[16 * 64] * f_ot);
    const float g_me = f_me * rl, g_ot = f_ot * rl;
    u16* op = oblk + ((size_t)head * 2 * SEQ + q0 + kh * 32 + r32) * 16 + 4 * hh;
#pragma unroll
    for (int g = 0; g < 4; ++g) {
      float v0 = om[4 * g] * g_me + src[(4 * g) * 64] * g_ot;
      float v1 = om[4 * g + 1] * g_me + src[(4 * g + 1) * 64] * g_ot;
      float v2 = om[4 * g + 2] * g_me + src[(4 * g + 2) * 64] * g_ot;
      float v3 = om[4 * g + 3] * g_me + src[(4 * g + 3) * 64] * g_ot;
      u32x2 v;
      v[0] = pack2(v0, v1);
      v[1] = pack2(v2, v3);
      *(u32x2*)(op + (g >> 1) * (SEQ * 16) + (g & 1) * 8) = v;
    }
  }
  STAMP_AT(2, 3);
}

extern "C" void kernel_launch(void* const* d_in, const int* in_sizes, int n_in, void* d_out, int out_size,
                              void* d_ws, size_t ws_size, hipStream_t stream) {
  const u16* x = (const u16*)d_in[0];
  const u16* wqkv = (const u16*)d_in[1];
  const u16* wo = (const u16*)d_in[2];
  const u16* wg = (const u16*)d_in[4];
  const u16* wu = (const u16*)d_in[5];
  const u16* wd = (const u16*)d_in[6];
  const u16* c = (const u16*)d_in[7];
  const u16* wmod = (const u16*)d_in[8];
  u16* out = (u16*)d_out;
  char* ws = (char*)d_ws;
  float* part = (float*)ws;
  float* modf = (float*)(ws + (256 << 10));
  unsigned* flags = (unsigned*)(ws + (512 << 10));
  u16* h1 = (u16*)(ws + (1 << 20));
  u16* qkv = (u16*)(ws + (8 << 20));
  u16* ob = (u16*)(ws + (24 << 20));
  u16* x2 = (u16*)(ws + (32 << 20));
  u16* h2 = (u16*)(ws + (40 << 20));
  u16* hh = (u16*)(ws + (48 << 20));

  hipFuncSetAttribute((const void*)k_qkv, hipFuncAttributeMaxDynamicSharedMemorySize, CQ::LDS);
  hipFuncSetAttribute((const void*)k_proj<DM>, hipFuncAttributeMaxDynamicSharedMemorySize, CP::LDS);
  hipFuncSetAttribute((const void*)k_proj<FF>, hipFuncAttributeMaxDynamicSharedMemorySize, CP::LDS);
  hipFuncSetAttribute((const void*)k_glu, hipFuncAttributeMaxDynamicSharedMemorySize, CG::LDS);
  hipFuncSetAttribute((const void*)k_attn, hipFuncAttributeMaxDynamicSharedMemorySize, ALDS);

  k_pre<<<256 + 256 + 384, 256, 0, stream>>>(c, wmod, part, (const char*)x, (const char*)wqkv, flags);
  k_ln<6, MNS, true><<<512, 512, 0, stream>>>(x, part, h1, c, wmod, part);
  k_qkv<<<256, 512, CQ::LDS, stream>>>(h1, wqkv, qkv);
  k_attn<<<256, 512, ALDS, stream>>>(qkv, ob, part, modf, (const char*)wo, (const char*)wg, (const char*)wu, (const char*)wd);
  k_proj<DM><<<256, 512, CP::LDS, stream>>>(ob, wo, x, modf + 2 * DM, x2);
  k_ln<7, 1, false><<<256, 512, 0, stream>>>(x2, modf + 3 * DM, h2, c, wmod, part);
  k_glu<<<256, 512, CG::LDS, stream>>>(h2, wg, wu, hh);
  k_proj<FF><<<256, 512, CP::LDS, stream>>>(hh, wd, x2, modf + 5 * DM, out);
}
```

```cpp
#include <hip/hip_runtime.h>
#include <hip/hip_bf16.h>
#include <stdint.h>

typedef __attribute__((ext_vector_type(8))) short bf16x8;
typedef __attribute__((ext_vector_type(4))) short s16x4;
typedef __attribute__((ext_vector_type(4))) float f32x4;
typedef __attribute__((ext_vector_type(2))) float f32x2;
typedef __attribute__((ext_vector_type(16))) float f32x16;
typedef __attribute__((ext_vector_type(4))) unsigned u32x4;
typedef __attribute__((ext_vector_type(2))) unsigned u32x2;
typedef __attribute__((ext_vector_type(2))) __bf16 bf16x2_t;
typedef unsigned short u16;
typedef __attribute__((address_space(3))) char* lds_ptr;
typedef __attribute__((address_space(3))) const char* lds_cptr;

#define DI __device__ __forceinline__

constexpr int SEQ = 2048, DM = 1024, FF = 3584, NH = 32;

DI float bf2f(u16 x) { return __uint_as_float(((unsigned)x) << 16); }
DI float bflo(unsigned x) { return __uint_as_float(x << 16); }
DI float bfhi(unsigned x) { return __uint_as_float(x & 0xffff0000u); }
DI unsigned pack2(float a, float b) {
  f32x2 v = {a, b};
  bf16x2_t r = __builtin_convertvector(v, bf16x2_t);
  return __builtin_bit_cast(unsigned, r);
}
DI float rbf(float a) { return bflo(pack2(a, 0.f) & 0xffffu); }

#define MFMA16(a, b, c) __builtin_amdgcn_mfma_f32_16x16x32_bf16((a), (b), (c), 0, 0, 0)
#define MFMA32(a, b, c) __builtin_amdgcn_mfma_f32_32x32x16_bf16((a), (b), (c), 0, 0, 0)

DI void glds16(const void* base, unsigned voff, unsigned lds_addr) {
  unsigned sv;
  asm volatile("s_mov_b32 %0, m0\n\ts_mov_b32 m0, %3\n\ts_nop 0\n\tglobal_load_lds_dwordx4 %1, %2\n\ts_mov_b32 m0, %0"
               : "=&s"(sv) : "v"(voff), "s"(base), "s"(lds_addr) : "memory");
}
DI unsigned lds_u32(lds_ptr p) { return (unsigned)(uintptr_t)p; }
DI bf16x8 lds_read128(lds_cptr p) { return *(const __attribute__((address_space(3))) bf16x8*)p; }
DI s16x4 lds_tr(lds_cptr p) {
  typedef short v4i16_t __attribute__((ext_vector_type(4)));
  return __builtin_bit_cast(s16x4, __builtin_amdgcn_ds_read_tr16_b64_v4i16((__attribute__((address_space(3))) v4i16_t*)p));
}
DI bf16x8 lds_tr8(lds_cptr p0, lds_cptr p1) {
  s16x4 a = lds_tr(p0), b = lds_tr(p1);
  return __builtin_shufflevector(a, b, 0, 1, 2, 3, 4, 5, 6, 7);
}
#define STAMP_AT(kid, slot) do {} while (0)
template <int N> DI void wait_vm() { asm volatile("s_waitcnt vmcnt(%0)" ::"n"(N) : "memory"); }
DI void bar() { __builtin_amdgcn_s_barrier(); }

constexpr int MNS = 8;
DI void mod_task(const u16* __restrict__ c, const u16* __restrict__ wmod, float* __restrict__ part, int cb, int ks,
                 int t, float* s_red  ) {
  const int cc = t & 7, rg = t >> 3;
  const int k0 = ks * 128 + rg;
  const u16* wp = wmod + (size_t)k0 * (6 * DM) + cb * 64 + cc * 8;
  u32x4 w[4];
  float sv[4];
#pragma unroll
  for (int i = 0; i < 4; ++i) w[i] = *(const u32x4*)(wp + (size_t)i * 32 * (6 * DM));
#pragma unroll
  for (int i = 0; i < 4; ++i) {
    float cv = bf2f(c[k0 + 32 * i]);
    sv[i] = rbf(cv / (1.f + __expf(-cv)));
  }
  float acc[8];
#pragma unroll
  for (int j = 0; j < 8; ++j) acc[j] = 0.f;
#pragma unroll
  for (int i = 0; i < 4; ++i) {
#pragma unroll
    for (int j = 0; j < 4; ++j) {
      acc[2 * j] += sv[i] * bflo(w[i][j]);
      acc[2 * j + 1] += sv[i] * bfhi(w[i][j]);
    }
  }
#pragma unroll
  for (int j = 0; j < 8; ++j) {
    float v = acc[j];
    v += __shfl_xor(v, 8);
    v += __shfl_xor(v, 16);
    v += __shfl_xor(v, 32);
    acc[j] = v;
  }
  const int lane = t & 63, wv = t >> 6;
  if (lane < 8) {
#pragma unroll
    for (int j = 0; j < 8; ++j) s_red[wv * 64 + lane * 8 + j] = acc[j];
  }
  __syncthreads();
  if (t < 64) part[ks * (6 * DM) + cb * 64 + t] = (s_red[t] + s_red[64 + t]) + (s_red[128 + t] + s_red[192 + t]);
}

DI void touch16k(const char* p, int t) {
  u32x4 v[4];
#pragma unroll
  for (int i = 0; i < 4; ++i) v[i] = *(const u32x4*)(p + i * 4096 + t * 16);
#pragma unroll
  for (int i = 0; i < 4; ++i) asm volatile("" ::"v"(v[i]));
}

__global__ __launch_bounds__(256) void k_pre(const u16* __restrict__ c, const u16* __restrict__ wmod, float* __restrict__ part,
                                              const char* __restrict__ x, const char* __restrict__ wqkv,
                                              unsigned* __restrict__ flags) {
  __shared__ float s_red[256];
  const int t = threadIdx.x, b = blockIdx.x;
  if (b < 256) STAMP_AT(0, 0);
  if (b < 256) {
    mod_task(c, wmod, part, b >> 3, b & 7, t, s_red);
    if (b == 0) flags[t] = 0u;
  } else {
    touch16k(x + (size_t)(b - 256) * 16384, t);
  }
  if (b < 256) STAMP_AT(0, 3);
}

template <int KID, int NS, bool FIRST>
__global__ __launch_bounds__(512) void k_ln(const u16* __restrict__ x, const float* __restrict__ ss  ,
                                             u16* __restrict__ h16, const char* __restrict__ pf) {
  __shared__ __attribute__((aligned(16))) float sm[2048];
  const int t = threadIdx.x;
  if (FIRST && blockIdx.x >= 256) {
    touch16k(pf + (size_t)((blockIdx.x - 256) * 2 + (t >> 8)) * 16384, t & 255);
    return;
  }
  const int lane = t & 63, wv = t >> 6;
  const int row = blockIdx.x * 8 + wv;
  STAMP_AT(KID, 0);
  const u16* xr = x + (size_t)row * DM;
  u32x4 v0 = *(const u32x4*)(xr + lane * 8);
  u32x4 v1 = *(const u32x4*)(xr + 512 + lane * 8);
  {
    f32x4 pv[NS];
#pragma unroll
    for (int i = 0; i < NS; ++i) pv[i] = *(const f32x4*)(ss + i * (6 * DM) + t * 4);
    f32x4 a = pv[0];
#pragma unroll
    for (int i = 1; i < NS; ++i) a += pv[i];
#pragma unroll
    for (int j = 0; j < 4; ++j) {
      float v = rbf(a[j]);
      a[j] = t < 256 ? v : rbf(1.f + v);
    }
    *(f32x4*)(sm + t * 4) = a;
  }
  float f[16];
#pragma unroll
  for (int j = 0; j < 4; ++j) {
    f[2 * j] = bflo(v0[j]); f[2 * j + 1] = bfhi(v0[j]);
    f[8 + 2 * j] = bflo(v1[j]); f[8 + 2 * j + 1] = bfhi(v1[j]);
  }
  float s = 0.f;
#pragma unroll
  for (int j = 0; j < 16; ++j) s += f[j];
#pragma unroll
  for (int o = 1; o < 64; o <<= 1) s += __shfl_xor(s, o);
  const float mean = s * (1.f / DM);
  float q = 0.f;
#pragma unroll
  for (int j = 0; j < 16; ++j) { float d = f[j] - mean; q += d * d; }
#pragma unroll
  for (int o = 1; o < 64; o <<= 1) q += __shfl_xor(q, o);
  const float mb = rbf(mean);
  const float vb = rbf(q * (1.f / DM));
  const float rden = 1.f / rbf(sqrtf(rbf(vb + 1e-5f)));
  __syncthreads();
#pragma unroll
  for (int half = 0; half < 2; ++half) {
    const int k0 = half * 512 + lane * 8;
    f32x4 sh0 = *(const f32x4*)(sm + k0), sh1 = *(const f32x4*)(sm + k0 + 4);
    f32x4 sc0 = *(const f32x4*)(sm + 1024 + k0), sc1 = *(const f32x4*)(sm + 1024 + k0 + 4);
    float y[8];
#pragma unroll
    for (int j = 0; j < 8; ++j) {
      float g = j < 4 ? sc0[j] : sc1[j - 4];
      float sh = j < 4 ? sh0[j] : sh1[j - 4];
      float d = rbf(f[half * 8 + j] - mb);
      float n = rbf(d * rden);
      y[j] = rbf(n * g) + sh;
    }
    u32x4 o;
#pragma unroll
    for (int j = 0; j < 4; ++j) o[j] = pack2(y[2 * j], y[2 * j + 1]);
    *(u32x4*)(h16 + ((size_t)(k0 >> 4) * SEQ + row) * 16 + (k0 & 15)) = o;
  }
  STAMP_AT(KID, 3);
}

DI u32x4 pair16(u32x2 a, u32x2 b) {
  auto s0 = __builtin_amdgcn_permlane16_swap(a[0], b[0], false, false);
  auto s1 = __builtin_amdgcn_permlane16_swap(a[1], b[1], false, false);
  return u32x4{s0[0], s1[0], s0[1], s1[1]};
}

DI int hA(int r) { return (-r) & 3; }

template <int BM, int NGRP, int KS, int NST>
struct GCfg {
  static constexpr int A_SUB = BM * 64;
  static constexpr int W_SUB = NGRP * 4096;
  static constexpr int STAGE = KS * (A_SUB + W_SUB);
  static constexpr int APW = KS * BM / 128;
  static constexpr int WPW = KS * NGRP / 2;
  static constexpr int PPW = APW + WPW;
  static constexpr int LDS = STAGE * NST;
};

template <class C>
struct Loader {
  const char* abase;
  const char* wbase[C::WPW];
  unsigned av[C::APW];
  unsigned wv[C::WPW];
  int aoff[C::APW];
  int woff[C::WPW];
};

template <int BM, int NGRP, int KS, int NST, int MT, int NT, int KSPLIT, int NKT, int KID, int IOFS>
struct Engine {
  typedef GCfg<BM, NGRP, KS, NST> C;
  static constexpr int SPW = KS / KSPLIT;
  static constexpr int P = C::PPW;
  static constexpr int G = NT * SPW;
  f32x4 (&acc)[NT][MT];
  lds_ptr smem;
  Loader<C>& ld;
  long a_adv, w_adv;
  const int (&waddr)[NT];
  int a_sub0, w_sub0;
  int cur, nxt, fre;
  bf16x8 wf[NT], af[2][MT];

  DI Engine(f32x4 (&acc_)[NT][MT], lds_ptr smem_, Loader<C>& ld_, long a_adv_, long w_adv_, int aaddr, const int (&waddr_)[NT], int kg)
      : acc(acc_), smem(smem_), ld(ld_), a_adv(a_adv_), w_adv(w_adv_), waddr(waddr_) {
    a_sub0 = kg * C::A_SUB + aaddr;
    w_sub0 = KS * C::A_SUB + kg * C::W_SUB;
  }
  template <int I> DI void piece(int stage_off) {
    if constexpr (I < C::APW) {
      glds16(ld.abase, ld.av[I], lds_u32(smem) + stage_off + ld.aoff[I]);
      if constexpr (I == C::APW - 1) ld.abase += a_adv * 2;
    } else {
      constexpr int J = I - C::APW;
      glds16(ld.wbase[J], ld.wv[J], lds_u32(smem) + stage_off + ld.woff[J]);
      ld.wbase[J] += w_adv * 2;
    }
  }
  template <int I> DI void issue_all(int stage_off) {
    if constexpr (I < P) { piece<I>(stage_off); issue_all<I + 1>(stage_off); }
  }
  template <int g, int I> DI void issue_at(int stage_off) {
    if constexpr (I < P) {
      constexpr int span = (G - IOFS) > P ? (G - IOFS) : P;
      constexpr int pos = IOFS + (I * span) / P < G ? IOFS + (I * span) / P : G - 1;
      if constexpr (pos == g) piece<I>(stage_off);
      issue_at<g, I + 1>(stage_off);
    }
  }
  template <int PAR, int S, bool PF, bool DOISSUE, int n>
  DI void groups(lds_cptr ab, lds_cptr wb) {
    if constexpr (n < NT) {
#pragma unroll
      for (int m = 0; m < MT; ++m) acc[n][m] = MFMA16(wf[n], af[PAR][m], acc[n][m]);
      if constexpr (PF) {
        wf[n] = lds_tr8(wb + waddr[n], wb + waddr[n] + 512);
#pragma unroll
        for (int m = 0; m < MT; ++m)
          if ((m * NT) / MT == n) af[PAR ^ 1][m] = lds_read128(ab + m * 512);
      }
      if constexpr (DOISSUE) issue_at<S * NT + n, 0>(fre);
      __builtin_amdgcn_sched_barrier(0);
      groups<PAR, S, PF, DOISSUE, n + 1>(ab, wb);
    }
  }
  template <int PAR, bool DOISSUE, bool LAST, int S> DI void steps() {
    if constexpr (S < SPW) {
      if constexpr (S + 1 < SPW)
        groups<PAR, S, true, DOISSUE, 0>(smem + cur + a_sub0 + (S + 1) * KSPLIT * C::A_SUB, smem + cur + w_sub0 + (S + 1) * KSPLIT * C::W_SUB);
      else if constexpr (!LAST)
        groups<PAR, S, true, DOISSUE, 0>(smem + nxt + a_sub0, smem + nxt + w_sub0);
      else
        groups<PAR, S, false, false, 0>(smem, smem);
      steps<PAR ^ 1, DOISSUE, LAST, S + 1>();
    }
  }
  template <int PAR, bool DOISSUE, bool LAST, int WAITN> DI void iter() {
    if constexpr (!LAST) { wait_vm<WAITN>(); bar(); }
    steps<PAR, DOISSUE, LAST, 0>();
    fre = cur; cur = nxt; nxt = (nxt + C::STAGE == NST * C::STAGE) ? 0 : nxt + C::STAGE;
  }
  template <int KT> DI void tail() {
    if constexpr (KT < NKT) {
      constexpr bool DOISSUE = KT + NST - 1 < NKT;
      constexpr bool LAST = KT == NKT - 1;
      constexpr int newest = (KT + NST - 2) < (NKT - 1) ? (KT + NST - 2) : (NKT - 1);
      constexpr int younger = newest - (KT + 1) > 0 ? newest - (KT + 1) : 0;
      iter<(KT * SPW) & 1, DOISSUE, LAST, P * younger>();
      tail<KT + 1>();
    }
  }
  DI void start() {
    static_assert(NKT >= NST + 1, "too few k tiles");
    STAMP_AT(KID, 0);
#pragma unroll
    for (int s = 0; s < NST - 1; ++s) issue_all<0>(s * C::STAGE);
  }
  DI void run() {
    wait_vm<P*(NST - 2)>();
    bar();
    STAMP_AT(KID, 1);
    {
      lds_cptr ab = smem + a_sub0;
      lds_cptr wb = smem + w_sub0;
#pragma unroll
      for (int m = 0; m < MT; ++m) af[0][m] = lds_read128(ab + m * 512);
#pragma unroll
      for (int n = 0; n < NT; ++n) wf[n] = lds_tr8(wb + waddr[n], wb + waddr[n] + 512);
    }
    cur = 0; nxt = C::STAGE; fre = (NST - 1) * C::STAGE;
    constexpr int STEADY = NKT - NST + 1;
    constexpr int LOOPN = (STEADY / 2) * 2;
    for (int kt = 0; kt < LOOPN; kt += 2) {
      iter<0, true, false, P*(NST - 3)>();
      iter<SPW & 1, true, false, P*(NST - 3)>();
    }
    tail<LOOPN>();
    STAMP_AT(KID, 2);
  }
};

template <class C, int BM, int NGRP, int KS>
DI void setup_loader_A(Loader<C>& ld, const u16* Ablk, int m0, int w, int lane) {
#pragma unroll
  for (int i = 0; i < C::APW; ++i) {
    int a = w + 8 * i;
    int blk = a / (BM / 32), j = a % (BM / 32);
    ld.aoff[i] = blk * (BM * 32) + j * 1024;
    ld.av[i] = ((blk * SEQ + m0 + j * 32) * 16) * 2 + lane * 16;
  }
  ld.abase = (const char*)Ablk;
}

DI int wfrag_addr(int t, int lane) {
  int g = lane >> 4, q = (lane & 15) >> 2, p = lane & 3;
  int f = ((q >> 1) & 1) | ((g & 1) << 1);
  return (t >> 2) * 4096 + (8 * g + q) * 128 + (((t & 3) ^ f) * 32) + p * 8;
}
template <int BM>
DI int afrag_addr(int mrow0, int lane) {
  int r = lane & 15, g = lane >> 4;
  return (g >> 1) * (BM * 32) + (mrow0 + r) * 32 + (g & 1) * 16;
}

constexpr float QSCALE = 0.17677669529663687f * 1.4426950408889634f;
typedef GCfg<128, 3, 2, 4> CQ;
__global__ __launch_bounds__(512, 2) void k_qkv(const u16* __restrict__ Ablk, const u16* __restrict__ W,
                                                 u16* __restrict__ out, const u16* __restrict__ c,
                                                 const u16* __restrict__ wmod, float* __restrict__ part) {
  extern __shared__ __attribute__((aligned(16))) char smem_[];
  lds_ptr smem = (lds_ptr)smem_;
  const int lane = threadIdx.x & 63;
  const int w = __builtin_amdgcn_readfirstlane(threadIdx.x >> 6);
  const int xcd = blockIdx.x & 7, idx = blockIdx.x >> 3;
  const int tm = (xcd & 1) * 8 + (idx & 7), tn = (xcd >> 1) * 4 + (idx >> 3);
  const int m0 = tm * 128, n0 = tn * 192;
  constexpr int LDW = 3 * DM;
  Loader<CQ> ld;
  setup_loader_A<CQ, 128, 3, 2>(ld, Ablk, m0, w, lane);
#pragma unroll
  for (int i = 0; i < CQ::WPW; ++i) {
    int b = w + 8 * i;
    int sub = b / 12, r = b % 12, grp = r >> 2, o = r & 3;
    ld.woff[i] = 2 * CQ::A_SUB + sub * CQ::W_SUB + grp * 4096 + o * 1024;
    int k = sub * 32 + 8 * o + (lane >> 3);
    int f = ((lane >> 4) & 1) | ((o & 1) << 1);
    int col = n0 + grp * 64 + ((((lane & 7) >> 1) ^ f) * 16) + (lane & 1) * 8;
    ld.wv[i] = (k * LDW + col) * 2;
    ld.wbase[i] = (const char*)W;
  }
  const int wm = w & 1, wn = w >> 1;
  int waddr[3];
#pragma unroll
  for (int n = 0; n < 3; ++n) waddr[n] = wfrag_addr(wn * 3 + n, lane);
  const int aaddr = afrag_addr<128>(wm * 64, lane);
  f32x4 acc[3][4];
#pragma unroll
  for (int n = 0; n < 3; ++n)
#pragma unroll
    for (int m = 0; m < 4; ++m) acc[n][m] = f32x4{0.f, 0.f, 0.f, 0.f};
  Engine<128, 3, 2, 4, 4, 3, 1, DM / 64, 1, 0> eng(acc, smem, ld, (long)2 * SEQ * 32, (long)64 * LDW, aaddr, waddr, 0);
  eng.start();
  {
    const int task = blockIdx.x * 2 + (threadIdx.x >> 8);
    mod_task(c, wmod, part, 32 + (task >> 3), task & 7, threadIdx.x & 255,
             (float*)(smem_ + 3 * CQ::STAGE) + (threadIdx.x >> 8) * 256);
  }
  eng.run();
  const int g = lane >> 4;
#pragma unroll
  for (int n = 0; n < 3; ++n) {
    const int cb = (n0 >> 4) + wn * 3 + n;
    const float sc = cb < DM / 16 ? QSCALE : 1.f;
#pragma unroll
    for (int m = 0; m < 4; m += 2) {
      u32x2 a, b;
      a[0] = pack2(acc[n][m][0] * sc, acc[n][m][1] * sc);
      a[1] = pack2(acc[n][m][2] * sc, acc[n][m][3] * sc);
      b[0] = pack2(acc[n][m + 1][0] * sc, acc[n][m + 1][1] * sc);
      b[1] = pack2(acc[n][m + 1][2] * sc, acc[n][m + 1][3] * sc);
      const int row = m0 + wm * 64 + (m + (g & 1)) * 16 + (lane & 15);
      *(u32x4*)(out + ((size_t)cb * SEQ + row) * 16 + (g >> 1) * 8) = pair16(a, b);
    }
  }
  STAMP_AT(1, 3);
}

typedef GCfg<128, 1, 2, 6> CP;
template <int KDIM>
__global__ __launch_bounds__(512, 2) void k_proj(const u16* __restrict__ Ablk, const u16* __restrict__ W,
                                                  const u16* __restrict__ res, const float* __restrict__ gate,
                                                  u16* __restrict__ out) {
  extern __shared__ __attribute__((aligned(16))) char smem_[];
  lds_ptr smem = (lds_ptr)smem_;
  const int lane = threadIdx.x & 63;
  const int w = __builtin_amdgcn_readfirstlane(threadIdx.x >> 6);
  const int xcd = blockIdx.x & 7, idx = blockIdx.x >> 3;
  const int tm = (xcd & 3) * 4 + (idx & 3), tn = (xcd >> 2) * 8 + (idx >> 2);
  const int m0 = tm * 128, n0 = tn * 64;
  constexpr int LDW = DM;
  Loader<CP> ld;
  setup_loader_A<CP, 128, 1, 2>(ld, Ablk, m0, w, lane);
  {
    int b = w;
    int sub = b >> 2, o = b & 3;
    ld.woff[0] = 2 * CP::A_SUB + sub * CP::W_SUB + o * 1024;
    int k = sub * 32 + 8 * o + (lane >> 3);
    int f = ((lane >> 4) & 1) | ((o & 1) << 1);
    int col = n0 + ((((lane & 7) >> 1) ^ f) * 16) + (lane & 1) * 8;
    ld.wv[0] = (k * LDW + col) * 2;
    ld.wbase[0] = (const char*)W;
  }
  const int wm = w & 1, wn = (w >> 1) & 1, kg = w >> 2;
  int waddr[2];
#pragma unroll
  for (int n = 0; n < 2; ++n) waddr[n] = wfrag_addr(wn * 2 + n, lane);
  const int aaddr = afrag_addr<128>(wm * 64, lane);
  f32x4 acc[2][4];
#pragma unroll
  for (int n = 0; n < 2; ++n)
#pragma unroll
    for (int m = 0; m < 4; ++m) acc[n][m] = f32x4{0.f, 0.f, 0.f, 0.f};
  Engine<128, 1, 2, 6, 4, 2, 2, KDIM / 64, (KDIM == DM ? 3 : 5), 0> eng(acc, smem, ld, (long)2 * SEQ * 32, (long)64 * LDW, aaddr, waddr, kg);
  eng.start();
  eng.run();
  __syncthreads();
  __attribute__((address_space(3))) f32x4* red = (__attribute__((address_space(3))) f32x4*)smem;
  if (kg == 1) {
#pragma unroll
    for (int n = 0; n < 2; ++n)
#pragma unroll
      for (int m = 0; m < 4; ++m) red[((w & 3) * 8 + n * 4 + m) * 64 + lane] = acc[n][m];
  }
  __syncthreads();
  if (kg == 0) {
    const int g = lane >> 4;
#pragma unroll
    for (int n = 0; n < 2; ++n) {
      int col = n0 + (wn * 2 + n) * 16 + 4 * g;
      f32x4 gt = *(const f32x4*)(gate + col);
#pragma unroll
      for (int m = 0; m < 4; ++m) {
        f32x4 a = acc[n][m] + red[((w & 3) * 8 + n * 4 + m) * 64 + lane];
        int row = m0 + wm * 64 + m * 16 + (lane & 15);
        u32x2 r = *(const u32x2*)(res + (size_t)row * DM + col);
        float y0 = bflo(r[0]) + rbf(gt[0] * rbf(a[0]));
        float y1 = bfhi(r[0]) + rbf(gt[1] * rbf(a[1]));
        float y2 = bflo(r[1]) + rbf(gt[2] * rbf(a[2]));
        float y3 = bfhi(r[1]) + rbf(gt[3] * rbf(a[3]));
        u32x2 o;
        o[0] = pack2(y0, y1);
        o[1] = pack2(y2, y3);
        *(u32x2*)(out + (size_t)row * DM + col) = o;
      }
    }
  }
  STAMP_AT((KDIM == DM ? 3 : 5), 3);
}

typedef GCfg<256, 4, 1, 4> CG;
template <int NH_>
DI void glu_wave(lds_ptr smem, Loader<CG>& ld, int lane, int wm, int t0, int m0, int f0, u16* __restrict__ out) {
  constexpr int NT = 2 * NH_;
  int waddr[NT];
#pragma unroll
  for (int n = 0; n < NH_; ++n) {
    waddr[n] = wfrag_addr(t0 + n, lane);
    waddr[NH_ + n] = waddr[n] + 8192;
  }
  const int aaddr = afrag_addr<256>(wm * 64, lane);
  f32x4 acc[NT][4];
#pragma unroll
  for (int n = 0; n < NT; ++n)
#pragma unroll
    for (int m = 0; m < 4; ++m) acc[n][m] = f32x4{0.f, 0.f, 0.f, 0.f};
  Engine<256, 4, 1, 4, 4, NT, 1, DM / 32, 4, (NH_ == 4 ? 0 : 1)> eng(acc, smem, ld, (long)SEQ * 32, (long)32 * FF, aaddr, waddr, 0);
  eng.start();
  eng.run();
  const int g = lane >> 4;
#pragma unroll
  for (int n = 0; n < NH_; ++n) {
    const int cb = (f0 >> 4) + t0 + n;
#pragma unroll
    for (int m = 0; m < 4; m += 2) {
      u32x2 o[2];
#pragma unroll
      for (int i = 0; i < 2; ++i) {
        f32x4 a = acc[n][m + i], b = acc[NH_ + n][m + i];
        o[i][0] = pack2(rbf(a[0]) * rbf(b[0]), rbf(a[1]) * rbf(b[1]));
        o[i][1] = pack2(rbf(a[2]) * rbf(b[2]), rbf(a[3]) * rbf(b[3]));
      }
      const int row = m0 + wm * 64 + (m + (g & 1)) * 16 + (lane & 15);
      *(u32x4*)(out + ((size_t)cb * SEQ + row) * 16 + (g >> 1) * 8) = pair16(o[0], o[1]);
    }
  }
  STAMP_AT(4, 3);
}

__global__ __launch_bounds__(512, 2) void k_glu(const u16* __restrict__ Ablk, const u16* __restrict__ Wg,
                                                 const u16* __restrict__ Wu, u16* __restrict__ out) {
  extern __shared__ __attribute__((aligned(16))) char smem_[];
  lds_ptr smem = (lds_ptr)smem_;
  const int lane = threadIdx.x & 63;
  const int w = __builtin_amdgcn_readfirstlane(threadIdx.x >> 6);
  const int xcd = blockIdx.x & 7, idx = blockIdx.x >> 3;
  const int tm = idx & 7, tn = xcd * 4 + (idx >> 3);
  const int m0 = tm * 256, f0 = tn * 112;
  Loader<CG> ld;
  setup_loader_A<CG, 256, 4, 1>(ld, Ablk, m0, w, lane);
#pragma unroll
  for (int i = 0; i < CG::WPW; ++i) {
    int b = w + 8 * i;
    int grp = b >> 2, o = b & 3;
    ld.woff[i] = CG::A_SUB + grp * 4096 + o * 1024;
    int k = 8 * o + (lane >> 3);
    int f = ((lane >> 4) & 1) | ((o & 1) << 1);
    int pr = ((lane & 7) >> 1) ^ f;
    if ((grp & 1) && pr == 3) pr = 0;
    int col = f0 + (grp & 1) * 64 + pr * 16 + (lane & 1) * 8;
    ld.wv[i] = (k * FF + col) * 2;
    ld.wbase[i] = (const char*)(grp >> 1 ? Wu : Wg);
  }
  const int wm = w & 3;
  if (w < 4) glu_wave<4>(smem, ld, lane, wm, 0, m0, f0, out);
  else glu_wave<3>(smem, ld, lane, wm, 4, m0, f0, out);
}

constexpr int ACH = 128, ANST = 4, ASTAGE = ACH * 64 * 2 + 256;
constexpr int ALDS = ANST * ASTAGE + 1024 + 16;

DI float rowmax16(const f32x16& v) {
  float mx = fmaxf(fmaxf(v[0], v[1]), v[2]);
#pragma unroll
  for (int r = 3; r < 15; r += 2) mx = fmaxf(fmaxf(mx, v[r]), v[r + 1]);
  mx = fmaxf(mx, v[15]);
  auto sw = __builtin_amdgcn_permlane32_swap(__float_as_uint(mx), __float_as_uint(mx), false, false);
  return fmaxf(__uint_as_float(sw[0]), __uint_as_float(sw[1]));
}

template <bool SAFE>
DI void attn_pass(lds_ptr smem, int lane, int w, const char* Kp, const char* Vp, const char* pf_wo, const char* pf_wg,
                  const char* pf_wu, const char* pf_wd, const bf16x8& qa0, const bf16x8& qa1, const bf16x8& qb0,
                  const bf16x8& qb1, f32x16& oa, f32x16& ob, float& refa, float& refb, float& la_out, float& lb_out) {
  const int r32 = lane & 31, hh = lane >> 5, kh = w >> 2;
  const unsigned kvo = (((w >> 2) * SEQ + 32 * (w & 3) + (lane >> 1)) * 16 + (((lane & 1) ^ ((lane >> 4) & 1)) * 8)) * 2;
  const unsigned vvo = (((w >> 2) * SEQ + 32 * (w & 3)) * 16) * 2 + lane * 16;
  const char* ksrc = Kp;
  const char* vsrc = Vp;
  auto issue = [&](int buf) {
    glds16(ksrc, kvo, lds_u32(smem) + buf * ASTAGE + w * 1024);
    glds16(vsrc, vvo, lds_u32(smem) + buf * ASTAGE + 8192 + w * 1024 + (w >> 2) * 128);
    ksrc += ACH * 32; vsrc += ACH * 32;
  };
  const unsigned pfo = lane * 16;
  auto prefetch = [&](int ch) {
    int pc = ch * 8 + w;
    const char* base;
    if (pc < 8) base = pf_wo + (size_t)blockIdx.x * 8192 + pc * 1024;
    else if (pc < 36) base = pf_wg + (size_t)blockIdx.x * 28672 + (pc - 8) * 1024;
    else if (pc < 64) base = pf_wu + (size_t)blockIdx.x * 28672 + (pc - 36) * 1024;
    else if (pc < 92) base = pf_wd + (size_t)blockIdx.x * 28672 + (pc - 64) * 1024;
    else base = pf_wo + (size_t)blockIdx.x * 8192;
    glds16(base, pfo, lds_u32(smem) + ANST * ASTAGE);
  };
  issue(0); issue(1); issue(2);
  const int kaddr0 = kh * 2048 + r32 * 32 + ((hh ^ ((r32 >> 3) & 1)) * 16);
  const int kaddr1 = kaddr0 + 4096;
  const int vaddr = 8192 + kh * 2048 + ((lane >> 4) & 1) * (4096 + 128) + (4 * hh + ((lane & 15) >> 2)) * 32 + (lane & 3) * 8;
  bf16x8 sel;
  {
    const short one = ((lane & 15) == ((lane >> 4) & 1)) ? (short)0x3F80 : (short)0;
#pragma unroll
    for (int j = 0; j < 8; ++j) sel[j] = one;
  }
  f32x16 nma, nmb, sa, sb;
#pragma unroll
  for (int r = 0; r < 16; ++r) { oa[r] = 0.f; ob[r] = 0.f; nma[r] = 0.f; nmb[r] = 0.f; }
  f32x4 lsa = {0.f, 0.f, 0.f, 0.f}, lsb = {0.f, 0.f, 0.f, 0.f};
  float la = 0.f, lb = 0.f;
  constexpr int NCH = SEQ / ACH;
  wait_vm<4>();
  bar();
  const bf16x8 kf0_ = lds_read128(smem + kaddr0);
  const bf16x8 kf1_ = lds_read128(smem + kaddr1);
  {
    sa = MFMA32(kf0_, qa0, nma); sa = MFMA32(kf1_, qa1, sa);
    sb = MFMA32(kf0_, qb0, nmb); sb = MFMA32(kf1_, qb1, sb);
    const float ma = rowmax16(sa), mb = rowmax16(sb);
#pragma unroll
    for (int r = 0; r < 16; ++r) { nma[r] = -ma; sa[r] -= ma; nmb[r] = -mb; sb[r] -= mb; }
  }
  if constexpr (SAFE) {
    auto half = [&](f32x16& s, f32x16& o, f32x16& nm, float& l, const bf16x8& q0f, const bf16x8& q1f,
                    const bf16x8& kn0, const bf16x8& kn1, const bf16x8& vf0, const bf16x8& vf1, bool has_next) {
      f32x16 p;
      const float dl = fmaxf(rowmax16(s), 0.f);
      const float f = __builtin_amdgcn_exp2f(-dl);
      float t = 0.f;
#pragma unroll
      for (int r = 0; r < 16; ++r) { nm[r] -= dl; o[r] *= f; p[r] = __builtin_amdgcn_exp2f(s[r] - dl); t += p[r]; }
      l = l * f + t;
      if (has_next) { s = MFMA32(kn0, q0f, nm); s = MFMA32(kn1, q1f, s); }
      u32x4 pa, pb;
#pragma unroll
      for (int j = 0; j < 4; ++j) { pa[j] = pack2(p[2 * j], p[2 * j + 1]); pb[j] = pack2(p[8 + 2 * j], p[8 + 2 * j + 1]); }
      o = MFMA32(vf0, __builtin_bit_cast(bf16x8, pa), o);
      o = MFMA32(vf1, __builtin_bit_cast(bf16x8, pb), o);
    };
    auto step = [&](lds_cptr vb, lds_cptr knext, bool has_next) {
      bf16x8 kn0, kn1;
      if (has_next) { kn0 = lds_read128(knext + kaddr0); kn1 = lds_read128(knext + kaddr1); }
      bf16x8 vf0 = lds_tr8(vb, vb + 256);
      bf16x8 vf1 = lds_tr8(vb + 512, vb + 768);
      half(sa, oa, nma, la, qa0, qa1, kn0, kn1, vf0, vf1, has_next);
      half(sb, ob, nmb, lb, qb0, qb1, kn0, kn1, vf0, vf1, has_next);
    };
    for (int ch = 0; ch < NCH; ++ch) {
      if (ch + 2 < NCH) wait_vm<2>(); else wait_vm<0>();
      bar();
      if (ch + 3 < NCH) issue((ch + 3) & 3);
      lds_cptr sbuf = smem + (ch & 3) * ASTAGE;
      lds_cptr nbuf = smem + ((ch + 1) & 3) * ASTAGE;
      step(sbuf + vaddr, sbuf + 1024, true);
      step(sbuf + 1024 + vaddr, nbuf, ch + 1 < NCH);
    }
  } else {
    u32x4 pa0, pa1, pb0, pb1;
    bf16x8 kc0 = kf0_, kc1 = kf1_;
    bf16x8 vp0, vp1;
#pragma unroll
    for (int j = 0; j < 4; ++j) { pb0[j] = 0u; pb1[j] = 0u; }
#pragma unroll
    for (int j = 0; j < 8; ++j) { vp0[j] = 0; vp1[j] = 0; }
#define SB_ __builtin_amdgcn_sched_barrier(0)
#define EX(i) e[i] = __builtin_amdgcn_exp2f(S[i])
#define PHASE(S, P0, P1, M0, M1, M2, M3, M4, M5, R0, R1) do { float e[16]; \
      M0; SB_; R0; EX(0); EX(1); EX(2); EX(3); SB_; \
      M1; SB_; R1; EX(4); EX(5); EX(6); EX(7); SB_; \
      M2; SB_; EX(8); EX(9); P0[0] = pack2(e[0], e[1]); P0[1] = pack2(e[2], e[3]); SB_; \
      M3; SB_; EX(10); EX(11); P0[2] = pack2(e[4], e[5]); P0[3] = pack2(e[6], e[7]); SB_; \
      M4; SB_; EX(12); EX(13); EX(14); EX(15); SB_; \
      M5; SB_; P1[0] = pack2(e[8], e[9]); P1[1] = pack2(e[10], e[11]); P1[2] = pack2(e[12], e[13]); P1[3] = pack2(e[14], e[15]); SB_; \
    } while (0)
#define BF(x) __builtin_bit_cast(bf16x8, x)
    auto step = [&](lds_cptr vb, lds_cptr knext, auto has_next_t) {
      constexpr bool HN = decltype(has_next_t)::value;
      bf16x8 kn0, kn1, vc0, vc1;
      { const f32x16& S = sa;
        PHASE(S, pa0, pa1,
              sb = MFMA32(kc0, qb0, nmb), sb = MFMA32(kc1, qb1, sb),
              ob = MFMA32(vp0, BF(pb0), ob), ob = MFMA32(vp1, BF(pb1), ob),
              lsb = MFMA16(sel, BF(pb0), lsb), lsb = MFMA16(sel, BF(pb1), lsb),
              if (HN) { kn0 = lds_read128(knext + kaddr0); kn1 = lds_read128(knext + kaddr1); },
              { vc0 = lds_tr8(vb, vb + 256); vc1 = lds_tr8(vb + 512, vb + 768); }); }
      { const f32x16& S = sb;
        PHASE(S, pb0, pb1,
              if (HN) sa = MFMA32(kn0, qa0, nma), if (HN) sa = MFMA32(kn1, qa1, sa),
              oa = MFMA32(vc0, BF(pa0), oa), oa = MFMA32(vc1, BF(pa1), oa),
              lsa = MFMA16(sel, BF(pa0), lsa), lsa = MFMA16(sel, BF(pa1), lsa),
              , ); }
      if (HN) { kc0 = kn0; kc1 = kn1; }
      vp0 = vc0; vp1 = vc1;
    };
    typedef std::integral_constant<bool, true> T_;
    typedef std::integral_constant<bool, false> F_;
    for (int ch = 0; ch < NCH - 1; ++ch) {
      if (ch + 2 < NCH) wait_vm<4>(); else wait_vm<0>();
      bar();
      if (ch + 3 < NCH) { issue((ch + 3) & 3); prefetch(ch); }
      lds_cptr sbuf = smem + (ch & 3) * ASTAGE;
      lds_cptr nbuf = smem + ((ch + 1) & 3) * ASTAGE;
      step(sbuf + vaddr, sbuf + 1024, T_{});
      step(sbuf + 1024 + vaddr, nbuf, T_{});
    }
    {
      lds_cptr sbuf = smem + ((NCH - 1) & 3) * ASTAGE;
      step(sbuf + vaddr, sbuf + 1024, T_{});
      step(sbuf + 1024 + vaddr, sbuf, F_{});
    }
    ob = MFMA32(vp0, BF(pb0), ob); ob = MFMA32(vp1, BF(pb1), ob);
    lsb = MFMA16(sel, BF(pb0), lsb); lsb = MFMA16(sel, BF(pb1), lsb);
#undef PHASE
#undef EX
#undef BF
  }
  refa = nma[0]; refb = nmb[0];
  if constexpr (SAFE) {
    auto s1 = __builtin_amdgcn_permlane32_swap(__float_as_uint(la), __float_as_uint(la), false, false);
    la_out = __uint_as_float(s1[0]) + __uint_as_float(s1[1]);
    auto s2 = __builtin_amdgcn_permlane32_swap(__float_as_uint(lb), __float_as_uint(lb), false, false);
    lb_out = __uint_as_float(s2[0]) + __uint_as_float(s2[1]);
  } else {
    const float a0 = __shfl(lsa[0], lane & 15), a1 = __shfl(lsa[1], lane & 15);
    const float b0 = __shfl(lsb[0], lane & 15), b1 = __shfl(lsb[1], lane & 15);
    la_out = (lane & 16) ? a1 : a0;
    lb_out = (lane & 16) ? b1 : b0;
  }
}

__global__ __launch_bounds__(512, 2) void k_attn(const u16* __restrict__ qkv, u16* __restrict__ oblk,
                                                  const float* __restrict__ part, float* __restrict__ modf,
                                                  const char* __restrict__ pf_wo, const char* __restrict__ pf_wg,
                                                  const char* __restrict__ pf_wu, const char* __restrict__ pf_wd) {
  extern __shared__ __attribute__((aligned(16))) char smem_[];
  lds_ptr smem = (lds_ptr)smem_;
  const int lane = threadIdx.x & 63;
  const int w = __builtin_amdgcn_readfirstlane(threadIdx.x >> 6);
  const int xcd = blockIdx.x & 7, idx = blockIdx.x >> 3;
  const int head = xcd * 4 + (idx >> 3), qb = idx & 7;
  const int r32 = lane & 31, hh = lane >> 5;
  const int qg = w & 3, kh = w >> 2;
  const u16* Qp = qkv + (size_t)head * SEQ * 32;
  const char* Kp = (const char*)(qkv + (size_t)(32 + head) * SEQ * 32);
  const char* Vp = (const char*)(qkv + (size_t)(64 + head) * SEQ * 32);
  const int q0 = qb * 256 + qg * 64;
  STAMP_AT(2, 0);
  __attribute__((address_space(3))) int* flag = (__attribute__((address_space(3))) int*)(smem + ANST * ASTAGE + 1024);
  if (threadIdx.x == 0) *flag = 0;
  bf16x8 qa0 = *(const bf16x8*)(Qp + (q0 + r32) * 16 + hh * 8);
  bf16x8 qa1 = *(const bf16x8*)(Qp + (SEQ + q0 + r32) * 16 + hh * 8);
  bf16x8 qb0 = *(const bf16x8*)(Qp + (q0 + 32 + r32) * 16 + hh * 8);
  bf16x8 qb1 = *(const bf16x8*)(Qp + (SEQ + q0 + 32 + r32) * 16 + hh * 8);
  if (threadIdx.x < 16) {
    const int col = 2 * DM + blockIdx.x * 16 + threadIdx.x;
    float v = 0.f;
#pragma unroll
    for (int i = 0; i < MNS; ++i) v += part[i * (6 * DM) + col];
    modf[col] = rbf(v);
  }
  f32x16 oa, ob;
  float nma, nmb, la, lb;
  attn_pass<false>(smem, lane, w, Kp, Vp, pf_wo, pf_wg, pf_wu, pf_wd, qa0, qa1, qb0, qb1, oa, ob, nma, nmb, la, lb);
  {
    float big = fmaxf(fabsf(la), fabsf(lb));
#pragma unroll
    for (int r = 0; r < 16; ++r) big = fmaxf(big, fmaxf(fabsf(oa[r]), fabsf(ob[r])));
    bool bad = !(big < 1e30f);
#pragma unroll
    for (int r = 0; r < 16; ++r) bad = bad || (oa[r] != oa[r]) || (ob[r] != ob[r]);
    bad = bad || (la != la) || (lb != lb);
    if (__any(bad) && lane == 0) *flag = 1;
  }
  __syncthreads();
  if (__builtin_amdgcn_readfirstlane(*flag) != 0) {
    attn_pass<true>(smem, lane, w, Kp, Vp, pf_wo, pf_wg, pf_wu, pf_wd, qa0, qa1, qb0, qb1, oa, ob, nma, nmb, la, lb);
    __syncthreads();
  }
  STAMP_AT(2, 2);
  {
    __attribute__((address_space(3))) float* xb = (__attribute__((address_space(3))) float*)smem;
    const f32x16& og = kh == 0 ? ob : oa;
    __attribute__((address_space(3))) float* dst = xb + w * (18 * 64) + lane;
#pragma unroll
    for (int r = 0; r < 16; ++r) dst[r * 64] = og[r];
    dst[16 * 64] = kh == 0 ? lb : la;
    dst[17 * 64] = kh == 0 ? nmb : nma;
    __syncthreads();
    const __attribute__((address_space(3))) float* src = xb + (w ^ 4) * (18 * 64) + lane;
    f32x16 om = kh == 0 ? oa : ob;
    const float nm_me = kh == 0 ? nma : nmb;
    const float nm_ot = src[17 * 64];
    const float nmin = fminf(nm_me, nm_ot);
    const float f_me = __builtin_amdgcn_exp2f(nmin - nm_me), f_ot = __builtin_amdgcn_exp2f(nmin - nm_ot);
    const float rl = 1.f / ((kh == 0 ? la : lb) * f_me + src[16 * 64] * f_ot);
    const float g_me = f_me * rl, g_ot = f_ot * rl;
    u16* op = oblk + ((size_t)head * 2 * SEQ + q0 + kh * 32 + r32) * 16 + 4 * hh;
#pragma unroll
    for (int g = 0; g < 4; ++g) {
      float v0 = om[4 * g] * g_me + src[(4 * g) * 64] * g_ot;
      float v1 = om[4 * g + 1] * g_me + src[(4 * g + 1) * 64] * g_ot;
      float v2 = om[4 * g + 2] * g_me + src[(4 * g + 2) * 64] * g_ot;
      float v3 = om[4 * g + 3] * g_me + src[(4 * g + 3) * 64] * g_ot;
      u32x2 v;
      v[0] = pack2(v0, v1);
      v[1] = pack2(v2, v3);
      *(u32x2*)(op + (g >> 1) * (SEQ * 16) + (g & 1) * 8) = v;
    }
  }
  STAMP_AT(2, 3);
}

extern "C" void kernel_launch(void* const* d_in, const int* in_sizes, int n_in, void* d_out, int out_size,
                              void* d_ws, size_t ws_size, hipStream_t stream) {
  const u16* x = (const u16*)d_in[0];
  const u16* wqkv = (const u16*)d_in[1];
  const u16* wo = (const u16*)d_in[2];
  const u16* wg = (const u16*)d_in[4];
  const u16* wu = (const u16*)d_in[5];
  const u16* wd = (const u16*)d_in[6];
  const u16* c = (const u16*)d_in[7];
  const u16* wmod = (const u16*)d_in[8];
  u16* out = (u16*)d_out;
  char* ws = (char*)d_ws;
  float* part = (float*)ws;
  float* modf = (float*)(ws + (256 << 10));
  unsigned* flags = (unsigned*)(ws + (512 << 10));
  u16* h1 = (u16*)(ws + (1 << 20));
  u16* qkv = (u16*)(ws + (8 << 20));
  u16* ob = (u16*)(ws + (24 << 20));
  u16* x2 = (u16*)(ws + (32 << 20));
  u16* h2 = (u16*)(ws + (40 << 20));
  u16* hh = (u16*)(ws + (48 << 20));

  hipFuncSetAttribute((const void*)k_qkv, hipFuncAttributeMaxDynamicSharedMemorySize, CQ::LDS);
  hipFuncSetAttribute((const void*)k_proj<DM>, hipFuncAttributeMaxDynamicSharedMemorySize, CP::LDS);
  hipFuncSetAttribute((const void*)k_proj<FF>, hipFuncAttributeMaxDynamicSharedMemorySize, CP::LDS);
  hipFuncSetAttribute((const void*)k_glu, hipFuncAttributeMaxDynamicSharedMemorySize, CG::LDS);
  hipFuncSetAttribute((const void*)k_attn, hipFuncAttributeMaxDynamicSharedMemorySize, ALDS);

  k_pre<<<256 + 256, 256, 0, stream>>>(c, wmod, part, (const char*)x, (const char*)wqkv, flags);
  k_ln<6, MNS, true><<<256 + 192, 512, 0, stream>>>(x, part, h1, (const char*)wqkv);
  k_qkv<<<256, 512, CQ::LDS, stream>>>(h1, wqkv, qkv, c, wmod, part);
  k_attn<<<256, 512, ALDS, stream>>>(qkv, ob, part, modf, (const char*)wo, (const char*)wg, (const char*)wu, (const char*)wd);
  k_proj<DM><<<256, 512, CP::LDS, stream>>>(ob, wo, x, modf + 2 * DM, x2);
  k_ln<7, 1, false><<<256, 512, 0, stream>>>(x2, modf + 3 * DM, h2, (const char*)wqkv);
  k_glu<<<256, 512, CG::LDS, stream>>>(h2, wg, wu, hh);
  k_proj<FF><<<256, 512, CP::LDS, stream>>>(hh, wd, x2, modf + 5 * DM, out);
}
```

```cpp
#include <hip/hip_runtime.h>
#include <hip/hip_bf16.h>
#include <stdint.h>

typedef __attribute__((ext_vector_type(8))) short bf16x8;
typedef __attribute__((ext_vector_type(4))) short s16x4;
typedef __attribute__((ext_vector_type(4))) float f32x4;
typedef __attribute__((ext_vector_type(2))) float f32x2;
typedef __attribute__((ext_vector_type(16))) float f32x16;
typedef __attribute__((ext_vector_type(4))) unsigned u32x4;
typedef __attribute__((ext_vector_type(2))) unsigned u32x2;
typedef __attribute__((ext_vector_type(2))) __bf16 bf16x2_t;
typedef unsigned short u16;
typedef __attribute__((address_space(3))) char* lds_ptr;
typedef __attribute__((address_space(3))) const char* lds_cptr;

#define DI __device__ __forceinline__

constexpr int SEQ = 2048, DM = 1024, FF = 3584, NH = 32;

DI float bf2f(u16 x) { return __uint_as_float(((unsigned)x) << 16); }
DI float bflo(unsigned x) { return __uint_as_float(x << 16); }
DI float bfhi(unsigned x) { return __uint_as_float(x & 0xffff0000u); }
DI unsigned pack2(float a, float b) {
  f32x2 v = {a, b};
  bf16x2_t r = __builtin_convertvector(v, bf16x2_t);
  return __builtin_bit_cast(unsigned, r);
}
DI float rbf(float a) { return bflo(pack2(a, 0.f) & 0xffffu); }

#define MFMA16(a, b, c) __builtin_amdgcn_mfma_f32_16x16x32_bf16((a), (b), (c), 0, 0, 0)
#define MFMA32(a, b, c) __builtin_amdgcn_mfma_f32_32x32x16_bf16((a), (b), (c), 0, 0, 0)

DI void glds16(const void* base, unsigned voff, unsigned lds_addr) {
  unsigned sv;
  asm volatile("s_mov_b32 %0, m0\n\ts_mov_b32 m0, %3\n\ts_nop 0\n\tglobal_load_lds_dwordx4 %1, %2\n\ts_mov_b32 m0, %0"
               : "=&s"(sv) : "v"(voff), "s"(base), "s"(lds_addr) : "memory");
}
DI unsigned lds_u32(lds_ptr p) { return (unsigned)(uintptr_t)p; }
DI bf16x8 lds_read128(lds_cptr p) { return *(const __attribute__((address_space(3))) bf16x8*)p; }
DI s16x4 lds_tr(lds_cptr p) {
  typedef short v4i16_t __attribute__((ext_vector_type(4)));
  return __builtin_bit_cast(s16x4, __builtin_amdgcn_ds_read_tr16_b64_v4i16((__attribute__((address_space(3))) v4i16_t*)p));
}
DI bf16x8 lds_tr8(lds_cptr p0, lds_cptr p1) {
  s16x4 a = lds_tr(p0), b = lds_tr(p1);
  return __builtin_shufflevector(a, b, 0, 1, 2, 3, 4, 5, 6, 7);
}
#define STAMP_AT(kid, slot) do {} while (0)
template <int N> DI void wait_vm() { asm volatile("s_waitcnt vmcnt(%0)" ::"n"(N) : "memory"); }
DI void bar() { __builtin_amdgcn_s_barrier(); }

constexpr int MNS = 8;
DI void mod_task(const u16* __restrict__ c, const u16* __restrict__ wmod, float* __restrict__ part, int cb, int ks,
                 int t, float* s_red  ) {
  const int cc = t & 7, rg = t >> 3;
  const int k0 = ks * 128 + rg;
  const u16* wp = wmod + (size_t)k0 * (6 * DM) + cb * 64 + cc * 8;
  u32x4 w[4];
  float sv[4];
#pragma unroll
  for (int i = 0; i < 4; ++i) w[i] = *(const u32x4*)(wp + (size_t)i * 32 * (6 * DM));
#pragma unroll
  for (int i = 0; i < 4; ++i) {
    float cv = bf2f(c[k0 + 32 * i]);
    sv[i] = rbf(cv / (1.f + __expf(-cv)));
  }
  float acc[8];
#pragma unroll
  for (int j = 0; j < 8; ++j) acc[j] = 0.f;
#pragma unroll
  for (int i = 0; i < 4; ++i) {
#pragma unroll
    for (int j = 0; j < 4; ++j) {
      acc[2 * j] += sv[i] * bflo(w[i][j]);
      acc[2 * j + 1] += sv[i] * bfhi(w[i][j]);
    }
  }
#pragma unroll
  for (int j = 0; j < 8; ++j) {
    float v = acc[j];
    v += __shfl_xor(v, 8);
    v += __shfl_xor(v, 16);
    v += __shfl_xor(v, 32);
    acc[j] = v;
  }
  const int lane = t & 63, wv = t >> 6;
  if (lane < 8) {
#pragma unroll
    for (int j = 0; j < 8; ++j) s_red[wv * 64 + lane * 8 + j] = acc[j];
  }
  __syncthreads();
  if (t < 64) part[ks * (6 * DM) + cb * 64 + t] = (s_red[t] + s_red[64 + t]) + (s_red[128 + t] + s_red[192 + t]);
}

DI void touch16k(const char* p, int t) {
  u32x4 v[4];
#pragma unroll
  for (int i = 0; i < 4; ++i) v[i] = *(const u32x4*)(p + i * 4096 + t * 16);
#pragma unroll
  for (int i = 0; i < 4; ++i) asm volatile("" ::"v"(v[i]));
}

__global__ __launch_bounds__(256) void k_pre(const u16* __restrict__ c, const u16* __restrict__ wmod, float* __restrict__ part,
                                              const char* __restrict__ x, const char* __restrict__ wqkv,
                                              unsigned* __restrict__ flags) {
  __shared__ float s_red[256];
  const int t = threadIdx.x, b = blockIdx.x;
  if (b < 256) STAMP_AT(0, 0);
  if (b < 256) {
    mod_task(c, wmod, part, b >> 3, b & 7, t, s_red);
    if (b == 0) flags[t] = 0u;
  } else {
    touch16k(x + (size_t)(b - 256) * 16384, t);
  }
  if (b < 256) STAMP_AT(0, 3);
}

template <int KID, int NS, bool FIRST>
__global__ __launch_bounds__(512) void k_ln(const u16* __restrict__ x, const float* __restrict__ ss  ,
                                             u16* __restrict__ h16, const char* __restrict__ pf) {
  __shared__ __attribute__((aligned(16))) float sm[2048];
  const int t = threadIdx.x;
  if (FIRST && blockIdx.x >= 256) {
    touch16k(pf + (size_t)((blockIdx.x - 256) * 2 + (t >> 8)) * 16384, t & 255);
    return;
  }
  const int lane = t & 63, wv = t >> 6;
  const int row = blockIdx.x * 8 + wv;
  STAMP_AT(KID, 0);
  const u16* xr = x + (size_t)row * DM;
  u32x4 v0 = *(const u32x4*)(xr + lane * 8);
  u32x4 v1 = *(const u32x4*)(xr + 512 + lane * 8);
  {
    f32x4 pv[NS];
#pragma unroll
    for (int i = 0; i < NS; ++i) pv[i] = *(const f32x4*)(ss + i * (6 * DM) + t * 4);
    f32x4 a = pv[0];
#pragma unroll
    for (int i = 1; i < NS; ++i) a += pv[i];
#pragma unroll
    for (int j = 0; j < 4; ++j) {
      float v = rbf(a[j]);
      a[j] = t < 256 ? v : rbf(1.f + v);
    }
    *(f32x4*)(sm + t * 4) = a;
  }
  float f[16];
#pragma unroll
  for (int j = 0; j < 4; ++j) {
    f[2 * j] = bflo(v0[j]); f[2 * j + 1] = bfhi(v0[j]);
    f[8 + 2 * j] = bflo(v1[j]); f[8 + 2 * j + 1] = bfhi(v1[j]);
  }
  float s = 0.f;
#pragma unroll
  for (int j = 0; j < 16; ++j) s += f[j];
#pragma unroll
  for (int o = 1; o < 64; o <<= 1) s += __shfl_xor(s, o);
  const float mean = s * (1.f / DM);
  float q = 0.f;
#pragma unroll
  for (int j = 0; j < 16; ++j) { float d = f[j] - mean; q += d * d; }
#pragma unroll
  for (int o = 1; o < 64; o <<= 1) q += __shfl_xor(q, o);
  const float mb = rbf(mean);
  const float vb = rbf(q * (1.f / DM));
  const float rden = 1.f / rbf(sqrtf(rbf(vb + 1e-5f)));
  __syncthreads();
#pragma unroll
  for (int half = 0; half < 2; ++half) {
    const int k0 = half * 512 + lane * 8;
    f32x4 sh0 = *(const f32x4*)(sm + k0), sh1 = *(const f32x4*)(sm + k0 + 4);
    f32x4 sc0 = *(const f32x4*)(sm + 1024 + k0), sc1 = *(const f32x4*)(sm + 1024 + k0 + 4);
    float y[8];
#pragma unroll
    for (int j = 0; j < 8; ++j) {
      float g = j < 4 ? sc0[j] : sc1[j - 4];
      float sh = j < 4 ? sh0[j] : sh1[j - 4];
      float d = rbf(f[half * 8 + j] - mb);
      float n = rbf(d * rden);
      y[j] = rbf(n * g) + sh;
    }
    u32x4 o;
#pragma unroll
    for (int j = 0; j < 4; ++j) o[j] = pack2(y[2 * j], y[2 * j + 1]);
    *(u32x4*)(h16 + ((size_t)(k0 >> 4) * SEQ + row) * 16 + (k0 & 15)) = o;
  }
  STAMP_AT(KID, 3);
}

DI u32x4 pair16(u32x2 a, u32x2 b) {
  auto s0 = __builtin_amdgcn_permlane16_swap(a[0], b[0], false, false);
  auto s1 = __builtin_amdgcn_permlane16_swap(a[1], b[1], false, false);
  return u32x4{s0[0], s1[0], s0[1], s1[1]};
}

DI int hA(int r) { return (-r) & 3; }

template <int BM, int NGRP, int KS, int NST>
struct GCfg {
  static constexpr int A_SUB = BM * 64;
  static constexpr int W_SUB = NGRP * 4096;
  static constexpr int STAGE = KS * (A_SUB + W_SUB);
  static constexpr int APW = KS * BM / 128;
  static constexpr int WPW = KS * NGRP / 2;
  static constexpr int PPW = APW + WPW;
  static constexpr int LDS = STAGE * NST;
};

template <class C>
struct Loader {
  const char* abase;
  const char* wbase[C::WPW];
  unsigned av[C::APW];
  unsigned wv[C::WPW];
  int aoff[C::APW];
  int woff[C::WPW];
};

template <int BM, int NGRP, int KS, int NST, int MT, int NT, int KSPLIT, int NKT, int KID, int IOFS>
struct Engine {
  typedef GCfg<BM, NGRP, KS, NST> C;
  static constexpr int SPW = KS / KSPLIT;
  static constexpr int P = C::PPW;
  static constexpr int G = NT * SPW;
  f32x4 (&acc)[NT][MT];
  lds_ptr smem;
  Loader<C>& ld;
  long a_adv, w_adv;
  const int (&waddr)[NT];
  int a_sub0, w_sub0;
  int cur, nxt, fre;
  bf16x8 wf[NT], af[2][MT];

  DI Engine(f32x4 (&acc_)[NT][MT], lds_ptr smem_, Loader<C>& ld_, long a_adv_, long w_adv_, int aaddr, const int (&waddr_)[NT], int kg)
      : acc(acc_), smem(smem_), ld(ld_), a_adv(a_adv_), w_adv(w_adv_), waddr(waddr_) {
    a_sub0 = kg * C::A_SUB + aaddr;
    w_sub0 = KS * C::A_SUB + kg * C::W_SUB;
  }
  template <int I> DI void piece(int stage_off) {
    if constexpr (I < C::APW) {
      glds16(ld.abase, ld.av[I], lds_u32(smem) + stage_off + ld.aoff[I]);
      if constexpr (I == C::APW - 1) ld.abase += a_adv * 2;
    } else {
      constexpr int J = I - C::APW;
      glds16(ld.wbase[J], ld.wv[J], lds_u32(smem) + stage_off + ld.woff[J]);
      ld.wbase[J] += w_adv * 2;
    }
  }
  template <int I> DI void issue_all(int stage_off) {
    if constexpr (I < P) { piece<I>(stage_off); issue_all<I + 1>(stage_off); }
  }
  template <int g, int I> DI void issue_at(int stage_off) {
    if constexpr (I < P) {
      constexpr int span = (G - IOFS) > P ? (G - IOFS) : P;
      constexpr int pos = IOFS + (I * span) / P < G ? IOFS + (I * span) / P : G - 1;
      if constexpr (pos == g) piece<I>(stage_off);
      issue_at<g, I + 1>(stage_off);
    }
  }
  template <int PAR, int S, bool PF, bool DOISSUE, int n>
  DI void groups(lds_cptr ab, lds_cptr wb) {
    if constexpr (n < NT) {
#pragma unroll
      for (int m = 0; m < MT; ++m) acc[n][m] = MFMA16(wf[n], af[PAR][m], acc[n][m]);
      if constexpr (PF) {
        wf[n] = lds_tr8(wb + waddr[n], wb + waddr[n] + 512);
#pragma unroll
        for (int m = 0; m < MT; ++m)
          if ((m * NT) / MT == n) af[PAR ^ 1][m] = lds_read128(ab + m * 512);
      }
      if constexpr (DOISSUE) issue_at<S * NT + n, 0>(fre);
      __builtin_amdgcn_sched_barrier(0);
      groups<PAR, S, PF, DOISSUE, n + 1>(ab, wb);
    }
  }
  template <int PAR, bool DOISSUE, bool LAST, int S> DI void steps() {
    if constexpr (S < SPW) {
      if constexpr (S + 1 < SPW)
        groups<PAR, S, true, DOISSUE, 0>(smem + cur + a_sub0 + (S + 1) * KSPLIT * C::A_SUB, smem + cur + w_sub0 + (S + 1) * KSPLIT * C::W_SUB);
      else if constexpr (!LAST)
        groups<PAR, S, true, DOISSUE, 0>(smem + nxt + a_sub0, smem + nxt + w_sub0);
      else
        groups<PAR, S, false, false, 0>(smem, smem);
      steps<PAR ^ 1, DOISSUE, LAST, S + 1>();
    }
  }
  template <int PAR, bool DOISSUE, bool LAST, int WAITN> DI void iter() {
    if constexpr (!LAST) { wait_vm<WAITN>(); bar(); }
    steps<PAR, DOISSUE, LAST, 0>();
    fre = cur; cur = nxt; nxt = (nxt + C::STAGE == NST * C::STAGE) ? 0 : nxt + C::STAGE;
  }
  template <int KT> DI void tail() {
    if constexpr (KT < NKT) {
      constexpr bool DOISSUE = KT + NST - 1 < NKT;
      constexpr bool LAST = KT == NKT - 1;
      constexpr int newest = (KT + NST - 2) < (NKT - 1) ? (KT + NST - 2) : (NKT - 1);
      constexpr int younger = newest - (KT + 1) > 0 ? newest - (KT + 1) : 0;
      iter<(KT * SPW) & 1, DOISSUE, LAST, P * younger>();
      tail<KT + 1>();
    }
  }
  DI void start() {
    static_assert(NKT >= NST + 1, "too few k tiles");
    STAMP_AT(KID, 0);
#pragma unroll
    for (int s = 0; s < NST - 1; ++s) issue_all<0>(s * C::STAGE);
  }
  DI void run() {
    wait_vm<P*(NST - 2)>();
    bar();
    STAMP_AT(KID, 1);
    {
      lds_cptr ab = smem + a_sub0;
      lds_cptr wb = smem + w_sub0;
#pragma unroll
      for (int m = 0; m < MT; ++m) af[0][m] = lds_read128(ab + m * 512);
#pragma unroll
      for (int n = 0; n < NT; ++n) wf[n] = lds_tr8(wb + waddr[n], wb + waddr[n] + 512);
    }
    cur = 0; nxt = C::STAGE; fre = (NST - 1) * C::STAGE;
    constexpr int STEADY = NKT - NST + 1;
    constexpr int LOOPN = (STEADY / 2) * 2;
    for (int kt = 0; kt < LOOPN; kt += 2) {
      iter<0, true, false, P*(NST - 3)>();
      iter<SPW & 1, true, false, P*(NST - 3)>();
    }
    tail<LOOPN>();
    STAMP_AT(KID, 2);
  }
};

template <class C, int BM, int NGRP, int KS>
DI void setup_loader_A(Loader<C>& ld, const u16* Ablk, int m0, int w, int lane) {
#pragma unroll
  for (int i = 0; i < C::APW; ++i) {
    int a = w + 8 * i;
    int blk = a / (BM / 32), j = a % (BM / 32);
    ld.aoff[i] = blk * (BM * 32) + j * 1024;
    ld.av[i] = ((blk * SEQ + m0 + j * 32) * 16) * 2 + lane * 16;
  }
  ld.abase = (const char*)Ablk;
}

DI int wfrag_addr(int t, int lane) {
  int g = lane >> 4, q = (lane & 15) >> 2, p = lane & 3;
  int f = ((q >> 1) & 1) | ((g & 1) << 1);
  return (t >> 2) * 4096 + (8 * g + q) * 128 + (((t & 3) ^ f) * 32) + p * 8;
}
template <int BM>
DI int afrag_addr(int mrow0, int lane) {
  int r = lane & 15, g = lane >> 4;
  return (g >> 1) * (BM * 32) + (mrow0 + r) * 32 + (g & 1) * 16;
}

constexpr float QSCALE = 0.17677669529663687f * 1.4426950408889634f;
typedef GCfg<128, 3, 2, 4> CQ;
__global__ __launch_bounds__(512, 2) void k_qkv(const u16* __restrict__ Ablk, const u16* __restrict__ W,
                                                 u16* __restrict__ out) {
  extern __shared__ __attribute__((aligned(16))) char smem_[];
  lds_ptr smem = (lds_ptr)smem_;
  const int lane = threadIdx.x & 63;
  const int w = __builtin_amdgcn_readfirstlane(threadIdx.x >> 6);
  const int xcd = blockIdx.x & 7, idx = blockIdx.x >> 3;
  const int tm = (xcd & 1) * 8 + (idx & 7), tn = (xcd >> 1) * 4 + (idx >> 3);
  const int m0 = tm * 128, n0 = tn * 192;
  constexpr int LDW = 3 * DM;
  Loader<CQ> ld;
  setup_loader_A<CQ, 128, 3, 2>(ld, Ablk, m0, w, lane);
#pragma unroll
  for (int i = 0; i < CQ::WPW; ++i) {
    int b = w + 8 * i;
    int sub = b / 12, r = b % 12, grp = r >> 2, o = r & 3;
    ld.woff[i] = 2 * CQ::A_SUB + sub * CQ::W_SUB + grp * 4096 + o * 1024;
    int k = sub * 32 + 8 * o + (lane >> 3);
    int f = ((lane >> 4) & 1) | ((o & 1) << 1);
    int col = n0 + grp * 64 + ((((lane & 7) >> 1) ^ f) * 16) + (lane & 1) * 8;
    ld.wv[i] = (k * LDW + col) * 2;
    ld.wbase[i] = (const char*)W;
  }
  const int wm = w & 1, wn = w >> 1;
  int waddr[3];
#pragma unroll
  for (int n = 0; n < 3; ++n) waddr[n] = wfrag_addr(wn * 3 + n, lane);
  const int aaddr = afrag_addr<128>(wm * 64, lane);
  f32x4 acc[3][4];
#pragma unroll
  for (int n = 0; n < 3; ++n)
#pragma unroll
    for (int m = 0; m < 4; ++m) acc[n][m] = f32x4{0.f, 0.f, 0.f, 0.f};
  Engine<128, 3, 2, 4, 4, 3, 1, DM / 64, 1, 0> eng(acc, smem, ld, (long)2 * SEQ * 32, (long)64 * LDW, aaddr, waddr, 0);
  eng.start();
  eng.run();
  const int g = lane >> 4;
#pragma unroll
  for (int n = 0; n < 3; ++n) {
    const int cb = (n0 >> 4) + wn * 3 + n;
    const float sc = cb < DM / 16 ? QSCALE : 1.f;
#pragma unroll
    for (int m = 0; m < 4; m += 2) {
      u32x2 a, b;
      a[0] = pack2(acc[n][m][0] * sc, acc[n][m][1] * sc);
      a[1] = pack2(acc[n][m][2] * sc, acc[n][m][3] * sc);
      b[0] = pack2(acc[n][m + 1][0] * sc, acc[n][m + 1][1] * sc);
      b[1] = pack2(acc[n][m + 1][2] * sc, acc[n][m + 1][3] * sc);
      const int row = m0 + wm * 64 + (m + (g & 1)) * 16 + (lane & 15);
      *(u32x4*)(out + ((size_t)cb * SEQ + row) * 16 + (g >> 1) * 8) = pair16(a, b);
    }
  }
  STAMP_AT(1, 3);
}

typedef GCfg<128, 1, 2, 6> CP;
template <int KDIM>
__global__ __launch_bounds__(512, 2) void k_proj(const u16* __restrict__ Ablk, const u16* __restrict__ W,
                                                  const u16* __restrict__ res, const float* __restrict__ gate,
                                                  u16* __restrict__ out) {
  extern __shared__ __attribute__((aligned(16))) char smem_[];
  lds_ptr smem = (lds_ptr)smem_;
  const int lane = threadIdx.x & 63;
  const int w = __builtin_amdgcn_readfirstlane(threadIdx.x >> 6);
  const int xcd = blockIdx.x & 7, idx = blockIdx.x >> 3;
  const int tm = (xcd & 3) * 4 + (idx & 3), tn = (xcd >> 2) * 8 + (idx >> 2);
  const int m0 = tm * 128, n0 = tn * 64;
  constexpr int LDW = DM;
  Loader<CP> ld;
  setup_loader_A<CP, 128, 1, 2>(ld, Ablk, m0, w, lane);
  {
    int b = w;
    int sub = b >> 2, o = b & 3;
    ld.woff[0] = 2 * CP::A_SUB + sub * CP::W_SUB + o * 1024;
    int k = sub * 32 + 8 * o + (lane >> 3);
    int f = ((lane >> 4) & 1) | ((o & 1) << 1);
    int col = n0 + ((((lane & 7) >> 1) ^ f) * 16) + (lane & 1) * 8;
    ld.wv[0] = (k * LDW + col) * 2;
    ld.wbase[0] = (const char*)W;
  }
  const int wm = w & 1, wn = (w >> 1) & 1, kg = w >> 2;
  int waddr[2];
#pragma unroll
  for (int n = 0; n < 2; ++n) waddr[n] = wfrag_addr(wn * 2 + n, lane);
  const int aaddr = afrag_addr<128>(wm * 64, lane);
  f32x4 acc[2][4];
#pragma unroll
  for (int n = 0; n < 2; ++n)
#pragma unroll
    for (int m = 0; m < 4; ++m) acc[n][m] = f32x4{0.f, 0.f, 0.f, 0.f};
  Engine<128, 1, 2, 6, 4, 2, 2, KDIM / 64, (KDIM == DM ? 3 : 5), 0> eng(acc, smem, ld, (long)2 * SEQ * 32, (long)64 * LDW, aaddr, waddr, kg);
  eng.start();
  eng.run();
  __syncthreads();
  __attribute__((address_space(3))) f32x4* red = (__attribute__((address_space(3))) f32x4*)smem;
  if (kg == 1) {
#pragma unroll
    for (int n = 0; n < 2; ++n)
#pragma unroll
      for (int m = 0; m < 4; ++m) red[((w & 3) * 8 + n * 4 + m) * 64 + lane] = acc[n][m];
  }
  __syncthreads();
  if (kg == 0) {
    const int g = lane >> 4;
#pragma unroll
    for (int n = 0; n < 2; ++n) {
      int col = n0 + (wn * 2 + n) * 16 + 4 * g;
      f32x4 gt = *(const f32x4*)(gate + col);
#pragma unroll
      for (int i = 1; i < MNS; ++i) gt += *(const f32x4*)(gate + i * (6 * DM) + col);
#pragma unroll
      for (int j = 0; j < 4; ++j) gt[j] = rbf(gt[j]);
#pragma unroll
      for (int m = 0; m < 4; ++m) {
        f32x4 a = acc[n][m] + red[((w & 3) * 8 + n * 4 + m) * 64 + lane];
        int row = m0 + wm * 64 + m * 16 + (lane & 15);
        u32x2 r = *(const u32x2*)(res + (size_t)row * DM + col);
        float y0 = bflo(r[0]) + rbf(gt[0] * rbf(a[0]));
        float y1 = bfhi(r[0]) + rbf(gt[1] * rbf(a[1]));
        float y2 = bflo(r[1]) + rbf(gt[2] * rbf(a[2]));
        float y3 = bfhi(r[1]) + rbf(gt[3] * rbf(a[3]));
        u32x2 o;
        o[0] = pack2(y0, y1);
        o[1] = pack2(y2, y3);
        *(u32x2*)(out + (size_t)row * DM + col) = o;
      }
    }
  }
  STAMP_AT((KDIM == DM ? 3 : 5), 3);
}

typedef GCfg<256, 4, 1, 4> CG;
template <int NH_>
DI void glu_wave(lds_ptr smem, Loader<CG>& ld, int lane, int wm, int t0, int m0, int f0, u16* __restrict__ out) {
  constexpr int NT = 2 * NH_;
  int waddr[NT];
#pragma unroll
  for (int n = 0; n < NH_; ++n) {
    waddr[n] = wfrag_addr(t0 + n, lane);
    waddr[NH_ + n] = waddr[n] + 8192;
  }
  const int aaddr = afrag_addr<256>(wm * 64, lane);
  f32x4 acc[NT][4];
#pragma unroll
  for (int n = 0; n < NT; ++n)
#pragma unroll
    for (int m = 0; m < 4; ++m) acc[n][m] = f32x4{0.f, 0.f, 0.f, 0.f};
  Engine<256, 4, 1, 4, 4, NT, 1, DM / 32, 4, (NH_ == 4 ? 0 : 1)> eng(acc, smem, ld, (long)SEQ * 32, (long)32 * FF, aaddr, waddr, 0);
  eng.start();
  eng.run();
  const int g = lane >> 4;
#pragma unroll
  for (int n = 0; n < NH_; ++n) {
    const int cb = (f0 >> 4) + t0 + n;
#pragma unroll
    for (int m = 0; m < 4; m += 2) {
      u32x2 o[2];
#pragma unroll
      for (int i = 0; i < 2; ++i) {
        f32x4 a = acc[n][m + i], b = acc[NH_ + n][m + i];
        o[i][0] = pack2(rbf(a[0]) * rbf(b[0]), rbf(a[1]) * rbf(b[1]));
        o[i][1] = pack2(rbf(a[2]) * rbf(b[2]), rbf(a[3]) * rbf(b[3]));
      }
      const int row = m0 + wm * 64 + (m + (g & 1)) * 16 + (lane & 15);
      *(u32x4*)(out + ((size_t)cb * SEQ + row) * 16 + (g >> 1) * 8) = pair16(o[0], o[1]);
    }
  }
  STAMP_AT(4, 3);
}

__global__ __launch_bounds__(512, 2) void k_glu(const u16* __restrict__ Ablk, const u16* __restrict__ Wg,
                                                 const u16* __restrict__ Wu, u16* __restrict__ out) {
  extern __shared__ __attribute__((aligned(16))) char smem_[];
  lds_ptr smem = (lds_ptr)smem_;
  const int lane = threadIdx.x & 63;
  const int w = __builtin_amdgcn_readfirstlane(threadIdx.x >> 6);
  const int xcd = blockIdx.x & 7, idx = blockIdx.x >> 3;
  const int tm = idx & 7, tn = xcd * 4 + (idx >> 3);
  const int m0 = tm * 256, f0 = tn * 112;
  Loader<CG> ld;
  setup_loader_A<CG, 256, 4, 1>(ld, Ablk, m0, w, lane);
#pragma unroll
  for (int i = 0; i < CG::WPW; ++i) {
    int b = w + 8 * i;
    int grp = b >> 2, o = b & 3;
    ld.woff[i] = CG::A_SUB + grp * 4096 + o * 1024;
    int k = 8 * o + (lane >> 3);
    int f = ((lane >> 4) & 1) | ((o & 1) << 1);
    int pr = ((lane & 7) >> 1) ^ f;
    if ((grp & 1) && pr == 3) pr = 0;
    int col = f0 + (grp & 1) * 64 + pr * 16 + (lane & 1) * 8;
    ld.wv[i] = (k * FF + col) * 2;
    ld.wbase[i] = (const char*)(grp >> 1 ? Wu : Wg);
  }
  const int wm = w & 3;
  if (w < 4) glu_wave<4>(smem, ld, lane, wm, 0, m0, f0, out);
  else glu_wave<3>(smem, ld, lane, wm, 4, m0, f0, out);
}

constexpr int ACH = 128, ANST = 4, ASTAGE = ACH * 64 * 2 + 256;
constexpr int ALDS = ANST * ASTAGE + 1024 + 16;

DI float rowmax16(const f32x16& v) {
  float mx = fmaxf(fmaxf(v[0], v[1]), v[2]);
#pragma unroll
  for (int r = 3; r < 15; r += 2) mx = fmaxf(fmaxf(mx, v[r]), v[r + 1]);
  mx = fmaxf(mx, v[15]);
  auto sw = __builtin_amdgcn_permlane32_swap(__float_as_uint(mx), __float_as_uint(mx), false, false);
  return fmaxf(__uint_as_float(sw[0]), __uint_as_float(sw[1]));
}

template <bool SAFE>
DI void attn_pass(lds_ptr smem, int lane, int w, const char* Kp, const char* Vp, const char* pf_wm, const char* pf_wo, const char* pf_wg,
                  const char* pf_wu, const char* pf_wd, const bf16x8& qa0, const bf16x8& qa1, const bf16x8& qb0,
                  const bf16x8& qb1, f32x16& oa, f32x16& ob, float& refa, float& refb, float& la_out, float& lb_out) {
  const int r32 = lane & 31, hh = lane >> 5, kh = w >> 2;
  const unsigned kvo = ((32 * (w & 3) + (lane >> 1)) * 16 + (((lane & 1) ^ ((lane >> 4) & 1)) * 8)) * 2;
  const unsigned vvo = ((32 * (w & 3)) * 16) * 2 + lane * 16;
  const char* ksrc = Kp;
  const char* vsrc = Vp;
  auto issue = [&](int buf) {
    if (w < 4) {
      const unsigned l0 = lds_u32(smem) + buf * ASTAGE + w * 1024;
      glds16(ksrc, kvo, l0);
      glds16(ksrc + SEQ * 32, kvo, l0 + 4096);
      glds16(vsrc, vvo, l0 + 8192);
      glds16(vsrc + SEQ * 32, vvo, l0 + 8192 + 4096 + 128);
    }
    ksrc += ACH * 32; vsrc += ACH * 32;
  };
  const unsigned pfo = lane * 16;
  auto prefetch = [&](int ch) {
    if (w >= 4) {
#pragma unroll
      for (int i = 0; i < 3; ++i) {
        int pc = (ch * 3 + i) * 4 + (w - 4);
        const char* base;
        if (pc < 32) base = pf_wm + (size_t)(blockIdx.x * 4 + (pc >> 3)) * (12 * DM) + 4 * DM + (pc & 7) * 1024;
        else if (pc < 40) base = pf_wo + (size_t)blockIdx.x * 8192 + (pc - 32) * 1024;
        else if (pc < 68) base = pf_wg + (size_t)blockIdx.x * 28672 + (pc - 40) * 1024;
        else if (pc < 96) base = pf_wu + (size_t)blockIdx.x * 28672 + (pc - 68) * 1024;
        else if (pc < 124) base = pf_wd + (size_t)blockIdx.x * 28672 + (pc - 96) * 1024;
        else break;
        glds16(base, pfo, lds_u32(smem) + ANST * ASTAGE);
      }
    }
  };
  issue(0); issue(1); issue(2);
  const int kaddr0 = kh * 2048 + r32 * 32 + ((hh ^ ((r32 >> 3) & 1)) * 16);
  const int kaddr1 = kaddr0 + 4096;
  const int vaddr = 8192 + kh * 2048 + ((lane >> 4) & 1) * (4096 + 128) + (4 * hh + ((lane & 15) >> 2)) * 32 + (lane & 3) * 8;
  bf16x8 sel;
  {
    const short one = ((lane & 15) == ((lane >> 4) & 1)) ? (short)0x3F80 : (short)0;
#pragma unroll
    for (int j = 0; j < 8; ++j) sel[j] = one;
  }
  f32x16 nma, nmb, sa, sb;
#pragma unroll
  for (int r = 0; r < 16; ++r) { oa[r] = 0.f; ob[r] = 0.f; nma[r] = 0.f; nmb[r] = 0.f; }
  f32x4 lsa = {0.f, 0.f, 0.f, 0.f}, lsb = {0.f, 0.f, 0.f, 0.f};
  float la = 0.f, lb = 0.f;
  constexpr int NCH = SEQ / ACH;
  if (w < 4) wait_vm<8>();
  bar();
  const bf16x8 kf0_ = lds_read128(smem + kaddr0);
  const bf16x8 kf1_ = lds_read128(smem + kaddr1);
  {
    sa = MFMA32(kf0_, qa0, nma); sa = MFMA32(kf1_, qa1, sa);
    sb = MFMA32(kf0_, qb0, nmb); sb = MFMA32(kf1_, qb1, sb);
    const float ma = rowmax16(sa), mb = rowmax16(sb);
#pragma unroll
    for (int r = 0; r < 16; ++r) { nma[r] = -ma; sa[r] -= ma; nmb[r] = -mb; sb[r] -= mb; }
  }
  if constexpr (SAFE) {
    auto half = [&](f32x16& s, f32x16& o, f32x16& nm, float& l, const bf16x8& q0f, const bf16x8& q1f,
                    const bf16x8& kn0, const bf16x8& kn1, const bf16x8& vf0, const bf16x8& vf1, bool has_next) {
      f32x16 p;
      const float dl = fmaxf(rowmax16(s), 0.f);
      const float f = __builtin_amdgcn_exp2f(-dl);
      float t = 0.f;
#pragma unroll
      for (int r = 0; r < 16; ++r) { nm[r] -= dl; o[r] *= f; p[r] = __builtin_amdgcn_exp2f(s[r] - dl); t += p[r]; }
      l = l * f + t;
      if (has_next) { s = MFMA32(kn0, q0f, nm); s = MFMA32(kn1, q1f, s); }
      u32x4 pa, pb;
#pragma unroll
      for (int j = 0; j < 4; ++j) { pa[j] = pack2(p[2 * j], p[2 * j + 1]); pb[j] = pack2(p[8 + 2 * j], p[8 + 2 * j + 1]); }
      o = MFMA32(vf0, __builtin_bit_cast(bf16x8, pa), o);
      o = MFMA32(vf1, __builtin_bit_cast(bf16x8, pb), o);
    };
    auto step = [&](lds_cptr vb, lds_cptr knext, bool has_next) {
      bf16x8 kn0, kn1;
      if (has_next) { kn0 = lds_read128(knext + kaddr0); kn1 = lds_read128(knext + kaddr1); }
      bf16x8 vf0 = lds_tr8(vb, vb + 256);
      bf16x8 vf1 = lds_tr8(vb + 512, vb + 768);
      half(sa, oa, nma, la, qa0, qa1, kn0, kn1, vf0, vf1, has_next);
      half(sb, ob, nmb, lb, qb0, qb1, kn0, kn1, vf0, vf1, has_next);
    };
    for (int ch = 0; ch < NCH; ++ch) {
      if (w < 4) { if (ch + 2 < NCH) wait_vm<4>(); else wait_vm<0>(); }
      bar();
      if (ch + 3 < NCH) issue((ch + 3) & 3);
      lds_cptr sbuf = smem + (ch & 3) * ASTAGE;
      lds_cptr nbuf = smem + ((ch + 1) & 3) * ASTAGE;
      step(sbuf + vaddr, sbuf + 1024, true);
      step(sbuf + 1024 + vaddr, nbuf, ch + 1 < NCH);
    }
  } else {
    u32x4 pa0, pa1, pb0, pb1;
    bf16x8 kc0 = kf0_, kc1 = kf1_;
    bf16x8 vp0, vp1;
#pragma unroll
    for (int j = 0; j < 4; ++j) { pb0[j] = 0u; pb1[j] = 0u; }
#pragma unroll
    for (int j = 0; j < 8; ++j) { vp0[j] = 0; vp1[j] = 0; }
#define SB_ __builtin_amdgcn_sched_barrier(0)
#define EX(i) e[i] = __builtin_amdgcn_exp2f(S[i])
#define PHASE(S, P0, P1, M0, M1, M2, M3, M4, M5, R0, R1) do { float e[16]; \
      M0; SB_; R0; EX(0); EX(1); EX(2); EX(3); SB_; \
      M1; SB_; R1; EX(4); EX(5); EX(6); EX(7); SB_; \
      M2; SB_; EX(8); EX(9); P0[0] = pack2(e[0], e[1]); P0[1] = pack2(e[2], e[3]); SB_; \
      M3; SB_; EX(10); EX(11); P0[2] = pack2(e[4], e[5]); P0[3] = pack2(e[6], e[7]); SB_; \
      M4; SB_; EX(12); EX(13); EX(14); EX(15); SB_; \
      M5; SB_; P1[0] = pack2(e[8], e[9]); P1[1] = pack2(e[10], e[11]); P1[2] = pack2(e[12], e[13]); P1[3] = pack2(e[14], e[15]); SB_; \
    } while (0)
#define BF(x) __builtin_bit_cast(bf16x8, x)
    auto step = [&](lds_cptr vb, lds_cptr knext, auto has_next_t) {
      constexpr bool HN = decltype(has_next_t)::value;
      bf16x8 kn0, kn1, vc0, vc1;
      { const f32x16& S = sa;
        PHASE(S, pa0, pa1,
              sb = MFMA32(kc0, qb0, nmb), sb = MFMA32(kc1, qb1, sb),
              ob = MFMA32(vp0, BF(pb0), ob), ob = MFMA32(vp1, BF(pb1), ob),
              lsb = MFMA16(sel, BF(pb0), lsb), lsb = MFMA16(sel, BF(pb1), lsb),
              if (HN) { kn0 = lds_read128(knext + kaddr0); kn1 = lds_read128(knext + kaddr1); },
              { vc0 = lds_tr8(vb, vb + 256); vc1 = lds_tr8(vb + 512, vb + 768); }); }
      { const f32x16& S = sb;
        PHASE(S, pb0, pb1,
              if (HN) sa = MFMA32(kn0, qa0, nma), if (HN) sa = MFMA32(kn1, qa1, sa),
              oa = MFMA32(vc0, BF(pa0), oa), oa = MFMA32(vc1, BF(pa1), oa),
              lsa = MFMA16(sel, BF(pa0), lsa), lsa = MFMA16(sel, BF(pa1), lsa),
              , ); }
      if (HN) { kc0 = kn0; kc1 = kn1; }
      vp0 = vc0; vp1 = vc1;
    };
    typedef std::integral_constant<bool, true> T_;
    typedef std::integral_constant<bool, false> F_;
    for (int ch = 0; ch < NCH - 1; ++ch) {
      if (w < 4) { if (ch + 2 < NCH) wait_vm<4>(); else wait_vm<0>(); }
      bar();
      if (ch + 3 < NCH) issue((ch + 3) & 3);
      prefetch(ch);
      lds_cptr sbuf = smem + (ch & 3) * ASTAGE;
      lds_cptr nbuf = smem + ((ch + 1) & 3) * ASTAGE;
      step(sbuf + vaddr, sbuf + 1024, T_{});
      step(sbuf + 1024 + vaddr, nbuf, T_{});
    }
    {
      lds_cptr sbuf = smem + ((NCH - 1) & 3) * ASTAGE;
      step(sbuf + vaddr, sbuf + 1024, T_{});
      step(sbuf + 1024 + vaddr, sbuf, F_{});
    }
    ob = MFMA32(vp0, BF(pb0), ob); ob = MFMA32(vp1, BF(pb1), ob);
    lsb = MFMA16(sel, BF(pb0), lsb); lsb = MFMA16(sel, BF(pb1), lsb);
#undef PHASE
#undef EX
#undef BF
  }
  refa = nma[0]; refb = nmb[0];
  if constexpr (SAFE) {
    auto s1 = __builtin_amdgcn_permlane32_swap(__float_as_uint(la), __float_as_uint(la), false, false);
    la_out = __uint_as_float(s1[0]) + __uint_as_float(s1[1]);
    auto s2 = __builtin_amdgcn_permlane32_swap(__float_as_uint(lb), __float_as_uint(lb), false, false);
    lb_out = __uint_as_float(s2[0]) + __uint_as_float(s2[1]);
  } else {
    const float a0 = __shfl(lsa[0], lane & 15), a1 = __shfl(lsa[1], lane & 15);
    const float b0 = __shfl(lsb[0], lane & 15), b1 = __shfl(lsb[1], lane & 15);
    la_out = (lane & 16) ? a1 : a0;
    lb_out = (lane & 16) ? b1 : b0;
  }
}

__global__ __launch_bounds__(512, 2) void k_attn(const u16* __restrict__ qkv, u16* __restrict__ oblk,
                                                  const u16* __restrict__ c, const u16* __restrict__ wmod, float* __restrict__ part,
                                                  const char* __restrict__ pf_wo, const char* __restrict__ pf_wg,
                                                  const char* __restrict__ pf_wu, const char* __restrict__ pf_wd) {
  extern __shared__ __attribute__((aligned(16))) char smem_[];
  lds_ptr smem = (lds_ptr)smem_;
  const int lane = threadIdx.x & 63;
  const int w = __builtin_amdgcn_readfirstlane(threadIdx.x >> 6);
  const int xcd = blockIdx.x & 7, idx = blockIdx.x >> 3;
  const int head = xcd * 4 + (idx >> 3), qb = idx & 7;
  const int r32 = lane & 31, hh = lane >> 5;
  const int qg = w & 3, kh = w >> 2;
  const u16* Qp = qkv + (size_t)head * SEQ * 32;
  const char* Kp = (const char*)(qkv + (size_t)(32 + head) * SEQ * 32);
  const char* Vp = (const char*)(qkv + (size_t)(64 + head) * SEQ * 32);
  const int q0 = qb * 256 + qg * 64;
  STAMP_AT(2, 0);
  __attribute__((address_space(3))) int* flag = (__attribute__((address_space(3))) int*)(smem + ANST * ASTAGE + 1024);
  if (threadIdx.x == 0) *flag = 0;
  bf16x8 qa0 = *(const bf16x8*)(Qp + (q0 + r32) * 16 + hh * 8);
  bf16x8 qa1 = *(const bf16x8*)(Qp + (SEQ + q0 + r32) * 16 + hh * 8);
  bf16x8 qb0 = *(const bf16x8*)(Qp + (q0 + 32 + r32) * 16 + hh * 8);
  bf16x8 qb1 = *(const bf16x8*)(Qp + (SEQ + q0 + 32 + r32) * 16 + hh * 8);
  f32x16 oa, ob;
  float nma, nmb, la, lb;
  attn_pass<false>(smem, lane, w, Kp, Vp, (const char*)wmod, pf_wo, pf_wg, pf_wu, pf_wd, qa0, qa1, qb0, qb1, oa, ob, nma, nmb, la, lb);
  {
    float big = fmaxf(fabsf(la), fabsf(lb));
#pragma unroll
    for (int r = 0; r < 16; ++r) big = fmaxf(big, fmaxf(fabsf(oa[r]), fabsf(ob[r])));
    bool bad = !(big < 1e30f);
#pragma unroll
    for (int r = 0; r < 16; ++r) bad = bad || (oa[r] != oa[r]) || (ob[r] != ob[r]);
    bad = bad || (la != la) || (lb != lb);
    if (__any(bad) && lane == 0) *flag = 1;
  }
  __syncthreads();
  if (__builtin_amdgcn_readfirstlane(*flag) != 0) {
    attn_pass<true>(smem, lane, w, Kp, Vp, (const char*)wmod, pf_wo, pf_wg, pf_wu, pf_wd, qa0, qa1, qb0, qb1, oa, ob, nma, nmb, la, lb);
    __syncthreads();
  }
  STAMP_AT(2, 2);
  {
    __attribute__((address_space(3))) float* xb = (__attribute__((address_space(3))) float*)smem;
    const f32x16& og = kh == 0 ? ob : oa;
    __attribute__((address_space(3))) float* dst = xb + w * (18 * 64) + lane;
#pragma unroll
    for (int r = 0; r < 16; ++r) dst[r * 64] = og[r];
    dst[16 * 64] = kh == 0 ? lb : la;
    dst[17 * 64] = kh == 0 ? nmb : nma;
    __syncthreads();
    const __attribute__((address_space(3))) float* src = xb + (w ^ 4) * (18 * 64) + lane;
    f32x16 om = kh == 0 ? oa : ob;
    const float nm_me = kh == 0 ? nma : nmb;
    const float nm_ot = src[17 * 64];
    const float nmin = fminf(nm_me, nm_ot);
    const float f_me = __builtin_amdgcn_exp2f(nmin - nm_me), f_ot = __builtin_amdgcn_exp2f(nmin - nm_ot);
    const float rl = 1.f / ((kh == 0 ? la : lb) * f_me + src[16 * 64] * f_ot);
    const float g_me = f_me * rl, g_ot = f_ot * rl;
    u16* op = oblk + ((size_t)head * 2 * SEQ + q0 + kh * 32 + r32) * 16 + 4 * hh;
#pragma unroll
    for (int g = 0; g < 4; ++g) {
      float v0 = om[4 * g] * g_me + src[(4 * g) * 64] * g_ot;
      float v1 = om[4 * g + 1] * g_me + src[(4 * g + 1) * 64] * g_ot;
      float v2 = om[4 * g + 2] * g_me + src[(4 * g + 2) * 64] * g_ot;
      float v3 = om[4 * g + 3] * g_me + src[(4 * g + 3) * 64] * g_ot;
      u32x2 v;
      v[0] = pack2(v0, v1);
      v[1] = pack2(v2, v3);
      *(u32x2*)(op + (g >> 1) * (SEQ * 16) + (g & 1) * 8) = v;
    }
  }
  {
    wait_vm<0>();
    const int task = blockIdx.x * 2 + (threadIdx.x >> 8);
    mod_task(c, wmod, part, 32 + (task >> 3), task & 7, threadIdx.x & 255,
             (float*)(smem_ + 40960) + (threadIdx.x >> 8) * 256);
  }
  STAMP_AT(2, 3);
}

extern "C" void kernel_launch(void* const* d_in, const int* in_sizes, int n_in, void* d_out, int out_size,
                              void* d_ws, size_t ws_size, hipStream_t stream) {
  const u16* x = (const u16*)d_in[0];
  const u16* wqkv = (const u16*)d_in[1];
  const u16* wo = (const u16*)d_in[2];
  const u16* wg = (const u16*)d_in[4];
  const u16* wu = (const u16*)d_in[5];
  const u16* wd = (const u16*)d_in[6];
  const u16* c = (const u16*)d_in[7];
  const u16* wmod = (const u16*)d_in[8];
  u16* out = (u16*)d_out;
  char* ws = (char*)d_ws;
  float* part = (float*)ws;
  float* modf = (float*)(ws + (256 << 10));
  unsigned* flags = (unsigned*)(ws + (512 << 10));
  u16* h1 = (u16*)(ws + (1 << 20));
  u16* qkv = (u16*)(ws + (8 << 20));
  u16* ob = (u16*)(ws + (24 << 20));
  u16* x2 = (u16*)(ws + (32 << 20));
  u16* h2 = (u16*)(ws + (40 << 20));
  u16* hh = (u16*)(ws + (48 << 20));

  hipFuncSetAttribute((const void*)k_qkv, hipFuncAttributeMaxDynamicSharedMemorySize, CQ::LDS);
  hipFuncSetAttribute((const void*)k_proj<DM>, hipFuncAttributeMaxDynamicSharedMemorySize, CP::LDS);
  hipFuncSetAttribute((const void*)k_proj<FF>, hipFuncAttributeMaxDynamicSharedMemorySize, CP::LDS);
  hipFuncSetAttribute((const void*)k_glu, hipFuncAttributeMaxDynamicSharedMemorySize, CG::LDS);
  hipFuncSetAttribute((const void*)k_attn, hipFuncAttributeMaxDynamicSharedMemorySize, ALDS);

  k_pre<<<256 + 256, 256, 0, stream>>>(c, wmod, part, (const char*)x, (const char*)wqkv, flags);
  k_ln<6, MNS, true><<<256 + 192, 512, 0, stream>>>(x, part, h1, (const char*)wqkv);
  k_qkv<<<256, 512, CQ::LDS, stream>>>(h1, wqkv, qkv);
  k_attn<<<256, 512, ALDS, stream>>>(qkv, ob, c, wmod, part, (const char*)wo, (const char*)wg, (const char*)wu, (const char*)wd);
  k_proj<DM><<<256, 512, CP::LDS, stream>>>(ob, wo, x, part + 2 * DM, x2);
  k_ln<7, MNS, false><<<256, 512, 0, stream>>>(x2, part + 3 * DM, h2, (const char*)wqkv);
  k_glu<<<256, 512, CG::LDS, stream>>>(h2, wg, wu, hh);
  k_proj<FF><<<256, 512, CP::LDS, stream>>>(hh, wd, x2, part + 5 * DM, out);
}
```

```cpp
#include <hip/hip_runtime.h>
#include <hip/hip_bf16.h>
#include <stdint.h>

typedef __attribute__((ext_vector_type(8))) short bf16x8;
typedef __attribute__((ext_vector_type(4))) short s16x4;
typedef __attribute__((ext_vector_type(4))) float f32x4;
typedef __attribute__((ext_vector_type(2))) float f32x2;
typedef __attribute__((ext_vector_type(16))) float f32x16;
typedef __attribute__((ext_vector_type(4))) unsigned u32x4;
typedef __attribute__((ext_vector_type(2))) unsigned u32x2;
typedef __attribute__((ext_vector_type(2))) __bf16 bf16x2_t;
typedef unsigned short u16;
typedef __attribute__((address_space(3))) char* lds_ptr;
typedef __attribute__((address_space(3))) const char* lds_cptr;

#define DI __device__ __forceinline__

constexpr int SEQ = 2048, DM = 1024, FF = 3584, NH = 32;

DI float bf2f(u16 x) { return __uint_as_float(((unsigned)x) << 16); }
DI float bflo(unsigned x) { return __uint_as_float(x << 16); }
DI float bfhi(unsigned x) { return __uint_as_float(x & 0xffff0000u); }
DI unsigned pack2(float a, float b) {
  f32x2 v = {a, b};
  bf16x2_t r = __builtin_convertvector(v, bf16x2_t);
  return __builtin_bit_cast(unsigned, r);
}
DI float rbf(float a) { return bflo(pack2(a, 0.f) & 0xffffu); }

#define MFMA16(a, b, c) __builtin_amdgcn_mfma_f32_16x16x32_bf16((a), (b), (c), 0, 0, 0)
#define MFMA32(a, b, c) __builtin_amdgcn_mfma_f32_32x32x16_bf16((a), (b), (c), 0, 0, 0)

DI void glds16(const void* base, unsigned voff, unsigned lds_addr) {
  unsigned sv;
  asm volatile("s_mov_b32 %0, m0\n\ts_mov_b32 m0, %3\n\ts_nop 0\n\tglobal_load_lds_dwordx4 %1, %2\n\ts_mov_b32 m0, %0"
               : "=&s"(sv) : "v"(voff), "s"(base), "s"(lds_addr) : "memory");
}
DI unsigned lds_u32(lds_ptr p) { return (unsigned)(uintptr_t)p; }
DI bf16x8 lds_read128(lds_cptr p) { return *(const __attribute__((address_space(3))) bf16x8*)p; }
DI s16x4 lds_tr(lds_cptr p) {
  typedef short v4i16_t __attribute__((ext_vector_type(4)));
  return __builtin_bit_cast(s16x4, __builtin_amdgcn_ds_read_tr16_b64_v4i16((__attribute__((address_space(3))) v4i16_t*)p));
}
DI bf16x8 lds_tr8(lds_cptr p0, lds_cptr p1) {
  s16x4 a = lds_tr(p0), b = lds_tr(p1);
  return __builtin_shufflevector(a, b, 0, 1, 2, 3, 4, 5, 6, 7);
}
#define STAMP_AT(kid, slot) do {} while (0)
template <int N> DI void wait_vm() { asm volatile("s_waitcnt vmcnt(%0)" ::"n"(N) : "memory"); }
DI void bar() { __builtin_amdgcn_s_barrier(); }

constexpr int MNS = 8;
struct ModTask { u32x4 w[4]; u16 cv[4]; };
DI void mod_load(ModTask& mt, const u16* __restrict__ c, const u16* __restrict__ wmod, int cb, int ks, int t) {
  const int cc = t & 7, rg = t >> 3;
  const int k0 = ks * 128 + rg;
  const u16* wp = wmod + (size_t)k0 * (6 * DM) + cb * 64 + cc * 8;
#pragma unroll
  for (int i = 0; i < 4; ++i) mt.w[i] = *(const u32x4*)(wp + (size_t)i * 32 * (6 * DM));
#pragma unroll
  for (int i = 0; i < 4; ++i) mt.cv[i] = c[k0 + 32 * i];
}
DI void mod_finish(const ModTask& mt, float* __restrict__ part, int cb, int ks, int t, float* s_red  ) {
  float acc[8];
#pragma unroll
  for (int j = 0; j < 8; ++j) acc[j] = 0.f;
#pragma unroll
  for (int i = 0; i < 4; ++i) {
    const float cv = bf2f(mt.cv[i]);
    const float sv = rbf(cv / (1.f + __expf(-cv)));
#pragma unroll
    for (int j = 0; j < 4; ++j) {
      acc[2 * j] += sv * bflo(mt.w[i][j]);
      acc[2 * j + 1] += sv * bfhi(mt.w[i][j]);
    }
  }
#pragma unroll
  for (int j = 0; j < 8; ++j) {
    float v = acc[j];
    v += __shfl_xor(v, 8);
    v += __shfl_xor(v, 16);
    v += __shfl_xor(v, 32);
    acc[j] = v;
  }
  const int lane = t & 63, wv = t >> 6;
  if (lane < 8) {
#pragma unroll
    for (int j = 0; j < 8; ++j) s_red[wv * 64 + lane * 8 + j] = acc[j];
  }
  __syncthreads();
  if (t < 64) part[ks * (6 * DM) + cb * 64 + t] = (s_red[t] + s_red[64 + t]) + (s_red[128 + t] + s_red[192 + t]);
}
DI f32x4 gate4(const float* __restrict__ gate, int col) {
  f32x4 gt = *(const f32x4*)(gate + col);
#pragma unroll
  for (int i = 1; i < MNS; ++i) gt += *(const f32x4*)(gate + i * (6 * DM) + col);
#pragma unroll
  for (int j = 0; j < 4; ++j) gt[j] = rbf(gt[j]);
  return gt;
}

DI void touch16k(const char* p, int t) {
  u32x4 v[4];
#pragma unroll
  for (int i = 0; i < 4; ++i) v[i] = *(const u32x4*)(p + i * 4096 + t * 16);
#pragma unroll
  for (int i = 0; i < 4; ++i) asm volatile("" ::"v"(v[i]));
}

__global__ __launch_bounds__(256) void k_pre(const u16* __restrict__ c, const u16* __restrict__ wmod, float* __restrict__ part,
                                              const char* __restrict__ x, const char* __restrict__ wqkv,
                                              unsigned* __restrict__ flags) {
  __shared__ float s_red[256];
  const int t = threadIdx.x, b = blockIdx.x;
  if (b < 256) STAMP_AT(0, 0);
  if (b < 256) {
    ModTask mt;
    mod_load(mt, c, wmod, b >> 3, b & 7, t);
    mod_finish(mt, part, b >> 3, b & 7, t, s_red);
    if (b == 0) flags[t * 16] = 0u;
  } else {
    touch16k(x + (size_t)(b - 256) * 16384, t);
  }
  if (b < 256) STAMP_AT(0, 3);
}

template <int KID, int NS, bool FIRST>
__global__ __launch_bounds__(512) void k_ln(const u16* __restrict__ x, const float* __restrict__ ss  ,
                                             u16* __restrict__ h16, const char* __restrict__ pf) {
  __shared__ __attribute__((aligned(16))) float sm[2048];
  const int t = threadIdx.x;
  if (FIRST && blockIdx.x >= 256) {
    touch16k(pf + (size_t)((blockIdx.x - 256) * 2 + (t >> 8)) * 16384, t & 255);
    return;
  }
  const int lane = t & 63, wv = t >> 6;
  const int row = blockIdx.x * 8 + wv;
  STAMP_AT(KID, 0);
  const u16* xr = x + (size_t)row * DM;
  u32x4 v0 = *(const u32x4*)(xr + lane * 8);
  u32x4 v1 = *(const u32x4*)(xr + 512 + lane * 8);
  {
    f32x4 pv[NS];
#pragma unroll
    for (int i = 0; i < NS; ++i) pv[i] = *(const f32x4*)(ss + i * (6 * DM) + t * 4);
    f32x4 a = pv[0];
#pragma unroll
    for (int i = 1; i < NS; ++i) a += pv[i];
#pragma unroll
    for (int j = 0; j < 4; ++j) {
      float v = rbf(a[j]);
      a[j] = t < 256 ? v : rbf(1.f + v);
    }
    *(f32x4*)(sm + t * 4) = a;
  }
  float f[16];
#pragma unroll
  for (int j = 0; j < 4; ++j) {
    f[2 * j] = bflo(v0[j]); f[2 * j + 1] = bfhi(v0[j]);
    f[8 + 2 * j] = bflo(v1[j]); f[8 + 2 * j + 1] = bfhi(v1[j]);
  }
  float s = 0.f;
#pragma unroll
  for (int j = 0; j < 16; ++j) s += f[j];
#pragma unroll
  for (int o = 1; o < 64; o <<= 1) s += __shfl_xor(s, o);
  const float mean = s * (1.f / DM);
  float q = 0.f;
#pragma unroll
  for (int j = 0; j < 16; ++j) { float d = f[j] - mean; q += d * d; }
#pragma unroll
  for (int o = 1; o < 64; o <<= 1) q += __shfl_xor(q, o);
  const float mb = rbf(mean);
  const float vb = rbf(q * (1.f / DM));
  const float rden = 1.f / rbf(sqrtf(rbf(vb + 1e-5f)));
  __syncthreads();
#pragma unroll
  for (int half = 0; half < 2; ++half) {
    const int k0 = half * 512 + lane * 8;
    f32x4 sh0 = *(const f32x4*)(sm + k0), sh1 = *(const f32x4*)(sm + k0 + 4);
    f32x4 sc0 = *(const f32x4*)(sm + 1024 + k0), sc1 = *(const f32x4*)(sm + 1024 + k0 + 4);
    float y[8];
#pragma unroll
    for (int j = 0; j < 8; ++j) {
      float g = j < 4 ? sc0[j] : sc1[j - 4];
      float sh = j < 4 ? sh0[j] : sh1[j - 4];
      float d = rbf(f[half * 8 + j] - mb);
      float n = rbf(d * rden);
      y[j] = rbf(n * g) + sh;
    }
    u32x4 o;
#pragma unroll
    for (int j = 0; j < 4; ++j) o[j] = pack2(y[2 * j], y[2 * j + 1]);
    *(u32x4*)(h16 + ((size_t)(k0 >> 4) * SEQ + row) * 16 + (k0 & 15)) = o;
  }
  STAMP_AT(KID, 3);
}

DI u32x4 pair16(u32x2 a, u32x2 b) {
  auto s0 = __builtin_amdgcn_permlane16_swap(a[0], b[0], false, false);
  auto s1 = __builtin_amdgcn_permlane16_swap(a[1], b[1], false, false);
  return u32x4{s0[0], s1[0], s0[1], s1[1]};
}

DI int hA(int r) { return (-r) & 3; }

template <int BM, int NGRP, int KS, int NST>
struct GCfg {
  static constexpr int A_SUB = BM * 64;
  static constexpr int W_SUB = NGRP * 4096;
  static constexpr int STAGE = KS * (A_SUB + W_SUB);
  static constexpr int APW = KS * BM / 128;
  static constexpr int WPW = KS * NGRP / 2;
  static constexpr int PPW = APW + WPW;
  static constexpr int LDS = STAGE * NST;
};

template <class C>
struct Loader {
  const char* abase;
  const char* wbase[C::WPW];
  unsigned av[C::APW];
  unsigned wv[C::WPW];
  int aoff[C::APW];
  int woff[C::WPW];
};

template <int BM, int NGRP, int KS, int NST, int MT, int NT, int KSPLIT, int NKT, int KID, int IOFS>
struct Engine {
  typedef GCfg<BM, NGRP, KS, NST> C;
  static constexpr int SPW = KS / KSPLIT;
  static constexpr int P = C::PPW;
  static constexpr int G = NT * SPW;
  f32x4 (&acc)[NT][MT];
  lds_ptr smem;
  Loader<C>& ld;
  long a_adv, w_adv;
  const int (&waddr)[NT];
  int a_sub0, w_sub0;
  int cur, nxt, fre;
  bf16x8 wf[NT], af[2][MT];

  DI Engine(f32x4 (&acc_)[NT][MT], lds_ptr smem_, Loader<C>& ld_, long a_adv_, long w_adv_, int aaddr, const int (&waddr_)[NT], int kg)
      : acc(acc_), smem(smem_), ld(ld_), a_adv(a_adv_), w_adv(w_adv_), waddr(waddr_) {
    a_sub0 = kg * C::A_SUB + aaddr;
    w_sub0 = KS * C::A_SUB + kg * C::W_SUB;
  }
  template <int I> DI void piece(int stage_off) {
    if constexpr (I < C::APW) {
      glds16(ld.abase, ld.av[I], lds_u32(smem) + stage_off + ld.aoff[I]);
      if constexpr (I == C::APW - 1) ld.abase += a_adv * 2;
    } else {
      constexpr int J = I - C::APW;
      glds16(ld.wbase[J], ld.wv[J], lds_u32(smem) + stage_off + ld.woff[J]);
      ld.wbase[J] += w_adv * 2;
    }
  }
  template <int I> DI void issue_all(int stage_off) {
    if constexpr (I < P) { piece<I>(stage_off); issue_all<I + 1>(stage_off); }
  }
  template <int g, int I> DI void issue_at(int stage_off) {
    if constexpr (I < P) {
      constexpr int span = (G - IOFS) > P ? (G - IOFS) : P;
      constexpr int pos = IOFS + (I * span) / P < G ? IOFS + (I * span) / P : G - 1;
      if constexpr (pos == g) piece<I>(stage_off);
      issue_at<g, I + 1>(stage_off);
    }
  }
  template <int PAR, int S, bool PF, bool DOISSUE, int n>
  DI void groups(lds_cptr ab, lds_cptr wb) {
    if constexpr (n < NT) {
#pragma unroll
      for (int m = 0; m < MT; ++m) acc[n][m] = MFMA16(wf[n], af[PAR][m], acc[n][m]);
      if constexpr (PF) {
        wf[n] = lds_tr8(wb + waddr[n], wb + waddr[n] + 512);
#pragma unroll
        for (int m = 0; m < MT; ++m)
          if ((m * NT) / MT == n) af[PAR ^ 1][m] = lds_read128(ab + m * 512);
      }
      if constexpr (DOISSUE) issue_at<S * NT + n, 0>(fre);
      __builtin_amdgcn_sched_barrier(0);
      groups<PAR, S, PF, DOISSUE, n + 1>(ab, wb);
    }
  }
  template <int PAR, bool DOISSUE, bool LAST, int S> DI void steps() {
    if constexpr (S < SPW) {
      if constexpr (S + 1 < SPW)
        groups<PAR, S, true, DOISSUE, 0>(smem + cur + a_sub0 + (S + 1) * KSPLIT * C::A_SUB, smem + cur + w_sub0 + (S + 1) * KSPLIT * C::W_SUB);
      else if constexpr (!LAST)
        groups<PAR, S, true, DOISSUE, 0>(smem + nxt + a_sub0, smem + nxt + w_sub0);
      else
        groups<PAR, S, false, false, 0>(smem, smem);
      steps<PAR ^ 1, DOISSUE, LAST, S + 1>();
    }
  }
  template <int PAR, bool DOISSUE, bool LAST, int WAITN> DI void iter() {
    if constexpr (!LAST) { wait_vm<WAITN>(); bar(); }
    steps<PAR, DOISSUE, LAST, 0>();
    fre = cur; cur = nxt; nxt = (nxt + C::STAGE == NST * C::STAGE) ? 0 : nxt + C::STAGE;
  }
  template <int KT> DI void tail() {
    if constexpr (KT < NKT) {
      constexpr bool DOISSUE = KT + NST - 1 < NKT;
      constexpr bool LAST = KT == NKT - 1;
      constexpr int newest = (KT + NST - 2) < (NKT - 1) ? (KT + NST - 2) : (NKT - 1);
      constexpr int younger = newest - (KT + 1) > 0 ? newest - (KT + 1) : 0;
      iter<(KT * SPW) & 1, DOISSUE, LAST, P * younger>();
      tail<KT + 1>();
    }
  }
  DI void start() {
    static_assert(NKT >= NST + 1, "too few k tiles");
    STAMP_AT(KID, 0);
#pragma unroll
    for (int s = 0; s < NST - 1; ++s) issue_all<0>(s * C::STAGE);
  }
  DI void run() {
    wait_vm<P*(NST - 2)>();
    bar();
    STAMP_AT(KID, 1);
    {
      lds_cptr ab = smem + a_sub0;
      lds_cptr wb = smem + w_sub0;
#pragma unroll
      for (int m = 0; m < MT; ++m) af[0][m] = lds_read128(ab + m * 512);
#pragma unroll
      for (int n = 0; n < NT; ++n) wf[n] = lds_tr8(wb + waddr[n], wb + waddr[n] + 512);
    }
    cur = 0; nxt = C::STAGE; fre = (NST - 1) * C::STAGE;
    constexpr int STEADY = NKT - NST + 1;
    constexpr int LOOPN = (STEADY / 2) * 2;
    for (int kt = 0; kt < LOOPN; kt += 2) {
      iter<0, true, false, P*(NST - 3)>();
      iter<SPW & 1, true, false, P*(NST - 3)>();
    }
    tail<LOOPN>();
    STAMP_AT(KID, 2);
  }
};

template <class C, int BM, int NGRP, int KS>
DI void setup_loader_A(Loader<C>& ld, const u16* Ablk, int m0, int w, int lane) {
#pragma unroll
  for (int i = 0; i < C::APW; ++i) {
    int a = w + 8 * i;
    int blk = a / (BM / 32), j = a % (BM / 32);
    ld.aoff[i] = blk * (BM * 32) + j * 1024;
    ld.av[i] = ((blk * SEQ + m0 + j * 32) * 16) * 2 + lane * 16;
  }
  ld.abase = (const char*)Ablk;
}

DI int wfrag_addr(int t, int lane) {
  int g = lane >> 4, q = (lane & 15) >> 2, p = lane & 3;
  int f = ((q >> 1) & 1) | ((g & 1) << 1);
  return (t >> 2) * 4096 + (8 * g + q) * 128 + (((t & 3) ^ f) * 32) + p * 8;
}
template <int BM>
DI int afrag_addr(int mrow0, int lane) {
  int r = lane & 15, g = lane >> 4;
  return (g >> 1) * (BM * 32) + (mrow0 + r) * 32 + (g & 1) * 16;
}

constexpr float QSCALE = 0.17677669529663687f * 1.4426950408889634f;
typedef GCfg<128, 3, 2, 4> CQ;
__global__ __launch_bounds__(512, 2) void k_qkv(const u16* __restrict__ Ablk, const u16* __restrict__ W,
                                                 u16* __restrict__ out) {
  extern __shared__ __attribute__((aligned(16))) char smem_[];
  lds_ptr smem = (lds_ptr)smem_;
  const int lane = threadIdx.x & 63;
  const int w = __builtin_amdgcn_readfirstlane(threadIdx.x >> 6);
  const int xcd = blockIdx.x & 7, idx = blockIdx.x >> 3;
  const int tm = (xcd & 1) * 8 + (idx & 7), tn = (xcd >> 1) * 4 + (idx >> 3);
  const int m0 = tm * 128, n0 = tn * 192;
  constexpr int LDW = 3 * DM;
  Loader<CQ> ld;
  setup_loader_A<CQ, 128, 3, 2>(ld, Ablk, m0, w, lane);
#pragma unroll
  for (int i = 0; i < CQ::WPW; ++i) {
    int b = w + 8 * i;
    int sub = b / 12, r = b % 12, grp = r >> 2, o = r & 3;
    ld.woff[i] = 2 * CQ::A_SUB + sub * CQ::W_SUB + grp * 4096 + o * 1024;
    int k = sub * 32 + 8 * o + (lane >> 3);
    int f = ((lane >> 4) & 1) | ((o & 1) << 1);
    int col = n0 + grp * 64 + ((((lane & 7) >> 1) ^ f) * 16) + (lane & 1) * 8;
    ld.wv[i] = (k * LDW + col) * 2;
    ld.wbase[i] = (const char*)W;
  }
  const int wm = w & 1, wn = w >> 1;
  int waddr[3];
#pragma unroll
  for (int n = 0; n < 3; ++n) waddr[n] = wfrag_addr(wn * 3 + n, lane);
  const int aaddr = afrag_addr<128>(wm * 64, lane);
  f32x4 acc[3][4];
#pragma unroll
  for (int n = 0; n < 3; ++n)
#pragma unroll
    for (int m = 0; m < 4; ++m) acc[n][m] = f32x4{0.f, 0.f, 0.f, 0.f};
  Engine<128, 3, 2, 4, 4, 3, 1, DM / 64, 1, 0> eng(acc, smem, ld, (long)2 * SEQ * 32, (long)64 * LDW, aaddr, waddr, 0);
  eng.start();
  eng.run();
  const int g = lane >> 4;
#pragma unroll
  for (int n = 0; n < 3; ++n) {
    const int cb = (n0 >> 4) + wn * 3 + n;
    const float sc = cb < DM / 16 ? QSCALE : 1.f;
#pragma unroll
    for (int m = 0; m < 4; m += 2) {
      u32x2 a, b;
      a[0] = pack2(acc[n][m][0] * sc, acc[n][m][1] * sc);
      a[1] = pack2(acc[n][m][2] * sc, acc[n][m][3] * sc);
      b[0] = pack2(acc[n][m + 1][0] * sc, acc[n][m + 1][1] * sc);
      b[1] = pack2(acc[n][m + 1][2] * sc, acc[n][m + 1][3] * sc);
      const int row = m0 + wm * 64 + (m + (g & 1)) * 16 + (lane & 15);
      *(u32x4*)(out + ((size_t)cb * SEQ + row) * 16 + (g >> 1) * 8) = pair16(a, b);
    }
  }
  STAMP_AT(1, 3);
}

typedef GCfg<128, 1, 2, 6> CP;
template <int KDIM>
__global__ __launch_bounds__(512, 2) void k_proj(const u16* __restrict__ Ablk, const u16* __restrict__ W,
                                                  const u16* __restrict__ res, const float* __restrict__ gate,
                                                  u16* __restrict__ out) {
  extern __shared__ __attribute__((aligned(16))) char smem_[];
  lds_ptr smem = (lds_ptr)smem_;
  const int lane = threadIdx.x & 63;
  const int w = __builtin_amdgcn_readfirstlane(threadIdx.x >> 6);
  const int xcd = blockIdx.x & 7, idx = blockIdx.x >> 3;
  const int tm = (xcd & 3) * 4 + (idx & 3), tn = (xcd >> 2) * 8 + (idx >> 2);
  const int m0 = tm * 128, n0 = tn * 64;
  constexpr int LDW = DM;
  Loader<CP> ld;
  setup_loader_A<CP, 128, 1, 2>(ld, Ablk, m0, w, lane);
  {
    int b = w;
    int sub = b >> 2, o = b & 3;
    ld.woff[0] = 2 * CP::A_SUB + sub * CP::W_SUB + o * 1024;
    int k = sub * 32 + 8 * o + (lane >> 3);
    int f = ((lane >> 4) & 1) | ((o & 1) << 1);
    int col = n0 + ((((lane & 7) >> 1) ^ f) * 16) + (lane & 1) * 8;
    ld.wv[0] = (k * LDW + col) * 2;
    ld.wbase[0] = (const char*)W;
  }
  const int wm = w & 1, wn = (w >> 1) & 1, kg = w >> 2;
  int waddr[2];
#pragma unroll
  for (int n = 0; n < 2; ++n) waddr[n] = wfrag_addr(wn * 2 + n, lane);
  const int aaddr = afrag_addr<128>(wm * 64, lane);
  f32x4 acc[2][4];
#pragma unroll
  for (int n = 0; n < 2; ++n)
#pragma unroll
    for (int m = 0; m < 4; ++m) acc[n][m] = f32x4{0.f, 0.f, 0.f, 0.f};
  Engine<128, 1, 2, 6, 4, 2, 2, KDIM / 64, (KDIM == DM ? 3 : 5), 0> eng(acc, smem, ld, (long)2 * SEQ * 32, (long)64 * LDW, aaddr, waddr, kg);
  f32x4 gts[2];
#pragma unroll
  for (int n = 0; n < 2; ++n) gts[n] = gate4(gate, n0 + (wn * 2 + n) * 16 + 4 * (lane >> 4));
  eng.start();
  eng.run();
  __syncthreads();
  __attribute__((address_space(3))) f32x4* red = (__attribute__((address_space(3))) f32x4*)smem;
  if (kg == 1) {
#pragma unroll
    for (int n = 0; n < 2; ++n)
#pragma unroll
      for (int m = 0; m < 4; ++m) red[((w & 3) * 8 + n * 4 + m) * 64 + lane] = acc[n][m];
  }
  __syncthreads();
  if (kg == 0) {
    const int g = lane >> 4;
#pragma unroll
    for (int n = 0; n < 2; ++n) {
      int col = n0 + (wn * 2 + n) * 16 + 4 * g;
      const f32x4 gt = gts[n];
#pragma unroll
      for (int m = 0; m < 4; ++m) {
        f32x4 a = acc[n][m] + red[((w & 3) * 8 + n * 4 + m) * 64 + lane];
        int row = m0 + wm * 64 + m * 16 + (lane & 15);
        u32x2 r = *(const u32x2*)(res + (size_t)row * DM + col);
        float y0 = bflo(r[0]) + rbf(gt[0] * rbf(a[0]));
        float y1 = bfhi(r[0]) + rbf(gt[1] * rbf(a[1]));
        float y2 = bflo(r[1]) + rbf(gt[2] * rbf(a[2]));
        float y3 = bfhi(r[1]) + rbf(gt[3] * rbf(a[3]));
        u32x2 o;
        o[0] = pack2(y0, y1);
        o[1] = pack2(y2, y3);
        *(u32x2*)(out + (size_t)row * DM + col) = o;
      }
    }
  }
  STAMP_AT((KDIM == DM ? 3 : 5), 3);
}

typedef GCfg<128, 2, 2, 4> CD;
__global__ __launch_bounds__(512, 2) void k_down(const u16* __restrict__ Ablk, const u16* __restrict__ W,
                                                  const u16* __restrict__ res, const float* __restrict__ gate,
                                                  u16* __restrict__ out, float* __restrict__ xbuf, unsigned* flags) {
  extern __shared__ __attribute__((aligned(16))) char smem_[];
  lds_ptr smem = (lds_ptr)smem_;
  const int lane = threadIdx.x & 63;
  const int w = __builtin_amdgcn_readfirstlane(threadIdx.x >> 6);
  const int xcd = blockIdx.x & 7, idx = blockIdx.x >> 3;
  const int h = idx & 1, t = idx >> 1;
  const int tm = (xcd & 3) * 4 + (t & 3), tn = (xcd >> 2) * 4 + (t >> 2);
  const int m0 = tm * 128, n0 = tn * 128;
  constexpr int LDW = DM, KH = FF / 2;
  Loader<CD> ld;
  setup_loader_A<CD, 128, 2, 2>(ld, Ablk + (size_t)h * (KH / 16) * SEQ * 16, m0, w, lane);
#pragma unroll
  for (int i = 0; i < CD::WPW; ++i) {
    int b = w + 8 * i;
    int sub = b >> 3, grp = (b >> 2) & 1, o = b & 3;
    ld.woff[i] = 2 * CD::A_SUB + sub * CD::W_SUB + grp * 4096 + o * 1024;
    int k = sub * 32 + 8 * o + (lane >> 3);
    int f = ((lane >> 4) & 1) | ((o & 1) << 1);
    int col = n0 + grp * 64 + ((((lane & 7) >> 1) ^ f) * 16) + (lane & 1) * 8;
    ld.wv[i] = (k * LDW + col) * 2;
    ld.wbase[i] = (const char*)(W + (size_t)h * KH * LDW);
  }
  const int wm = w & 1, wn = (w >> 1) & 1, kg = w >> 2;
  int waddr[4];
#pragma unroll
  for (int n = 0; n < 4; ++n) waddr[n] = wfrag_addr(wn * 4 + n, lane);
  const int aaddr = afrag_addr<128>(wm * 64, lane);
  f32x4 acc[4][4];
#pragma unroll
  for (int n = 0; n < 4; ++n)
#pragma unroll
    for (int m = 0; m < 4; ++m) acc[n][m] = f32x4{0.f, 0.f, 0.f, 0.f};
  const int g = lane >> 4;
  f32x4 gts[2];
#pragma unroll
  for (int n = 0; n < 2; ++n) gts[n] = gate4(gate, n0 + (wn * 4 + kg * 2 + n) * 16 + 4 * g);
  Engine<128, 2, 2, 4, 4, 4, 2, KH / 64, 5, 0> eng(acc, smem, ld, (long)2 * SEQ * 32, (long)64 * LDW, aaddr, waddr, kg);
  eng.start();
  eng.run();
  __syncthreads();
  __attribute__((address_space(3))) f32x4* red = (__attribute__((address_space(3))) f32x4*)smem;
  f32x4 keep[2][4];
#pragma unroll
  for (int n = 0; n < 2; ++n)
#pragma unroll
    for (int m = 0; m < 4; ++m) {
      red[(w * 8 + n * 4 + m) * 64 + lane] = kg == 0 ? acc[2 + n][m] : acc[n][m];
      keep[n][m] = kg == 0 ? acc[n][m] : acc[2 + n][m];
    }
  __syncthreads();
#pragma unroll
  for (int n = 0; n < 2; ++n)
#pragma unroll
    for (int m = 0; m < 4; ++m) keep[n][m] += red[((w ^ 4) * 8 + n * 4 + m) * 64 + lane];
  const int slot = (wn * 2 + kg) * 8192 + lane * 16;
  if (wm != h) {
    auto rs = __builtin_amdgcn_make_buffer_rsrc((void*)((char*)xbuf + (size_t)blockIdx.x * 32768), 0, 32768, 0x00020000);
#pragma unroll
    for (int n = 0; n < 2; ++n)
#pragma unroll
      for (int m = 0; m < 4; ++m)
        __builtin_amdgcn_raw_buffer_store_b128(__builtin_bit_cast(u32x4, keep[n][m]), rs, slot + (n * 4 + m) * 1024, 0, 16);
  }
  asm volatile("s_waitcnt vmcnt(0)" ::: "memory");
  __syncthreads();
  if (threadIdx.x == 0) {
    __hip_atomic_store(flags + blockIdx.x * 16, 1u, __ATOMIC_RELAXED, __HIP_MEMORY_SCOPE_AGENT);
    const unsigned* pf = flags + (blockIdx.x ^ 8) * 16;
    for (int spin = 0; spin < (1 << 22); ++spin) {
      if (__hip_atomic_load(pf, __ATOMIC_RELAXED, __HIP_MEMORY_SCOPE_AGENT) != 0u) break;
      __builtin_amdgcn_s_sleep(1);
    }
    __builtin_amdgcn_fence(__ATOMIC_ACQUIRE, "agent");
    asm volatile("s_waitcnt vmcnt(0)" ::: "memory");
  }
  __syncthreads();
  if (wm == h) {
    const char* src = (const char*)xbuf + (size_t)(blockIdx.x ^ 8) * 32768 + slot;
#pragma unroll
    for (int n = 0; n < 2; ++n) {
      const int col = n0 + (wn * 4 + kg * 2 + n) * 16 + 4 * g;
      const f32x4 gt = gts[n];
#pragma unroll
      for (int m = 0; m < 4; m += 2) {
        u32x2 o[2];
#pragma unroll
        for (int i = 0; i < 2; ++i) {
          f32x4 a = keep[n][m + i] + *(const f32x4*)(src + (n * 4 + m + i) * 1024);
          const int row = m0 + wm * 64 + (m + i) * 16 + (lane & 15);
          u32x2 r = *(const u32x2*)(res + (size_t)row * DM + col);
          o[i][0] = pack2(bflo(r[0]) + rbf(gt[0] * rbf(a[0])), bfhi(r[0]) + rbf(gt[1] * rbf(a[1])));
          o[i][1] = pack2(bflo(r[1]) + rbf(gt[2] * rbf(a[2])), bfhi(r[1]) + rbf(gt[3] * rbf(a[3])));
        }
        const int row = m0 + wm * 64 + (m + (g & 1)) * 16 + (lane & 15);
        *(u32x4*)(out + (size_t)row * DM + n0 + (wn * 4 + kg * 2 + n) * 16 + (g >> 1) * 8) = pair16(o[0], o[1]);
      }
    }
  }
  STAMP_AT(5, 3);
}

typedef GCfg<256, 4, 1, 4> CG;
template <int NH_>
DI void glu_wave(lds_ptr smem, Loader<CG>& ld, int lane, int wm, int t0, int m0, int f0, u16* __restrict__ out) {
  constexpr int NT = 2 * NH_;
  int waddr[NT];
#pragma unroll
  for (int n = 0; n < NH_; ++n) {
    waddr[n] = wfrag_addr(t0 + n, lane);
    waddr[NH_ + n] = waddr[n] + 8192;
  }
  const int aaddr = afrag_addr<256>(wm * 64, lane);
  f32x4 acc[NT][4];
#pragma unroll
  for (int n = 0; n < NT; ++n)
#pragma unroll
    for (int m = 0; m < 4; ++m) acc[n][m] = f32x4{0.f, 0.f, 0.f, 0.f};
  Engine<256, 4, 1, 4, 4, NT, 1, DM / 32, 4, (NH_ == 4 ? 0 : 1)> eng(acc, smem, ld, (long)SEQ * 32, (long)32 * FF, aaddr, waddr, 0);
  eng.start();
  eng.run();
  const int g = lane >> 4;
#pragma unroll
  for (int n = 0; n < NH_; ++n) {
    const int cb = (f0 >> 4) + t0 + n;
#pragma unroll
    for (int m = 0; m < 4; m += 2) {
      u32x2 o[2];
#pragma unroll
      for (int i = 0; i < 2; ++i) {
        f32x4 a = acc[n][m + i], b = acc[NH_ + n][m + i];
        o[i][0] = pack2(rbf(a[0]) * rbf(b[0]), rbf(a[1]) * rbf(b[1]));
        o[i][1] = pack2(rbf(a[2]) * rbf(b[2]), rbf(a[3]) * rbf(b[3]));
      }
      const int row = m0 + wm * 64 + (m + (g & 1)) * 16 + (lane & 15);
      *(u32x4*)(out + ((size_t)cb * SEQ + row) * 16 + (g >> 1) * 8) = pair16(o[0], o[1]);
    }
  }
  STAMP_AT(4, 3);
}

__global__ __launch_bounds__(512, 2) void k_glu(const u16* __restrict__ Ablk, const u16* __restrict__ Wg,
                                                 const u16* __restrict__ Wu, u16* __restrict__ out) {
  extern __shared__ __attribute__((aligned(16))) char smem_[];
  lds_ptr smem = (lds_ptr)smem_;
  const int lane = threadIdx.x & 63;
  const int w = __builtin_amdgcn_readfirstlane(threadIdx.x >> 6);
  const int xcd = blockIdx.x & 7, idx = blockIdx.x >> 3;
  const int tm = idx & 7, tn = xcd * 4 + (idx >> 3);
  const int m0 = tm * 256, f0 = tn * 112;
  Loader<CG> ld;
  setup_loader_A<CG, 256, 4, 1>(ld, Ablk, m0, w, lane);
#pragma unroll
  for (int i = 0; i < CG::WPW; ++i) {
    int b = w + 8 * i;
    int grp = b >> 2, o = b & 3;
    ld.woff[i] = CG::A_SUB + grp * 4096 + o * 1024;
    int k = 8 * o + (lane >> 3);
    int f = ((lane >> 4) & 1) | ((o & 1) << 1);
    int pr = ((lane & 7) >> 1) ^ f;
    if ((grp & 1) && pr == 3) pr = 0;
    int col = f0 + (grp & 1) * 64 + pr * 16 + (lane & 1) * 8;
    ld.wv[i] = (k * FF + col) * 2;
    ld.wbase[i] = (const char*)(grp >> 1 ? Wu : Wg);
  }
  const int wm = w & 3;
  if (w < 4) glu_wave<4>(smem, ld, lane, wm, 0, m0, f0, out);
  else glu_wave<3>(smem, ld, lane, wm, 4, m0, f0, out);
}

constexpr int ACH = 128, ANST = 4, ASTAGE = ACH * 64 * 2 + 256;
constexpr int ALDS = ANST * ASTAGE + 1024 + 16;

DI float rowmax16(const f32x16& v) {
  float mx = fmaxf(fmaxf(v[0], v[1]), v[2]);
#pragma unroll
  for (int r = 3; r < 15; r += 2) mx = fmaxf(fmaxf(mx, v[r]), v[r + 1]);
  mx = fmaxf(mx, v[15]);
  auto sw = __builtin_amdgcn_permlane32_swap(__float_as_uint(mx), __float_as_uint(mx), false, false);
  return fmaxf(__uint_as_float(sw[0]), __uint_as_float(sw[1]));
}

template <bool SAFE>
DI void attn_pass(lds_ptr smem, int lane, int w, const char* Kp, const char* Vp, const char* pf_wm, const char* pf_wo, const char* pf_wg,
                  const char* pf_wu, const char* pf_wd, const bf16x8& qa0, const bf16x8& qa1, const bf16x8& qb0,
                  const bf16x8& qb1, f32x16& oa, f32x16& ob, float& refa, float& refb, float& la_out, float& lb_out) {
  const int r32 = lane & 31, hh = lane >> 5, kh = w >> 2;
  const unsigned kvo = ((32 * (w & 3) + (lane >> 1)) * 16 + (((lane & 1) ^ ((lane >> 4) & 1)) * 8)) * 2;
  const unsigned vvo = ((32 * (w & 3)) * 16) * 2 + lane * 16;
  const char* ksrc = Kp;
  const char* vsrc = Vp;
  auto issue = [&](int buf) {
    if (w < 4) {
      const unsigned l0 = lds_u32(smem) + buf * ASTAGE + w * 1024;
      glds16(ksrc, kvo, l0);
      glds16(ksrc + SEQ * 32, kvo, l0 + 4096);
      glds16(vsrc, vvo, l0 + 8192);
      glds16(vsrc + SEQ * 32, vvo, l0 + 8192 + 4096 + 128);
    }
    ksrc += ACH * 32; vsrc += ACH * 32;
  };
  const unsigned pfo = lane * 16;
  auto prefetch = [&](int ch) {
    if (w >= 4) {
#pragma unroll
      for (int i = 0; i < 3; ++i) {
        int pc = (ch * 3 + i) * 4 + (w - 4);
        const char* base;
        if (pc < 32) base = pf_wm + (size_t)(blockIdx.x * 4 + (pc >> 3)) * (12 * DM) + 4 * DM + (pc & 7) * 1024;
        else if (pc < 40) base = pf_wo + (size_t)blockIdx.x * 8192 + (pc - 32) * 1024;
        else if (pc < 68) base = pf_wg + (size_t)blockIdx.x * 28672 + (pc - 40) * 1024;
        else if (pc < 96) base = pf_wu + (size_t)blockIdx.x * 28672 + (pc - 68) * 1024;
        else if (pc < 124) base = pf_wd + (size_t)blockIdx.x * 28672 + (pc - 96) * 1024;
        else break;
        glds16(base, pfo, lds_u32(smem) + ANST * ASTAGE);
      }
    }
  };
  issue(0); issue(1); issue(2);
  const int kaddr0 = kh * 2048 + r32 * 32 + ((hh ^ ((r32 >> 3) & 1)) * 16);
  const int kaddr1 = kaddr0 + 4096;
  const int vaddr = 8192 + kh * 2048 + ((lane >> 4) & 1) * (4096 + 128) + (4 * hh + ((lane & 15) >> 2)) * 32 + (lane & 3) * 8;
  bf16x8 sel;
  {
    const short one = ((lane & 15) == ((lane >> 4) & 1)) ? (short)0x3F80 : (short)0;
#pragma unroll
    for (int j = 0; j < 8; ++j) sel[j] = one;
  }
  f32x16 nma, nmb, sa, sb;
#pragma unroll
  for (int r = 0; r < 16; ++r) { oa[r] = 0.f; ob[r] = 0.f; nma[r] = 0.f; nmb[r] = 0.f; }
  f32x4 lsa = {0.f, 0.f, 0.f, 0.f}, lsb = {0.f, 0.f, 0.f, 0.f};
  float la = 0.f, lb = 0.f;
  constexpr int NCH = SEQ / ACH;
  if (w < 4) wait_vm<8>();
  bar();
  const bf16x8 kf0_ = lds_read128(smem + kaddr0);
  const bf16x8 kf1_ = lds_read128(smem + kaddr1);
  {
    sa = MFMA32(kf0_, qa0, nma); sa = MFMA32(kf1_, qa1, sa);
    sb = MFMA32(kf0_, qb0, nmb); sb = MFMA32(kf1_, qb1, sb);
    const float ma = rowmax16(sa), mb = rowmax16(sb);
#pragma unroll
    for (int r = 0; r < 16; ++r) { nma[r] = -ma; sa[r] -= ma; nmb[r] = -mb; sb[r] -= mb; }
  }
  if constexpr (SAFE) {
    auto half = [&](f32x16& s, f32x16& o, f32x16& nm, float& l, const bf16x8& q0f, const bf16x8& q1f,
                    const bf16x8& kn0, const bf16x8& kn1, const bf16x8& vf0, const bf16x8& vf1, bool has_next) {
      f32x16 p;
      const float dl = fmaxf(rowmax16(s), 0.f);
      const float f = __builtin_amdgcn_exp2f(-dl);
      float t = 0.f;
#pragma unroll
      for (int r = 0; r < 16; ++r) { nm[r] -= dl; o[r] *= f; p[r] = __builtin_amdgcn_exp2f(s[r] - dl); t += p[r]; }
      l = l * f + t;
      if (has_next) { s = MFMA32(kn0, q0f, nm); s = MFMA32(kn1, q1f, s); }
      u32x4 pa, pb;
#pragma unroll
      for (int j = 0; j < 4; ++j) { pa[j] = pack2(p[2 * j], p[2 * j + 1]); pb[j] = pack2(p[8 + 2 * j], p[8 + 2 * j + 1]); }
      o = MFMA32(vf0, __builtin_bit_cast(bf16x8, pa), o);
      o = MFMA32(vf1, __builtin_bit_cast(bf16x8, pb), o);
    };
    auto step = [&](lds_cptr vb, lds_cptr knext, bool has_next) {
      bf16x8 kn0, kn1;
      if (has_next) { kn0 = lds_read128(knext + kaddr0); kn1 = lds_read128(knext + kaddr1); }
      bf16x8 vf0 = lds_tr8(vb, vb + 256);
      bf16x8 vf1 = lds_tr8(vb + 512, vb + 768);
      half(sa, oa, nma, la, qa0, qa1, kn0, kn1, vf0, vf1, has_next);
      half(sb, ob, nmb, lb, qb0, qb1, kn0, kn1, vf0, vf1, has_next);
    };
    for (int ch = 0; ch < NCH; ++ch) {
      if (w < 4) { if (ch + 2 < NCH) wait_vm<4>(); else wait_vm<0>(); }
      bar();
      if (ch + 3 < NCH) issue((ch + 3) & 3);
      lds_cptr sbuf = smem + (ch & 3) * ASTAGE;
      lds_cptr nbuf = smem + ((ch + 1) & 3) * ASTAGE;
      step(sbuf + vaddr, sbuf + 1024, true);
      step(sbuf + 1024 + vaddr, nbuf, ch + 1 < NCH);
    }
  } else {
    u32x4 pa0, pa1, pb0, pb1;
    bf16x8 kc0 = kf0_, kc1 = kf1_;
    bf16x8 vp0, vp1;
#pragma unroll
    for (int j = 0; j < 4; ++j) { pb0[j] = 0u; pb1[j] = 0u; }
#pragma unroll
    for (int j = 0; j < 8; ++j) { vp0[j] = 0; vp1[j] = 0; }
#define SB_ __builtin_amdgcn_sched_barrier(0)
#define EX(i) e[i] = __builtin_amdgcn_exp2f(S[i])
#define PHASE(S, P0, P1, M0, M1, M2, M3, M4, M5, R0, R1) do { float e[16]; \
      M0; SB_; R0; EX(0); EX(1); EX(2); EX(3); SB_; \
      M1; SB_; R1; EX(4); EX(5); EX(6); EX(7); SB_; \
      M2; SB_; EX(8); EX(9); P0[0] = pack2(e[0], e[1]); P0[1] = pack2(e[2], e[3]); SB_; \
      M3; SB_; EX(10); EX(11); P0[2] = pack2(e[4], e[5]); P0[3] = pack2(e[6], e[7]); SB_; \
      M4; SB_; EX(12); EX(13); EX(14); EX(15); SB_; \
      M5; SB_; P1[0] = pack2(e[8], e[9]); P1[1] = pack2(e[10], e[11]); P1[2] = pack2(e[12], e[13]); P1[3] = pack2(e[14], e[15]); SB_; \
    } while (0)
#define BF(x) __builtin_bit_cast(bf16x8, x)
    auto step = [&](lds_cptr vb, lds_cptr knext, auto has_next_t) {
      constexpr bool HN = decltype(has_next_t)::value;
      bf16x8 kn0, kn1, vc0, vc1;
      { const f32x16& S = sa;
        PHASE(S, pa0, pa1,
              sb = MFMA32(kc0, qb0, nmb), sb = MFMA32(kc1, qb1, sb),
              ob = MFMA32(vp0, BF(pb0), ob), ob = MFMA32(vp1, BF(pb1), ob),
              lsb = MFMA16(sel, BF(pb0), lsb), lsb = MFMA16(sel, BF(pb1), lsb),
              if (HN) { kn0 = lds_read128(knext + kaddr0); kn1 = lds_read128(knext + kaddr1); },
              { vc0 = lds_tr8(vb, vb + 256); vc1 = lds_tr8(vb + 512, vb + 768); }); }
      { const f32x16& S = sb;
        PHASE(S, pb0, pb1,
              if (HN) sa = MFMA32(kn0, qa0, nma), if (HN) sa = MFMA32(kn1, qa1, sa),
              oa = MFMA32(vc0, BF(pa0), oa), oa = MFMA32(vc1, BF(pa1), oa),
              lsa = MFMA16(sel, BF(pa0), lsa), lsa = MFMA16(sel, BF(pa1), lsa),
              , ); }
      if (HN) { kc0 = kn0; kc1 = kn1; }
      vp0 = vc0; vp1 = vc1;
    };
    typedef std::integral_constant<bool, true> T_;
    typedef std::integral_constant<bool, false> F_;
    for (int ch = 0; ch < NCH - 1; ++ch) {
      if (w < 4) { if (ch + 2 < NCH) wait_vm<4>(); else wait_vm<0>(); }
      bar();
      if (ch + 3 < NCH) issue((ch + 3) & 3);
      prefetch(ch);
      lds_cptr sbuf = smem + (ch & 3) * ASTAGE;
      lds_cptr nbuf = smem + ((ch + 1) & 3) * ASTAGE;
      step(sbuf + vaddr, sbuf + 1024, T_{});
      step(sbuf + 1024 + vaddr, nbuf, T_{});
    }
    {
      lds_cptr sbuf = smem + ((NCH - 1) & 3) * ASTAGE;
      step(sbuf + vaddr, sbuf + 1024, T_{});
      step(sbuf + 1024 + vaddr, sbuf, F_{});
    }
    ob = MFMA32(vp0, BF(pb0), ob); ob = MFMA32(vp1, BF(pb1), ob);
    lsb = MFMA16(sel, BF(pb0), lsb); lsb = MFMA16(sel, BF(pb1), lsb);
#undef PHASE
#undef EX
#undef BF
  }
  refa = nma[0]; refb = nmb[0];
  if constexpr (SAFE) {
    auto s1 = __builtin_amdgcn_permlane32_swap(__float_as_uint(la), __float_as_uint(la), false, false);
    la_out = __uint_as_float(s1[0]) + __uint_as_float(s1[1]);
    auto s2 = __builtin_amdgcn_permlane32_swap(__float_as_uint(lb), __float_as_uint(lb), false, false);
    lb_out = __uint_as_float(s2[0]) + __uint_as_float(s2[1]);
  } else {
    const float a0 = __shfl(lsa[0], lane & 15), a1 = __shfl(lsa[1], lane & 15);
    const float b0 = __shfl(lsb[0], lane & 15), b1 = __shfl(lsb[1], lane & 15);
    la_out = (lane & 16) ? a1 : a0;
    lb_out = (lane & 16) ? b1 : b0;
  }
}

__global__ __launch_bounds__(512, 2) void k_attn(const u16* __restrict__ qkv, u16* __restrict__ oblk,
                                                  const u16* __restrict__ c, const u16* __restrict__ wmod, float* __restrict__ part,
                                                  const char* __restrict__ pf_wo, const char* __restrict__ pf_wg,
                                                  const char* __restrict__ pf_wu, const char* __restrict__ pf_wd) {
  extern __shared__ __attribute__((aligned(16))) char smem_[];
  lds_ptr smem = (lds_ptr)smem_;
  const int lane = threadIdx.x & 63;
  const int w = __builtin_amdgcn_readfirstlane(threadIdx.x >> 6);
  const int xcd = blockIdx.x & 7, idx = blockIdx.x >> 3;
  const int head = xcd * 4 + (idx >> 3), qb = idx & 7;
  const int r32 = lane & 31, hh = lane >> 5;
  const int qg = w & 3, kh = w >> 2;
  const u16* Qp = qkv + (size_t)head * SEQ * 32;
  const char* Kp = (const char*)(qkv + (size_t)(32 + head) * SEQ * 32);
  const char* Vp = (const char*)(qkv + (size_t)(64 + head) * SEQ * 32);
  const int q0 = qb * 256 + qg * 64;
  STAMP_AT(2, 0);
  __attribute__((address_space(3))) int* flag = (__attribute__((address_space(3))) int*)(smem + ANST * ASTAGE + 1024);
  if (threadIdx.x == 0) *flag = 0;
  bf16x8 qa0 = *(const bf16x8*)(Qp + (q0 + r32) * 16 + hh * 8);
  bf16x8 qa1 = *(const bf16x8*)(Qp + (SEQ + q0 + r32) * 16 + hh * 8);
  bf16x8 qb0 = *(const bf16x8*)(Qp + (q0 + 32 + r32) * 16 + hh * 8);
  bf16x8 qb1 = *(const bf16x8*)(Qp + (SEQ + q0 + 32 + r32) * 16 + hh * 8);
  f32x16 oa, ob;
  float nma, nmb, la, lb;
  attn_pass<false>(smem, lane, w, Kp, Vp, (const char*)wmod, pf_wo, pf_wg, pf_wu, pf_wd, qa0, qa1, qb0, qb1, oa, ob, nma, nmb, la, lb);
  {
    float big = fmaxf(fabsf(la), fabsf(lb));
#pragma unroll
    for (int r = 0; r < 16; ++r) big = fmaxf(big, fmaxf(fabsf(oa[r]), fabsf(ob[r])));
    bool bad = !(big < 1e30f);
#pragma unroll
    for (int r = 0; r < 16; ++r) bad = bad || (oa[r] != oa[r]) || (ob[r] != ob[r]);
    bad = bad || (la != la) || (lb != lb);
    if (__any(bad) && lane == 0) *flag = 1;
  }
  __syncthreads();
  if (__builtin_amdgcn_readfirstlane(*flag) != 0) {
    attn_pass<true>(smem, lane, w, Kp, Vp, (const char*)wmod, pf_wo, pf_wg, pf_wu, pf_wd, qa0, qa1, qb0, qb1, oa, ob, nma, nmb, la, lb);
    __syncthreads();
  }
  STAMP_AT(2, 2);
  const int task = blockIdx.x * 2 + (threadIdx.x >> 8);
  ModTask mt;
  mod_load(mt, c, wmod, 32 + (task >> 3), task & 7, threadIdx.x & 255);
  {
    __attribute__((address_space(3))) float* xb = (__attribute__((address_space(3))) float*)smem;
    const f32x16& og = kh == 0 ? ob : oa;
    __attribute__((address_space(3))) float* dst = xb + w * (18 * 64) + lane;
#pragma unroll
    for (int r = 0; r < 16; ++r) dst[r * 64] = og[r];
    dst[16 * 64] = kh == 0 ? lb : la;
    dst[17 * 64] = kh == 0 ? nmb : nma;
    __syncthreads();
    const __attribute__((address_space(3))) float* src = xb + (w ^ 4) * (18 * 64) + lane;
    f32x16 om = kh == 0 ? oa : ob;
    const float nm_me = kh == 0 ? nma : nmb;
    const float nm_ot = src[17 * 64];
    const float nmin = fminf(nm_me, nm_ot);
    const float f_me = __builtin_amdgcn_exp2f(nmin - nm_me), f_ot = __builtin_amdgcn_exp2f(nmin - nm_ot);
    const float rl = 1.f / ((kh == 0 ? la : lb) * f_me + src[16 * 64] * f_ot);
    const float g_me = f_me * rl, g_ot = f_ot * rl;
    u16* op = oblk + ((size_t)head * 2 * SEQ + q0 + kh * 32 + r32) * 16 + 4 * hh;
#pragma unroll
    for (int g = 0; g < 4; ++g) {
      float v0 = om[4 * g] * g_me + src[(4 * g) * 64] * g_ot;
      float v1 = om[4 * g + 1] * g_me + src[(4 * g + 1) * 64] * g_ot;
      float v2 = om[4 * g + 2] * g_me + src[(4 * g + 2) * 64] * g_ot;
      float v3 = om[4 * g + 3] * g_me + src[(4 * g + 3) * 64] * g_ot;
      u32x2 v;
      v[0] = pack2(v0, v1);
      v[1] = pack2(v2, v3);
      *(u32x2*)(op + (g >> 1) * (SEQ * 16) + (g & 1) * 8) = v;
    }
  }
  mod_finish(mt, part, 32 + (task >> 3), task & 7, threadIdx.x & 255, (float*)(smem_ + 40960) + (threadIdx.x >> 8) * 256);
  wait_vm<0>();
  STAMP_AT(2, 3);
}

extern "C" void kernel_launch(void* const* d_in, const int* in_sizes, int n_in, void* d_out, int out_size,
                              void* d_ws, size_t ws_size, hipStream_t stream) {
  const u16* x = (const u16*)d_in[0];
  const u16* wqkv = (const u16*)d_in[1];
  const u16* wo = (const u16*)d_in[2];
  const u16* wg = (const u16*)d_in[4];
  const u16* wu = (const u16*)d_in[5];
  const u16* wd = (const u16*)d_in[6];
  const u16* c = (const u16*)d_in[7];
  const u16* wmod = (const u16*)d_in[8];
  u16* out = (u16*)d_out;
  char* ws = (char*)d_ws;
  float* part = (float*)ws;
  float* modf = (float*)(ws + (256 << 10));
  unsigned* flags = (unsigned*)(ws + (512 << 10));
  u16* h1 = (u16*)(ws + (1 << 20));
  u16* qkv = (u16*)(ws + (8 << 20));
  u16* ob = (u16*)(ws + (24 << 20));
  u16* x2 = (u16*)(ws + (32 << 20));
  u16* h2 = (u16*)(ws + (40 << 20));
  u16* hh = (u16*)(ws + (48 << 20));
  float* xbuf = (float*)(ws + (64 << 20));

  hipFuncSetAttribute((const void*)k_qkv, hipFuncAttributeMaxDynamicSharedMemorySize, CQ::LDS);
  hipFuncSetAttribute((const void*)k_proj<DM>, hipFuncAttributeMaxDynamicSharedMemorySize, CP::LDS);
  hipFuncSetAttribute((const void*)k_down, hipFuncAttributeMaxDynamicSharedMemorySize, CD::LDS);
  hipFuncSetAttribute((const void*)k_glu, hipFuncAttributeMaxDynamicSharedMemorySize, CG::LDS);
  hipFuncSetAttribute((const void*)k_attn, hipFuncAttributeMaxDynamicSharedMemorySize, ALDS);

  k_pre<<<256 + 256, 256, 0, stream>>>(c, wmod, part, (const char*)x, (const char*)wqkv, flags);
  k_ln<6, MNS, true><<<256 + 192, 512, 0, stream>>>(x, part, h1, (const char*)wqkv);
  k_qkv<<<256, 512, CQ::LDS, stream>>>(h1, wqkv, qkv);
  k_attn<<<256, 512, ALDS, stream>>>(qkv, ob, c, wmod, part, (const char*)wo, (const char*)wg, (const char*)wu, (const char*)wd);
  k_proj<DM><<<256, 512, CP::LDS, stream>>>(ob, wo, x, part + 2 * DM, x2);
  k_ln<7, MNS, false><<<256, 512, 0, stream>>>(x2, part + 3 * DM, h2, (const char*)wqkv);
  k_glu<<<256, 512, CG::LDS, stream>>>(h2, wg, wu, hh);
  k_down<<<256, 512, CD::LDS, stream>>>(hh, wd, x2, part + 5 * DM, out, xbuf, flags);
}
```

```cpp
#include <hip/hip_runtime.h>
#include <hip/hip_bf16.h>
#include <stdint.h>

typedef __attribute__((ext_vector_type(8))) short bf16x8;
typedef __attribute__((ext_vector_type(4))) short s16x4;
typedef __attribute__((ext_vector_type(4))) float f32x4;
typedef __attribute__((ext_vector_type(2))) float f32x2;
typedef __attribute__((ext_vector_type(16))) float f32x16;
typedef __attribute__((ext_vector_type(4))) unsigned u32x4;
typedef __attribute__((ext_vector_type(2))) unsigned u32x2;
typedef __attribute__((ext_vector_type(2))) __bf16 bf16x2_t;
typedef unsigned short u16;
typedef __attribute__((address_space(3))) char* lds_ptr;
typedef __attribute__((address_space(3))) const char* lds_cptr;

#define DI __device__ __forceinline__

constexpr int SEQ = 2048, DM = 1024, FF = 3584, NH = 32;

DI float bf2f(u16 x) { return __uint_as_float(((unsigned)x) << 16); }
DI float bflo(unsigned x) { return __uint_as_float(x << 16); }
DI float bfhi(unsigned x) { return __uint_as_float(x & 0xffff0000u); }
DI unsigned pack2(float a, float b) {
  f32x2 v = {a, b};
  bf16x2_t r = __builtin_convertvector(v, bf16x2_t);
  return __builtin_bit_cast(unsigned, r);
}
DI float rbf(float a) { return bflo(pack2(a, 0.f) & 0xffffu); }

#define MFMA16(a, b, c) __builtin_amdgcn_mfma_f32_16x16x32_bf16((a), (b), (c), 0, 0, 0)
#define MFMA32(a, b, c) __builtin_amdgcn_mfma_f32_32x32x16_bf16((a), (b), (c), 0, 0, 0)

DI void glds16(const void* base, unsigned voff, unsigned lds_addr) {
  unsigned sv;
  asm volatile("s_mov_b32 %0, m0\n\ts_mov_b32 m0, %3\n\ts_nop 0\n\tglobal_load_lds_dwordx4 %1, %2\n\ts_mov_b32 m0, %0"
               : "=&s"(sv) : "v"(voff), "s"(base), "s"(lds_addr) : "memory");
}
DI unsigned lds_u32(lds_ptr p) { return (unsigned)(uintptr_t)p; }
DI bf16x8 lds_read128(lds_cptr p) { return *(const __attribute__((address_space(3))) bf16x8*)p; }
DI s16x4 lds_tr(lds_cptr p) {
  typedef short v4i16_t __attribute__((ext_vector_type(4)));
  return __builtin_bit_cast(s16x4, __builtin_amdgcn_ds_read_tr16_b64_v4i16((__attribute__((address_space(3))) v4i16_t*)p));
}
DI bf16x8 lds_tr8(lds_cptr p0, lds_cptr p1) {
  s16x4 a = lds_tr(p0), b = lds_tr(p1);
  return __builtin_shufflevector(a, b, 0, 1, 2, 3, 4, 5, 6, 7);
}
#define STAMP_AT(kid, slot) do {} while (0)
template <int N> DI void wait_vm() { asm volatile("s_waitcnt vmcnt(%0)" ::"n"(N) : "memory"); }
DI void bar() { __builtin_amdgcn_s_barrier(); }

constexpr int MNS = 8;
struct ModTask { u32x4 w[4]; u16 cv[4]; };
DI void mod_load(ModTask& mt, const u16* __restrict__ c, const u16* __restrict__ wmod, int cb, int ks, int t) {
  const int cc = t & 7, rg = t >> 3;
  const int k0 = ks * 128 + rg;
  const u16* wp = wmod + (size_t)k0 * (6 * DM) + cb * 64 + cc * 8;
#pragma unroll
  for (int i = 0; i < 4; ++i) mt.w[i] = *(const u32x4*)(wp + (size_t)i * 32 * (6 * DM));
#pragma unroll
  for (int i = 0; i < 4; ++i) mt.cv[i] = c[k0 + 32 * i];
}
DI void mod_finish(const ModTask& mt, float* __restrict__ part, int cb, int ks, int t, float* s_red  ) {
  float acc[8];
#pragma unroll
  for (int j = 0; j < 8; ++j) acc[j] = 0.f;
#pragma unroll
  for (int i = 0; i < 4; ++i) {
    const float cv = bf2f(mt.cv[i]);
    const float sv = rbf(cv / (1.f + __expf(-cv)));
#pragma unroll
    for (int j = 0; j < 4; ++j) {
      acc[2 * j] += sv * bflo(mt.w[i][j]);
      acc[2 * j + 1] += sv * bfhi(mt.w[i][j]);
    }
  }
#pragma unroll
  for (int j = 0; j < 8; ++j) {
    float v = acc[j];
    v += __shfl_xor(v, 8);
    v += __shfl_xor(v, 16);
    v += __shfl_xor(v, 32);
    acc[j] = v;
  }
  const int lane = t & 63, wv = t >> 6;
  if (lane < 8) {
#pragma unroll
    for (int j = 0; j < 8; ++j) s_red[wv * 64 + lane * 8 + j] = acc[j];
  }
  __syncthreads();
  if (t < 64) part[ks * (6 * DM) + cb * 64 + t] = (s_red[t] + s_red[64 + t]) + (s_red[128 + t] + s_red[192 + t]);
}
DI f32x4 gate4(const float* __restrict__ gate, int col) {
  f32x4 gt = *(const f32x4*)(gate + col);
#pragma unroll
  for (int i = 1; i < MNS; ++i) gt += *(const f32x4*)(gate + i * (6 * DM) + col);
#pragma unroll
  for (int j = 0; j < 4; ++j) gt[j] = rbf(gt[j]);
  return gt;
}

DI void touch16k(const char* p, int t) {
  u32x4 v[4];
#pragma unroll
  for (int i = 0; i < 4; ++i) v[i] = *(const u32x4*)(p + i * 4096 + t * 16);
#pragma unroll
  for (int i = 0; i < 4; ++i) asm volatile("" ::"v"(v[i]));
}

__global__ __launch_bounds__(256) void k_pre(const u16* __restrict__ c, const u16* __restrict__ wmod, float* __restrict__ part,
                                              const char* __restrict__ x, const char* __restrict__ wqkv,
                                              unsigned* __restrict__ flags) {
  __shared__ float s_red[256];
  const int t = threadIdx.x, b = blockIdx.x;
  if (b < 256) STAMP_AT(0, 0);
  if (b < 256) {
    ModTask mt;
    mod_load(mt, c, wmod, b >> 3, b & 7, t);
    mod_finish(mt, part, b >> 3, b & 7, t, s_red);
    if (b == 0) flags[t * 16] = 0u;
  } else {
    touch16k(x + (size_t)(b - 256) * 16384, t);
  }
  if (b < 256) STAMP_AT(0, 3);
}

template <int KID, int NS, bool FIRST>
__global__ __launch_bounds__(512) void k_ln(const u16* __restrict__ x, const float* __restrict__ ss  ,
                                             u16* __restrict__ h16, const char* __restrict__ pf) {
  __shared__ __attribute__((aligned(16))) float sm[2048];
  const int t = threadIdx.x;
  if (FIRST && blockIdx.x >= 256) {
    touch16k(pf + (size_t)((blockIdx.x - 256) * 2 + (t >> 8)) * 16384, t & 255);
    return;
  }
  const int lane = t & 63, wv = t >> 6;
  const int row = blockIdx.x * 8 + wv;
  STAMP_AT(KID, 0);
  const u16* xr = x + (size_t)row * DM;
  u32x4 v0 = *(const u32x4*)(xr + lane * 8);
  u32x4 v1 = *(const u32x4*)(xr + 512 + lane * 8);
  {
    f32x4 pv[NS];
#pragma unroll
    for (int i = 0; i < NS; ++i) pv[i] = *(const f32x4*)(ss + i * (6 * DM) + t * 4);
    f32x4 a = pv[0];
#pragma unroll
    for (int i = 1; i < NS; ++i) a += pv[i];
#pragma unroll
    for (int j = 0; j < 4; ++j) {
      float v = rbf(a[j]);
      a[j] = t < 256 ? v : rbf(1.f + v);
    }
    *(f32x4*)(sm + t * 4) = a;
  }
  float f[16];
#pragma unroll
  for (int j = 0; j < 4; ++j) {
    f[2 * j] = bflo(v0[j]); f[2 * j + 1] = bfhi(v0[j]);
    f[8 + 2 * j] = bflo(v1[j]); f[8 + 2 * j + 1] = bfhi(v1[j]);
  }
  float s = 0.f;
#pragma unroll
  for (int j = 0; j < 16; ++j) s += f[j];
#pragma unroll
  for (int o = 1; o < 64; o <<= 1) s += __shfl_xor(s, o);
  const float mean = s * (1.f / DM);
  float q = 0.f;
#pragma unroll
  for (int j = 0; j < 16; ++j) { float d = f[j] - mean; q += d * d; }
#pragma unroll
  for (int o = 1; o < 64; o <<= 1) q += __shfl_xor(q, o);
  const float mb = rbf(mean);
  const float vb = rbf(q * (1.f / DM));
  const float rden = 1.f / rbf(sqrtf(rbf(vb + 1e-5f)));
  __syncthreads();
#pragma unroll
  for (int half = 0; half < 2; ++half) {
    const int k0 = half * 512 + lane * 8;
    f32x4 sh0 = *(const f32x4*)(sm + k0), sh1 = *(const f32x4*)(sm + k0 + 4);
    f32x4 sc0 = *(const f32x4*)(sm + 1024 + k0), sc1 = *(const f32x4*)(sm + 1024 + k0 + 4);
    float y[8];
#pragma unroll
    for (int j = 0; j < 8; ++j) {
      float g = j < 4 ? sc0[j] : sc1[j - 4];
      float sh = j < 4 ? sh0[j] : sh1[j - 4];
      float d = rbf(f[half * 8 + j] - mb);
      float n = rbf(d * rden);
      y[j] = rbf(n * g) + sh;
    }
    u32x4 o;
#pragma unroll
    for (int j = 0; j < 4; ++j) o[j] = pack2(y[2 * j], y[2 * j + 1]);
    *(u32x4*)(h16 + ((size_t)(k0 >> 4) * SEQ + row) * 16 + (k0 & 15)) = o;
  }
  STAMP_AT(KID, 3);
}

DI u32x4 pair16(u32x2 a, u32x2 b) {
  auto s0 = __builtin_amdgcn_permlane16_swap(a[0], b[0], false, false);
  auto s1 = __builtin_amdgcn_permlane16_swap(a[1], b[1], false, false);
  return u32x4{s0[0], s1[0], s0[1], s1[1]};
}

DI int hA(int r) { return (-r) & 3; }

template <int BM, int NGRP, int KS, int NST, int NLW_ = 8>
struct GCfg {
  static constexpr int NLW = NLW_;
  static constexpr int A_SUB = BM * 64;
  static constexpr int W_SUB = NGRP * 4096;
  static constexpr int STAGE = KS * (A_SUB + W_SUB);
  static constexpr int APW = KS * BM / 16 / NLW;
  static constexpr int WPW = KS * NGRP * 4 / NLW;
  static constexpr int PPW = APW + WPW;
  static constexpr int LDS = STAGE * NST;
};

template <class C>
struct Loader {
  const char* abase;
  const char* wbase[C::WPW];
  unsigned av[C::APW];
  unsigned wv[C::WPW];
  int aoff[C::APW];
  int woff[C::WPW];
};

template <int BM, int NGRP, int KS, int NST, int MT, int NT, int KSPLIT, int NKT, int KID, int IOFS, int NLW = 8, bool LOADS = (NLW == 8)>
struct Engine {
  typedef GCfg<BM, NGRP, KS, NST, NLW> C;
  static constexpr bool DED = !LOADS;
  static constexpr int SPW = KS / KSPLIT;
  static constexpr int P = C::PPW;
  static constexpr int G = NT * SPW;
  f32x4 (&acc)[NT][MT];
  lds_ptr smem;
  Loader<C>& ld;
  long a_adv, w_adv;
  const int (&waddr)[NT];
  int a_sub0, w_sub0;
  int cur, nxt, fre;
  bf16x8 wf[NT], af[2][MT];

  DI Engine(f32x4 (&acc_)[NT][MT], lds_ptr smem_, Loader<C>& ld_, long a_adv_, long w_adv_, int aaddr, const int (&waddr_)[NT], int kg)
      : acc(acc_), smem(smem_), ld(ld_), a_adv(a_adv_), w_adv(w_adv_), waddr(waddr_) {
    a_sub0 = kg * C::A_SUB + aaddr;
    w_sub0 = KS * C::A_SUB + kg * C::W_SUB;
  }
  template <int I> DI void piece(int stage_off) {
    if constexpr (I < C::APW) {
      glds16(ld.abase, ld.av[I], lds_u32(smem) + stage_off + ld.aoff[I]);
      if constexpr (I == C::APW - 1) ld.abase += a_adv * 2;
    } else {
      constexpr int J = I - C::APW;
      glds16(ld.wbase[J], ld.wv[J], lds_u32(smem) + stage_off + ld.woff[J]);
      ld.wbase[J] += w_adv * 2;
    }
  }
  template <int I> DI void issue_all(int stage_off) {
    if constexpr (I < P) { piece<I>(stage_off); issue_all<I + 1>(stage_off); }
  }
  template <int g, int I> DI void issue_at(int stage_off) {
    if constexpr (I < P) {
      constexpr int span = (G - IOFS) > P ? (G - IOFS) : P;
      constexpr int pos = IOFS + (I * span) / P < G ? IOFS + (I * span) / P : G - 1;
      if constexpr (pos == g) piece<I>(stage_off);
      issue_at<g, I + 1>(stage_off);
    }
  }
  template <int PAR, int S, bool PF, bool DOISSUE, int n>
  DI void groups(lds_cptr ab, lds_cptr wb) {
    if constexpr (n < NT) {
#pragma unroll
      for (int m = 0; m < MT; ++m) acc[n][m] = MFMA16(wf[n], af[PAR][m], acc[n][m]);
      if constexpr (PF) {
        wf[n] = lds_tr8(wb + waddr[n], wb + waddr[n] + 512);
#pragma unroll
        for (int m = 0; m < MT; ++m)
          if ((m * NT) / MT == n) af[PAR ^ 1][m] = lds_read128(ab + m * 512);
      }
      if constexpr (DOISSUE && !DED) issue_at<S * NT + n, 0>(fre);
      __builtin_amdgcn_sched_barrier(0);
      groups<PAR, S, PF, DOISSUE, n + 1>(ab, wb);
    }
  }
  template <int PAR, bool DOISSUE, bool LAST, int S> DI void steps() {
    if constexpr (S < SPW) {
      if constexpr (S + 1 < SPW)
        groups<PAR, S, true, DOISSUE, 0>(smem + cur + a_sub0 + (S + 1) * KSPLIT * C::A_SUB, smem + cur + w_sub0 + (S + 1) * KSPLIT * C::W_SUB);
      else if constexpr (!LAST)
        groups<PAR, S, true, DOISSUE, 0>(smem + nxt + a_sub0, smem + nxt + w_sub0);
      else
        groups<PAR, S, false, false, 0>(smem, smem);
      steps<PAR ^ 1, DOISSUE, LAST, S + 1>();
    }
  }
  template <int PAR, bool DOISSUE, bool LAST, int WAITN> DI void iter() {
    if constexpr (!LAST) { if constexpr (!DED) wait_vm<WAITN>(); bar(); }
    steps<PAR, DOISSUE, LAST, 0>();
    fre = cur; cur = nxt; nxt = (nxt + C::STAGE == NST * C::STAGE) ? 0 : nxt + C::STAGE;
  }
  template <int KT> DI void tail() {
    if constexpr (KT < NKT) {
      constexpr bool DOISSUE = KT + NST - 1 < NKT;
      constexpr bool LAST = KT == NKT - 1;
      constexpr int newest = (KT + NST - 2) < (NKT - 1) ? (KT + NST - 2) : (NKT - 1);
      constexpr int younger = newest - (KT + 1) > 0 ? newest - (KT + 1) : 0;
      iter<(KT * SPW) & 1, DOISSUE, LAST, P * younger>();
      tail<KT + 1>();
    }
  }
  DI void start() {
    static_assert(NKT >= NST + 1, "too few k tiles");
    STAMP_AT(KID, 0);
    if constexpr (!DED) {
#pragma unroll
      for (int s = 0; s < NST - 1; ++s) issue_all<0>(s * C::STAGE);
    }
  }
  template <int KT> DI void ltail() {
    if constexpr (KT < NKT - 1) {
      constexpr int newest = (KT + NST - 2) < (NKT - 1) ? (KT + NST - 2) : (NKT - 1);
      constexpr int younger = newest - (KT + 1) > 0 ? newest - (KT + 1) : 0;
      wait_vm<P * younger>();
      bar();
      ltail<KT + 1>();
    }
  }
  DI void loader_run() {
#pragma unroll
    for (int s = 0; s < NST - 1; ++s) issue_all<0>(s * C::STAGE);
    wait_vm<P*(NST - 2)>();
    bar();
    fre = (NST - 1) * C::STAGE;
    for (int kt = 0; kt < NKT - NST + 1; ++kt) {
      wait_vm<P*(NST - 3)>();
      bar();
      issue_all<0>(fre);
      fre = (fre + C::STAGE == NST * C::STAGE) ? 0 : fre + C::STAGE;
    }
    ltail<NKT - NST + 1>();
  }
  DI void run() {
    if constexpr (!DED) wait_vm<P*(NST - 2)>();
    bar();
    STAMP_AT(KID, 1);
    {
      lds_cptr ab = smem + a_sub0;
      lds_cptr wb = smem + w_sub0;
#pragma unroll
      for (int m = 0; m < MT; ++m) af[0][m] = lds_read128(ab + m * 512);
#pragma unroll
      for (int n = 0; n < NT; ++n) wf[n] = lds_tr8(wb + waddr[n], wb + waddr[n] + 512);
    }
    cur = 0; nxt = C::STAGE; fre = (NST - 1) * C::STAGE;
    constexpr int STEADY = NKT - NST + 1;
    constexpr int LOOPN = (STEADY / 2) * 2;
    for (int kt = 0; kt < LOOPN; kt += 2) {
      iter<0, true, false, P*(NST - 3)>();
      iter<SPW & 1, true, false, P*(NST - 3)>();
    }
    tail<LOOPN>();
    STAMP_AT(KID, 2);
  }
};

template <class C, int BM, int NGRP, int KS>
DI void setup_loader_A(Loader<C>& ld, const u16* Ablk, int m0, int w, int lane) {
#pragma unroll
  for (int i = 0; i < C::APW; ++i) {
    int a = w + C::NLW * i;
    int blk = a / (BM / 32), j = a % (BM / 32);
    ld.aoff[i] = blk * (BM * 32) + j * 1024;
    ld.av[i] = ((blk * SEQ + m0 + j * 32) * 16) * 2 + lane * 16;
  }
  ld.abase = (const char*)Ablk;
}

DI int wfrag_addr(int t, int lane) {
  int g = lane >> 4, q = (lane & 15) >> 2, p = lane & 3;
  int f = ((q >> 1) & 1) | ((g & 1) << 1);
  return (t >> 2) * 4096 + (8 * g + q) * 128 + (((t & 3) ^ f) * 32) + p * 8;
}
template <int BM>
DI int afrag_addr(int mrow0, int lane) {
  int r = lane & 15, g = lane >> 4;
  return (g >> 1) * (BM * 32) + (mrow0 + r) * 32 + (g & 1) * 16;
}

constexpr float QSCALE = 0.17677669529663687f * 1.4426950408889634f;
typedef GCfg<128, 3, 2, 4, 4> CQ;
__global__ __launch_bounds__(768, 3) void k_qkv(const u16* __restrict__ Ablk, const u16* __restrict__ W,
                                                 u16* __restrict__ out) {
  extern __shared__ __attribute__((aligned(16))) char smem_[];
  lds_ptr smem = (lds_ptr)smem_;
  const int lane = threadIdx.x & 63;
  const int w = __builtin_amdgcn_readfirstlane(threadIdx.x >> 6);
  const int xcd = blockIdx.x & 7, idx = blockIdx.x >> 3;
  const int tm = (xcd & 1) * 8 + (idx & 7), tn = (xcd >> 1) * 4 + (idx >> 3);
  const int m0 = tm * 128, n0 = tn * 192;
  constexpr int LDW = 3 * DM;
  Loader<CQ> ld;
  const int lw = w & 3;
  setup_loader_A<CQ, 128, 3, 2>(ld, Ablk, m0, lw, lane);
#pragma unroll
  for (int i = 0; i < CQ::WPW; ++i) {
    int b = lw + 4 * i;
    int sub = b / 12, r = b % 12, grp = r >> 2, o = r & 3;
    ld.woff[i] = 2 * CQ::A_SUB + sub * CQ::W_SUB + grp * 4096 + o * 1024;
    int k = sub * 32 + 8 * o + (lane >> 3);
    int f = ((lane >> 4) & 1) | ((o & 1) << 1);
    int col = n0 + grp * 64 + ((((lane & 7) >> 1) ^ f) * 16) + (lane & 1) * 8;
    ld.wv[i] = (k * LDW + col) * 2;
    ld.wbase[i] = (const char*)W;
  }
  const int wm = w & 1, wn = (w >> 1) & 3;
  int waddr[3];
#pragma unroll
  for (int n = 0; n < 3; ++n) waddr[n] = wfrag_addr(wn * 3 + n, lane);
  const int aaddr = afrag_addr<128>(wm * 64, lane);
  f32x4 acc[3][4];
#pragma unroll
  for (int n = 0; n < 3; ++n)
#pragma unroll
    for (int m = 0; m < 4; ++m) acc[n][m] = f32x4{0.f, 0.f, 0.f, 0.f};
  Engine<128, 3, 2, 4, 4, 3, 1, DM / 64, 1, 0, 4> eng(acc, smem, ld, (long)2 * SEQ * 32, (long)64 * LDW, aaddr, waddr, 0);
  if (w >= 8) { eng.loader_run(); return; }
  eng.start();
  eng.run();
  const int g = lane >> 4;
#pragma unroll
  for (int n = 0; n < 3; ++n) {
    const int cb = (n0 >> 4) + wn * 3 + n;
    const float sc = cb < DM / 16 ? QSCALE : 1.f;
#pragma unroll
    for (int m = 0; m < 4; m += 2) {
      u32x2 a, b;
      a[0] = pack2(acc[n][m][0] * sc, acc[n][m][1] * sc);
      a[1] = pack2(acc[n][m][2] * sc, acc[n][m][3] * sc);
      b[0] = pack2(acc[n][m + 1][0] * sc, acc[n][m + 1][1] * sc);
      b[1] = pack2(acc[n][m + 1][2] * sc, acc[n][m + 1][3] * sc);
      const int row = m0 + wm * 64 + (m + (g & 1)) * 16 + (lane & 15);
      *(u32x4*)(out + ((size_t)cb * SEQ + row) * 16 + (g >> 1) * 8) = pair16(a, b);
    }
  }
  STAMP_AT(1, 3);
}

typedef GCfg<128, 1, 2, 6, 4> CP;
template <int KDIM>
__global__ __launch_bounds__(768, 3) void k_proj(const u16* __restrict__ Ablk, const u16* __restrict__ W,
                                                  const u16* __restrict__ res, const float* __restrict__ gate,
                                                  u16* __restrict__ out) {
  extern __shared__ __attribute__((aligned(16))) char smem_[];
  lds_ptr smem = (lds_ptr)smem_;
  const int lane = threadIdx.x & 63;
  const int w = __builtin_amdgcn_readfirstlane(threadIdx.x >> 6);
  const int xcd = blockIdx.x & 7, idx = blockIdx.x >> 3;
  const int tm = (xcd & 3) * 4 + (idx & 3), tn = (xcd >> 2) * 8 + (idx >> 2);
  const int m0 = tm * 128, n0 = tn * 64;
  constexpr int LDW = DM;
  Loader<CP> ld;
  const int lw = w & 3;
  setup_loader_A<CP, 128, 1, 2>(ld, Ablk, m0, lw, lane);
#pragma unroll
  for (int i = 0; i < CP::WPW; ++i) {
    int b = lw + 4 * i;
    int sub = b >> 2, o = b & 3;
    ld.woff[i] = 2 * CP::A_SUB + sub * CP::W_SUB + o * 1024;
    int k = sub * 32 + 8 * o + (lane >> 3);
    int f = ((lane >> 4) & 1) | ((o & 1) << 1);
    int col = n0 + ((((lane & 7) >> 1) ^ f) * 16) + (lane & 1) * 8;
    ld.wv[i] = (k * LDW + col) * 2;
    ld.wbase[i] = (const char*)W;
  }
  const int wm = w & 1, wn = (w >> 1) & 1, kg = (w >> 2) & 1;
  int waddr[2];
#pragma unroll
  for (int n = 0; n < 2; ++n) waddr[n] = wfrag_addr(wn * 2 + n, lane);
  const int aaddr = afrag_addr<128>(wm * 64, lane);
  f32x4 acc[2][4];
#pragma unroll
  for (int n = 0; n < 2; ++n)
#pragma unroll
    for (int m = 0; m < 4; ++m) acc[n][m] = f32x4{0.f, 0.f, 0.f, 0.f};
  Engine<128, 1, 2, 6, 4, 2, 2, KDIM / 64, (KDIM == DM ? 3 : 5), 0, 4> eng(acc, smem, ld, (long)2 * SEQ * 32, (long)64 * LDW, aaddr, waddr, kg);
  if (w >= 8) { eng.loader_run(); return; }
  eng.start();
  f32x4 gts[2];
#pragma unroll
  for (int n = 0; n < 2; ++n) gts[n] = gate4(gate, n0 + (wn * 2 + n) * 16 + 4 * (lane >> 4));
  eng.run();
  __syncthreads();
  __attribute__((address_space(3))) f32x4* red = (__attribute__((address_space(3))) f32x4*)smem;
  if (kg == 1) {
#pragma unroll
    for (int n = 0; n < 2; ++n)
#pragma unroll
      for (int m = 0; m < 4; ++m) red[((w & 3) * 8 + n * 4 + m) * 64 + lane] = acc[n][m];
  }
  __syncthreads();
  if (kg == 0) {
    const int g = lane >> 4;
#pragma unroll
    for (int n = 0; n < 2; ++n) {
      int col = n0 + (wn * 2 + n) * 16 + 4 * g;
      const f32x4 gt = gts[n];
#pragma unroll
      for (int m = 0; m < 4; ++m) {
        f32x4 a = acc[n][m] + red[((w & 3) * 8 + n * 4 + m) * 64 + lane];
        int row = m0 + wm * 64 + m * 16 + (lane & 15);
        u32x2 r = *(const u32x2*)(res + (size_t)row * DM + col);
        float y0 = bflo(r[0]) + rbf(gt[0] * rbf(a[0]));
        float y1 = bfhi(r[0]) + rbf(gt[1] * rbf(a[1]));
        float y2 = bflo(r[1]) + rbf(gt[2] * rbf(a[2]));
        float y3 = bfhi(r[1]) + rbf(gt[3] * rbf(a[3]));
        u32x2 o;
        o[0] = pack2(y0, y1);
        o[1] = pack2(y2, y3);
        *(u32x2*)(out + (size_t)row * DM + col) = o;
      }
    }
  }
  STAMP_AT((KDIM == DM ? 3 : 5), 3);
}

constexpr int DK0 = 32, DK1 = FF / 64 - DK0;
typedef GCfg<128, 2, 2, 4, 4> CD;
template <int NKT>
DI void down_main(f32x4 (&acc)[4][4], lds_ptr smem, Loader<CD>& ld, int aaddr, const int (&waddr)[4], int kg, int w) {
  Engine<128, 2, 2, 4, 4, 4, 2, NKT, 5, 0, 4> eng(acc, smem, ld, (long)2 * SEQ * 32, (long)64 * DM, aaddr, waddr, kg);
  if (w >= 8) { eng.loader_run(); return; }
  eng.start();
  eng.run();
}
__global__ __launch_bounds__(768, 3) void k_down(const u16* __restrict__ Ablk, const u16* __restrict__ W,
                                                  const u16* __restrict__ res, const float* __restrict__ gate,
                                                  u16* __restrict__ out, float* __restrict__ xbuf, unsigned* flags) {
  extern __shared__ __attribute__((aligned(16))) char smem_[];
  lds_ptr smem = (lds_ptr)smem_;
  const int lane = threadIdx.x & 63;
  const int w = __builtin_amdgcn_readfirstlane(threadIdx.x >> 6);
  const int xcd = blockIdx.x & 7, idx = blockIdx.x >> 3;
  const int h = xcd >> 2;
  const int tm = (xcd & 1) * 8 + (idx & 7), tn = ((xcd >> 1) & 1) * 4 + (idx >> 3);
  const int tile = tm * 8 + tn;
  const int m0 = tm * 128, n0 = tn * 128;
  constexpr int LDW = DM;
  const int kbeg = h * DK0 * 64;
  Loader<CD> ld;
  const int lw = w & 3;
  setup_loader_A<CD, 128, 2, 2>(ld, Ablk + (size_t)(kbeg / 16) * SEQ * 16, m0, lw, lane);
#pragma unroll
  for (int i = 0; i < CD::WPW; ++i) {
    int b = lw + 4 * i;
    int sub = b >> 3, grp = (b >> 2) & 1, o = b & 3;
    ld.woff[i] = 2 * CD::A_SUB + sub * CD::W_SUB + grp * 4096 + o * 1024;
    int k = sub * 32 + 8 * o + (lane >> 3);
    int f = ((lane >> 4) & 1) | ((o & 1) << 1);
    int col = n0 + grp * 64 + ((((lane & 7) >> 1) ^ f) * 16) + (lane & 1) * 8;
    ld.wv[i] = (k * LDW + col) * 2;
    ld.wbase[i] = (const char*)(W + (size_t)kbeg * LDW);
  }
  const int wm = w & 1, wn = (w >> 1) & 1, kg = (w >> 2) & 1;
  int waddr[4];
#pragma unroll
  for (int n = 0; n < 4; ++n) waddr[n] = wfrag_addr(wn * 4 + n, lane);
  const int aaddr = afrag_addr<128>(wm * 64, lane);
  f32x4 acc[4][4];
#pragma unroll
  for (int n = 0; n < 4; ++n)
#pragma unroll
    for (int m = 0; m < 4; ++m) acc[n][m] = f32x4{0.f, 0.f, 0.f, 0.f};
  const int g = lane >> 4;
  if (h == 0) down_main<DK0>(acc, smem, ld, aaddr, waddr, kg, w);
  else down_main<DK1>(acc, smem, ld, aaddr, waddr, kg, w);
  if (w >= 8) return;
  if (h == 0 && threadIdx.x == 0) {
    const unsigned* pf = flags + tile * 16;
    for (int spin = 0; spin < (1 << 22); ++spin) {
      if (__hip_atomic_load(pf, __ATOMIC_RELAXED, __HIP_MEMORY_SCOPE_AGENT) != 0u) break;
      __builtin_amdgcn_s_sleep(1);
    }
  }
  u32x2 rr[2][4];
  f32x4 gts[2];
  if (h == 0) {
#pragma unroll
    for (int n = 0; n < 2; ++n) gts[n] = gate4(gate, n0 + (wn * 4 + kg * 2 + n) * 16 + 4 * g);
#pragma unroll
    for (int n = 0; n < 2; ++n)
#pragma unroll
      for (int m = 0; m < 4; ++m)
        rr[n][m] = *(const u32x2*)(res + (size_t)(m0 + wm * 64 + m * 16 + (lane & 15)) * DM + n0 + (wn * 4 + kg * 2 + n) * 16 + 4 * g);
  }
  __syncthreads();
  auto rs = __builtin_amdgcn_make_buffer_rsrc((void*)((char*)xbuf + (size_t)tile * 65536), 0, 65536, 0x00020000);
  const int slot = w * 8192 + lane * 16;
  f32x4 ot[2][4];
  if (h == 0) {
#pragma unroll
    for (int n = 0; n < 2; ++n)
#pragma unroll
      for (int m = 0; m < 4; ++m)
        ot[n][m] = __builtin_bit_cast(f32x4, __builtin_amdgcn_raw_buffer_load_b128(rs, slot + (n * 4 + m) * 1024, 0, 16));
  }
  __attribute__((address_space(3))) f32x4* red = (__attribute__((address_space(3))) f32x4*)smem;
  f32x4 keep[2][4];
#pragma unroll
  for (int n = 0; n < 2; ++n)
#pragma unroll
    for (int m = 0; m < 4; ++m) {
      red[(w * 8 + n * 4 + m) * 64 + lane] = kg == 0 ? acc[2 + n][m] : acc[n][m];
      keep[n][m] = kg == 0 ? acc[n][m] : acc[2 + n][m];
    }
  asm volatile("s_waitcnt lgkmcnt(0)" ::: "memory");
  __builtin_amdgcn_s_barrier();
  asm volatile("" ::: "memory");
#pragma unroll
  for (int n = 0; n < 2; ++n)
#pragma unroll
    for (int m = 0; m < 4; ++m) keep[n][m] += red[((w ^ 4) * 8 + n * 4 + m) * 64 + lane];
  if (h == 1) {
#pragma unroll
    for (int n = 0; n < 2; ++n)
#pragma unroll
      for (int m = 0; m < 4; ++m)
        __builtin_amdgcn_raw_buffer_store_b128(__builtin_bit_cast(u32x4, keep[n][m]), rs, slot + (n * 4 + m) * 1024, 0, 16);
    asm volatile("s_waitcnt vmcnt(0)" ::: "memory");
    __builtin_amdgcn_s_barrier();
    if (threadIdx.x == 0) __hip_atomic_store(flags + tile * 16, 1u, __ATOMIC_RELAXED, __HIP_MEMORY_SCOPE_AGENT);
  } else {
#pragma unroll
    for (int n = 0; n < 2; ++n) {
      const f32x4 gt = gts[n];
#pragma unroll
      for (int m = 0; m < 4; m += 2) {
        u32x2 o[2];
#pragma unroll
        for (int i = 0; i < 2; ++i) {
          f32x4 a = keep[n][m + i] + ot[n][m + i];
          u32x2 r = rr[n][m + i];
          o[i][0] = pack2(bflo(r[0]) + rbf(gt[0] * rbf(a[0])), bfhi(r[0]) + rbf(gt[1] * rbf(a[1])));
          o[i][1] = pack2(bflo(r[1]) + rbf(gt[2] * rbf(a[2])), bfhi(r[1]) + rbf(gt[3] * rbf(a[3])));
        }
        const int row = m0 + wm * 64 + (m + (g & 1)) * 16 + (lane & 15);
        *(u32x4*)(out + (size_t)row * DM + n0 + (wn * 4 + kg * 2 + n) * 16 + (g >> 1) * 8) = pair16(o[0], o[1]);
      }
    }
  }
  STAMP_AT(5, 3);
}

typedef GCfg<256, 4, 1, 4, 4> CG;
template <int NH_>
DI void glu_wave(lds_ptr smem, Loader<CG>& ld, int lane, int wm, int t0, int m0, int f0, u16* __restrict__ out) {
  constexpr int NT = 2 * NH_;
  int waddr[NT];
#pragma unroll
  for (int n = 0; n < NH_; ++n) {
    waddr[n] = wfrag_addr(t0 + n, lane);
    waddr[NH_ + n] = waddr[n] + 8192;
  }
  const int aaddr = afrag_addr<256>(wm * 64, lane);
  f32x4 acc[NT][4];
#pragma unroll
  for (int n = 0; n < NT; ++n)
#pragma unroll
    for (int m = 0; m < 4; ++m) acc[n][m] = f32x4{0.f, 0.f, 0.f, 0.f};
  Engine<256, 4, 1, 4, 4, NT, 1, DM / 32, 4, 0, 4, (NH_ == 3)> eng(acc, smem, ld, (long)SEQ * 32, (long)32 * FF, aaddr, waddr, 0);
  eng.start();
  eng.run();
  const int g = lane >> 4;
#pragma unroll
  for (int n = 0; n < NH_; ++n) {
    const int cb = (f0 >> 4) + t0 + n;
#pragma unroll
    for (int m = 0; m < 4; m += 2) {
      u32x2 o[2];
#pragma unroll
      for (int i = 0; i < 2; ++i) {
        f32x4 a = acc[n][m + i], b = acc[NH_ + n][m + i];
        o[i][0] = pack2(rbf(a[0]) * rbf(b[0]), rbf(a[1]) * rbf(b[1]));
        o[i][1] = pack2(rbf(a[2]) * rbf(b[2]), rbf(a[3]) * rbf(b[3]));
      }
      const int row = m0 + wm * 64 + (m + (g & 1)) * 16 + (lane & 15);
      *(u32x4*)(out + ((size_t)cb * SEQ + row) * 16 + (g >> 1) * 8) = pair16(o[0], o[1]);
    }
  }
  STAMP_AT(4, 3);
}

__global__ __launch_bounds__(512, 2) void k_glu(const u16* __restrict__ Ablk, const u16* __restrict__ Wg,
                                                 const u16* __restrict__ Wu, u16* __restrict__ out) {
  extern __shared__ __attribute__((aligned(16))) char smem_[];
  lds_ptr smem = (lds_ptr)smem_;
  const int lane = threadIdx.x & 63;
  const int w = __builtin_amdgcn_readfirstlane(threadIdx.x >> 6);
  const int xcd = blockIdx.x & 7, idx = blockIdx.x >> 3;
  const int tm = idx & 7, tn = xcd * 4 + (idx >> 3);
  const int m0 = tm * 256, f0 = tn * 112;
  Loader<CG> ld;
  const int lw = w & 3;
  setup_loader_A<CG, 256, 4, 1>(ld, Ablk, m0, lw, lane);
#pragma unroll
  for (int i = 0; i < CG::WPW; ++i) {
    int b = lw + 4 * i;
    int grp = b >> 2, o = b & 3;
    ld.woff[i] = CG::A_SUB + grp * 4096 + o * 1024;
    int k = 8 * o + (lane >> 3);
    int f = ((lane >> 4) & 1) | ((o & 1) << 1);
    int pr = ((lane & 7) >> 1) ^ f;
    if ((grp & 1) && pr == 3) pr = 0;
    int col = f0 + (grp & 1) * 64 + pr * 16 + (lane & 1) * 8;
    ld.wv[i] = (k * FF + col) * 2;
    ld.wbase[i] = (const char*)(grp >> 1 ? Wu : Wg);
  }
  const int wm = w & 3;
  if (w < 4) glu_wave<4>(smem, ld, lane, wm, 0, m0, f0, out);
  else glu_wave<3>(smem, ld, lane, wm, 4, m0, f0, out);
}

constexpr int ACH = 128, ANST = 4, ASTAGE = ACH * 64 * 2 + 256;
constexpr int ALDS = ANST * ASTAGE + 1024 + 16;

DI float rowmax16(const f32x16& v) {
  float mx = fmaxf(fmaxf(v[0], v[1]), v[2]);
#pragma unroll
  for (int r = 3; r < 15; r += 2) mx = fmaxf(fmaxf(mx, v[r]), v[r + 1]);
  mx = fmaxf(mx, v[15]);
  auto sw = __builtin_amdgcn_permlane32_swap(__float_as_uint(mx), __float_as_uint(mx), false, false);
  return fmaxf(__uint_as_float(sw[0]), __uint_as_float(sw[1]));
}

template <bool SAFE>
DI void attn_pass(lds_ptr smem, int lane, int w, const char* Kp, const char* Vp, const char* pf_wm, const char* pf_wo, const char* pf_wg,
                  const char* pf_wu, const char* pf_wd, const bf16x8& qa0, const bf16x8& qa1, const bf16x8& qb0,
                  const bf16x8& qb1, f32x16& oa, f32x16& ob, float& refa, float& refb, float& la_out, float& lb_out) {
  const int r32 = lane & 31, hh = lane >> 5, kh = w >> 2;
  const unsigned kvo = ((32 * (w & 3) + (lane >> 1)) * 16 + (((lane & 1) ^ ((lane >> 4) & 1)) * 8)) * 2;
  const unsigned vvo = ((32 * (w & 3)) * 16) * 2 + lane * 16;
  const char* ksrc = Kp;
  const char* vsrc = Vp;
  auto issue = [&](int buf) {
    if (w < 4) {
      const unsigned l0 = lds_u32(smem) + buf * ASTAGE + w * 1024;
      glds16(ksrc, kvo, l0);
      glds16(ksrc + SEQ * 32, kvo, l0 + 4096);
      glds16(vsrc, vvo, l0 + 8192);
      glds16(vsrc + SEQ * 32, vvo, l0 + 8192 + 4096 + 128);
    }
    ksrc += ACH * 32; vsrc += ACH * 32;
  };
  const unsigned pfo = lane * 16;
  auto prefetch = [&](int ch) {
    if (w >= 4) {
#pragma unroll
      for (int i = 0; i < 3; ++i) {
        int pc = (ch * 3 + i) * 4 + (w - 4);
        const char* base;
        if (pc < 32) base = pf_wm + (size_t)(blockIdx.x * 4 + (pc >> 3)) * (12 * DM) + 4 * DM + (pc & 7) * 1024;
        else if (pc < 40) base = pf_wo + (size_t)blockIdx.x * 8192 + (pc - 32) * 1024;
        else if (pc < 68) base = pf_wg + (size_t)blockIdx.x * 28672 + (pc - 40) * 1024;
        else if (pc < 96) base = pf_wu + (size_t)blockIdx.x * 28672 + (pc - 68) * 1024;
        else if (pc < 124) base = pf_wd + (size_t)blockIdx.x * 28672 + (pc - 96) * 1024;
        else break;
        glds16(base, pfo, lds_u32(smem) + ANST * ASTAGE);
      }
    }
  };
  issue(0); issue(1); issue(2);
  const int kaddr0 = kh * 2048 + r32 * 32 + ((hh ^ ((r32 >> 3) & 1)) * 16);
  const int kaddr1 = kaddr0 + 4096;
  const int vaddr = 8192 + kh * 2048 + ((lane >> 4) & 1) * (4096 + 128) + (4 * hh + ((lane & 15) >> 2)) * 32 + (lane & 3) * 8;
  bf16x8 sel;
  {
    const short one = ((lane & 15) == ((lane >> 4) & 1)) ? (short)0x3F80 : (short)0;
#pragma unroll
    for (int j = 0; j < 8; ++j) sel[j] = one;
  }
  f32x16 nma, nmb, sa, sb;
#pragma unroll
  for (int r = 0; r < 16; ++r) { oa[r] = 0.f; ob[r] = 0.f; nma[r] = 0.f; nmb[r] = 0.f; }
  f32x4 lsa = {0.f, 0.f, 0.f, 0.f}, lsb = {0.f, 0.f, 0.f, 0.f};
  float la = 0.f, lb = 0.f;
  constexpr int NCH = SEQ / ACH;
  if (w < 4) wait_vm<8>();
  bar();
  const bf16x8 kf0_ = lds_read128(smem + kaddr0);
  const bf16x8 kf1_ = lds_read128(smem + kaddr1);
  {
    sa = MFMA32(kf0_, qa0, nma); sa = MFMA32(kf1_, qa1, sa);
    sb = MFMA32(kf0_, qb0, nmb); sb = MFMA32(kf1_, qb1, sb);
    const float ma = rowmax16(sa), mb = rowmax16(sb);
#pragma unroll
    for (int r = 0; r < 16; ++r) { nma[r] = -ma; sa[r] -= ma; nmb[r] = -mb; sb[r] -= mb; }
  }
  if constexpr (SAFE) {
    auto half = [&](f32x16& s, f32x16& o, f32x16& nm, float& l, const bf16x8& q0f, const bf16x8& q1f,
                    const bf16x8& kn0, const bf16x8& kn1, const bf16x8& vf0, const bf16x8& vf1, bool has_next) {
      f32x16 p;
      const float dl = fmaxf(rowmax16(s), 0.f);
      const float f = __builtin_amdgcn_exp2f(-dl);
      float t = 0.f;
#pragma unroll
      for (int r = 0; r < 16; ++r) { nm[r] -= dl; o[r] *= f; p[r] = __builtin_amdgcn_exp2f(s[r] - dl); t += p[r]; }
      l = l * f + t;
      if (has_next) { s = MFMA32(kn0, q0f, nm); s = MFMA32(kn1, q1f, s); }
      u32x4 pa, pb;
#pragma unroll
      for (int j = 0; j < 4; ++j) { pa[j] = pack2(p[2 * j], p[2 * j + 1]); pb[j] = pack2(p[8 + 2 * j], p[8 + 2 * j + 1]); }
      o = MFMA32(vf0, __builtin_bit_cast(bf16x8, pa), o);
      o = MFMA32(vf1, __builtin_bit_cast(bf16x8, pb), o);
    };
    auto step = [&](lds_cptr vb, lds_cptr knext, bool has_next) {
      bf16x8 kn0, kn1;
      if (has_next) { kn0 = lds_read128(knext + kaddr0); kn1 = lds_read128(knext + kaddr1); }
      bf16x8 vf0 = lds_tr8(vb, vb + 256);
      bf16x8 vf1 = lds_tr8(vb + 512, vb + 768);
      half(sa, oa, nma, la, qa0, qa1, kn0, kn1, vf0, vf1, has_next);
      half(sb, ob, nmb, lb, qb0, qb1, kn0, kn1, vf0, vf1, has_next);
    };
    for (int ch = 0; ch < NCH; ++ch) {
      if (w < 4) { if (ch + 2 < NCH) wait_vm<4>(); else wait_vm<0>(); }
      bar();
      if (ch + 3 < NCH) issue((ch + 3) & 3);
      lds_cptr sbuf = smem + (ch & 3) * ASTAGE;
      lds_cptr nbuf = smem + ((ch + 1) & 3) * ASTAGE;
      step(sbuf + vaddr, sbuf + 1024, true);
      step(sbuf + 1024 + vaddr, nbuf, ch + 1 < NCH);
    }
  } else {
    u32x4 pa0, pa1, pb0, pb1;
    bf16x8 kc0 = kf0_, kc1 = kf1_;
    bf16x8 vp0, vp1;
#pragma unroll
    for (int j = 0; j < 4; ++j) { pb0[j] = 0u; pb1[j] = 0u; }
#pragma unroll
    for (int j = 0; j < 8; ++j) { vp0[j] = 0; vp1[j] = 0; }
#define SB_ __builtin_amdgcn_sched_barrier(0)
#define EX(i) e[i] = __builtin_amdgcn_exp2f(S[i])
#define PHASE(S, P0, P1, M0, M1, M2, M3, M4, M5, R0, R1) do { float e[16]; \
      M0; SB_; R0; EX(0); EX(1); EX(2); EX(3); SB_; \
      M1; SB_; R1; EX(4); EX(5); EX(6); EX(7); SB_; \
      M2; SB_; EX(8); EX(9); P0[0] = pack2(e[0], e[1]); P0[1] = pack2(e[2], e[3]); SB_; \
      M3; SB_; EX(10); EX(11); P0[2] = pack2(e[4], e[5]); P0[3] = pack2(e[6], e[7]); SB_; \
      M4; SB_; EX(12); EX(13); EX(14); EX(15); SB_; \
      M5; SB_; P1[0] = pack2(e[8], e[9]); P1[1] = pack2(e[10], e[11]); P1[2] = pack2(e[12], e[13]); P1[3] = pack2(e[14], e[15]); SB_; \
    } while (0)
#define BF(x) __builtin_bit_cast(bf16x8, x)
    auto step = [&](lds_cptr vb, lds_cptr knext, auto has_next_t) {
      constexpr bool HN = decltype(has_next_t)::value;
      bf16x8 kn0, kn1, vc0, vc1;
      { const f32x16& S = sa;
        PHASE(S, pa0, pa1,
              sb = MFMA32(kc0, qb0, nmb), sb = MFMA32(kc1, qb1, sb),
              ob = MFMA32(vp0, BF(pb0), ob), ob = MFMA32(vp1, BF(pb1), ob),
              lsb = MFMA16(sel, BF(pb0), lsb), lsb = MFMA16(sel, BF(pb1), lsb),
              if (HN) { kn0 = lds_read128(knext + kaddr0); kn1 = lds_read128(knext + kaddr1); },
              { vc0 = lds_tr8(vb, vb + 256); vc1 = lds_tr8(vb + 512, vb + 768); }); }
      { const f32x16& S = sb;
        PHASE(S, pb0, pb1,
              if (HN) sa = MFMA32(kn0, qa0, nma), if (HN) sa = MFMA32(kn1, qa1, sa),
              oa = MFMA32(vc0, BF(pa0), oa), oa = MFMA32(vc1, BF(pa1), oa),
              lsa = MFMA16(sel, BF(pa0), lsa), lsa = MFMA16(sel, BF(pa1), lsa),
              , ); }
      if (HN) { kc0 = kn0; kc1 = kn1; }
      vp0 = vc0; vp1 = vc1;
    };
    typedef std::integral_constant<bool, true> T_;
    typedef std::integral_constant<bool, false> F_;
    for (int ch = 0; ch < NCH - 1; ++ch) {
      if (w < 4) { if (ch + 2 < NCH) wait_vm<4>(); else wait_vm<0>(); }
      bar();
      if (ch + 3 < NCH) issue((ch + 3) & 3);
      prefetch(ch);
      lds_cptr sbuf = smem + (ch & 3) * ASTAGE;
      lds_cptr nbuf = smem + ((ch + 1) & 3) * ASTAGE;
      step(sbuf + vaddr, sbuf + 1024, T_{});
      step(sbuf + 1024 + vaddr, nbuf, T_{});
    }
    {
      lds_cptr sbuf = smem + ((NCH - 1) & 3) * ASTAGE;
      step(sbuf + vaddr, sbuf + 1024, T_{});
      step(sbuf + 1024 + vaddr, sbuf, F_{});
    }
    ob = MFMA32(vp0, BF(pb0), ob); ob = MFMA32(vp1, BF(pb1), ob);
    lsb = MFMA16(sel, BF(pb0), lsb); lsb = MFMA16(sel, BF(pb1), lsb);
#undef PHASE
#undef EX
#undef BF
  }
  refa = nma[0]; refb = nmb[0];
  if constexpr (SAFE) {
    auto s1 = __builtin_amdgcn_permlane32_swap(__float_as_uint(la), __float_as_uint(la), false, false);
    la_out = __uint_as_float(s1[0]) + __uint_as_float(s1[1]);
    auto s2 = __builtin_amdgcn_permlane32_swap(__float_as_uint(lb), __float_as_uint(lb), false, false);
    lb_out = __uint_as_float(s2[0]) + __uint_as_float(s2[1]);
  } else {
    const float a0 = __shfl(lsa[0], lane & 15), a1 = __shfl(lsa[1], lane & 15);
    const float b0 = __shfl(lsb[0], lane & 15), b1 = __shfl(lsb[1], lane & 15);
    la_out = (lane & 16) ? a1 : a0;
    lb_out = (lane & 16) ? b1 : b0;
  }
}

__global__ __launch_bounds__(512, 2) void k_attn(const u16* __restrict__ qkv, u16* __restrict__ oblk,
                                                  const u16* __restrict__ c, const u16* __restrict__ wmod, float* __restrict__ part,
                                                  const char* __restrict__ pf_wo, const char* __restrict__ pf_wg,
                                                  const char* __restrict__ pf_wu, const char* __restrict__ pf_wd) {
  extern __shared__ __attribute__((aligned(16))) char smem_[];
  lds_ptr smem = (lds_ptr)smem_;
  const int lane = threadIdx.x & 63;
  const int w = __builtin_amdgcn_readfirstlane(threadIdx.x >> 6);
  const int xcd = blockIdx.x & 7, idx = blockIdx.x >> 3;
  const int head = xcd * 4 + (idx >> 3), qb = idx & 7;
  const int r32 = lane & 31, hh = lane >> 5;
  const int qg = w & 3, kh = w >> 2;
  const u16* Qp = qkv + (size_t)head * SEQ * 32;
  const char* Kp = (const char*)(qkv + (size_t)(32 + head) * SEQ * 32);
  const char* Vp = (const char*)(qkv + (size_t)(64 + head) * SEQ * 32);
  const int q0 = qb * 256 + qg * 64;
  STAMP_AT(2, 0);
  __attribute__((address_space(3))) int* flag = (__attribute__((address_space(3))) int*)(smem + ANST * ASTAGE + 1024);
  if (threadIdx.x == 0) *flag = 0;
  bf16x8 qa0 = *(const bf16x8*)(Qp + (q0 + r32) * 16 + hh * 8);
  bf16x8 qa1 = *(const bf16x8*)(Qp + (SEQ + q0 + r32) * 16 + hh * 8);
  bf16x8 qb0 = *(const bf16x8*)(Qp + (q0 + 32 + r32) * 16 + hh * 8);
  bf16x8 qb1 = *(const bf16x8*)(Qp + (SEQ + q0 + 32 + r32) * 16 + hh * 8);
  f32x16 oa, ob;
  float nma, nmb, la, lb;
  attn_pass<false>(smem, lane, w, Kp, Vp, (const char*)wmod, pf_wo, pf_wg, pf_wu, pf_wd, qa0, qa1, qb0, qb1, oa, ob, nma, nmb, la, lb);
  {
    float big = fmaxf(fabsf(la), fabsf(lb));
#pragma unroll
    for (int r = 0; r < 16; ++r) big = fmaxf(big, fmaxf(fabsf(oa[r]), fabsf(ob[r])));
    bool bad = !(big < 1e30f);
#pragma unroll
    for (int r = 0; r < 16; ++r) bad = bad || (oa[r] != oa[r]) || (ob[r] != ob[r]);
    bad = bad || (la != la) || (lb != lb);
    if (__any(bad) && lane == 0) *flag = 1;
  }
  __syncthreads();
  if (__builtin_amdgcn_readfirstlane(*flag) != 0) {
    attn_pass<true>(smem, lane, w, Kp, Vp, (const char*)wmod, pf_wo, pf_wg, pf_wu, pf_wd, qa0, qa1, qb0, qb1, oa, ob, nma, nmb, la, lb);
    __syncthreads();
  }
  STAMP_AT(2, 2);
  const int task = blockIdx.x * 2 + (threadIdx.x >> 8);
  ModTask mt;
  mod_load(mt, c, wmod, 32 + (task >> 3), task & 7, threadIdx.x & 255);
  {
    __attribute__((address_space(3))) float* xb = (__attribute__((address_space(3))) float*)smem;
    const f32x16& og = kh == 0 ? ob : oa;
    __attribute__((address_space(3))) float* dst = xb + w * (18 * 64) + lane;
#pragma unroll
    for (int r = 0; r < 16; ++r) dst[r * 64] = og[r];
    dst[16 * 64] = kh == 0 ? lb : la;
    dst[17 * 64] = kh == 0 ? nmb : nma;
    __syncthreads();
    const __attribute__((address_space(3))) float* src = xb + (w ^ 4) * (18 * 64) + lane;
    f32x16 om = kh == 0 ? oa : ob;
    const float nm_me = kh == 0 ? nma : nmb;
    const float nm_ot = src[17 * 64];
    const float nmin = fminf(nm_me, nm_ot);
    const float f_me = __builtin_amdgcn_exp2f(nmin - nm_me), f_ot = __builtin_amdgcn_exp2f(nmin - nm_ot);
    const float rl = 1.f / ((kh == 0 ? la : lb) * f_me + src[16 * 64] * f_ot);
    const float g_me = f_me * rl, g_ot = f_ot * rl;
    u16* op = oblk + ((size_t)head * 2 * SEQ + q0 + kh * 32 + r32) * 16 + 4 * hh;
#pragma unroll
    for (int g = 0; g < 4; ++g) {
      float v0 = om[4 * g] * g_me + src[(4 * g) * 64] * g_ot;
      float v1 = om[4 * g + 1] * g_me + src[(4 * g + 1) * 64] * g_ot;
      float v2 = om[4 * g + 2] * g_me + src[(4 * g + 2) * 64] * g_ot;
      float v3 = om[4 * g + 3] * g_me + src[(4 * g + 3) * 64] * g_ot;
      u32x2 v;
      v[0] = pack2(v0, v1);
      v[1] = pack2(v2, v3);
      *(u32x2*)(op + (g >> 1) * (SEQ * 16) + (g & 1) * 8) = v;
    }
  }
  mod_finish(mt, part, 32 + (task >> 3), task & 7, threadIdx.x & 255, (float*)(smem_ + 40960) + (threadIdx.x >> 8) * 256);
  wait_vm<0>();
  STAMP_AT(2, 3);
}

extern "C" void kernel_launch(void* const* d_in, const int* in_sizes, int n_in, void* d_out, int out_size,
                              void* d_ws, size_t ws_size, hipStream_t stream) {
  const u16* x = (const u16*)d_in[0];
  const u16* wqkv = (const u16*)d_in[1];
  const u16* wo = (const u16*)d_in[2];
  const u16* wg = (const u16*)d_in[4];
  const u16* wu = (const u16*)d_in[5];
  const u16* wd = (const u16*)d_in[6];
  const u16* c = (const u16*)d_in[7];
  const u16* wmod = (const u16*)d_in[8];
  u16* out = (u16*)d_out;
  char* ws = (char*)d_ws;
  float* part = (float*)ws;
  float* modf = (float*)(ws + (256 << 10));
  unsigned* flags = (unsigned*)(ws + (512 << 10));
  u16* h1 = (u16*)(ws + (1 << 20));
  u16* qkv = (u16*)(ws + (8 << 20));
  u16* ob = (u16*)(ws + (24 << 20));
  u16* x2 = (u16*)(ws + (32 << 20));
  u16* h2 = (u16*)(ws + (40 << 20));
  u16* hh = (u16*)(ws + (48 << 20));
  float* xbuf = (float*)(ws + (64 << 20));

  hipFuncSetAttribute((const void*)k_qkv, hipFuncAttributeMaxDynamicSharedMemorySize, CQ::LDS);
  hipFuncSetAttribute((const void*)k_proj<DM>, hipFuncAttributeMaxDynamicSharedMemorySize, CP::LDS);
  hipFuncSetAttribute((const void*)k_down, hipFuncAttributeMaxDynamicSharedMemorySize, CD::LDS);
  hipFuncSetAttribute((const void*)k_glu, hipFuncAttributeMaxDynamicSharedMemorySize, CG::LDS);
  hipFuncSetAttribute((const void*)k_attn, hipFuncAttributeMaxDynamicSharedMemorySize, ALDS);

  k_pre<<<256 + 256, 256, 0, stream>>>(c, wmod, part, (const char*)x, (const char*)wqkv, flags);
  k_ln<6, MNS, true><<<256 + 192, 512, 0, stream>>>(x, part, h1, (const char*)wqkv);
  k_qkv<<<256, 768, CQ::LDS, stream>>>(h1, wqkv, qkv);
  k_attn<<<256, 512, ALDS, stream>>>(qkv, ob, c, wmod, part, (const char*)wo, (const char*)wg, (const char*)wu, (const char*)wd);
  k_proj<DM><<<256, 768, CP::LDS, stream>>>(ob, wo, x, part + 2 * DM, x2);
  k_ln<7, MNS, false><<<256, 512, 0, stream>>>(x2, part + 3 * DM, h2, (const char*)wqkv);
  k_glu<<<256, 512, CG::LDS, stream>>>(h2, wg, wu, hh);
  k_down<<<256, 768, CD::LDS, stream>>>(hh, wd, x2, part + 5 * DM, out, xbuf, flags);
}
```

```cpp
#include <hip/hip_runtime.h>
#include <hip/hip_bf16.h>
#include <stdint.h>

typedef __attribute__((ext_vector_type(8))) short bf16x8;
typedef __attribute__((ext_vector_type(4))) short s16x4;
typedef __attribute__((ext_vector_type(4))) float f32x4;
typedef __attribute__((ext_vector_type(2))) float f32x2;
typedef __attribute__((ext_vector_type(16))) float f32x16;
typedef __attribute__((ext_vector_type(4))) unsigned u32x4;
typedef __attribute__((ext_vector_type(2))) unsigned u32x2;
typedef __attribute__((ext_vector_type(2))) __bf16 bf16x2_t;
typedef unsigned short u16;
typedef __attribute__((address_space(3))) char* lds_ptr;
typedef __attribute__((address_space(3))) const char* lds_cptr;

#define DI __device__ __forceinline__

constexpr int SEQ = 2048, DM = 1024, FF = 3584, NH = 32;

DI float bf2f(u16 x) { return __uint_as_float(((unsigned)x) << 16); }
DI float bflo(unsigned x) { return __uint_as_float(x << 16); }
DI float bfhi(unsigned x) { return __uint_as_float(x & 0xffff0000u); }
DI unsigned pack2(float a, float b) {
  f32x2 v = {a, b};
  bf16x2_t r = __builtin_convertvector(v, bf16x2_t);
  return __builtin_bit_cast(unsigned, r);
}
DI float rbf(float a) { return bflo(pack2(a, 0.f) & 0xffffu); }

#define MFMA16(a, b, c) __builtin_amdgcn_mfma_f32_16x16x32_bf16((a), (b), (c), 0, 0, 0)
#define MFMA32(a, b, c) __builtin_amdgcn_mfma_f32_32x32x16_bf16((a), (b), (c), 0, 0, 0)

DI void glds16(const void* base, unsigned voff, unsigned lds_addr) {
  unsigned sv;
  asm volatile("s_mov_b32 %0, m0\n\ts_mov_b32 m0, %3\n\ts_nop 0\n\tglobal_load_lds_dwordx4 %1, %2\n\ts_mov_b32 m0, %0"
               : "=&s"(sv) : "v"(voff), "s"(base), "s"(lds_addr) : "memory");
}
DI unsigned lds_u32(lds_ptr p) { return (unsigned)(uintptr_t)p; }
DI bf16x8 lds_read128(lds_cptr p) { return *(const __attribute__((address_space(3))) bf16x8*)p; }
DI s16x4 lds_tr(lds_cptr p) {
  typedef short v4i16_t __attribute__((ext_vector_type(4)));
  return __builtin_bit_cast(s16x4, __builtin_amdgcn_ds_read_tr16_b64_v4i16((__attribute__((address_space(3))) v4i16_t*)p));
}
DI bf16x8 lds_tr8(lds_cptr p0, lds_cptr p1) {
  s16x4 a = lds_tr(p0), b = lds_tr(p1);
  return __builtin_shufflevector(a, b, 0, 1, 2, 3, 4, 5, 6, 7);
}
#define STAMP_AT(kid, slot) do {} while (0)
template <int N> DI void wait_vm() { asm volatile("s_waitcnt vmcnt(%0)" ::"n"(N) : "memory"); }
DI void bar() { __builtin_amdgcn_s_barrier(); }

constexpr int MNS = 8;
struct ModTask { u32x4 w[4]; u16 cv[4]; };
DI void mod_load(ModTask& mt, const u16* __restrict__ c, const u16* __restrict__ wmod, int cb, int ks, int t) {
  const int cc = t & 7, rg = t >> 3;
  const int k0 = ks * 128 + rg;
  const u16* wp = wmod + (size_t)k0 * (6 * DM) + cb * 64 + cc * 8;
#pragma unroll
  for (int i = 0; i < 4; ++i) mt.w[i] = *(const u32x4*)(wp + (size_t)i * 32 * (6 * DM));
#pragma unroll
  for (int i = 0; i < 4; ++i) mt.cv[i] = c[k0 + 32 * i];
}
DI void mod_finish(const ModTask& mt, float* __restrict__ part, int cb, int ks, int t, float* s_red  ) {
  float acc[8];
#pragma unroll
  for (int j = 0; j < 8; ++j) acc[j] = 0.f;
#pragma unroll
  for (int i = 0; i < 4; ++i) {
    const float cv = bf2f(mt.cv[i]);
    const float sv = rbf(cv / (1.f + __expf(-cv)));
#pragma unroll
    for (int j = 0; j < 4; ++j) {
      acc[2 * j] += sv * bflo(mt.w[i][j]);
      acc[2 * j + 1] += sv * bfhi(mt.w[i][j]);
    }
  }
#pragma unroll
  for (int j = 0; j < 8; ++j) {
    float v = acc[j];
    v += __shfl_xor(v, 8);
    v += __shfl_xor(v, 16);
    v += __shfl_xor(v, 32);
    acc[j] = v;
  }
  const int lane = t & 63, wv = t >> 6;
  if (lane < 8) {
#pragma unroll
    for (int j = 0; j < 8; ++j) s_red[wv * 64 + lane * 8 + j] = acc[j];
  }
  __syncthreads();
  if (t < 64) part[ks * (6 * DM) + cb * 64 + t] = (s_red[t] + s_red[64 + t]) + (s_red[128 + t] + s_red[192 + t]);
}
DI f32x4 gate4(const float* __restrict__ gate, int col) {
  f32x4 gt = *(const f32x4*)(gate + col);
#pragma unroll
  for (int i = 1; i < MNS; ++i) gt += *(const f32x4*)(gate + i * (6 * DM) + col);
#pragma unroll
  for (int j = 0; j < 4; ++j) gt[j] = rbf(gt[j]);
  return gt;
}

DI void touch16k(const char* p, int t) {
  u32x4 v[4];
#pragma unroll
  for (int i = 0; i < 4; ++i) v[i] = *(const u32x4*)(p + i * 4096 + t * 16);
#pragma unroll
  for (int i = 0; i < 4; ++i) asm volatile("" ::"v"(v[i]));
}

__global__ __launch_bounds__(256) void k_pre(const u16* __restrict__ c, const u16* __restrict__ wmod, float* __restrict__ part,
                                              const char* __restrict__ x, const char* __restrict__ wqkv,
                                              unsigned* __restrict__ flags) {
  __shared__ float s_red[256];
  const int t = threadIdx.x, b = blockIdx.x;
  if (b < 256) STAMP_AT(0, 0);
  if (b < 256) {
    ModTask mt;
    mod_load(mt, c, wmod, b >> 3, b & 7, t);
    mod_finish(mt, part, b >> 3, b & 7, t, s_red);
    if (b == 0) flags[t * 16] = 0u;
  } else {
    touch16k(x + (size_t)(b - 256) * 16384, t);
  }
  if (b < 256) STAMP_AT(0, 3);
}

template <int KID, int NS, bool FIRST>
__global__ __launch_bounds__(512) void k_ln(const u16* __restrict__ x, const float* __restrict__ ss  ,
                                             u16* __restrict__ h16, const char* __restrict__ pf) {
  __shared__ __attribute__((aligned(16))) float sm[2048];
  const int t = threadIdx.x;
  if (FIRST && blockIdx.x >= 256) {
    touch16k(pf + (size_t)((blockIdx.x - 256) * 2 + (t >> 8)) * 16384, t & 255);
    return;
  }
  const int lane = t & 63, wv = t >> 6;
  const int row = blockIdx.x * 8 + wv;
  STAMP_AT(KID, 0);
  const u16* xr = x + (size_t)row * DM;
  u32x4 v0 = *(const u32x4*)(xr + lane * 8);
  u32x4 v1 = *(const u32x4*)(xr + 512 + lane * 8);
  {
    f32x4 pv[NS];
#pragma unroll
    for (int i = 0; i < NS; ++i) pv[i] = *(const f32x4*)(ss + i * (6 * DM) + t * 4);
    f32x4 a = pv[0];
#pragma unroll
    for (int i = 1; i < NS; ++i) a += pv[i];
#pragma unroll
    for (int j = 0; j < 4; ++j) {
      float v = rbf(a[j]);
      a[j] = t < 256 ? v : rbf(1.f + v);
    }
    *(f32x4*)(sm + t * 4) = a;
  }
  float f[16];
#pragma unroll
  for (int j = 0; j < 4; ++j) {
    f[2 * j] = bflo(v0[j]); f[2 * j + 1] = bfhi(v0[j]);
    f[8 + 2 * j] = bflo(v1[j]); f[8 + 2 * j + 1] = bfhi(v1[j]);
  }
  float s = 0.f;
#pragma unroll
  for (int j = 0; j < 16; ++j) s += f[j];
#pragma unroll
  for (int o = 1; o < 64; o <<= 1) s += __shfl_xor(s, o);
  const float mean = s * (1.f / DM);
  float q = 0.f;
#pragma unroll
  for (int j = 0; j < 16; ++j) { float d = f[j] - mean; q += d * d; }
#pragma unroll
  for (int o = 1; o < 64; o <<= 1) q += __shfl_xor(q, o);
  const float mb = rbf(mean);
  const float vb = rbf(q * (1.f / DM));
  const float rden = 1.f / rbf(sqrtf(rbf(vb + 1e-5f)));
  __syncthreads();
#pragma unroll
  for (int half = 0; half < 2; ++half) {
    const int k0 = half * 512 + lane * 8;
    f32x4 sh0 = *(const f32x4*)(sm + k0), sh1 = *(const f32x4*)(sm + k0 + 4);
    f32x4 sc0 = *(const f32x4*)(sm + 1024 + k0), sc1 = *(const f32x4*)(sm + 1024 + k0 + 4);
    float y[8];
#pragma unroll
    for (int j = 0; j < 8; ++j) {
      float g = j < 4 ? sc0[j] : sc1[j - 4];
      float sh = j < 4 ? sh0[j] : sh1[j - 4];
      float d = rbf(f[half * 8 + j] - mb);
      float n = rbf(d * rden);
      y[j] = rbf(n * g) + sh;
    }
    u32x4 o;
#pragma unroll
    for (int j = 0; j < 4; ++j) o[j] = pack2(y[2 * j], y[2 * j + 1]);
    *(u32x4*)(h16 + ((size_t)(k0 >> 4) * SEQ + row) * 16 + (k0 & 15)) = o;
  }
  STAMP_AT(KID, 3);
}

DI void st16(void* p, u32x4 v) {
  *(u32x4*)p = v;
}
DI u32x4 pair16(u32x2 a, u32x2 b) {
  auto s0 = __builtin_amdgcn_permlane16_swap(a[0], b[0], false, false);
  auto s1 = __builtin_amdgcn_permlane16_swap(a[1], b[1], false, false);
  return u32x4{s0[0], s1[0], s0[1], s1[1]};
}

DI int hA(int r) { return (-r) & 3; }

template <int BM, int NGRP, int KS, int NST, int NLW_ = 8>
struct GCfg {
  static constexpr int NLW = NLW_;
  static constexpr int A_SUB = BM * 64;
  static constexpr int W_SUB = NGRP * 4096;
  static constexpr int STAGE = KS * (A_SUB + W_SUB);
  static constexpr int APW = KS * BM / 16 / NLW;
  static constexpr int WPW = KS * NGRP * 4 / NLW;
  static constexpr int PPW = APW + WPW;
  static constexpr int LDS = STAGE * NST;
};

template <class C>
struct Loader {
  const char* abase;
  const char* wbase[C::WPW];
  unsigned av[C::APW];
  unsigned wv[C::WPW];
  int aoff[C::APW];
  int woff[C::WPW];
};

template <int BM, int NGRP, int KS, int NST, int MT, int NT, int KSPLIT, int NKT, int KID, int IOFS, int NLW = 8, bool LOADS = (NLW == 8)>
struct Engine {
  typedef GCfg<BM, NGRP, KS, NST, NLW> C;
  static constexpr bool DED = !LOADS;
  static constexpr int SPW = KS / KSPLIT;
  static constexpr int P = C::PPW;
  static constexpr int G = NT * SPW;
  f32x4 (&acc)[NT][MT];
  lds_ptr smem;
  Loader<C>& ld;
  long a_adv, w_adv;
  const int (&waddr)[NT];
  int a_sub0, w_sub0;
  int cur, nxt, fre;
  bf16x8 wf[NT], af[2][MT];

  DI Engine(f32x4 (&acc_)[NT][MT], lds_ptr smem_, Loader<C>& ld_, long a_adv_, long w_adv_, int aaddr, const int (&waddr_)[NT], int kg)
      : acc(acc_), smem(smem_), ld(ld_), a_adv(a_adv_), w_adv(w_adv_), waddr(waddr_) {
    a_sub0 = kg * C::A_SUB + aaddr;
    w_sub0 = KS * C::A_SUB + kg * C::W_SUB;
  }
  template <int I> DI void piece(int stage_off) {
    if constexpr (I < C::APW) {
      glds16(ld.abase, ld.av[I], lds_u32(smem) + stage_off + ld.aoff[I]);
      if constexpr (I == C::APW - 1) ld.abase += a_adv * 2;
    } else {
      constexpr int J = I - C::APW;
      glds16(ld.wbase[J], ld.wv[J], lds_u32(smem) + stage_off + ld.woff[J]);
      ld.wbase[J] += w_adv * 2;
    }
  }
  template <int I> DI void issue_all(int stage_off) {
    if constexpr (I < P) { piece<I>(stage_off); issue_all<I + 1>(stage_off); }
  }
  template <int g, int I> DI void issue_at(int stage_off) {
    if constexpr (I < P) {
      constexpr int span = (G - IOFS) > P ? (G - IOFS) : P;
      constexpr int pos = IOFS + (I * span) / P < G ? IOFS + (I * span) / P : G - 1;
      if constexpr (pos == g) piece<I>(stage_off);
      issue_at<g, I + 1>(stage_off);
    }
  }
  template <int PAR, int S, bool PF, bool DOISSUE, int n>
  DI void groups(lds_cptr ab, lds_cptr wb) {
    if constexpr (n < NT) {
#pragma unroll
      for (int m = 0; m < MT; ++m) acc[n][m] = MFMA16(wf[n], af[PAR][m], acc[n][m]);
      if constexpr (PF) {
        wf[n] = lds_tr8(wb + waddr[n], wb + waddr[n] + 512);
#pragma unroll
        for (int m = 0; m < MT; ++m)
          if ((m * NT) / MT == n) af[PAR ^ 1][m] = lds_read128(ab + m * 512);
      }
      if constexpr (DOISSUE && !DED) issue_at<S * NT + n, 0>(fre);
      __builtin_amdgcn_sched_barrier(0);
      groups<PAR, S, PF, DOISSUE, n + 1>(ab, wb);
    }
  }
  template <int PAR, bool DOISSUE, bool LAST, int S> DI void steps() {
    if constexpr (S < SPW) {
      if constexpr (S + 1 < SPW)
        groups<PAR, S, true, DOISSUE, 0>(smem + cur + a_sub0 + (S + 1) * KSPLIT * C::A_SUB, smem + cur + w_sub0 + (S + 1) * KSPLIT * C::W_SUB);
      else if constexpr (!LAST)
        groups<PAR, S, true, DOISSUE, 0>(smem + nxt + a_sub0, smem + nxt + w_sub0);
      else
        groups<PAR, S, false, false, 0>(smem, smem);
      steps<PAR ^ 1, DOISSUE, LAST, S + 1>();
    }
  }
  template <int PAR, bool DOISSUE, bool LAST, int WAITN> DI void iter() {
    if constexpr (!LAST) { if constexpr (!DED) wait_vm<WAITN>(); bar(); }
    steps<PAR, DOISSUE, LAST, 0>();
    fre = cur; cur = nxt; nxt = (nxt + C::STAGE == NST * C::STAGE) ? 0 : nxt + C::STAGE;
  }
  template <int KT> DI void tail() {
    if constexpr (KT < NKT) {
      constexpr bool DOISSUE = KT + NST - 1 < NKT;
      constexpr bool LAST = KT == NKT - 1;
      constexpr int newest = (KT + NST - 2) < (NKT - 1) ? (KT + NST - 2) : (NKT - 1);
      constexpr int younger = newest - (KT + 1) > 0 ? newest - (KT + 1) : 0;
      iter<(KT * SPW) & 1, DOISSUE, LAST, P * younger>();
      tail<KT + 1>();
    }
  }
  DI void start() {
    static_assert(NKT >= NST + 1, "too few k tiles");
    STAMP_AT(KID, 0);
    if constexpr (!DED) {
#pragma unroll
      for (int s = 0; s < NST - 1; ++s) issue_all<0>(s * C::STAGE);
    }
  }
  template <int KT> DI void ltail() {
    if constexpr (KT < NKT - 1) {
      constexpr int newest = (KT + NST - 2) < (NKT - 1) ? (KT + NST - 2) : (NKT - 1);
      constexpr int younger = newest - (KT + 1) > 0 ? newest - (KT + 1) : 0;
      wait_vm<P * younger>();
      bar();
      ltail<KT + 1>();
    }
  }
  DI void loader_run() {
#pragma unroll
    for (int s = 0; s < NST - 1; ++s) issue_all<0>(s * C::STAGE);
    wait_vm<P*(NST - 2)>();
    bar();
    fre = (NST - 1) * C::STAGE;
    for (int kt = 0; kt < NKT - NST + 1; ++kt) {
      wait_vm<P*(NST - 3)>();
      bar();
      issue_all<0>(fre);
      fre = (fre + C::STAGE == NST * C::STAGE) ? 0 : fre + C::STAGE;
    }
    ltail<NKT - NST + 1>();
  }
  DI void run() {
    if constexpr (!DED) wait_vm<P*(NST - 2)>();
    bar();
    STAMP_AT(KID, 1);
    {
      lds_cptr ab = smem + a_sub0;
      lds_cptr wb = smem + w_sub0;
#pragma unroll
      for (int m = 0; m < MT; ++m) af[0][m] = lds_read128(ab + m * 512);
#pragma unroll
      for (int n = 0; n < NT; ++n) wf[n] = lds_tr8(wb + waddr[n], wb + waddr[n] + 512);
    }
    cur = 0; nxt = C::STAGE; fre = (NST - 1) * C::STAGE;
    constexpr int STEADY = NKT - NST + 1;
    constexpr int LOOPN = (STEADY / 2) * 2;
    for (int kt = 0; kt < LOOPN; kt += 2) {
      iter<0, true, false, P*(NST - 3)>();
      iter<SPW & 1, true, false, P*(NST - 3)>();
    }
    tail<LOOPN>();
    STAMP_AT(KID, 2);
  }
};

template <class C, int BM, int NGRP, int KS>
DI void setup_loader_A(Loader<C>& ld, const u16* Ablk, int m0, int w, int lane) {
#pragma unroll
  for (int i = 0; i < C::APW; ++i) {
    int a = w + C::NLW * i;
    int blk = a / (BM / 32), j = a % (BM / 32);
    ld.aoff[i] = blk * (BM * 32) + j * 1024;
    ld.av[i] = ((blk * SEQ + m0 + j * 32) * 16) * 2 + lane * 16;
  }
  ld.abase = (const char*)Ablk;
}

DI int wfrag_addr(int t, int lane) {
  int g = lane >> 4, q = (lane & 15) >> 2, p = lane & 3;
  int f = ((q >> 1) & 1) | ((g & 1) << 1);
  return (t >> 2) * 4096 + (8 * g + q) * 128 + (((t & 3) ^ f) * 32) + p * 8;
}
template <int BM>
DI int afrag_addr(int mrow0, int lane) {
  int r = lane & 15, g = lane >> 4;
  return (g >> 1) * (BM * 32) + (mrow0 + r) * 32 + (g & 1) * 16;
}

constexpr float QSCALE = 0.17677669529663687f * 1.4426950408889634f;
typedef GCfg<128, 3, 2, 4, 4> CQ;
__global__ __launch_bounds__(768, 3) void k_qkv(const u16* __restrict__ Ablk, const u16* __restrict__ W,
                                                 u16* __restrict__ out) {
  extern __shared__ __attribute__((aligned(16))) char smem_[];
  lds_ptr smem = (lds_ptr)smem_;
  const int lane = threadIdx.x & 63;
  const int w = __builtin_amdgcn_readfirstlane(threadIdx.x >> 6);
  const int xcd = blockIdx.x & 7, idx = blockIdx.x >> 3;
  const int tm = (xcd & 1) * 8 + (idx & 7), tn = (xcd >> 1) * 4 + (idx >> 3);
  const int m0 = tm * 128, n0 = tn * 192;
  constexpr int LDW = 3 * DM;
  Loader<CQ> ld;
  const int lw = w & 3;
  setup_loader_A<CQ, 128, 3, 2>(ld, Ablk, m0, lw, lane);
#pragma unroll
  for (int i = 0; i < CQ::WPW; ++i) {
    int b = lw + 4 * i;
    int sub = b / 12, r = b % 12, grp = r >> 2, o = r & 3;
    ld.woff[i] = 2 * CQ::A_SUB + sub * CQ::W_SUB + grp * 4096 + o * 1024;
    int k = sub * 32 + 8 * o + (lane >> 3);
    int f = ((lane >> 4) & 1) | ((o & 1) << 1);
    int col = n0 + grp * 64 + ((((lane & 7) >> 1) ^ f) * 16) + (lane & 1) * 8;
    ld.wv[i] = (k * LDW + col) * 2;
    ld.wbase[i] = (const char*)W;
  }
  const int wm = w & 1, wn = (w >> 1) & 3;
  int waddr[3];
#pragma unroll
  for (int n = 0; n < 3; ++n) waddr[n] = wfrag_addr(wn * 3 + n, lane);
  const int aaddr = afrag_addr<128>(wm * 64, lane);
  f32x4 acc[3][4];
#pragma unroll
  for (int n = 0; n < 3; ++n)
#pragma unroll
    for (int m = 0; m < 4; ++m) acc[n][m] = f32x4{0.f, 0.f, 0.f, 0.f};
  Engine<128, 3, 2, 4, 4, 3, 1, DM / 64, 1, 0, 4> eng(acc, smem, ld, (long)2 * SEQ * 32, (long)64 * LDW, aaddr, waddr, 0);
  if (w >= 8) { eng.loader_run(); return; }
  eng.start();
  eng.run();
  const int g = lane >> 4;
#pragma unroll
  for (int n = 0; n < 3; ++n) {
    const int cb = (n0 >> 4) + wn * 3 + n;
    const float sc = cb < DM / 16 ? QSCALE : 1.f;
#pragma unroll
    for (int m = 0; m < 4; m += 2) {
      u32x2 a, b;
      a[0] = pack2(acc[n][m][0] * sc, acc[n][m][1] * sc);
      a[1] = pack2(acc[n][m][2] * sc, acc[n][m][3] * sc);
      b[0] = pack2(acc[n][m + 1][0] * sc, acc[n][m + 1][1] * sc);
      b[1] = pack2(acc[n][m + 1][2] * sc, acc[n][m + 1][3] * sc);
      const int row = m0 + wm * 64 + (m + (g & 1)) * 16 + (lane & 15);
      st16(out + ((size_t)cb * SEQ + row) * 16 + (g >> 1) * 8, pair16(a, b));
    }
  }
  STAMP_AT(1, 3);
}

constexpr int WDP = DM + 64;
typedef GCfg<128, 1, 2, 6, 4> CP;
template <int KDIM, int LDW>
__global__ __launch_bounds__(768, 3) void k_proj(const u16* __restrict__ Ablk, const u16* __restrict__ W,
                                                  const u16* __restrict__ res, const float* __restrict__ gate,
                                                  u16* __restrict__ out) {
  extern __shared__ __attribute__((aligned(16))) char smem_[];
  lds_ptr smem = (lds_ptr)smem_;
  const int lane = threadIdx.x & 63;
  const int w = __builtin_amdgcn_readfirstlane(threadIdx.x >> 6);
  const int xcd = blockIdx.x & 7, idx = blockIdx.x >> 3;
  const int tm = (xcd & 3) * 4 + (idx & 3), tn = (xcd >> 2) * 8 + (idx >> 2);
  const int m0 = tm * 128, n0 = tn * 64;
  Loader<CP> ld;
  const int lw = w & 3;
  setup_loader_A<CP, 128, 1, 2>(ld, Ablk, m0, lw, lane);
#pragma unroll
  for (int i = 0; i < CP::WPW; ++i) {
    int b = lw + 4 * i;
    int sub = b >> 2, o = b & 3;
    ld.woff[i] = 2 * CP::A_SUB + sub * CP::W_SUB + o * 1024;
    int k = sub * 32 + 8 * o + (lane >> 3);
    int f = ((lane >> 4) & 1) | ((o & 1) << 1);
    int col = n0 + ((((lane & 7) >> 1) ^ f) * 16) + (lane & 1) * 8;
    ld.wv[i] = (k * LDW + col) * 2;
    ld.wbase[i] = (const char*)W;
  }
  const int wm = w & 1, wn = (w >> 1) & 1, kg = (w >> 2) & 1;
  int waddr[2];
#pragma unroll
  for (int n = 0; n < 2; ++n) waddr[n] = wfrag_addr(wn * 2 + n, lane);
  const int aaddr = afrag_addr<128>(wm * 64, lane);
  f32x4 acc[2][4];
#pragma unroll
  for (int n = 0; n < 2; ++n)
#pragma unroll
    for (int m = 0; m < 4; ++m) acc[n][m] = f32x4{0.f, 0.f, 0.f, 0.f};
  Engine<128, 1, 2, 6, 4, 2, 2, KDIM / 64, (KDIM == DM ? 3 : 5), 0, 4> eng(acc, smem, ld, (long)2 * SEQ * 32, (long)64 * LDW, aaddr, waddr, kg);
  __attribute__((address_space(3))) float* sgate = (__attribute__((address_space(3))) float*)(smem + CP::LDS);
  if (w >= 8) {
    if (w == 8) {
      float v = 0.f;
#pragma unroll
      for (int i = 0; i < MNS; ++i) v += gate[i * (6 * DM) + n0 + lane];
      sgate[lane] = rbf(v);
    }
    eng.loader_run();
    return;
  }
  eng.start();
  eng.run();
  __syncthreads();
  __attribute__((address_space(3))) f32x4* red = (__attribute__((address_space(3))) f32x4*)smem;
  if (kg == 1) {
#pragma unroll
    for (int n = 0; n < 2; ++n)
#pragma unroll
      for (int m = 0; m < 4; ++m) red[((w & 3) * 8 + n * 4 + m) * 64 + lane] = acc[n][m];
  }
  __syncthreads();
  if (kg == 0) {
    const int g = lane >> 4;
#pragma unroll
    for (int n = 0; n < 2; ++n) {
      int col = n0 + (wn * 2 + n) * 16 + 4 * g;
      const f32x4 gt = *(const __attribute__((address_space(3))) f32x4*)(sgate + (wn * 2 + n) * 16 + 4 * g);
#pragma unroll
      for (int m = 0; m < 4; ++m) {
        f32x4 a = acc[n][m] + red[((w & 3) * 8 + n * 4 + m) * 64 + lane];
        int row = m0 + wm * 64 + m * 16 + (lane & 15);
        u32x2 r = *(const u32x2*)(res + (size_t)row * DM + col);
        float y0 = bflo(r[0]) + rbf(gt[0] * rbf(a[0]));
        float y1 = bfhi(r[0]) + rbf(gt[1] * rbf(a[1]));
        float y2 = bflo(r[1]) + rbf(gt[2] * rbf(a[2]));
        float y3 = bfhi(r[1]) + rbf(gt[3] * rbf(a[3]));
        u32x2 o;
        o[0] = pack2(y0, y1);
        o[1] = pack2(y2, y3);
        *(u32x2*)(out + (size_t)row * DM + col) = o;
      }
    }
  }
  STAMP_AT((KDIM == DM ? 3 : 5), 3);
}

typedef GCfg<256, 4, 1, 4, 4> CG;
template <int NH_>
DI void glu_wave(lds_ptr smem, Loader<CG>& ld, int lane, int wm, int t0, int m0, int f0, u16* __restrict__ out) {
  constexpr int NT = 2 * NH_;
  int waddr[NT];
#pragma unroll
  for (int n = 0; n < NH_; ++n) {
    waddr[n] = wfrag_addr(t0 + n, lane);
    waddr[NH_ + n] = waddr[n] + 8192;
  }
  const int aaddr = afrag_addr<256>(wm * 64, lane);
  f32x4 acc[NT][4];
#pragma unroll
  for (int n = 0; n < NT; ++n)
#pragma unroll
    for (int m = 0; m < 4; ++m) acc[n][m] = f32x4{0.f, 0.f, 0.f, 0.f};
  Engine<256, 4, 1, 4, 4, NT, 1, DM / 32, 4, 0, 4, (NH_ == 3)> eng(acc, smem, ld, (long)SEQ * 32, (long)32 * FF, aaddr, waddr, 0);
  eng.start();
  eng.run();
  const int g = lane >> 4;
#pragma unroll
  for (int n = 0; n < NH_; ++n) {
    const int cb = (f0 >> 4) + t0 + n;
#pragma unroll
    for (int m = 0; m < 4; m += 2) {
      u32x2 o[2];
#pragma unroll
      for (int i = 0; i < 2; ++i) {
        f32x4 a = acc[n][m + i], b = acc[NH_ + n][m + i];
        o[i][0] = pack2(rbf(a[0]) * rbf(b[0]), rbf(a[1]) * rbf(b[1]));
        o[i][1] = pack2(rbf(a[2]) * rbf(b[2]), rbf(a[3]) * rbf(b[3]));
      }
      const int row = m0 + wm * 64 + (m + (g & 1)) * 16 + (lane & 15);
      st16(out + ((size_t)cb * SEQ + row) * 16 + (g >> 1) * 8, pair16(o[0], o[1]));
    }
  }
  STAMP_AT(4, 3);
}

__global__ __launch_bounds__(512, 2) void k_glu(const u16* __restrict__ Ablk, const u16* __restrict__ Wg,
                                                 const u16* __restrict__ Wu, u16* __restrict__ out,
                                                 const u16* __restrict__ wd, u16* __restrict__ wdp) {
  extern __shared__ __attribute__((aligned(16))) char smem_[];
  lds_ptr smem = (lds_ptr)smem_;
  const int lane = threadIdx.x & 63;
  const int w = __builtin_amdgcn_readfirstlane(threadIdx.x >> 6);
  const int xcd = blockIdx.x & 7, idx = blockIdx.x >> 3;
  const int tm = idx & 7, tn = xcd * 4 + (idx >> 3);
  const int m0 = tm * 256, f0 = tn * 112;
  Loader<CG> ld;
  const int lw = w & 3;
  setup_loader_A<CG, 256, 4, 1>(ld, Ablk, m0, lw, lane);
#pragma unroll
  for (int i = 0; i < CG::WPW; ++i) {
    int b = lw + 4 * i;
    int grp = b >> 2, o = b & 3;
    ld.woff[i] = CG::A_SUB + grp * 4096 + o * 1024;
    int k = 8 * o + (lane >> 3);
    int f = ((lane >> 4) & 1) | ((o & 1) << 1);
    int pr = ((lane & 7) >> 1) ^ f;
    if ((grp & 1) && pr == 3) pr = 0;
    int col = f0 + (grp & 1) * 64 + pr * 16 + (lane & 1) * 8;
    ld.wv[i] = (k * FF + col) * 2;
    ld.wbase[i] = (const char*)(grp >> 1 ? Wu : Wg);
  }
  const int wm = w & 3;
  if (w < 4) {
    u32x4 cp[7];
#pragma unroll
    for (int i = 0; i < 7; ++i) {
      const int pc = w + 4 * i, row = blockIdx.x * 14 + (pc >> 1);
      cp[i] = *(const u32x4*)(wd + (size_t)row * DM + (pc & 1) * 512 + lane * 8);
    }
#pragma unroll
    for (int i = 0; i < 7; ++i) {
      const int pc = w + 4 * i, row = blockIdx.x * 14 + (pc >> 1);
      *(u32x4*)(wdp + (size_t)row * WDP + (pc & 1) * 512 + lane * 8) = cp[i];
    }
    glu_wave<4>(smem, ld, lane, wm, 0, m0, f0, out);
  }
  else glu_wave<3>(smem, ld, lane, wm, 4, m0, f0, out);
}

constexpr int ACH = 128, ANST = 4, ASTAGE = ACH * 64 * 2 + 256;
constexpr int ALDS = ANST * ASTAGE + 1024 + 16;

DI float rowmax16(const f32x16& v) {
  float mx = fmaxf(fmaxf(v[0], v[1]), v[2]);
#pragma unroll
  for (int r = 3; r < 15; r += 2) mx = fmaxf(fmaxf(mx, v[r]), v[r + 1]);
  mx = fmaxf(mx, v[15]);
  auto sw = __builtin_amdgcn_permlane32_swap(__float_as_uint(mx), __float_as_uint(mx), false, false);
  return fmaxf(__uint_as_float(sw[0]), __uint_as_float(sw[1]));
}

template <bool SAFE>
DI void attn_pass(lds_ptr smem, int lane, int w, const char* Kp, const char* Vp, const char* pf_wm, const char* pf_wo, const char* pf_wg,
                  const char* pf_wu, const char* pf_wd, const bf16x8& qa0, const bf16x8& qa1, const bf16x8& qb0,
                  const bf16x8& qb1, f32x16& oa, f32x16& ob, float& refa, float& refb, float& la_out, float& lb_out,
                  ModTask& mt, const u16* c) {
  const int r32 = lane & 31, hh = lane >> 5, kh = w >> 2;
  const unsigned kvo = ((32 * (w & 3) + (lane >> 1)) * 16 + (((lane & 1) ^ ((lane >> 4) & 1)) * 8)) * 2;
  const unsigned vvo = ((32 * (w & 3)) * 16) * 2 + lane * 16;
  const char* ksrc = Kp;
  const char* vsrc = Vp;
  auto issue = [&](int buf) {
    if (w < 4) {
      const unsigned l0 = lds_u32(smem) + buf * ASTAGE + w * 1024;
      glds16(ksrc, kvo, l0);
      glds16(ksrc + SEQ * 32, kvo, l0 + 4096);
      glds16(vsrc, vvo, l0 + 8192);
      glds16(vsrc + SEQ * 32, vvo, l0 + 8192 + 4096 + 128);
    }
    ksrc += ACH * 32; vsrc += ACH * 32;
  };
  const unsigned pfo = lane * 16;
  auto prefetch = [&](int ch) {
    if (w >= 4) {
#pragma unroll
      for (int i = 0; i < 3; ++i) {
        int pc = (ch * 3 + i) * 4 + (w - 4);
        const char* base;
        if (pc < 32) base = pf_wm + (size_t)(blockIdx.x * 4 + (pc >> 3)) * (12 * DM) + 4 * DM + (pc & 7) * 1024;
        else if (pc < 40) base = pf_wo + (size_t)blockIdx.x * 8192 + (pc - 32) * 1024;
        else if (pc < 68) base = pf_wg + (size_t)blockIdx.x * 28672 + (pc - 40) * 1024;
        else if (pc < 96) base = pf_wu + (size_t)blockIdx.x * 28672 + (pc - 68) * 1024;
        else if (pc < 124) base = pf_wd + (size_t)blockIdx.x * 28672 + (pc - 96) * 1024;
        else break;
        glds16(base, pfo, lds_u32(smem) + ANST * ASTAGE);
      }
    }
  };
  issue(0); issue(1); issue(2);
  const int kaddr0 = kh * 2048 + r32 * 32 + ((hh ^ ((r32 >> 3) & 1)) * 16);
  const int kaddr1 = kaddr0 + 4096;
  const int vaddr = 8192 + kh * 2048 + ((lane >> 4) & 1) * (4096 + 128) + (4 * hh + ((lane & 15) >> 2)) * 32 + (lane & 3) * 8;
  bf16x8 sel;
  {
    const short one = ((lane & 15) == ((lane >> 4) & 1)) ? (short)0x3F80 : (short)0;
#pragma unroll
    for (int j = 0; j < 8; ++j) sel[j] = one;
  }
  f32x16 nma, nmb, sa, sb;
#pragma unroll
  for (int r = 0; r < 16; ++r) { oa[r] = 0.f; ob[r] = 0.f; nma[r] = 0.f; nmb[r] = 0.f; }
  f32x4 lsa = {0.f, 0.f, 0.f, 0.f}, lsb = {0.f, 0.f, 0.f, 0.f};
  float la = 0.f, lb = 0.f;
  constexpr int NCH = SEQ / ACH;
  if (w < 4) wait_vm<8>();
  bar();
  const bf16x8 kf0_ = lds_read128(smem + kaddr0);
  const bf16x8 kf1_ = lds_read128(smem + kaddr1);
  {
    sa = MFMA32(kf0_, qa0, nma); sa = MFMA32(kf1_, qa1, sa);
    sb = MFMA32(kf0_, qb0, nmb); sb = MFMA32(kf1_, qb1, sb);
    const float ma = rowmax16(sa), mb = rowmax16(sb);
#pragma unroll
    for (int r = 0; r < 16; ++r) { nma[r] = -ma; sa[r] -= ma; nmb[r] = -mb; sb[r] -= mb; }
  }
  if constexpr (SAFE) {
    auto half = [&](f32x16& s, f32x16& o, f32x16& nm, float& l, const bf16x8& q0f, const bf16x8& q1f,
                    const bf16x8& kn0, const bf16x8& kn1, const bf16x8& vf0, const bf16x8& vf1, bool has_next) {
      f32x16 p;
      const float dl = fmaxf(rowmax16(s), 0.f);
      const float f = __builtin_amdgcn_exp2f(-dl);
      float t = 0.f;
#pragma unroll
      for (int r = 0; r < 16; ++r) { nm[r] -= dl; o[r] *= f; p[r] = __builtin_amdgcn_exp2f(s[r] - dl); t += p[r]; }
      l = l * f + t;
      if (has_next) { s = MFMA32(kn0, q0f, nm); s = MFMA32(kn1, q1f, s); }
      u32x4 pa, pb;
#pragma unroll
      for (int j = 0; j < 4; ++j) { pa[j] = pack2(p[2 * j], p[2 * j + 1]); pb[j] = pack2(p[8 + 2 * j], p[8 + 2 * j + 1]); }
      o = MFMA32(vf0, __builtin_bit_cast(bf16x8, pa), o);
      o = MFMA32(vf1, __builtin_bit_cast(bf16x8, pb), o);
    };
    auto step = [&](lds_cptr vb, lds_cptr knext, bool has_next) {
      bf16x8 kn0, kn1;
      if (has_next) { kn0 = lds_read128(knext + kaddr0); kn1 = lds_read128(knext + kaddr1); }
      bf16x8 vf0 = lds_tr8(vb, vb + 256);
      bf16x8 vf1 = lds_tr8(vb + 512, vb + 768);
      half(sa, oa, nma, la, qa0, qa1, kn0, kn1, vf0, vf1, has_next);
      half(sb, ob, nmb, lb, qb0, qb1, kn0, kn1, vf0, vf1, has_next);
    };
    for (int ch = 0; ch < NCH; ++ch) {
      if (w < 4) { if (ch + 2 < NCH) wait_vm<4>(); else wait_vm<0>(); }
      bar();
      if (ch + 3 < NCH) issue((ch + 3) & 3);
      lds_cptr sbuf = smem + (ch & 3) * ASTAGE;
      lds_cptr nbuf = smem + ((ch + 1) & 3) * ASTAGE;
      step(sbuf + vaddr, sbuf + 1024, true);
      step(sbuf + 1024 + vaddr, nbuf, ch + 1 < NCH);
    }
  } else {
    u32x4 pa0, pa1, pb0, pb1;
    bf16x8 kc0 = kf0_, kc1 = kf1_;
    bf16x8 vp0, vp1;
#pragma unroll
    for (int j = 0; j < 4; ++j) { pb0[j] = 0u; pb1[j] = 0u; }
#pragma unroll
    for (int j = 0; j < 8; ++j) { vp0[j] = 0; vp1[j] = 0; }
#define SB_ __builtin_amdgcn_sched_barrier(0)
#define EX(i) e[i] = __builtin_amdgcn_exp2f(S[i])
#define PHASE(S, P0, P1, M0, M1, M2, M3, M4, M5, R0, R1) do { float e[16]; \
      M0; SB_; R0; EX(0); EX(1); EX(2); EX(3); SB_; \
      M1; SB_; R1; EX(4); EX(5); EX(6); EX(7); SB_; \
      M2; SB_; EX(8); EX(9); P0[0] = pack2(e[0], e[1]); P0[1] = pack2(e[2], e[3]); SB_; \
      M3; SB_; EX(10); EX(11); P0[2] = pack2(e[4], e[5]); P0[3] = pack2(e[6], e[7]); SB_; \
      M4; SB_; EX(12); EX(13); EX(14); EX(15); SB_; \
      M5; SB_; P1[0] = pack2(e[8], e[9]); P1[1] = pack2(e[10], e[11]); P1[2] = pack2(e[12], e[13]); P1[3] = pack2(e[14], e[15]); SB_; \
    } while (0)
#define BF(x) __builtin_bit_cast(bf16x8, x)
    auto step = [&](lds_cptr vb, lds_cptr knext, auto has_next_t) {
      constexpr bool HN = decltype(has_next_t)::value;
      bf16x8 kn0, kn1, vc0, vc1;
      { const f32x16& S = sa;
        PHASE(S, pa0, pa1,
              sb = MFMA32(kc0, qb0, nmb), sb = MFMA32(kc1, qb1, sb),
              ob = MFMA32(vp0, BF(pb0), ob), ob = MFMA32(vp1, BF(pb1), ob),
              lsb = MFMA16(sel, BF(pb0), lsb), lsb = MFMA16(sel, BF(pb1), lsb),
              if (HN) { kn0 = lds_read128(knext + kaddr0); kn1 = lds_read128(knext + kaddr1); },
              { vc0 = lds_tr8(vb, vb + 256); vc1 = lds_tr8(vb + 512, vb + 768); }); }
      { const f32x16& S = sb;
        PHASE(S, pb0, pb1,
              if (HN) sa = MFMA32(kn0, qa0, nma), if (HN) sa = MFMA32(kn1, qa1, sa),
              oa = MFMA32(vc0, BF(pa0), oa), oa = MFMA32(vc1, BF(pa1), oa),
              lsa = MFMA16(sel, BF(pa0), lsa), lsa = MFMA16(sel, BF(pa1), lsa),
              , ); }
      if (HN) { kc0 = kn0; kc1 = kn1; }
      vp0 = vc0; vp1 = vc1;
    };
    typedef std::integral_constant<bool, true> T_;
    typedef std::integral_constant<bool, false> F_;
    for (int ch = 0; ch < NCH - 1; ++ch) {
      if (w < 4) { if (ch + 2 < NCH) wait_vm<4>(); else wait_vm<0>(); }
      bar();
      if (ch + 3 < NCH) issue((ch + 3) & 3);
      prefetch(ch);
      lds_cptr sbuf = smem + (ch & 3) * ASTAGE;
      lds_cptr nbuf = smem + ((ch + 1) & 3) * ASTAGE;
      step(sbuf + vaddr, sbuf + 1024, T_{});
      step(sbuf + 1024 + vaddr, nbuf, T_{});
    }
    {
      const int task = blockIdx.x * 2 + (threadIdx.x >> 8);
      mod_load(mt, c, (const u16*)pf_wm, 32 + (task >> 3), task & 7, threadIdx.x & 255);
      lds_cptr sbuf = smem + ((NCH - 1) & 3) * ASTAGE;
      step(sbuf + vaddr, sbuf + 1024, T_{});
      step(sbuf + 1024 + vaddr, sbuf, F_{});
    }
    ob = MFMA32(vp0, BF(pb0), ob); ob = MFMA32(vp1, BF(pb1), ob);
    lsb = MFMA16(sel, BF(pb0), lsb); lsb = MFMA16(sel, BF(pb1), lsb);
#undef PHASE
#undef EX
#undef BF
  }
  refa = nma[0]; refb = nmb[0];
  if constexpr (SAFE) {
    auto s1 = __builtin_amdgcn_permlane32_swap(__float_as_uint(la), __float_as_uint(la), false, false);
    la_out = __uint_as_float(s1[0]) + __uint_as_float(s1[1]);
    auto s2 = __builtin_amdgcn_permlane32_swap(__float_as_uint(lb), __float_as_uint(lb), false, false);
    lb_out = __uint_as_float(s2[0]) + __uint_as_float(s2[1]);
  } else {
    const float a0 = __shfl(lsa[0], lane & 15), a1 = __shfl(lsa[1], lane & 15);
    const float b0 = __shfl(lsb[0], lane & 15), b1 = __shfl(lsb[1], lane & 15);
    la_out = (lane & 16) ? a1 : a0;
    lb_out = (lane & 16) ? b1 : b0;
  }
}

__global__ __launch_bounds__(512, 2) void k_attn(const u16* __restrict__ qkv, u16* __restrict__ oblk,
                                                  const u16* __restrict__ c, const u16* __restrict__ wmod, float* __restrict__ part,
                                                  const char* __restrict__ pf_wo, const char* __restrict__ pf_wg,
                                                  const char* __restrict__ pf_wu, const char* __restrict__ pf_wd) {
  extern __shared__ __attribute__((aligned(16))) char smem_[];
  lds_ptr smem = (lds_ptr)smem_;
  const int lane = threadIdx.x & 63;
  const int w = __builtin_amdgcn_readfirstlane(threadIdx.x >> 6);
  const int xcd = blockIdx.x & 7, idx = blockIdx.x >> 3;
  const int head = xcd * 4 + (idx >> 3), qb = idx & 7;
  const int r32 = lane & 31, hh = lane >> 5;
  const int qg = w & 3, kh = w >> 2;
  const u16* Qp = qkv + (size_t)head * SEQ * 32;
  const char* Kp = (const char*)(qkv + (size_t)(32 + head) * SEQ * 32);
  const char* Vp = (const char*)(qkv + (size_t)(64 + head) * SEQ * 32);
  const int q0 = qb * 256 + qg * 64;
  STAMP_AT(2, 0);
  __attribute__((address_space(3))) int* flag = (__attribute__((address_space(3))) int*)(smem + ANST * ASTAGE + 1024);
  if (threadIdx.x == 0) *flag = 0;
  bf16x8 qa0 = *(const bf16x8*)(Qp + (q0 + r32) * 16 + hh * 8);
  bf16x8 qa1 = *(const bf16x8*)(Qp + (SEQ + q0 + r32) * 16 + hh * 8);
  bf16x8 qb0 = *(const bf16x8*)(Qp + (q0 + 32 + r32) * 16 + hh * 8);
  bf16x8 qb1 = *(const bf16x8*)(Qp + (SEQ + q0 + 32 + r32) * 16 + hh * 8);
  f32x16 oa, ob;
  float nma, nmb, la, lb;
  ModTask mt;
  attn_pass<false>(smem, lane, w, Kp, Vp, (const char*)wmod, pf_wo, pf_wg, pf_wu, pf_wd, qa0, qa1, qb0, qb1, oa, ob, nma, nmb, la, lb, mt, c);
  {
    float big = fmaxf(fabsf(la), fabsf(lb));
#pragma unroll
    for (int r = 0; r < 16; ++r) big = fmaxf(big, fmaxf(fabsf(oa[r]), fabsf(ob[r])));
    bool bad = !(big < 1e30f);
#pragma unroll
    for (int r = 0; r < 16; ++r) bad = bad || (oa[r] != oa[r]) || (ob[r] != ob[r]);
    bad = bad || (la != la) || (lb != lb);
    if (__any(bad) && lane == 0) *flag = 1;
  }
  __syncthreads();
  if (__builtin_amdgcn_readfirstlane(*flag) != 0) {
    attn_pass<true>(smem, lane, w, Kp, Vp, (const char*)wmod, pf_wo, pf_wg, pf_wu, pf_wd, qa0, qa1, qb0, qb1, oa, ob, nma, nmb, la, lb, mt, c);
    __syncthreads();
  }
  STAMP_AT(2, 2);
  const int task = blockIdx.x * 2 + (threadIdx.x >> 8);
  {
    __attribute__((address_space(3))) float* xb = (__attribute__((address_space(3))) float*)smem;
    const f32x16& og = kh == 0 ? ob : oa;
    __attribute__((address_space(3))) float* dst = xb + w * (18 * 64) + lane;
#pragma unroll
    for (int r = 0; r < 16; ++r) dst[r * 64] = og[r];
    dst[16 * 64] = kh == 0 ? lb : la;
    dst[17 * 64] = kh == 0 ? nmb : nma;
    __syncthreads();
    const __attribute__((address_space(3))) float* src = xb + (w ^ 4) * (18 * 64) + lane;
    f32x16 om = kh == 0 ? oa : ob;
    const float nm_me = kh == 0 ? nma : nmb;
    const float nm_ot = src[17 * 64];
    const float nmin = fminf(nm_me, nm_ot);
    const float f_me = __builtin_amdgcn_exp2f(nmin - nm_me), f_ot = __builtin_amdgcn_exp2f(nmin - nm_ot);
    const float rl = 1.f / ((kh == 0 ? la : lb) * f_me + src[16 * 64] * f_ot);
    const float g_me = f_me * rl, g_ot = f_ot * rl;
    u16* op = oblk + ((size_t)head * 2 * SEQ + q0 + kh * 32 + r32) * 16 + 4 * hh;
#pragma unroll
    for (int g = 0; g < 4; ++g) {
      float v0 = om[4 * g] * g_me + src[(4 * g) * 64] * g_ot;
      float v1 = om[4 * g + 1] * g_me + src[(4 * g + 1) * 64] * g_ot;
      float v2 = om[4 * g + 2] * g_me + src[(4 * g + 2) * 64] * g_ot;
      float v3 = om[4 * g + 3] * g_me + src[(4 * g + 3) * 64] * g_ot;
      u32x2 v;
      v[0] = pack2(v0, v1);
      v[1] = pack2(v2, v3);
      *(u32x2*)(op + (g >> 1) * (SEQ * 16) + (g & 1) * 8) = v;
    }
  }
  mod_finish(mt, part, 32 + (task >> 3), task & 7, threadIdx.x & 255, (float*)(smem_ + 40960) + (threadIdx.x >> 8) * 256);
  wait_vm<0>();
  STAMP_AT(2, 3);
}

extern "C" void kernel_launch(void* const* d_in, const int* in_sizes, int n_in, void* d_out, int out_size,
                              void* d_ws, size_t ws_size, hipStream_t stream) {
  const u16* x = (const u16*)d_in[0];
  const u16* wqkv = (const u16*)d_in[1];
  const u16* wo = (const u16*)d_in[2];
  const u16* wg = (const u16*)d_in[4];
  const u16* wu = (const u16*)d_in[5];
  const u16* wd = (const u16*)d_in[6];
  const u16* c = (const u16*)d_in[7];
  const u16* wmod = (const u16*)d_in[8];
  u16* out = (u16*)d_out;
  char* ws = (char*)d_ws;
  float* part = (float*)ws;
  float* modf = (float*)(ws + (256 << 10));
  unsigned* flags = (unsigned*)(ws + (512 << 10));
  u16* h1 = (u16*)(ws + (1 << 20));
  u16* qkv = (u16*)(ws + (8 << 20));
  u16* ob = (u16*)(ws + (24 << 20));
  u16* x2 = (u16*)(ws + (32 << 20));
  u16* h2 = (u16*)(ws + (40 << 20));
  u16* hh = (u16*)(ws + (48 << 20));
  u16* wdp = (u16*)(ws + (64 << 20));

  hipFuncSetAttribute((const void*)k_qkv, hipFuncAttributeMaxDynamicSharedMemorySize, CQ::LDS);
  hipFuncSetAttribute((const void*)k_proj<DM, DM>, hipFuncAttributeMaxDynamicSharedMemorySize, CP::LDS + 256);
  hipFuncSetAttribute((const void*)k_proj<FF, WDP>, hipFuncAttributeMaxDynamicSharedMemorySize, CP::LDS + 256);
  hipFuncSetAttribute((const void*)k_glu, hipFuncAttributeMaxDynamicSharedMemorySize, CG::LDS);
  hipFuncSetAttribute((const void*)k_attn, hipFuncAttributeMaxDynamicSharedMemorySize, ALDS);

  k_pre<<<256 + 256, 256, 0, stream>>>(c, wmod, part, (const char*)x, (const char*)wqkv, flags);
  k_ln<6, MNS, true><<<256 + 192, 512, 0, stream>>>(x, part, h1, (const char*)wqkv);
  k_qkv<<<256, 768, CQ::LDS, stream>>>(h1, wqkv, qkv);
  k_attn<<<256, 512, ALDS, stream>>>(qkv, ob, c, wmod, part, (const char*)wo, (const char*)wg, (const char*)wu, (const char*)wd);
  k_proj<DM, DM><<<256, 768, CP::LDS + 256, stream>>>(ob, wo, x, part + 2 * DM, x2);
  k_ln<7, MNS, false><<<256, 512, 0, stream>>>(x2, part + 3 * DM, h2, (const char*)wqkv);
  k_glu<<<256, 512, CG::LDS, stream>>>(h2, wg, wu, hh, wd, wdp);
  k_proj<FF, WDP><<<256, 768, CP::LDS + 256, stream>>>(hh, wdp, x2, part + 5 * DM, out);
}
```

```cpp
#include <hip/hip_runtime.h>
#include <hip/hip_bf16.h>
#include <stdint.h>

typedef __attribute__((ext_vector_type(8))) short bf16x8;
typedef __attribute__((ext_vector_type(4))) short s16x4;
typedef __attribute__((ext_vector_type(4))) float f32x4;
typedef __attribute__((ext_vector_type(2))) float f32x2;
typedef __attribute__((ext_vector_type(16))) float f32x16;
typedef __attribute__((ext_vector_type(4))) unsigned u32x4;
typedef __attribute__((ext_vector_type(2))) unsigned u32x2;
typedef __attribute__((ext_vector_type(2))) __bf16 bf16x2_t;
typedef unsigned short u16;
typedef __attribute__((address_space(3))) char* lds_ptr;
typedef __attribute__((address_space(3))) const char* lds_cptr;

#define DI __device__ __forceinline__

constexpr int SEQ = 2048, DM = 1024, FF = 3584, NH = 32;

DI float bf2f(u16 x) { return __uint_as_float(((unsigned)x) << 16); }
DI float bflo(unsigned x) { return __uint_as_float(x << 16); }
DI float bfhi(unsigned x) { return __uint_as_float(x & 0xffff0000u); }
DI unsigned pack2(float a, float b) {
  f32x2 v = {a, b};
  bf16x2_t r = __builtin_convertvector(v, bf16x2_t);
  return __builtin_bit_cast(unsigned, r);
}
DI float rbf(float a) { return bflo(pack2(a, 0.f) & 0xffffu); }

#define MFMA16(a, b, c) __builtin_amdgcn_mfma_f32_16x16x32_bf16((a), (b), (c), 0, 0, 0)
#define MFMA32(a, b, c) __builtin_amdgcn_mfma_f32_32x32x16_bf16((a), (b), (c), 0, 0, 0)

DI void glds16(const void* base, unsigned voff, unsigned lds_addr) {
  unsigned sv;
  asm volatile("s_mov_b32 %0, m0\n\ts_mov_b32 m0, %3\n\ts_nop 0\n\tglobal_load_lds_dwordx4 %1, %2\n\ts_mov_b32 m0, %0"
               : "=&s"(sv) : "v"(voff), "s"(base), "s"(lds_addr) : "memory");
}
DI unsigned lds_u32(lds_ptr p) { return (unsigned)(uintptr_t)p; }
DI bf16x8 lds_read128(lds_cptr p) { return *(const __attribute__((address_space(3))) bf16x8*)p; }
DI s16x4 lds_tr(lds_cptr p) {
  typedef short v4i16_t __attribute__((ext_vector_type(4)));
  return __builtin_bit_cast(s16x4, __builtin_amdgcn_ds_read_tr16_b64_v4i16((__attribute__((address_space(3))) v4i16_t*)p));
}
DI bf16x8 lds_tr8(lds_cptr p0, lds_cptr p1) {
  s16x4 a = lds_tr(p0), b = lds_tr(p1);
  return __builtin_shufflevector(a, b, 0, 1, 2, 3, 4, 5, 6, 7);
}
#define STAMP_AT(kid, slot) do {} while (0)
template <int N> DI void wait_vm() { asm volatile("s_waitcnt vmcnt(%0)" ::"n"(N) : "memory"); }
DI void bar() { __builtin_amdgcn_s_barrier(); }

constexpr int MNS = 8;
struct ModTask { u32x4 w[4]; u16 cv[4]; };
DI void mod_load(ModTask& mt, const u16* __restrict__ c, const u16* __restrict__ wmod, int cb, int ks, int t) {
  const int cc = t & 7, rg = t >> 3;
  const int k0 = ks * 128 + rg;
  const u16* wp = wmod + (size_t)k0 * (6 * DM) + cb * 64 + cc * 8;
#pragma unroll
  for (int i = 0; i < 4; ++i) mt.w[i] = *(const u32x4*)(wp + (size_t)i * 32 * (6 * DM));
#pragma unroll
  for (int i = 0; i < 4; ++i) mt.cv[i] = c[k0 + 32 * i];
}
DI void mod_finish(const ModTask& mt, float* __restrict__ part, int cb, int ks, int t, float* s_red  ) {
  float acc[8];
#pragma unroll
  for (int j = 0; j < 8; ++j) acc[j] = 0.f;
#pragma unroll
  for (int i = 0; i < 4; ++i) {
    const float cv = bf2f(mt.cv[i]);
    const float sv = rbf(cv / (1.f + __expf(-cv)));
#pragma unroll
    for (int j = 0; j < 4; ++j) {
      acc[2 * j] += sv * bflo(mt.w[i][j]);
      acc[2 * j + 1] += sv * bfhi(mt.w[i][j]);
    }
  }
#pragma unroll
  for (int j = 0; j < 8; ++j) {
    float v = acc[j];
    v += __shfl_xor(v, 8);
    v += __shfl_xor(v, 16);
    v += __shfl_xor(v, 32);
    acc[j] = v;
  }
  const int lane = t & 63, wv = t >> 6;
  if (lane < 8) {
#pragma unroll
    for (int j = 0; j < 8; ++j) s_red[wv * 64 + lane * 8 + j] = acc[j];
  }
  __syncthreads();
  if (t < 64) part[ks * (6 * DM) + cb * 64 + t] = (s_red[t] + s_red[64 + t]) + (s_red[128 + t] + s_red[192 + t]);
}
DI f32x4 gate4(const float* __restrict__ gate, int col) {
  f32x4 gt = *(const f32x4*)(gate + col);
#pragma unroll
  for (int i = 1; i < MNS; ++i) gt += *(const f32x4*)(gate + i * (6 * DM) + col);
#pragma unroll
  for (int j = 0; j < 4; ++j) gt[j] = rbf(gt[j]);
  return gt;
}

DI void touch16k(const char* p, int t) {
  u32x4 v[4];
#pragma unroll
  for (int i = 0; i < 4; ++i) v[i] = *(const u32x4*)(p + i * 4096 + t * 16);
#pragma unroll
  for (int i = 0; i < 4; ++i) asm volatile("" ::"v"(v[i]));
}

__global__ __launch_bounds__(256) void k_pre(const u16* __restrict__ c, const u16* __restrict__ wmod, float* __restrict__ part,
                                              const char* __restrict__ x, const char* __restrict__ wqkv,
                                              unsigned* __restrict__ flags) {
  __shared__ float s_red[256];
  const int t = threadIdx.x, b = blockIdx.x;
  if (b < 256) STAMP_AT(0, 0);
  if (b < 256) {
    ModTask mt;
    mod_load(mt, c, wmod, b >> 3, b & 7, t);
    mod_finish(mt, part, b >> 3, b & 7, t, s_red);
    if (b == 0) flags[t * 16] = 0u;
  } else {
    touch16k(x + (size_t)(b - 256) * 16384, t);
  }
  if (b < 256) STAMP_AT(0, 3);
}

constexpr int WDP = DM + 64;
template <int KID, int NS, int NPF>
__global__ __launch_bounds__(512) void k_ln(const u16* __restrict__ x, const float* __restrict__ ss  ,
                                             u16* __restrict__ h16, const char* __restrict__ pf,
                                             const u16* __restrict__ wsrc, u16* __restrict__ wdst) {
  __shared__ __attribute__((aligned(16))) float sm[2048];
  const int t = threadIdx.x;
  if (blockIdx.x >= 256 + NPF) {
    const int r0 = (blockIdx.x - 256 - NPF) * 16 + (t >> 7);
    u32x4 v[4];
#pragma unroll
    for (int i = 0; i < 4; ++i) v[i] = *(const u32x4*)(wsrc + (size_t)(r0 + 4 * i) * DM + (t & 127) * 8);
#pragma unroll
    for (int i = 0; i < 4; ++i) *(u32x4*)(wdst + (size_t)(r0 + 4 * i) * WDP + (t & 127) * 8) = v[i];
    return;
  }
  if (blockIdx.x >= 256) {
    touch16k(pf + (size_t)((blockIdx.x - 256) * 2 + (t >> 8)) * 16384, t & 255);
    return;
  }
  const int lane = t & 63, wv = t >> 6;
  const int row = blockIdx.x * 8 + wv;
  STAMP_AT(KID, 0);
  const u16* xr = x + (size_t)row * DM;
  u32x4 v0 = *(const u32x4*)(xr + lane * 8);
  u32x4 v1 = *(const u32x4*)(xr + 512 + lane * 8);
  {
    f32x4 pv[NS];
#pragma unroll
    for (int i = 0; i < NS; ++i) pv[i] = *(const f32x4*)(ss + i * (6 * DM) + t * 4);
    f32x4 a = pv[0];
#pragma unroll
    for (int i = 1; i < NS; ++i) a += pv[i];
#pragma unroll
    for (int j = 0; j < 4; ++j) {
      float v = rbf(a[j]);
      a[j] = t < 256 ? v : rbf(1.f + v);
    }
    *(f32x4*)(sm + t * 4) = a;
  }
  float f[16];
#pragma unroll
  for (int j = 0; j < 4; ++j) {
    f[2 * j] = bflo(v0[j]); f[2 * j + 1] = bfhi(v0[j]);
    f[8 + 2 * j] = bflo(v1[j]); f[8 + 2 * j + 1] = bfhi(v1[j]);
  }
  float s = 0.f;
#pragma unroll
  for (int j = 0; j < 16; ++j) s += f[j];
#pragma unroll
  for (int o = 1; o < 64; o <<= 1) s += __shfl_xor(s, o);
  const float mean = s * (1.f / DM);
  float q = 0.f;
#pragma unroll
  for (int j = 0; j < 16; ++j) { float d = f[j] - mean; q += d * d; }
#pragma unroll
  for (int o = 1; o < 64; o <<= 1) q += __shfl_xor(q, o);
  const float mb = rbf(mean);
  const float vb = rbf(q * (1.f / DM));
  const float rden = 1.f / rbf(sqrtf(rbf(vb + 1e-5f)));
  __syncthreads();
#pragma unroll
  for (int half = 0; half < 2; ++half) {
    const int k0 = half * 512 + lane * 8;
    f32x4 sh0 = *(const f32x4*)(sm + k0), sh1 = *(const f32x4*)(sm + k0 + 4);
    f32x4 sc0 = *(const f32x4*)(sm + 1024 + k0), sc1 = *(const f32x4*)(sm + 1024 + k0 + 4);
    float y[8];
#pragma unroll
    for (int j = 0; j < 8; ++j) {
      float g = j < 4 ? sc0[j] : sc1[j - 4];
      float sh = j < 4 ? sh0[j] : sh1[j - 4];
      float d = rbf(f[half * 8 + j] - mb);
      float n = rbf(d * rden);
      y[j] = rbf(n * g) + sh;
    }
    u32x4 o;
#pragma unroll
    for (int j = 0; j < 4; ++j) o[j] = pack2(y[2 * j], y[2 * j + 1]);
    *(u32x4*)(h16 + ((size_t)(k0 >> 4) * SEQ + row) * 16 + (k0 & 15)) = o;
  }
  STAMP_AT(KID, 3);
}

DI void st16(void* p, u32x4 v) {
  *(u32x4*)p = v;
}
DI u32x4 pair16(u32x2 a, u32x2 b) {
  auto s0 = __builtin_amdgcn_permlane16_swap(a[0], b[0], false, false);
  auto s1 = __builtin_amdgcn_permlane16_swap(a[1], b[1], false, false);
  return u32x4{s0[0], s1[0], s0[1], s1[1]};
}

DI int hA(int r) { return (-r) & 3; }

template <int BM, int NGRP, int KS, int NST, int NLW_ = 8>
struct GCfg {
  static constexpr int NLW = NLW_;
  static constexpr int A_SUB = BM * 64;
  static constexpr int W_SUB = NGRP * 4096;
  static constexpr int STAGE = KS * (A_SUB + W_SUB);
  static constexpr int APW = KS * BM / 16 / NLW;
  static constexpr int WPW = KS * NGRP * 4 / NLW;
  static constexpr int PPW = APW + WPW;
  static constexpr int LDS = STAGE * NST;
};

template <class C>
struct Loader {
  const char* abase;
  const char* wbase[C::WPW];
  unsigned av[C::APW];
  unsigned wv[C::WPW];
  int aoff[C::APW];
  int woff[C::WPW];
};

template <int BM, int NGRP, int KS, int NST, int MT, int NT, int KSPLIT, int NKT, int KID, int IOFS, int NLW = 8, bool LOADS = (NLW == 8)>
struct Engine {
  typedef GCfg<BM, NGRP, KS, NST, NLW> C;
  static constexpr bool DED = !LOADS;
  static constexpr int SPW = KS / KSPLIT;
  static constexpr int P = C::PPW;
  static constexpr int G = NT * SPW;
  f32x4 (&acc)[NT][MT];
  lds_ptr smem;
  Loader<C>& ld;
  long a_adv, w_adv;
  const int (&waddr)[NT];
  int a_sub0, w_sub0;
  int cur, nxt, fre;
  bf16x8 wf[NT], af[2][MT];

  DI Engine(f32x4 (&acc_)[NT][MT], lds_ptr smem_, Loader<C>& ld_, long a_adv_, long w_adv_, int aaddr, const int (&waddr_)[NT], int kg)
      : acc(acc_), smem(smem_), ld(ld_), a_adv(a_adv_), w_adv(w_adv_), waddr(waddr_) {
    a_sub0 = kg * C::A_SUB + aaddr;
    w_sub0 = KS * C::A_SUB + kg * C::W_SUB;
  }
  template <int I> DI void piece(int stage_off) {
    if constexpr (I < C::APW) {
      glds16(ld.abase, ld.av[I], lds_u32(smem) + stage_off + ld.aoff[I]);
      if constexpr (I == C::APW - 1) ld.abase += a_adv * 2;
    } else {
      constexpr int J = I - C::APW;
      glds16(ld.wbase[J], ld.wv[J], lds_u32(smem) + stage_off + ld.woff[J]);
      ld.wbase[J] += w_adv * 2;
    }
  }
  template <int I> DI void issue_all(int stage_off) {
    if constexpr (I < P) { piece<I>(stage_off); issue_all<I + 1>(stage_off); }
  }
  template <int g, int I> DI void issue_at(int stage_off) {
    if constexpr (I < P) {
      constexpr int span = (G - IOFS) > P ? (G - IOFS) : P;
      constexpr int pos = IOFS + (I * span) / P < G ? IOFS + (I * span) / P : G - 1;
      if constexpr (pos == g) piece<I>(stage_off);
      issue_at<g, I + 1>(stage_off);
    }
  }
  template <int PAR, int S, bool PF, bool DOISSUE, int n>
  DI void groups(lds_cptr ab, lds_cptr wb) {
    if constexpr (n < NT) {
#pragma unroll
      for (int m = 0; m < MT; ++m) acc[n][m] = MFMA16(wf[n], af[PAR][m], acc[n][m]);
      if constexpr (PF) {
        wf[n] = lds_tr8(wb + waddr[n], wb + waddr[n] + 512);
#pragma unroll
        for (int m = 0; m < MT; ++m)
          if ((m * NT) / MT == n) af[PAR ^ 1][m] = lds_read128(ab + m * 512);
      }
      if constexpr (DOISSUE && !DED) issue_at<S * NT + n, 0>(fre);
      __builtin_amdgcn_sched_barrier(0);
      groups<PAR, S, PF, DOISSUE, n + 1>(ab, wb);
    }
  }
  template <int PAR, bool DOISSUE, bool LAST, int S> DI void steps() {
    if constexpr (S < SPW) {
      if constexpr (S + 1 < SPW)
        groups<PAR, S, true, DOISSUE, 0>(smem + cur + a_sub0 + (S + 1) * KSPLIT * C::A_SUB, smem + cur + w_sub0 + (S + 1) * KSPLIT * C::W_SUB);
      else if constexpr (!LAST)
        groups<PAR, S, true, DOISSUE, 0>(smem + nxt + a_sub0, smem + nxt + w_sub0);
      else
        groups<PAR, S, false, false, 0>(smem, smem);
      steps<PAR ^ 1, DOISSUE, LAST, S + 1>();
    }
  }
  template <int PAR, bool DOISSUE, bool LAST, int WAITN> DI void iter() {
    if constexpr (!LAST) { if constexpr (!DED) wait_vm<WAITN>(); bar(); }
    steps<PAR, DOISSUE, LAST, 0>();
    fre = cur; cur = nxt; nxt = (nxt + C::STAGE == NST * C::STAGE) ? 0 : nxt + C::STAGE;
  }
  template <int KT> DI void tail() {
    if constexpr (KT < NKT) {
      constexpr bool DOISSUE = KT + NST - 1 < NKT;
      constexpr bool LAST = KT == NKT - 1;
      constexpr int newest = (KT + NST - 2) < (NKT - 1) ? (KT + NST - 2) : (NKT - 1);
      constexpr int younger = newest - (KT + 1) > 0 ? newest - (KT + 1) : 0;
      iter<(KT * SPW) & 1, DOISSUE, LAST, P * younger>();
      tail<KT + 1>();
    }
  }
  DI void start() {
    static_assert(NKT >= NST + 1, "too few k tiles");
    STAMP_AT(KID, 0);
    if constexpr (!DED) {
#pragma unroll
      for (int s = 0; s < NST - 1; ++s) issue_all<0>(s * C::STAGE);
    }
  }
  template <int KT> DI void ltail() {
    if constexpr (KT < NKT - 1) {
      constexpr int newest = (KT + NST - 2) < (NKT - 1) ? (KT + NST - 2) : (NKT - 1);
      constexpr int younger = newest - (KT + 1) > 0 ? newest - (KT + 1) : 0;
      wait_vm<P * younger>();
      bar();
      ltail<KT + 1>();
    }
  }
  DI void loader_run() {
#pragma unroll
    for (int s = 0; s < NST - 1; ++s) issue_all<0>(s * C::STAGE);
    wait_vm<P*(NST - 2)>();
    bar();
    fre = (NST - 1) * C::STAGE;
    for (int kt = 0; kt < NKT - NST + 1; ++kt) {
      wait_vm<P*(NST - 3)>();
      bar();
      issue_all<0>(fre);
      fre = (fre + C::STAGE == NST * C::STAGE) ? 0 : fre + C::STAGE;
    }
    ltail<NKT - NST + 1>();
  }
  DI void run() {
    if constexpr (!DED) wait_vm<P*(NST - 2)>();
    bar();
    STAMP_AT(KID, 1);
    {
      lds_cptr ab = smem + a_sub0;
      lds_cptr wb = smem + w_sub0;
#pragma unroll
      for (int m = 0; m < MT; ++m) af[0][m] = lds_read128(ab + m * 512);
#pragma unroll
      for (int n = 0; n < NT; ++n) wf[n] = lds_tr8(wb + waddr[n], wb + waddr[n] + 512);
    }
    cur = 0; nxt = C::STAGE; fre = (NST - 1) * C::STAGE;
    constexpr int STEADY = NKT - NST + 1;
    constexpr int LOOPN = (STEADY / 2) * 2;
    for (int kt = 0; kt < LOOPN; kt += 2) {
      iter<0, true, false, P*(NST - 3)>();
      iter<SPW & 1, true, false, P*(NST - 3)>();
    }
    tail<LOOPN>();
    STAMP_AT(KID, 2);
  }
};

template <class C, int BM, int NGRP, int KS>
DI void setup_loader_A(Loader<C>& ld, const u16* Ablk, int m0, int w, int lane) {
#pragma unroll
  for (int i = 0; i < C::APW; ++i) {
    int a = w + C::NLW * i;
    int blk = a / (BM / 32), j = a % (BM / 32);
    ld.aoff[i] = blk * (BM * 32) + j * 1024;
    ld.av[i] = ((blk * SEQ + m0 + j * 32) * 16) * 2 + lane * 16;
  }
  ld.abase = (const char*)Ablk;
}

DI int wfrag_addr(int t, int lane) {
  int g = lane >> 4, q = (lane & 15) >> 2, p = lane & 3;
  int f = ((q >> 1) & 1) | ((g & 1) << 1);
  return (t >> 2) * 4096 + (8 * g + q) * 128 + (((t & 3) ^ f) * 32) + p * 8;
}
template <int BM>
DI int afrag_addr(int mrow0, int lane) {
  int r = lane & 15, g = lane >> 4;
  return (g >> 1) * (BM * 32) + (mrow0 + r) * 32 + (g & 1) * 16;
}

constexpr float QSCALE = 0.17677669529663687f * 1.4426950408889634f;
typedef GCfg<128, 3, 2, 4, 4> CQ;
__global__ __launch_bounds__(768, 3) void k_qkv(const u16* __restrict__ Ablk, const u16* __restrict__ W,
                                                 u16* __restrict__ out) {
  extern __shared__ __attribute__((aligned(16))) char smem_[];
  lds_ptr smem = (lds_ptr)smem_;
  const int lane = threadIdx.x & 63;
  const int w = __builtin_amdgcn_readfirstlane(threadIdx.x >> 6);
  const int xcd = blockIdx.x & 7, idx = blockIdx.x >> 3;
  const int tm = (xcd & 1) * 8 + (idx & 7), tn = (xcd >> 1) * 4 + (idx >> 3);
  const int m0 = tm * 128, n0 = tn * 192;
  constexpr int LDW = 3 * DM;
  Loader<CQ> ld;
  const int lw = w & 3;
  setup_loader_A<CQ, 128, 3, 2>(ld, Ablk, m0, lw, lane);
#pragma unroll
  for (int i = 0; i < CQ::WPW; ++i) {
    int b = lw + 4 * i;
    int sub = b / 12, r = b % 12, grp = r >> 2, o = r & 3;
    ld.woff[i] = 2 * CQ::A_SUB + sub * CQ::W_SUB + grp * 4096 + o * 1024;
    int k = sub * 32 + 8 * o + (lane >> 3);
    int f = ((lane >> 4) & 1) | ((o & 1) << 1);
    int col = n0 + grp * 64 + ((((lane & 7) >> 1) ^ f) * 16) + (lane & 1) * 8;
    ld.wv[i] = (k * LDW + col) * 2;
    ld.wbase[i] = (const char*)W;
  }
  const int wm = w & 1, wn = (w >> 1) & 3;
  int waddr[3];
#pragma unroll
  for (int n = 0; n < 3; ++n) waddr[n] = wfrag_addr(wn * 3 + n, lane);
  const int aaddr = afrag_addr<128>(wm * 64, lane);
  f32x4 acc[3][4];
#pragma unroll
  for (int n = 0; n < 3; ++n)
#pragma unroll
    for (int m = 0; m < 4; ++m) acc[n][m] = f32x4{0.f, 0.f, 0.f, 0.f};
  Engine<128, 3, 2, 4, 4, 3, 1, DM / 64, 1, 0, 4> eng(acc, smem, ld, (long)2 * SEQ * 32, (long)64 * LDW, aaddr, waddr, 0);
  if (w >= 8) { eng.loader_run(); return; }
  eng.start();
  eng.run();
  const int g = lane >> 4;
#pragma unroll
  for (int n = 0; n < 3; ++n) {
    const int cb = (n0 >> 4) + wn * 3 + n;
    const float sc = cb < DM / 16 ? QSCALE : 1.f;
#pragma unroll
    for (int m = 0; m < 4; m += 2) {
      u32x2 a, b;
      a[0] = pack2(acc[n][m][0] * sc, acc[n][m][1] * sc);
      a[1] = pack2(acc[n][m][2] * sc, acc[n][m][3] * sc);
      b[0] = pack2(acc[n][m + 1][0] * sc, acc[n][m + 1][1] * sc);
      b[1] = pack2(acc[n][m + 1][2] * sc, acc[n][m + 1][3] * sc);
      const int row = m0 + wm * 64 + (m + (g & 1)) * 16 + (lane & 15);
      st16(out + ((size_t)cb * SEQ + row) * 16 + (g >> 1) * 8, pair16(a, b));
    }
  }
  STAMP_AT(1, 3);
}

typedef GCfg<128, 1, 2, 6, 4> CP;
template <int KDIM, int LDW>
__global__ __launch_bounds__(768, 3) void k_proj(const u16* __restrict__ Ablk, const u16* __restrict__ W,
                                                  const u16* __restrict__ res, const float* __restrict__ gate,
                                                  u16* __restrict__ out) {
  extern __shared__ __attribute__((aligned(16))) char smem_[];
  lds_ptr smem = (lds_ptr)smem_;
  const int lane = threadIdx.x & 63;
  const int w = __builtin_amdgcn_readfirstlane(threadIdx.x >> 6);
  const int xcd = blockIdx.x & 7, idx = blockIdx.x >> 3;
  const int tm = (xcd & 3) * 4 + (idx & 3), tn = (xcd >> 2) * 8 + (idx >> 2);
  const int m0 = tm * 128, n0 = tn * 64;
  Loader<CP> ld;
  const int lw = w & 3;
  setup_loader_A<CP, 128, 1, 2>(ld, Ablk, m0, lw, lane);
#pragma unroll
  for (int i = 0; i < CP::WPW; ++i) {
    int b = lw + 4 * i;
    int sub = b >> 2, o = b & 3;
    ld.woff[i] = 2 * CP::A_SUB + sub * CP::W_SUB + o * 1024;
    int k = sub * 32 + 8 * o + (lane >> 3);
    int f = ((lane >> 4) & 1) | ((o & 1) << 1);
    int col = n0 + ((((lane & 7) >> 1) ^ f) * 16) + (lane & 1) * 8;
    ld.wv[i] = (k * LDW + col) * 2;
    ld.wbase[i] = (const char*)W;
  }
  const int wm = w & 1, wn = (w >> 1) & 1, kg = (w >> 2) & 1;
  int waddr[2];
#pragma unroll
  for (int n = 0; n < 2; ++n) waddr[n] = wfrag_addr(wn * 2 + n, lane);
  const int aaddr = afrag_addr<128>(wm * 64, lane);
  f32x4 acc[2][4];
#pragma unroll
  for (int n = 0; n < 2; ++n)
#pragma unroll
    for (int m = 0; m < 4; ++m) acc[n][m] = f32x4{0.f, 0.f, 0.f, 0.f};
  Engine<128, 1, 2, 6, 4, 2, 2, KDIM / 64, (KDIM == DM ? 3 : 5), 0, 4> eng(acc, smem, ld, (long)2 * SEQ * 32, (long)64 * LDW, aaddr, waddr, kg);
  __attribute__((address_space(3))) float* sgate = (__attribute__((address_space(3))) float*)(smem + CP::LDS);
  if (w >= 8) {
    if (w == 8) {
      float v = 0.f;
#pragma unroll
      for (int i = 0; i < MNS; ++i) v += gate[i * (6 * DM) + n0 + lane];
      sgate[lane] = rbf(v);
    }
    eng.loader_run();
    return;
  }
  eng.start();
  eng.run();
  __syncthreads();
  __attribute__((address_space(3))) f32x4* red = (__attribute__((address_space(3))) f32x4*)smem;
  if (kg == 1) {
#pragma unroll
    for (int n = 0; n < 2; ++n)
#pragma unroll
      for (int m = 0; m < 4; ++m) red[((w & 3) * 8 + n * 4 + m) * 64 + lane] = acc[n][m];
  }
  __syncthreads();
  if (kg == 0) {
    const int g = lane >> 4;
#pragma unroll
    for (int n = 0; n < 2; ++n) {
      int col = n0 + (wn * 2 + n) * 16 + 4 * g;
      const f32x4 gt = *(const __attribute__((address_space(3))) f32x4*)(sgate + (wn * 2 + n) * 16 + 4 * g);
#pragma unroll
      for (int m = 0; m < 4; ++m) {
        f32x4 a = acc[n][m] + red[((w & 3) * 8 + n * 4 + m) * 64 + lane];
        int row = m0 + wm * 64 + m * 16 + (lane & 15);
        u32x2 r = *(const u32x2*)(res + (size_t)row * DM + col);
        float y0 = bflo(r[0]) + rbf(gt[0] * rbf(a[0]));
        float y1 = bfhi(r[0]) + rbf(gt[1] * rbf(a[1]));
        float y2 = bflo(r[1]) + rbf(gt[2] * rbf(a[2]));
        float y3 = bfhi(r[1]) + rbf(gt[3] * rbf(a[3]));
        u32x2 o;
        o[0] = pack2(y0, y1);
        o[1] = pack2(y2, y3);
        *(u32x2*)(out + (size_t)row * DM + col) = o;
      }
    }
  }
  STAMP_AT((KDIM == DM ? 3 : 5), 3);
}

typedef GCfg<256, 4, 1, 4, 4> CG;
template <int NH_>
DI void glu_wave(lds_ptr smem, Loader<CG>& ld, int lane, int wm, int t0, int m0, int f0, u16* __restrict__ out) {
  constexpr int NT = 2 * NH_;
  int waddr[NT];
#pragma unroll
  for (int n = 0; n < NH_; ++n) {
    waddr[n] = wfrag_addr(t0 + n, lane);
    waddr[NH_ + n] = waddr[n] + 8192;
  }
  const int aaddr = afrag_addr<256>(wm * 64, lane);
  f32x4 acc[NT][4];
#pragma unroll
  for (int n = 0; n < NT; ++n)
#pragma unroll
    for (int m = 0; m < 4; ++m) acc[n][m] = f32x4{0.f, 0.f, 0.f, 0.f};
  Engine<256, 4, 1, 4, 4, NT, 1, DM / 32, 4, 0, 4, (NH_ == 3)> eng(acc, smem, ld, (long)SEQ * 32, (long)32 * FF, aaddr, waddr, 0);
  eng.start();
  eng.run();
  const int g = lane >> 4;
#pragma unroll
  for (int n = 0; n < NH_; ++n) {
    const int cb = (f0 >> 4) + t0 + n;
#pragma unroll
    for (int m = 0; m < 4; m += 2) {
      u32x2 o[2];
#pragma unroll
      for (int i = 0; i < 2; ++i) {
        f32x4 a = acc[n][m + i], b = acc[NH_ + n][m + i];
        o[i][0] = pack2(rbf(a[0]) * rbf(b[0]), rbf(a[1]) * rbf(b[1]));
        o[i][1] = pack2(rbf(a[2]) * rbf(b[2]), rbf(a[3]) * rbf(b[3]));
      }
      const int row = m0 + wm * 64 + (m + (g & 1)) * 16 + (lane & 15);
      st16(out + ((size_t)cb * SEQ + row) * 16 + (g >> 1) * 8, pair16(o[0], o[1]));
    }
  }
  STAMP_AT(4, 3);
}

__global__ __launch_bounds__(512, 2) void k_glu(const u16* __restrict__ Ablk, const u16* __restrict__ Wg,
                                                 const u16* __restrict__ Wu, u16* __restrict__ out) {
  extern __shared__ __attribute__((aligned(16))) char smem_[];
  lds_ptr smem = (lds_ptr)smem_;
  const int lane = threadIdx.x & 63;
  const int w = __builtin_amdgcn_readfirstlane(threadIdx.x >> 6);
  const int xcd = blockIdx.x & 7, idx = blockIdx.x >> 3;
  const int tm = idx & 7, tn = xcd * 4 + (idx >> 3);
  const int m0 = tm * 256, f0 = tn * 112;
  Loader<CG> ld;
  const int lw = w & 3;
  setup_loader_A<CG, 256, 4, 1>(ld, Ablk, m0, lw, lane);
#pragma unroll
  for (int i = 0; i < CG::WPW; ++i) {
    int b = lw + 4 * i;
    int grp = b >> 2, o = b & 3;
    ld.woff[i] = CG::A_SUB + grp * 4096 + o * 1024;
    int k = 8 * o + (lane >> 3);
    int f = ((lane >> 4) & 1) | ((o & 1) << 1);
    int pr = ((lane & 7) >> 1) ^ f;
    if ((grp & 1) && pr == 3) pr = 0;
    int col = f0 + (grp & 1) * 64 + pr * 16 + (lane & 1) * 8;
    ld.wv[i] = (k * FF + col) * 2;
    ld.wbase[i] = (const char*)(grp >> 1 ? Wu : Wg);
  }
  const int wm = w & 3;
  if (w < 4) glu_wave<4>(smem, ld, lane, wm, 0, m0, f0, out);
  else glu_wave<3>(smem, ld, lane, wm, 4, m0, f0, out);
}

constexpr int ACH = 128, ANST = 4, ASTAGE = ACH * 64 * 2 + 256;
constexpr int ALDS = ANST * ASTAGE + 1024 + 16;

DI float rowmax16(const f32x16& v) {
  float mx = fmaxf(fmaxf(v[0], v[1]), v[2]);
#pragma unroll
  for (int r = 3; r < 15; r += 2) mx = fmaxf(fmaxf(mx, v[r]), v[r + 1]);
  mx = fmaxf(mx, v[15]);
  auto sw = __builtin_amdgcn_permlane32_swap(__float_as_uint(mx), __float_as_uint(mx), false, false);
  return fmaxf(__uint_as_float(sw[0]), __uint_as_float(sw[1]));
}

template <bool SAFE>
DI void attn_pass(lds_ptr smem, int lane, int w, const char* Kp, const char* Vp, const char* pf_wm, const char* pf_wo, const char* pf_wg,
                  const char* pf_wu, const char* pf_wd, const bf16x8& qa0, const bf16x8& qa1, const bf16x8& qb0,
                  const bf16x8& qb1, f32x16& oa, f32x16& ob, float& refa, float& refb, float& la_out, float& lb_out,
                  ModTask& mt, const u16* c) {
  const int r32 = lane & 31, hh = lane >> 5, kh = w >> 2;
  const unsigned kvo = ((32 * (w & 3) + (lane >> 1)) * 16 + (((lane & 1) ^ ((lane >> 4) & 1)) * 8)) * 2;
  const unsigned vvo = ((32 * (w & 3)) * 16) * 2 + lane * 16;
  const char* ksrc = Kp;
  const char* vsrc = Vp;
  auto issue = [&](int buf) {
    if (w < 4) {
      const unsigned l0 = lds_u32(smem) + buf * ASTAGE + w * 1024;
      glds16(ksrc, kvo, l0);
      glds16(ksrc + SEQ * 32, kvo, l0 + 4096);
      glds16(vsrc, vvo, l0 + 8192);
      glds16(vsrc + SEQ * 32, vvo, l0 + 8192 + 4096 + 128);
    }
    ksrc += ACH * 32; vsrc += ACH * 32;
  };
  const unsigned pfo = lane * 16;
  auto prefetch = [&](int ch) {
    if (w >= 4) {
#pragma unroll
      for (int i = 0; i < 2; ++i) {
        int pc = (ch * 2 + i) * 4 + (w - 4);
        const char* base;
        if (pc < 32) base = pf_wm + (size_t)(blockIdx.x * 4 + (pc >> 3)) * (12 * DM) + 4 * DM + (pc & 7) * 1024;
        else if (pc < 60) base = pf_wd + (size_t)blockIdx.x * 28672 + (pc - 32) * 1024;
        else if (pc < 88) base = pf_wg + (size_t)blockIdx.x * 28672 + (pc - 60) * 1024;
        else if (pc < 116) base = pf_wu + (size_t)blockIdx.x * 28672 + (pc - 88) * 1024;
        else break;
        glds16(base, pfo, lds_u32(smem) + ANST * ASTAGE);
      }
    }
  };
  issue(0); issue(1); issue(2);
  const int kaddr0 = kh * 2048 + r32 * 32 + ((hh ^ ((r32 >> 3) & 1)) * 16);
  const int kaddr1 = kaddr0 + 4096;
  const int vaddr = 8192 + kh * 2048 + ((lane >> 4) & 1) * (4096 + 128) + (4 * hh + ((lane & 15) >> 2)) * 32 + (lane & 3) * 8;
  bf16x8 sel;
  {
    const short one = ((lane & 15) == ((lane >> 4) & 1)) ? (short)0x3F80 : (short)0;
#pragma unroll
    for (int j = 0; j < 8; ++j) sel[j] = one;
  }
  f32x16 nma, nmb, sa, sb;
#pragma unroll
  for (int r = 0; r < 16; ++r) { oa[r] = 0.f; ob[r] = 0.f; nma[r] = 0.f; nmb[r] = 0.f; }
  f32x4 lsa = {0.f, 0.f, 0.f, 0.f}, lsb = {0.f, 0.f, 0.f, 0.f};
  float la = 0.f, lb = 0.f;
  constexpr int NCH = SEQ / ACH;
  if (w < 4) wait_vm<8>();
  bar();
  const bf16x8 kf0_ = lds_read128(smem + kaddr0);
  const bf16x8 kf1_ = lds_read128(smem + kaddr1);
  {
    sa = MFMA32(kf0_, qa0, nma); sa = MFMA32(kf1_, qa1, sa);
    sb = MFMA32(kf0_, qb0, nmb); sb = MFMA32(kf1_, qb1, sb);
    const float ma = rowmax16(sa), mb = rowmax16(sb);
#pragma unroll
    for (int r = 0; r < 16; ++r) { nma[r] = -ma; sa[r] -= ma; nmb[r] = -mb; sb[r] -= mb; }
  }
  if constexpr (SAFE) {
    auto half = [&](f32x16& s, f32x16& o, f32x16& nm, float& l, const bf16x8& q0f, const bf16x8& q1f,
                    const bf16x8& kn0, const bf16x8& kn1, const bf16x8& vf0, const bf16x8& vf1, bool has_next) {
      f32x16 p;
      const float dl = fmaxf(rowmax16(s), 0.f);
      const float f = __builtin_amdgcn_exp2f(-dl);
      float t = 0.f;
#pragma unroll
      for (int r = 0; r < 16; ++r) { nm[r] -= dl; o[r] *= f; p[r] = __builtin_amdgcn_exp2f(s[r] - dl); t += p[r]; }
      l = l * f + t;
      if (has_next) { s = MFMA32(kn0, q0f, nm); s = MFMA32(kn1, q1f, s); }
      u32x4 pa, pb;
#pragma unroll
      for (int j = 0; j < 4; ++j) { pa[j] = pack2(p[2 * j], p[2 * j + 1]); pb[j] = pack2(p[8 + 2 * j], p[8 + 2 * j + 1]); }
      o = MFMA32(vf0, __builtin_bit_cast(bf16x8, pa), o);
      o = MFMA32(vf1, __builtin_bit_cast(bf16x8, pb), o);
    };
    auto step = [&](lds_cptr vb, lds_cptr knext, bool has_next) {
      bf16x8 kn0, kn1;
      if (has_next) { kn0 = lds_read128(knext + kaddr0); kn1 = lds_read128(knext + kaddr1); }
      bf16x8 vf0 = lds_tr8(vb, vb + 256);
      bf16x8 vf1 = lds_tr8(vb + 512, vb + 768);
      half(sa, oa, nma, la, qa0, qa1, kn0, kn1, vf0, vf1, has_next);
      half(sb, ob, nmb, lb, qb0, qb1, kn0, kn1, vf0, vf1, has_next);
    };
    for (int ch = 0; ch < NCH; ++ch) {
      if (w < 4) { if (ch + 2 < NCH) wait_vm<4>(); else wait_vm<0>(); }
      bar();
      if (ch + 3 < NCH) issue((ch + 3) & 3);
      lds_cptr sbuf = smem + (ch & 3) * ASTAGE;
      lds_cptr nbuf = smem + ((ch + 1) & 3) * ASTAGE;
      step(sbuf + vaddr, sbuf + 1024, true);
      step(sbuf + 1024 + vaddr, nbuf, ch + 1 < NCH);
    }
  } else {
    u32x4 pa0, pa1, pb0, pb1;
    bf16x8 kc0 = kf0_, kc1 = kf1_;
    bf16x8 vp0, vp1;
#pragma unroll
    for (int j = 0; j < 4; ++j) { pb0[j] = 0u; pb1[j] = 0u; }
#pragma unroll
    for (int j = 0; j < 8; ++j) { vp0[j] = 0; vp1[j] = 0; }
#define SB_ __builtin_amdgcn_sched_barrier(0)
#define EX(i) e[i] = __builtin_amdgcn_exp2f(S[i])
#define PHASE(S, P0, P1, M0, M1, M2, M3, M4, M5, R0, R1) do { float e[16]; \
      M0; SB_; R0; EX(0); EX(1); EX(2); EX(3); SB_; \
      M1; SB_; R1; EX(4); EX(5); EX(6); EX(7); SB_; \
      M2; SB_; EX(8); EX(9); P0[0] = pack2(e[0], e[1]); P0[1] = pack2(e[2], e[3]); SB_; \
      M3; SB_; EX(10); EX(11); P0[2] = pack2(e[4], e[5]); P0[3] = pack2(e[6], e[7]); SB_; \
      M4; SB_; EX(12); EX(13); EX(14); EX(15); SB_; \
      M5; SB_; P1[0] = pack2(e[8], e[9]); P1[1] = pack2(e[10], e[11]); P1[2] = pack2(e[12], e[13]); P1[3] = pack2(e[14], e[15]); SB_; \
    } while (0)
#define BF(x) __builtin_bit_cast(bf16x8, x)
    auto step = [&](lds_cptr vb, lds_cptr knext, auto has_next_t) {
      constexpr bool HN = decltype(has_next_t)::value;
      bf16x8 kn0, kn1, vc0, vc1;
      { const f32x16& S = sa;
        PHASE(S, pa0, pa1,
              sb = MFMA32(kc0, qb0, nmb), sb = MFMA32(kc1, qb1, sb),
              ob = MFMA32(vp0, BF(pb0), ob), ob = MFMA32(vp1, BF(pb1), ob),
              lsb = MFMA16(sel, BF(pb0), lsb), lsb = MFMA16(sel, BF(pb1), lsb),
              if (HN) { kn0 = lds_read128(knext + kaddr0); kn1 = lds_read128(knext + kaddr1); },
              { vc0 = lds_tr8(vb, vb + 256); vc1 = lds_tr8(vb + 512, vb + 768); }); }
      { const f32x16& S = sb;
        PHASE(S, pb0, pb1,
              if (HN) sa = MFMA32(kn0, qa0, nma), if (HN) sa = MFMA32(kn1, qa1, sa),
              oa = MFMA32(vc0, BF(pa0), oa), oa = MFMA32(vc1, BF(pa1), oa),
              lsa = MFMA16(sel, BF(pa0), lsa), lsa = MFMA16(sel, BF(pa1), lsa),
              , ); }
      if (HN) { kc0 = kn0; kc1 = kn1; }
      vp0 = vc0; vp1 = vc1;
    };
    typedef std::integral_constant<bool, true> T_;
    typedef std::integral_constant<bool, false> F_;
    for (int ch = 0; ch < NCH - 1; ++ch) {
      if (w < 4) { if (ch + 2 < NCH) wait_vm<4>(); else wait_vm<0>(); }
      bar();
      if (ch + 3 < NCH) issue((ch + 3) & 3);
      prefetch(ch);
      lds_cptr sbuf = smem + (ch & 3) * ASTAGE;
      lds_cptr nbuf = smem + ((ch + 1) & 3) * ASTAGE;
      step(sbuf + vaddr, sbuf + 1024, T_{});
      step(sbuf + 1024 + vaddr, nbuf, T_{});
    }
    {
      const int task = blockIdx.x * 2 + (threadIdx.x >> 8);
      mod_load(mt, c, (const u16*)pf_wm, 32 + (task >> 3), task & 7, threadIdx.x & 255);
      lds_cptr sbuf = smem + ((NCH - 1) & 3) * ASTAGE;
      step(sbuf + vaddr, sbuf + 1024, T_{});
      step(sbuf + 1024 + vaddr, sbuf, F_{});
    }
    ob = MFMA32(vp0, BF(pb0), ob); ob = MFMA32(vp1, BF(pb1), ob);
    lsb = MFMA16(sel, BF(pb0), lsb); lsb = MFMA16(sel, BF(pb1), lsb);
#undef PHASE
#undef EX
#undef BF
  }
  refa = nma[0]; refb = nmb[0];
  if constexpr (SAFE) {
    auto s1 = __builtin_amdgcn_permlane32_swap(__float_as_uint(la), __float_as_uint(la), false, false);
    la_out = __uint_as_float(s1[0]) + __uint_as_float(s1[1]);
    auto s2 = __builtin_amdgcn_permlane32_swap(__float_as_uint(lb), __float_as_uint(lb), false, false);
    lb_out = __uint_as_float(s2[0]) + __uint_as_float(s2[1]);
  } else {
    const float a0 = __shfl(lsa[0], lane & 15), a1 = __shfl(lsa[1], lane & 15);
    const float b0 = __shfl(lsb[0], lane & 15), b1 = __shfl(lsb[1], lane & 15);
    la_out = (lane & 16) ? a1 : a0;
    lb_out = (lane & 16) ? b1 : b0;
  }
}

__global__ __launch_bounds__(512, 2) void k_attn(const u16* __restrict__ qkv, u16* __restrict__ oblk,
                                                  const u16* __restrict__ c, const u16* __restrict__ wmod, float* __restrict__ part,
                                                  const char* __restrict__ pf_wo, const char* __restrict__ pf_wg,
                                                  const char* __restrict__ pf_wu, const char* __restrict__ pf_wd) {
  extern __shared__ __attribute__((aligned(16))) char smem_[];
  lds_ptr smem = (lds_ptr)smem_;
  const int lane = threadIdx.x & 63;
  const int w = __builtin_amdgcn_readfirstlane(threadIdx.x >> 6);
  const int xcd = blockIdx.x & 7, idx = blockIdx.x >> 3;
  const int head = xcd * 4 + (idx >> 3), qb = idx & 7;
  const int r32 = lane & 31, hh = lane >> 5;
  const int qg = w & 3, kh = w >> 2;
  const u16* Qp = qkv + (size_t)head * SEQ * 32;
  const char* Kp = (const char*)(qkv + (size_t)(32 + head) * SEQ * 32);
  const char* Vp = (const char*)(qkv + (size_t)(64 + head) * SEQ * 32);
  const int q0 = qb * 256 + qg * 64;
  STAMP_AT(2, 0);
  __attribute__((address_space(3))) int* flag = (__attribute__((address_space(3))) int*)(smem + ANST * ASTAGE + 1024);
  if (threadIdx.x == 0) *flag = 0;
  bf16x8 qa0 = *(const bf16x8*)(Qp + (q0 + r32) * 16 + hh * 8);
  bf16x8 qa1 = *(const bf16x8*)(Qp + (SEQ + q0 + r32) * 16 + hh * 8);
  bf16x8 qb0 = *(const bf16x8*)(Qp + (q0 + 32 + r32) * 16 + hh * 8);
  bf16x8 qb1 = *(const bf16x8*)(Qp + (SEQ + q0 + 32 + r32) * 16 + hh * 8);
  f32x16 oa, ob;
  float nma, nmb, la, lb;
  ModTask mt;
  attn_pass<false>(smem, lane, w, Kp, Vp, (const char*)wmod, pf_wo, pf_wg, pf_wu, pf_wd, qa0, qa1, qb0, qb1, oa, ob, nma, nmb, la, lb, mt, c);
  {
    float big = fmaxf(fabsf(la), fabsf(lb));
#pragma unroll
    for (int r = 0; r < 16; ++r) big = fmaxf(big, fmaxf(fabsf(oa[r]), fabsf(ob[r])));
    bool bad = !(big < 1e30f);
#pragma unroll
    for (int r = 0; r < 16; ++r) bad = bad || (oa[r] != oa[r]) || (ob[r] != ob[r]);
    bad = bad || (la != la) || (lb != lb);
    if (__any(bad) && lane == 0) *flag = 1;
  }
  __syncthreads();
  if (__builtin_amdgcn_readfirstlane(*flag) != 0) {
    attn_pass<true>(smem, lane, w, Kp, Vp, (const char*)wmod, pf_wo, pf_wg, pf_wu, pf_wd, qa0, qa1, qb0, qb1, oa, ob, nma, nmb, la, lb, mt, c);
    __syncthreads();
  }
  STAMP_AT(2, 2);
  const int task = blockIdx.x * 2 + (threadIdx.x >> 8);
  {
    __attribute__((address_space(3))) float* xb = (__attribute__((address_space(3))) float*)smem;
    const f32x16& og = kh == 0 ? ob : oa;
    __attribute__((address_space(3))) float* dst = xb + w * (18 * 64) + lane;
#pragma unroll
    for (int r = 0; r < 16; ++r) dst[r * 64] = og[r];
    dst[16 * 64] = kh == 0 ? lb : la;
    dst[17 * 64] = kh == 0 ? nmb : nma;
    __syncthreads();
    const __attribute__((address_space(3))) float* src = xb + (w ^ 4) * (18 * 64) + lane;
    f32x16 om = kh == 0 ? oa : ob;
    const float nm_me = kh == 0 ? nma : nmb;
    const float nm_ot = src[17 * 64];
    const float nmin = fminf(nm_me, nm_ot);
    const float f_me = __builtin_amdgcn_exp2f(nmin - nm_me), f_ot = __builtin_amdgcn_exp2f(nmin - nm_ot);
    const float rl = 1.f / ((kh == 0 ? la : lb) * f_me + src[16 * 64] * f_ot);
    const float g_me = f_me * rl, g_ot = f_ot * rl;
    u16* op = oblk + ((size_t)head * 2 * SEQ + q0 + kh * 32 + r32) * 16 + 4 * hh;
#pragma unroll
    for (int g = 0; g < 4; ++g) {
      float v0 = om[4 * g] * g_me + src[(4 * g) * 64] * g_ot;
      float v1 = om[4 * g + 1] * g_me + src[(4 * g + 1) * 64] * g_ot;
      float v2 = om[4 * g + 2] * g_me + src[(4 * g + 2) * 64] * g_ot;
      float v3 = om[4 * g + 3] * g_me + src[(4 * g + 3) * 64] * g_ot;
      u32x2 v;
      v[0] = pack2(v0, v1);
      v[1] = pack2(v2, v3);
      *(u32x2*)(op + (g >> 1) * (SEQ * 16) + (g & 1) * 8) = v;
    }
  }
  mod_finish(mt, part, 32 + (task >> 3), task & 7, threadIdx.x & 255, (float*)(smem_ + 40960) + (threadIdx.x >> 8) * 256);
  wait_vm<0>();
  STAMP_AT(2, 3);
}

extern "C" void kernel_launch(void* const* d_in, const int* in_sizes, int n_in, void* d_out, int out_size,
                              void* d_ws, size_t ws_size, hipStream_t stream) {
  const u16* x = (const u16*)d_in[0];
  const u16* wqkv = (const u16*)d_in[1];
  const u16* wo = (const u16*)d_in[2];
  const u16* wg = (const u16*)d_in[4];
  const u16* wu = (const u16*)d_in[5];
  const u16* wd = (const u16*)d_in[6];
  const u16* c = (const u16*)d_in[7];
  const u16* wmod = (const u16*)d_in[8];
  u16* out = (u16*)d_out;
  char* ws = (char*)d_ws;
  float* part = (float*)ws;
  float* modf = (float*)(ws + (256 << 10));
  unsigned* flags = (unsigned*)(ws + (512 << 10));
  u16* h1 = (u16*)(ws + (1 << 20));
  u16* qkv = (u16*)(ws + (8 << 20));
  u16* ob = (u16*)(ws + (24 << 20));
  u16* x2 = (u16*)(ws + (32 << 20));
  u16* h2 = (u16*)(ws + (40 << 20));
  u16* hh = (u16*)(ws + (48 << 20));
  u16* wdp = (u16*)(ws + (64 << 20));
  u16* wop = (u16*)(ws + (80 << 20));

  hipFuncSetAttribute((const void*)k_qkv, hipFuncAttributeMaxDynamicSharedMemorySize, CQ::LDS);
  hipFuncSetAttribute((const void*)k_proj<DM, WDP>, hipFuncAttributeMaxDynamicSharedMemorySize, CP::LDS + 256);
  hipFuncSetAttribute((const void*)k_proj<FF, WDP>, hipFuncAttributeMaxDynamicSharedMemorySize, CP::LDS + 256);
  hipFuncSetAttribute((const void*)k_glu, hipFuncAttributeMaxDynamicSharedMemorySize, CG::LDS);
  hipFuncSetAttribute((const void*)k_attn, hipFuncAttributeMaxDynamicSharedMemorySize, ALDS);

  k_pre<<<256 + 256, 256, 0, stream>>>(c, wmod, part, (const char*)x, (const char*)wqkv, flags);
  k_ln<6, MNS, 192><<<256 + 192 + 64, 512, 0, stream>>>(x, part, h1, (const char*)wqkv, wo, wop);
  k_qkv<<<256, 768, CQ::LDS, stream>>>(h1, wqkv, qkv);
  k_attn<<<256, 512, ALDS, stream>>>(qkv, ob, c, wmod, part, (const char*)wo, (const char*)wg, (const char*)wu, (const char*)wd);
  k_proj<DM, WDP><<<256, 768, CP::LDS + 256, stream>>>(ob, wop, x, part + 2 * DM, x2);
  k_ln<7, MNS, 0><<<256 + 224, 512, 0, stream>>>(x2, part + 3 * DM, h2, (const char*)wqkv, wd, wdp);
  k_glu<<<256, 512, CG::LDS, stream>>>(h2, wg, wu, hh);
  k_proj<FF, WDP><<<256, 768, CP::LDS + 256, stream>>>(hh, wdp, x2, part + 5 * DM, out);
}
```

```cpp
#include <hip/hip_runtime.h>
#include <hip/hip_bf16.h>
#include <stdint.h>

typedef __attribute__((ext_vector_type(8))) short bf16x8;
typedef __attribute__((ext_vector_type(4))) short s16x4;
typedef __attribute__((ext_vector_type(4))) float f32x4;
typedef __attribute__((ext_vector_type(2))) float f32x2;
typedef __attribute__((ext_vector_type(16))) float f32x16;
typedef __attribute__((ext_vector_type(4))) unsigned u32x4;
typedef __attribute__((ext_vector_type(2))) unsigned u32x2;
typedef __attribute__((ext_vector_type(2))) __bf16 bf16x2_t;
typedef unsigned short u16;
typedef __attribute__((address_space(3))) char* lds_ptr;
typedef __attribute__((address_space(3))) const char* lds_cptr;

#define DI __device__ __forceinline__

constexpr int SEQ = 2048, DM = 1024, FF = 3584, NH = 32;

DI float bf2f(u16 x) { return __uint_as_float(((unsigned)x) << 16); }
DI float bflo(unsigned x) { return __uint_as_float(x << 16); }
DI float bfhi(unsigned x) { return __uint_as_float(x & 0xffff0000u); }
DI unsigned pack2(float a, float b) {
  f32x2 v = {a, b};
  bf16x2_t r = __builtin_convertvector(v, bf16x2_t);
  return __builtin_bit_cast(unsigned, r);
}
DI float rbf(float a) { return bflo(pack2(a, 0.f) & 0xffffu); }

#define MFMA16(a, b, c) __builtin_amdgcn_mfma_f32_16x16x32_bf16((a), (b), (c), 0, 0, 0)
#define MFMA32(a, b, c) __builtin_amdgcn_mfma_f32_32x32x16_bf16((a), (b), (c), 0, 0, 0)

DI void glds16(const void* base, unsigned voff, unsigned lds_addr) {
  unsigned sv;
  asm volatile("s_mov_b32 %0, m0\n\ts_mov_b32 m0, %3\n\ts_nop 0\n\tglobal_load_lds_dwordx4 %1, %2\n\ts_mov_b32 m0, %0"
               : "=&s"(sv) : "v"(voff), "s"(base), "s"(lds_addr) : "memory");
}
DI unsigned lds_u32(lds_ptr p) { return (unsigned)(uintptr_t)p; }
DI bf16x8 lds_read128(lds_cptr p) { return *(const __attribute__((address_space(3))) bf16x8*)p; }
DI s16x4 lds_tr(lds_cptr p) {
  typedef short v4i16_t __attribute__((ext_vector_type(4)));
  return __builtin_bit_cast(s16x4, __builtin_amdgcn_ds_read_tr16_b64_v4i16((__attribute__((address_space(3))) v4i16_t*)p));
}
DI bf16x8 lds_tr8(lds_cptr p0, lds_cptr p1) {
  s16x4 a = lds_tr(p0), b = lds_tr(p1);
  return __builtin_shufflevector(a, b, 0, 1, 2, 3, 4, 5, 6, 7);
}
#define STAMP_AT(kid, slot) do {} while (0)
template <int N> DI void wait_vm() { asm volatile("s_waitcnt vmcnt(%0)" ::"n"(N) : "memory"); }
DI void bar() { __builtin_amdgcn_s_barrier(); }

constexpr int MNS = 8;
struct ModTask { u32x4 w[4]; u16 cv[4]; };
DI void mod_load(ModTask& mt, const u16* __restrict__ c, const u16* __restrict__ wmod, int cb, int ks, int t) {
  const int cc = t & 7, rg = t >> 3;
  const int k0 = ks * 128 + rg;
  const u16* wp = wmod + (size_t)k0 * (6 * DM) + cb * 64 + cc * 8;
#pragma unroll
  for (int i = 0; i < 4; ++i) mt.w[i] = *(const u32x4*)(wp + (size_t)i * 32 * (6 * DM));
#pragma unroll
  for (int i = 0; i < 4; ++i) mt.cv[i] = c[k0 + 32 * i];
}
DI void mod_finish(const ModTask& mt, float* __restrict__ part, int cb, int ks, int t, float* s_red  ) {
  float acc[8];
#pragma unroll
  for (int j = 0; j < 8; ++j) acc[j] = 0.f;
#pragma unroll
  for (int i = 0; i < 4; ++i) {
    const float cv = bf2f(mt.cv[i]);
    const float sv = rbf(cv / (1.f + __expf(-cv)));
#pragma unroll
    for (int j = 0; j < 4; ++j) {
      acc[2 * j] += sv * bflo(mt.w[i][j]);
      acc[2 * j + 1] += sv * bfhi(mt.w[i][j]);
    }
  }
#pragma unroll
  for (int j = 0; j < 8; ++j) {
    float v = acc[j];
    v += __shfl_xor(v, 8);
    v += __shfl_xor(v, 16);
    v += __shfl_xor(v, 32);
    acc[j] = v;
  }
  const int lane = t & 63, wv = t >> 6;
  if (lane < 8) {
#pragma unroll
    for (int j = 0; j < 8; ++j) s_red[wv * 64 + lane * 8 + j] = acc[j];
  }
  __syncthreads();
  if (t < 64) part[ks * (6 * DM) + cb * 64 + t] = (s_red[t] + s_red[64 + t]) + (s_red[128 + t] + s_red[192 + t]);
}
DI f32x4 gate4(const float* __restrict__ gate, int col) {
  f32x4 gt = *(const f32x4*)(gate + col);
#pragma unroll
  for (int i = 1; i < MNS; ++i) gt += *(const f32x4*)(gate + i * (6 * DM) + col);
#pragma unroll
  for (int j = 0; j < 4; ++j) gt[j] = rbf(gt[j]);
  return gt;
}

DI void touch16k(const char* p, int t) {
  u32x4 v[4];
#pragma unroll
  for (int i = 0; i < 4; ++i) v[i] = *(const u32x4*)(p + i * 4096 + t * 16);
#pragma unroll
  for (int i = 0; i < 4; ++i) asm volatile("" ::"v"(v[i]));
}

__global__ __launch_bounds__(256) void k_pre(const u16* __restrict__ c, const u16* __restrict__ wmod, float* __restrict__ part,
                                              const char* __restrict__ x, const char* __restrict__ wqkv,
                                              unsigned* __restrict__ flags) {
  __shared__ float s_red[256];
  const int t = threadIdx.x, b = blockIdx.x;
  if (b < 256) STAMP_AT(0, 0);
  if (b < 256) {
    ModTask mt;
    mod_load(mt, c, wmod, b >> 3, b & 7, t);
    mod_finish(mt, part, b >> 3, b & 7, t, s_red);
    if (b == 0) flags[t * 16] = 0u;
  } else {
    touch16k(x + (size_t)(b - 256) * 16384, t);
  }
  if (b < 256) STAMP_AT(0, 3);
}

constexpr int WDP = DM + 64;
template <int KID, int NS, int NPF>
__global__ __launch_bounds__(512) void k_ln(const u16* __restrict__ x, const float* __restrict__ ss  ,
                                             u16* __restrict__ h16, const char* __restrict__ pf,
                                             const u16* __restrict__ wsrc, u16* __restrict__ wdst) {
  __shared__ __attribute__((aligned(16))) float sm[2048];
  const int t = threadIdx.x;
  if (blockIdx.x >= 256 + NPF) {
    const int r0 = (blockIdx.x - 256 - NPF) * 16 + (t >> 7);
    u32x4 v[4];
#pragma unroll
    for (int i = 0; i < 4; ++i) v[i] = *(const u32x4*)(wsrc + (size_t)(r0 + 4 * i) * DM + (t & 127) * 8);
#pragma unroll
    for (int i = 0; i < 4; ++i) *(u32x4*)(wdst + (size_t)(r0 + 4 * i) * WDP + (t & 127) * 8) = v[i];
    return;
  }
  if (blockIdx.x >= 256) {
    touch16k(pf + (size_t)((blockIdx.x - 256) * 2 + (t >> 8)) * 16384, t & 255);
    return;
  }
  const int lane = t & 63, wv = t >> 6;
  const int row = blockIdx.x * 8 + wv;
  STAMP_AT(KID, 0);
  const u16* xr = x + (size_t)row * DM;
  u32x4 v0 = *(const u32x4*)(xr + lane * 8);
  u32x4 v1 = *(const u32x4*)(xr + 512 + lane * 8);
  {
    f32x4 pv[NS];
#pragma unroll
    for (int i = 0; i < NS; ++i) pv[i] = *(const f32x4*)(ss + i * (6 * DM) + t * 4);
    f32x4 a = pv[0];
#pragma unroll
    for (int i = 1; i < NS; ++i) a += pv[i];
#pragma unroll
    for (int j = 0; j < 4; ++j) {
      float v = rbf(a[j]);
      a[j] = t < 256 ? v : rbf(1.f + v);
    }
    *(f32x4*)(sm + t * 4) = a;
  }
  float f[16];
#pragma unroll
  for (int j = 0; j < 4; ++j) {
    f[2 * j] = bflo(v0[j]); f[2 * j + 1] = bfhi(v0[j]);
    f[8 + 2 * j] = bflo(v1[j]); f[8 + 2 * j + 1] = bfhi(v1[j]);
  }
  float s = 0.f;
#pragma unroll
  for (int j = 0; j < 16; ++j) s += f[j];
#pragma unroll
  for (int o = 1; o < 64; o <<= 1) s += __shfl_xor(s, o);
  const float mean = s * (1.f / DM);
  float q = 0.f;
#pragma unroll
  for (int j = 0; j < 16; ++j) { float d = f[j] - mean; q += d * d; }
#pragma unroll
  for (int o = 1; o < 64; o <<= 1) q += __shfl_xor(q, o);
  const float mb = rbf(mean);
  const float vb = rbf(q * (1.f / DM));
  const float rden = 1.f / rbf(sqrtf(rbf(vb + 1e-5f)));
  __syncthreads();
#pragma unroll
  for (int half = 0; half < 2; ++half) {
    const int k0 = half * 512 + lane * 8;
    f32x4 sh0 = *(const f32x4*)(sm + k0), sh1 = *(const f32x4*)(sm + k0 + 4);
    f32x4 sc0 = *(const f32x4*)(sm + 1024 + k0), sc1 = *(const f32x4*)(sm + 1024 + k0 + 4);
    float y[8];
#pragma unroll
    for (int j = 0; j < 8; ++j) {
      float g = j < 4 ? sc0[j] : sc1[j - 4];
      float sh = j < 4 ? sh0[j] : sh1[j - 4];
      float d = rbf(f[half * 8 + j] - mb);
      float n = rbf(d * rden);
      y[j] = rbf(n * g) + sh;
    }
    u32x4 o;
#pragma unroll
    for (int j = 0; j < 4; ++j) o[j] = pack2(y[2 * j], y[2 * j + 1]);
    *(u32x4*)(h16 + ((size_t)(k0 >> 4) * SEQ + row) * 16 + (k0 & 15)) = o;
  }
  STAMP_AT(KID, 3);
}

DI void st16(void* p, u32x4 v) {
  *(u32x4*)p = v;
}
DI u32x4 pair16(u32x2 a, u32x2 b) {
  auto s0 = __builtin_amdgcn_permlane16_swap(a[0], b[0], false, false);
  auto s1 = __builtin_amdgcn_permlane16_swap(a[1], b[1], false, false);
  return u32x4{s0[0], s1[0], s0[1], s1[1]};
}

DI int hA(int r) { return (-r) & 3; }

template <int BM, int NGRP, int KS, int NST, int NLW_ = 8>
struct GCfg {
  static constexpr int NLW = NLW_;
  static constexpr int A_SUB = BM * 64;
  static constexpr int W_SUB = NGRP * 4096;
  static constexpr int STAGE = KS * (A_SUB + W_SUB);
  static constexpr int APW = KS * BM / 16 / NLW;
  static constexpr int WPW = KS * NGRP * 4 / NLW;
  static constexpr int PPW = APW + WPW;
  static constexpr int LDS = STAGE * NST;
};

template <class C>
struct Loader {
  const char* abase;
  const char* wbase[C::WPW];
  unsigned av[C::APW];
  unsigned wv[C::WPW];
  int aoff[C::APW];
  int woff[C::WPW];
};

template <int BM, int NGRP, int KS, int NST, int MT, int NT, int KSPLIT, int NKT, int KID, int IOFS, int NLW = 8, bool LOADS = (NLW == 8)>
struct Engine {
  typedef GCfg<BM, NGRP, KS, NST, NLW> C;
  static constexpr bool DED = !LOADS;
  static constexpr int SPW = KS / KSPLIT;
  static constexpr int P = C::PPW;
  static constexpr int G = NT * SPW;
  f32x4 (&acc)[NT][MT];
  lds_ptr smem;
  Loader<C>& ld;
  long a_adv, w_adv;
  const int (&waddr)[NT];
  int a_sub0, w_sub0;
  int cur, nxt, fre;
  bf16x8 wf[NT], af[2][MT];

  DI Engine(f32x4 (&acc_)[NT][MT], lds_ptr smem_, Loader<C>& ld_, long a_adv_, long w_adv_, int aaddr, const int (&waddr_)[NT], int kg)
      : acc(acc_), smem(smem_), ld(ld_), a_adv(a_adv_), w_adv(w_adv_), waddr(waddr_) {
    a_sub0 = kg * C::A_SUB + aaddr;
    w_sub0 = KS * C::A_SUB + kg * C::W_SUB;
  }
  template <int I> DI void piece(int stage_off) {
    if constexpr (I < C::APW) {
      glds16(ld.abase, ld.av[I], lds_u32(smem) + stage_off + ld.aoff[I]);
      if constexpr (I == C::APW - 1) ld.abase += a_adv * 2;
    } else {
      constexpr int J = I - C::APW;
      glds16(ld.wbase[J], ld.wv[J], lds_u32(smem) + stage_off + ld.woff[J]);
      ld.wbase[J] += w_adv * 2;
    }
  }
  template <int I> DI void issue_all(int stage_off) {
    if constexpr (I < P) { piece<I>(stage_off); issue_all<I + 1>(stage_off); }
  }
  template <int g, int I> DI void issue_at(int stage_off) {
    if constexpr (I < P) {
      constexpr int span = (G - IOFS) > P ? (G - IOFS) : P;
      constexpr int pos = IOFS + (I * span) / P < G ? IOFS + (I * span) / P : G - 1;
      if constexpr (pos == g) piece<I>(stage_off);
      issue_at<g, I + 1>(stage_off);
    }
  }
  template <int PAR, int S, bool PF, bool DOISSUE, int n>
  DI void groups(lds_cptr ab, lds_cptr wb) {
    if constexpr (n < NT) {
#pragma unroll
      for (int m = 0; m < MT; ++m) acc[n][m] = MFMA16(wf[n], af[PAR][m], acc[n][m]);
      if constexpr (PF) {
        wf[n] = lds_tr8(wb + waddr[n], wb + waddr[n] + 512);
#pragma unroll
        for (int m = 0; m < MT; ++m)
          if ((m * NT) / MT == n) af[PAR ^ 1][m] = lds_read128(ab + m * 512);
      }
      if constexpr (DOISSUE && !DED) issue_at<S * NT + n, 0>(fre);
      __builtin_amdgcn_sched_barrier(0);
      groups<PAR, S, PF, DOISSUE, n + 1>(ab, wb);
    }
  }
  template <int PAR, bool DOISSUE, bool LAST, int S> DI void steps() {
    if constexpr (S < SPW) {
      if constexpr (S + 1 < SPW)
        groups<PAR, S, true, DOISSUE, 0>(smem + cur + a_sub0 + (S + 1) * KSPLIT * C::A_SUB, smem + cur + w_sub0 + (S + 1) * KSPLIT * C::W_SUB);
      else if constexpr (!LAST)
        groups<PAR, S, true, DOISSUE, 0>(smem + nxt + a_sub0, smem + nxt + w_sub0);
      else
        groups<PAR, S, false, false, 0>(smem, smem);
      steps<PAR ^ 1, DOISSUE, LAST, S + 1>();
    }
  }
  template <int PAR, bool DOISSUE, bool LAST, int WAITN> DI void iter() {
    if constexpr (!LAST) { if constexpr (!DED) wait_vm<WAITN>(); bar(); }
    steps<PAR, DOISSUE, LAST, 0>();
    fre = cur; cur = nxt; nxt = (nxt + C::STAGE == NST * C::STAGE) ? 0 : nxt + C::STAGE;
  }
  template <int KT> DI void tail() {
    if constexpr (KT < NKT) {
      constexpr bool DOISSUE = KT + NST - 1 < NKT;
      constexpr bool LAST = KT == NKT - 1;
      constexpr int newest = (KT + NST - 2) < (NKT - 1) ? (KT + NST - 2) : (NKT - 1);
      constexpr int younger = newest - (KT + 1) > 0 ? newest - (KT + 1) : 0;
      iter<(KT * SPW) & 1, DOISSUE, LAST, P * younger>();
      tail<KT + 1>();
    }
  }
  DI void start() {
    static_assert(NKT >= NST + 1, "too few k tiles");
    STAMP_AT(KID, 0);
    if constexpr (!DED) {
#pragma unroll
      for (int s = 0; s < NST - 1; ++s) issue_all<0>(s * C::STAGE);
    }
  }
  template <int KT> DI void ltail() {
    if constexpr (KT < NKT - 1) {
      constexpr int newest = (KT + NST - 2) < (NKT - 1) ? (KT + NST - 2) : (NKT - 1);
      constexpr int younger = newest - (KT + 1) > 0 ? newest - (KT + 1) : 0;
      wait_vm<P * younger>();
      bar();
      ltail<KT + 1>();
    }
  }
  DI void loader_run() {
#pragma unroll
    for (int s = 0; s < NST - 1; ++s) issue_all<0>(s * C::STAGE);
    wait_vm<P*(NST - 2)>();
    bar();
    fre = (NST - 1) * C::STAGE;
    for (int kt = 0; kt < NKT - NST + 1; ++kt) {
      wait_vm<P*(NST - 3)>();
      bar();
      issue_all<0>(fre);
      fre = (fre + C::STAGE == NST * C::STAGE) ? 0 : fre + C::STAGE;
    }
    ltail<NKT - NST + 1>();
  }
  DI void run() { run([] {}); }
  template <class Hook> DI void run(Hook hook) {
    if constexpr (!DED) wait_vm<P*(NST - 2)>();
    bar();
    STAMP_AT(KID, 1);
    {
      lds_cptr ab = smem + a_sub0;
      lds_cptr wb = smem + w_sub0;
#pragma unroll
      for (int m = 0; m < MT; ++m) af[0][m] = lds_read128(ab + m * 512);
#pragma unroll
      for (int n = 0; n < NT; ++n) wf[n] = lds_tr8(wb + waddr[n], wb + waddr[n] + 512);
    }
    hook();
    cur = 0; nxt = C::STAGE; fre = (NST - 1) * C::STAGE;
    constexpr int STEADY = NKT - NST + 1;
    constexpr int LOOPN = (STEADY / 2) * 2;
    for (int kt = 0; kt < LOOPN; kt += 2) {
      iter<0, true, false, P*(NST - 3)>();
      iter<SPW & 1, true, false, P*(NST - 3)>();
    }
    tail<LOOPN>();
    STAMP_AT(KID, 2);
  }
};

template <class C, int BM, int NGRP, int KS>
DI void setup_loader_A(Loader<C>& ld, const u16* Ablk, int m0, int w, int lane) {
#pragma unroll
  for (int i = 0; i < C::APW; ++i) {
    int a = w + C::NLW * i;
    int blk = a / (BM / 32), j = a % (BM / 32);
    ld.aoff[i] = blk * (BM * 32) + j * 1024;
    ld.av[i] = ((blk * SEQ + m0 + j * 32) * 16) * 2 + lane * 16;
  }
  ld.abase = (const char*)Ablk;
}

DI int wfrag_addr(int t, int lane) {
  int g = lane >> 4, q = (lane & 15) >> 2, p = lane & 3;
  int f = ((q >> 1) & 1) | ((g & 1) << 1);
  return (t >> 2) * 4096 + (8 * g + q) * 128 + (((t & 3) ^ f) * 32) + p * 8;
}
template <int BM>
DI int afrag_addr(int mrow0, int lane) {
  int r = lane & 15, g = lane >> 4;
  return (g >> 1) * (BM * 32) + (mrow0 + r) * 32 + (g & 1) * 16;
}

constexpr float QSCALE = 0.17677669529663687f * 1.4426950408889634f;
typedef GCfg<128, 3, 2, 4, 4> CQ;
__global__ __launch_bounds__(768, 3) void k_qkv(const u16* __restrict__ Ablk, const u16* __restrict__ W,
                                                 u16* __restrict__ out) {
  extern __shared__ __attribute__((aligned(16))) char smem_[];
  lds_ptr smem = (lds_ptr)smem_;
  const int lane = threadIdx.x & 63;
  const int w = __builtin_amdgcn_readfirstlane(threadIdx.x >> 6);
  const int xcd = blockIdx.x & 7, idx = blockIdx.x >> 3;
  const int tm = (xcd & 1) * 8 + (idx & 7), tn = (xcd >> 1) * 4 + (idx >> 3);
  const int m0 = tm * 128, n0 = tn * 192;
  constexpr int LDW = 3 * DM;
  Loader<CQ> ld;
  const int lw = w & 3;
  setup_loader_A<CQ, 128, 3, 2>(ld, Ablk, m0, lw, lane);
#pragma unroll
  for (int i = 0; i < CQ::WPW; ++i) {
    int b = lw + 4 * i;
    int sub = b / 12, r = b % 12, grp = r >> 2, o = r & 3;
    ld.woff[i] = 2 * CQ::A_SUB + sub * CQ::W_SUB + grp * 4096 + o * 1024;
    int k = sub * 32 + 8 * o + (lane >> 3);
    int f = ((lane >> 4) & 1) | ((o & 1) << 1);
    int col = n0 + grp * 64 + ((((lane & 7) >> 1) ^ f) * 16) + (lane & 1) * 8;
    ld.wv[i] = (k * LDW + col) * 2;
    ld.wbase[i] = (const char*)W;
  }
  const int wm = w & 1, wn = (w >> 1) & 3;
  int waddr[3];
#pragma unroll
  for (int n = 0; n < 3; ++n) waddr[n] = wfrag_addr(wn * 3 + n, lane);
  const int aaddr = afrag_addr<128>(wm * 64, lane);
  f32x4 acc[3][4];
#pragma unroll
  for (int n = 0; n < 3; ++n)
#pragma unroll
    for (int m = 0; m < 4; ++m) acc[n][m] = f32x4{0.f, 0.f, 0.f, 0.f};
  Engine<128, 3, 2, 4, 4, 3, 1, DM / 64, 1, 0, 4> eng(acc, smem, ld, (long)2 * SEQ * 32, (long)64 * LDW, aaddr, waddr, 0);
  if (w >= 8) { eng.loader_run(); return; }
  eng.start();
  eng.run();
  const int g = lane >> 4;
#pragma unroll
  for (int n = 0; n < 3; ++n) {
    const int cb = (n0 >> 4) + wn * 3 + n;
    const float sc = cb < DM / 16 ? QSCALE : 1.f;
#pragma unroll
    for (int m = 0; m < 4; m += 2) {
      u32x2 a, b;
      a[0] = pack2(acc[n][m][0] * sc, acc[n][m][1] * sc);
      a[1] = pack2(acc[n][m][2] * sc, acc[n][m][3] * sc);
      b[0] = pack2(acc[n][m + 1][0] * sc, acc[n][m + 1][1] * sc);
      b[1] = pack2(acc[n][m + 1][2] * sc, acc[n][m + 1][3] * sc);
      const int row = m0 + wm * 64 + (m + (g & 1)) * 16 + (lane & 15);
      st16(out + ((size_t)cb * SEQ + row) * 16 + (g >> 1) * 8, pair16(a, b));
    }
  }
  STAMP_AT(1, 3);
}

typedef GCfg<128, 1, 2, 6, 4> CP;
template <int KDIM, int LDW>
__global__ __launch_bounds__(768, 3) void k_proj(const u16* __restrict__ Ablk, const u16* __restrict__ W,
                                                  const u16* __restrict__ res, const float* __restrict__ gate,
                                                  u16* __restrict__ out) {
  extern __shared__ __attribute__((aligned(16))) char smem_[];
  lds_ptr smem = (lds_ptr)smem_;
  const int lane = threadIdx.x & 63;
  const int w = __builtin_amdgcn_readfirstlane(threadIdx.x >> 6);
  const int xcd = blockIdx.x & 7, idx = blockIdx.x >> 3;
  const int tm = (xcd & 3) * 4 + (idx & 3), tn = (xcd >> 2) * 8 + (idx >> 2);
  const int m0 = tm * 128, n0 = tn * 64;
  Loader<CP> ld;
  const int lw = w & 3;
  setup_loader_A<CP, 128, 1, 2>(ld, Ablk, m0, lw, lane);
#pragma unroll
  for (int i = 0; i < CP::WPW; ++i) {
    int b = lw + 4 * i;
    int sub = b >> 2, o = b & 3;
    ld.woff[i] = 2 * CP::A_SUB + sub * CP::W_SUB + o * 1024;
    int k = sub * 32 + 8 * o + (lane >> 3);
    int f = ((lane >> 4) & 1) | ((o & 1) << 1);
    int col = n0 + ((((lane & 7) >> 1) ^ f) * 16) + (lane & 1) * 8;
    ld.wv[i] = (k * LDW + col) * 2;
    ld.wbase[i] = (const char*)W;
  }
  const int wm = w & 1, wn = (w >> 1) & 1, kg = (w >> 2) & 1;
  int waddr[2];
#pragma unroll
  for (int n = 0; n < 2; ++n) waddr[n] = wfrag_addr(wn * 2 + n, lane);
  const int aaddr = afrag_addr<128>(wm * 64, lane);
  f32x4 acc[2][4];
#pragma unroll
  for (int n = 0; n < 2; ++n)
#pragma unroll
    for (int m = 0; m < 4; ++m) acc[n][m] = f32x4{0.f, 0.f, 0.f, 0.f};
  Engine<128, 1, 2, 6, 4, 2, 2, KDIM / 64, (KDIM == DM ? 3 : 5), 0, 4> eng(acc, smem, ld, (long)2 * SEQ * 32, (long)64 * LDW, aaddr, waddr, kg);
  if (w >= 8) { eng.loader_run(); return; }
  const int g = lane >> 4;
  const int colw = n0 + (wn * 2 + kg) * 16;
  u32x2 rr[4];
  f32x4 gp[MNS];
  eng.start();
  eng.run();
#pragma unroll
  for (int m = 0; m < 4; ++m)
    rr[m] = *(const u32x2*)(res + (size_t)(m0 + wm * 64 + m * 16 + (lane & 15)) * DM + colw + 4 * g);
#pragma unroll
  for (int i = 0; i < MNS; ++i) gp[i] = *(const f32x4*)(gate + i * (6 * DM) + colw + 4 * g);
  __builtin_amdgcn_s_barrier();
  __attribute__((address_space(3))) f32x4* red = (__attribute__((address_space(3))) f32x4*)smem;
  f32x4 kp[4];
  if (kg == 0) {
#pragma unroll
    for (int m = 0; m < 4; ++m) { red[(w * 4 + m) * 64 + lane] = acc[1][m]; kp[m] = acc[0][m]; }
  } else {
#pragma unroll
    for (int m = 0; m < 4; ++m) { red[(w * 4 + m) * 64 + lane] = acc[0][m]; kp[m] = acc[1][m]; }
  }
  asm volatile("s_waitcnt lgkmcnt(0)" ::: "memory");
  __builtin_amdgcn_s_barrier();
  asm volatile("" ::: "memory");
  {
    f32x4 gt = gp[0];
#pragma unroll
    for (int i = 1; i < MNS; ++i) gt += gp[i];
#pragma unroll
    for (int j = 0; j < 4; ++j) gt[j] = rbf(gt[j]);
#pragma unroll
    for (int m = 0; m < 4; m += 2) {
      u32x2 o[2];
#pragma unroll
      for (int i = 0; i < 2; ++i) {
        f32x4 a = kp[m + i] + red[((w ^ 4) * 4 + m + i) * 64 + lane];
        u32x2 r = rr[m + i];
        o[i][0] = pack2(bflo(r[0]) + rbf(gt[0] * rbf(a[0])), bfhi(r[0]) + rbf(gt[1] * rbf(a[1])));
        o[i][1] = pack2(bflo(r[1]) + rbf(gt[2] * rbf(a[2])), bfhi(r[1]) + rbf(gt[3] * rbf(a[3])));
      }
      const int row = m0 + wm * 64 + (m + (g & 1)) * 16 + (lane & 15);
      *(u32x4*)(out + (size_t)row * DM + colw + (g >> 1) * 8) = pair16(o[0], o[1]);
    }
  }
  STAMP_AT((KDIM == DM ? 3 : 5), 3);
}

typedef GCfg<256, 4, 1, 4, 4> CG;
template <int NH_>
DI void glu_wave(lds_ptr smem, Loader<CG>& ld, int lane, int wm, int t0, int m0, int f0, u16* __restrict__ out) {
  constexpr int NT = 2 * NH_;
  int waddr[NT];
#pragma unroll
  for (int n = 0; n < NH_; ++n) {
    waddr[n] = wfrag_addr(t0 + n, lane);
    waddr[NH_ + n] = waddr[n] + 8192;
  }
  const int aaddr = afrag_addr<256>(wm * 64, lane);
  f32x4 acc[NT][4];
#pragma unroll
  for (int n = 0; n < NT; ++n)
#pragma unroll
    for (int m = 0; m < 4; ++m) acc[n][m] = f32x4{0.f, 0.f, 0.f, 0.f};
  Engine<256, 4, 1, 4, 4, NT, 1, DM / 32, 4, 0, 4, (NH_ == 3)> eng(acc, smem, ld, (long)SEQ * 32, (long)32 * FF, aaddr, waddr, 0);
  eng.start();
  eng.run();
  const int g = lane >> 4;
#pragma unroll
  for (int n = 0; n < NH_; ++n) {
    const int cb = (f0 >> 4) + t0 + n;
#pragma unroll
    for (int m = 0; m < 4; m += 2) {
      u32x2 o[2];
#pragma unroll
      for (int i = 0; i < 2; ++i) {
        f32x4 a = acc[n][m + i], b = acc[NH_ + n][m + i];
        o[i][0] = pack2(rbf(a[0]) * rbf(b[0]), rbf(a[1]) * rbf(b[1]));
        o[i][1] = pack2(rbf(a[2]) * rbf(b[2]), rbf(a[3]) * rbf(b[3]));
      }
      const int row = m0 + wm * 64 + (m + (g & 1)) * 16 + (lane & 15);
      st16(out + ((size_t)cb * SEQ + row) * 16 + (g >> 1) * 8, pair16(o[0], o[1]));
    }
  }
  STAMP_AT(4, 3);
}

__global__ __launch_bounds__(512, 2) void k_glu(const u16* __restrict__ Ablk, const u16* __restrict__ Wg,
                                                 const u16* __restrict__ Wu, u16* __restrict__ out) {
  extern __shared__ __attribute__((aligned(16))) char smem_[];
  lds_ptr smem = (lds_ptr)smem_;
  const int lane = threadIdx.x & 63;
  const int w = __builtin_amdgcn_readfirstlane(threadIdx.x >> 6);
  const int xcd = blockIdx.x & 7, idx = blockIdx.x >> 3;
  const int tm = idx & 7, tn = xcd * 4 + (idx >> 3);
  const int m0 = tm * 256, f0 = tn * 112;
  Loader<CG> ld;
  const int lw = w & 3;
  setup_loader_A<CG, 256, 4, 1>(ld, Ablk, m0, lw, lane);
#pragma unroll
  for (int i = 0; i < CG::WPW; ++i) {
    int b = lw + 4 * i;
    int grp = b >> 2, o = b & 3;
    ld.woff[i] = CG::A_SUB + grp * 4096 + o * 1024;
    int k = 8 * o + (lane >> 3);
    int f = ((lane >> 4) & 1) | ((o & 1) << 1);
    int pr = ((lane & 7) >> 1) ^ f;
    if ((grp & 1) && pr == 3) pr = 0;
    int col = f0 + (grp & 1) * 64 + pr * 16 + (lane & 1) * 8;
    ld.wv[i] = (k * FF + col) * 2;
    ld.wbase[i] = (const char*)(grp >> 1 ? Wu : Wg);
  }
  const int wm = w & 3;
  if (w < 4) glu_wave<4>(smem, ld, lane, wm, 0, m0, f0, out);
  else glu_wave<3>(smem, ld, lane, wm, 4, m0, f0, out);
}

constexpr int ACH = 128, ANST = 4, ASTAGE = ACH * 64 * 2 + 256;
constexpr int ALDS = ANST * ASTAGE + 1024 + 16;

DI float rowmax16(const f32x16& v) {
  float mx = fmaxf(fmaxf(v[0], v[1]), v[2]);
#pragma unroll
  for (int r = 3; r < 15; r += 2) mx = fmaxf(fmaxf(mx, v[r]), v[r + 1]);
  mx = fmaxf(mx, v[15]);
  auto sw = __builtin_amdgcn_permlane32_swap(__float_as_uint(mx), __float_as_uint(mx), false, false);
  return fmaxf(__uint_as_float(sw[0]), __uint_as_float(sw[1]));
}

template <bool SAFE>
DI void attn_pass(lds_ptr smem, int lane, int w, const char* Kp, const char* Vp, const char* pf_wm, const char* pf_wo, const char* pf_wg,
                  const char* pf_wu, const char* pf_wd, const bf16x8& qa0, const bf16x8& qa1, const bf16x8& qb0,
                  const bf16x8& qb1, f32x16& oa, f32x16& ob, float& refa, float& refb, float& la_out, float& lb_out,
                  ModTask& mt, const u16* c) {
  const int r32 = lane & 31, hh = lane >> 5, kh = w >> 2;
  const unsigned kvo = ((32 * (w & 3) + (lane >> 1)) * 16 + (((lane & 1) ^ ((lane >> 4) & 1)) * 8)) * 2;
  const unsigned vvo = ((32 * (w & 3)) * 16) * 2 + lane * 16;
  const char* ksrc = Kp;
  const char* vsrc = Vp;
  auto issue = [&](int buf) {
    if (w < 4) {
      const unsigned l0 = lds_u32(smem) + buf * ASTAGE + w * 1024;
      glds16(ksrc, kvo, l0);
      glds16(ksrc + SEQ * 32, kvo, l0 + 4096);
      glds16(vsrc, vvo, l0 + 8192);
      glds16(vsrc + SEQ * 32, vvo, l0 + 8192 + 4096 + 128);
    }
    ksrc += ACH * 32; vsrc += ACH * 32;
  };
  const unsigned pfo = lane * 16;
  auto prefetch = [&](int ch) {
    if (w >= 4) {
#pragma unroll
      for (int i = 0; i < 3; ++i) {
        int pc = (ch * 3 + i) * 4 + (w - 4);
        const char* base;
        if (pc < 32) base = pf_wm + (size_t)(blockIdx.x * 4 + (pc >> 3)) * (12 * DM) + 4 * DM + (pc & 7) * 1024;
        else if (pc < 40) base = pf_wo + (size_t)blockIdx.x * 8192 + (pc - 32) * 1024;
        else if (pc < 68) base = pf_wd + (size_t)blockIdx.x * 28672 + (pc - 40) * 1024;
        else if (pc < 96) base = pf_wg + (size_t)blockIdx.x * 28672 + (pc - 68) * 1024;
        else if (pc < 124) base = pf_wu + (size_t)blockIdx.x * 28672 + (pc - 96) * 1024;
        else break;
        glds16(base, pfo, lds_u32(smem) + ANST * ASTAGE);
      }
    }
  };
  issue(0); issue(1); issue(2);
  const int kaddr0 = kh * 2048 + r32 * 32 + ((hh ^ ((r32 >> 3) & 1)) * 16);
  const int kaddr1 = kaddr0 + 4096;
  const int vaddr = 8192 + kh * 2048 + ((lane >> 4) & 1) * (4096 + 128) + (4 * hh + ((lane & 15) >> 2)) * 32 + (lane & 3) * 8;
  bf16x8 sel;
  {
    const short one = ((lane & 15) == ((lane >> 4) & 1)) ? (short)0x3F80 : (short)0;
#pragma unroll
    for (int j = 0; j < 8; ++j) sel[j] = one;
  }
  f32x16 nma, nmb, sa, sb;
#pragma unroll
  for (int r = 0; r < 16; ++r) { oa[r] = 0.f; ob[r] = 0.f; nma[r] = 0.f; nmb[r] = 0.f; }
  f32x4 lsa = {0.f, 0.f, 0.f, 0.f}, lsb = {0.f, 0.f, 0.f, 0.f};
  float la = 0.f, lb = 0.f;
  constexpr int NCH = SEQ / ACH;
  if (w < 4) wait_vm<8>();
  bar();
  const bf16x8 kf0_ = lds_read128(smem + kaddr0);
  const bf16x8 kf1_ = lds_read128(smem + kaddr1);
  {
    sa = MFMA32(kf0_, qa0, nma); sa = MFMA32(kf1_, qa1, sa);
    sb = MFMA32(kf0_, qb0, nmb); sb = MFMA32(kf1_, qb1, sb);
    const float ma = rowmax16(sa), mb = rowmax16(sb);
#pragma unroll
    for (int r = 0; r < 16; ++r) { nma[r] = -ma; sa[r] -= ma; nmb[r] = -mb; sb[r] -= mb; }
  }
  if constexpr (SAFE) {
    auto half = [&](f32x16& s, f32x16& o, f32x16& nm, float& l, const bf16x8& q0f, const bf16x8& q1f,
                    const bf16x8& kn0, const bf16x8& kn1, const bf16x8& vf0, const bf16x8& vf1, bool has_next) {
      f32x16 p;
      const float dl = fmaxf(rowmax16(s), 0.f);
      const float f = __builtin_amdgcn_exp2f(-dl);
      float t = 0.f;
#pragma unroll
      for (int r = 0; r < 16; ++r) { nm[r] -= dl; o[r] *= f; p[r] = __builtin_amdgcn_exp2f(s[r] - dl); t += p[r]; }
      l = l * f + t;
      if (has_next) { s = MFMA32(kn0, q0f, nm); s = MFMA32(kn1, q1f, s); }
      u32x4 pa, pb;
#pragma unroll
      for (int j = 0; j < 4; ++j) { pa[j] = pack2(p[2 * j], p[2 * j + 1]); pb[j] = pack2(p[8 + 2 * j], p[8 + 2 * j + 1]); }
      o = MFMA32(vf0, __builtin_bit_cast(bf16x8, pa), o);
      o = MFMA32(vf1, __builtin_bit_cast(bf16x8, pb), o);
    };
    auto step = [&](lds_cptr vb, lds_cptr knext, bool has_next) {
      bf16x8 kn0, kn1;
      if (has_next) { kn0 = lds_read128(knext + kaddr0); kn1 = lds_read128(knext + kaddr1); }
      bf16x8 vf0 = lds_tr8(vb, vb + 256);
      bf16x8 vf1 = lds_tr8(vb + 512, vb + 768);
      half(sa, oa, nma, la, qa0, qa1, kn0, kn1, vf0, vf1, has_next);
      half(sb, ob, nmb, lb, qb0, qb1, kn0, kn1, vf0, vf1, has_next);
    };
    for (int ch = 0; ch < NCH; ++ch) {
      if (w < 4) { if (ch + 2 < NCH) wait_vm<4>(); else wait_vm<0>(); }
      bar();
      if (ch + 3 < NCH) issue((ch + 3) & 3);
      lds_cptr sbuf = smem + (ch & 3) * ASTAGE;
      lds_cptr nbuf = smem + ((ch + 1) & 3) * ASTAGE;
      step(sbuf + vaddr, sbuf + 1024, true);
      step(sbuf + 1024 + vaddr, nbuf, ch + 1 < NCH);
    }
  } else {
    u32x4 pa0, pa1, pb0, pb1;
    bf16x8 kc0 = kf0_, kc1 = kf1_;
    bf16x8 vp0, vp1;
#pragma unroll
    for (int j = 0; j < 4; ++j) { pb0[j] = 0u; pb1[j] = 0u; }
#pragma unroll
    for (int j = 0; j < 8; ++j) { vp0[j] = 0; vp1[j] = 0; }
#define SB_ __builtin_amdgcn_sched_barrier(0)
#define EX(i) e[i] = __builtin_amdgcn_exp2f(S[i])
#define PHASE(S, P0, P1, M0, M1, M2, M3, M4, M5, R0, R1) do { float e[16]; \
      M0; SB_; R0; EX(0); EX(1); EX(2); EX(3); SB_; \
      M1; SB_; R1; EX(4); EX(5); EX(6); EX(7); SB_; \
      M2; SB_; EX(8); EX(9); P0[0] = pack2(e[0], e[1]); P0[1] = pack2(e[2], e[3]); SB_; \
      M3; SB_; EX(10); EX(11); P0[2] = pack2(e[4], e[5]); P0[3] = pack2(e[6], e[7]); SB_; \
      M4; SB_; EX(12); EX(13); EX(14); EX(15); SB_; \
      M5; SB_; P1[0] = pack2(e[8], e[9]); P1[1] = pack2(e[10], e[11]); P1[2] = pack2(e[12], e[13]); P1[3] = pack2(e[14], e[15]); SB_; \
    } while (0)
#define BF(x) __builtin_bit_cast(bf16x8, x)
    auto step = [&](lds_cptr vb, lds_cptr knext, auto has_next_t) {
      constexpr bool HN = decltype(has_next_t)::value;
      bf16x8 kn0, kn1, vc0, vc1;
      { const f32x16& S = sa;
        PHASE(S, pa0, pa1,
              sb = MFMA32(kc0, qb0, nmb), sb = MFMA32(kc1, qb1, sb),
              ob = MFMA32(vp0, BF(pb0), ob), ob = MFMA32(vp1, BF(pb1), ob),
              lsb = MFMA16(sel, BF(pb0), lsb), lsb = MFMA16(sel, BF(pb1), lsb),
              if (HN) { kn0 = lds_read128(knext + kaddr0); kn1 = lds_read128(knext + kaddr1); },
              { vc0 = lds_tr8(vb, vb + 256); vc1 = lds_tr8(vb + 512, vb + 768); }); }
      { const f32x16& S = sb;
        PHASE(S, pb0, pb1,
              if (HN) sa = MFMA32(kn0, qa0, nma), if (HN) sa = MFMA32(kn1, qa1, sa),
              oa = MFMA32(vc0, BF(pa0), oa), oa = MFMA32(vc1, BF(pa1), oa),
              lsa = MFMA16(sel, BF(pa0), lsa), lsa = MFMA16(sel, BF(pa1), lsa),
              , ); }
      if (HN) { kc0 = kn0; kc1 = kn1; }
      vp0 = vc0; vp1 = vc1;
    };
    typedef std::integral_constant<bool, true> T_;
    typedef std::integral_constant<bool, false> F_;
    for (int ch = 0; ch < NCH - 1; ++ch) {
      if (w < 4) { if (ch + 2 < NCH) wait_vm<4>(); else wait_vm<0>(); }
      bar();
      if (ch + 3 < NCH) issue((ch + 3) & 3);
      prefetch(ch);
      lds_cptr sbuf = smem + (ch & 3) * ASTAGE;
      lds_cptr nbuf = smem + ((ch + 1) & 3) * ASTAGE;
      step(sbuf + vaddr, sbuf + 1024, T_{});
      step(sbuf + 1024 + vaddr, nbuf, T_{});
    }
    {
      const int task = blockIdx.x * 2 + (threadIdx.x >> 8);
      mod_load(mt, c, (const u16*)pf_wm, 32 + (task >> 3), task & 7, threadIdx.x & 255);
      lds_cptr sbuf = smem + ((NCH - 1) & 3) * ASTAGE;
      step(sbuf + vaddr, sbuf + 1024, T_{});
      step(sbuf + 1024 + vaddr, sbuf, F_{});
    }
    ob = MFMA32(vp0, BF(pb0), ob); ob = MFMA32(vp1, BF(pb1), ob);
    lsb = MFMA16(sel, BF(pb0), lsb); lsb = MFMA16(sel, BF(pb1), lsb);
#undef PHASE
#undef EX
#undef BF
  }
  refa = nma[0]; refb = nmb[0];
  if constexpr (SAFE) {
    auto s1 = __builtin_amdgcn_permlane32_swap(__float_as_uint(la), __float_as_uint(la), false, false);
    la_out = __uint_as_float(s1[0]) + __uint_as_float(s1[1]);
    auto s2 = __builtin_amdgcn_permlane32_swap(__float_as_uint(lb), __float_as_uint(lb), false, false);
    lb_out = __uint_as_float(s2[0]) + __uint_as_float(s2[1]);
  } else {
    const float a0 = __shfl(lsa[0], lane & 15), a1 = __shfl(lsa[1], lane & 15);
    const float b0 = __shfl(lsb[0], lane & 15), b1 = __shfl(lsb[1], lane & 15);
    la_out = (lane & 16) ? a1 : a0;
    lb_out = (lane & 16) ? b1 : b0;
  }
}

__global__ __launch_bounds__(512, 2) void k_attn(const u16* __restrict__ qkv, u16* __restrict__ oblk,
                                                  const u16* __restrict__ c, const u16* __restrict__ wmod, float* __restrict__ part,
                                                  const char* __restrict__ pf_wo, const char* __restrict__ pf_wg,
                                                  const char* __restrict__ pf_wu, const char* __restrict__ pf_wd) {
  extern __shared__ __attribute__((aligned(16))) char smem_[];
  lds_ptr smem = (lds_ptr)smem_;
  const int lane = threadIdx.x & 63;
  const int w = __builtin_amdgcn_readfirstlane(threadIdx.x >> 6);
  const int xcd = blockIdx.x & 7, idx = blockIdx.x >> 3;
  const int head = xcd * 4 + (idx >> 3), qb = idx & 7;
  const int r32 = lane & 31, hh = lane >> 5;
  const int qg = w & 3, kh = w >> 2;
  const u16* Qp = qkv + (size_t)head * SEQ * 32;
  const char* Kp = (const char*)(qkv + (size_t)(32 + head) * SEQ * 32);
  const char* Vp = (const char*)(qkv + (size_t)(64 + head) * SEQ * 32);
  const int q0 = qb * 256 + qg * 64;
  STAMP_AT(2, 0);
  __attribute__((address_space(3))) int* flag = (__attribute__((address_space(3))) int*)(smem + ANST * ASTAGE + 1024);
  if (threadIdx.x == 0) *flag = 0;
  bf16x8 qa0 = *(const bf16x8*)(Qp + (q0 + r32) * 16 + hh * 8);
  bf16x8 qa1 = *(const bf16x8*)(Qp + (SEQ + q0 + r32) * 16 + hh * 8);
  bf16x8 qb0 = *(const bf16x8*)(Qp + (q0 + 32 + r32) * 16 + hh * 8);
  bf16x8 qb1 = *(const bf16x8*)(Qp + (SEQ + q0 + 32 + r32) * 16 + hh * 8);
  f32x16 oa, ob;
  float nma, nmb, la, lb;
  ModTask mt;
  attn_pass<false>(smem, lane, w, Kp, Vp, (const char*)wmod, pf_wo, pf_wg, pf_wu, pf_wd, qa0, qa1, qb0, qb1, oa, ob, nma, nmb, la, lb, mt, c);
  {
    float big = fmaxf(fabsf(la), fabsf(lb));
#pragma unroll
    for (int r = 0; r < 16; ++r) big = fmaxf(big, fmaxf(fabsf(oa[r]), fabsf(ob[r])));
    bool bad = !(big < 1e30f);
#pragma unroll
    for (int r = 0; r < 16; ++r) bad = bad || (oa[r] != oa[r]) || (ob[r] != ob[r]);
    bad = bad || (la != la) || (lb != lb);
    if (__any(bad) && lane == 0) *flag = 1;
  }
  __syncthreads();
  if (__builtin_amdgcn_readfirstlane(*flag) != 0) {
    attn_pass<true>(smem, lane, w, Kp, Vp, (const char*)wmod, pf_wo, pf_wg, pf_wu, pf_wd, qa0, qa1, qb0, qb1, oa, ob, nma, nmb, la, lb, mt, c);
    __syncthreads();
  }
  STAMP_AT(2, 2);
  const int task = blockIdx.x * 2 + (threadIdx.x >> 8);
  {
    __attribute__((address_space(3))) float* xb = (__attribute__((address_space(3))) float*)smem;
    const f32x16& og = kh == 0 ? ob : oa;
    __attribute__((address_space(3))) float* dst = xb + w * (18 * 64) + lane;
#pragma unroll
    for (int r = 0; r < 16; ++r) dst[r * 64] = og[r];
    dst[16 * 64] = kh == 0 ? lb : la;
    dst[17 * 64] = kh == 0 ? nmb : nma;
    __syncthreads();
    const __attribute__((address_space(3))) float* src = xb + (w ^ 4) * (18 * 64) + lane;
    f32x16 om = kh == 0 ? oa : ob;
    const float nm_me = kh == 0 ? nma : nmb;
    const float nm_ot = src[17 * 64];
    const float nmin = fminf(nm_me, nm_ot);
    const float f_me = __builtin_amdgcn_exp2f(nmin - nm_me), f_ot = __builtin_amdgcn_exp2f(nmin - nm_ot);
    const float rl = 1.f / ((kh == 0 ? la : lb) * f_me + src[16 * 64] * f_ot);
    const float g_me = f_me * rl, g_ot = f_ot * rl;
    u16* op = oblk + ((size_t)head * 2 * SEQ + q0 + kh * 32 + r32) * 16 + 4 * hh;
#pragma unroll
    for (int g = 0; g < 4; ++g) {
      float v0 = om[4 * g] * g_me + src[(4 * g) * 64] * g_ot;
      float v1 = om[4 * g + 1] * g_me + src[(4 * g + 1) * 64] * g_ot;
      float v2 = om[4 * g + 2] * g_me + src[(4 * g + 2) * 64] * g_ot;
      float v3 = om[4 * g + 3] * g_me + src[(4 * g + 3) * 64] * g_ot;
      u32x2 v;
      v[0] = pack2(v0, v1);
      v[1] = pack2(v2, v3);
      *(u32x2*)(op + (g >> 1) * (SEQ * 16) + (g & 1) * 8) = v;
    }
  }
  mod_finish(mt, part, 32 + (task >> 3), task & 7, threadIdx.x & 255, (float*)(smem_ + 40960) + (threadIdx.x >> 8) * 256);
  wait_vm<0>();
  STAMP_AT(2, 3);
}

extern "C" void kernel_launch(void* const* d_in, const int* in_sizes, int n_in, void* d_out, int out_size,
                              void* d_ws, size_t ws_size, hipStream_t stream) {
  const u16* x = (const u16*)d_in[0];
  const u16* wqkv = (const u16*)d_in[1];
  const u16* wo = (const u16*)d_in[2];
  const u16* wg = (const u16*)d_in[4];
  const u16* wu = (const u16*)d_in[5];
  const u16* wd = (const u16*)d_in[6];
  const u16* c = (const u16*)d_in[7];
  const u16* wmod = (const u16*)d_in[8];
  u16* out = (u16*)d_out;
  char* ws = (char*)d_ws;
  float* part = (float*)ws;
  float* modf = (float*)(ws + (256 << 10));
  unsigned* flags = (unsigned*)(ws + (512 << 10));
  u16* h1 = (u16*)(ws + (1 << 20));
  u16* qkv = (u16*)(ws + (8 << 20));
  u16* ob = (u16*)(ws + (24 << 20));
  u16* x2 = (u16*)(ws + (32 << 20));
  u16* h2 = (u16*)(ws + (40 << 20));
  u16* hh = (u16*)(ws + (48 << 20));
  u16* wdp = (u16*)(ws + (64 << 20));

  hipFuncSetAttribute((const void*)k_qkv, hipFuncAttributeMaxDynamicSharedMemorySize, CQ::LDS);
  hipFuncSetAttribute((const void*)k_proj<DM, DM>, hipFuncAttributeMaxDynamicSharedMemorySize, CP::LDS);
  hipFuncSetAttribute((const void*)k_proj<FF, WDP>, hipFuncAttributeMaxDynamicSharedMemorySize, CP::LDS);
  hipFuncSetAttribute((const void*)k_glu, hipFuncAttributeMaxDynamicSharedMemorySize, CG::LDS);
  hipFuncSetAttribute((const void*)k_attn, hipFuncAttributeMaxDynamicSharedMemorySize, ALDS);

  k_pre<<<256 + 256, 256, 0, stream>>>(c, wmod, part, (const char*)x, (const char*)wqkv, flags);
  k_ln<6, MNS, 192><<<256 + 192, 512, 0, stream>>>(x, part, h1, (const char*)wqkv, wd, wdp);
  k_qkv<<<256, 768, CQ::LDS, stream>>>(h1, wqkv, qkv);
  k_attn<<<256, 512, ALDS, stream>>>(qkv, ob, c, wmod, part, (const char*)wo, (const char*)wg, (const char*)wu, (const char*)wd);
  k_proj<DM, DM><<<256, 768, CP::LDS, stream>>>(ob, wo, x, part + 2 * DM, x2);
  k_ln<7, MNS, 0><<<256 + 224, 512, 0, stream>>>(x2, part + 3 * DM, h2, (const char*)wqkv, wd, wdp);
  k_glu<<<256, 512, CG::LDS, stream>>>(h2, wg, wu, hh);
  k_proj<FF, WDP><<<256, 768, CP::LDS, stream>>>(hh, wdp, x2, part + 5 * DM, out);
}
```

```cpp
#include <hip/hip_runtime.h>
#include <hip/hip_bf16.h>
#include <stdint.h>

typedef __attribute__((ext_vector_type(8))) short bf16x8;
typedef __attribute__((ext_vector_type(4))) short s16x4;
typedef __attribute__((ext_vector_type(4))) float f32x4;
typedef __attribute__((ext_vector_type(2))) float f32x2;
typedef __attribute__((ext_vector_type(16))) float f32x16;
typedef __attribute__((ext_vector_type(4))) unsigned u32x4;
typedef __attribute__((ext_vector_type(2))) unsigned u32x2;
typedef __attribute__((ext_vector_type(2))) __bf16 bf16x2_t;
typedef unsigned short u16;
typedef __attribute__((address_space(3))) char* lds_ptr;
typedef __attribute__((address_space(3))) const char* lds_cptr;

#define DI __device__ __forceinline__

constexpr int SEQ = 2048, DM = 1024, FF = 3584, NH = 32;

DI float bf2f(u16 x) { return __uint_as_float(((unsigned)x) << 16); }
DI float bflo(unsigned x) { return __uint_as_float(x << 16); }
DI float bfhi(unsigned x) { return __uint_as_float(x & 0xffff0000u); }
DI unsigned pack2(float a, float b) {
  f32x2 v = {a, b};
  bf16x2_t r = __builtin_convertvector(v, bf16x2_t);
  return __builtin_bit_cast(unsigned, r);
}
DI float rbf(float a) { return bflo(pack2(a, 0.f) & 0xffffu); }

#define MFMA16(a, b, c) __builtin_amdgcn_mfma_f32_16x16x32_bf16((a), (b), (c), 0, 0, 0)
#define MFMA32(a, b, c) __builtin_amdgcn_mfma_f32_32x32x16_bf16((a), (b), (c), 0, 0, 0)

DI void glds16(const void* base, unsigned voff, unsigned lds_addr) {
  unsigned sv;
  asm volatile("s_mov_b32 %0, m0\n\ts_mov_b32 m0, %3\n\ts_nop 0\n\tglobal_load_lds_dwordx4 %1, %2\n\ts_mov_b32 m0, %0"
               : "=&s"(sv) : "v"(voff), "s"(base), "s"(lds_addr) : "memory");
}
DI unsigned lds_u32(lds_ptr p) { return (unsigned)(uintptr_t)p; }
DI bf16x8 lds_read128(lds_cptr p) { return *(const __attribute__((address_space(3))) bf16x8*)p; }
DI s16x4 lds_tr(lds_cptr p) {
  typedef short v4i16_t __attribute__((ext_vector_type(4)));
  return __builtin_bit_cast(s16x4, __builtin_amdgcn_ds_read_tr16_b64_v4i16((__attribute__((address_space(3))) v4i16_t*)p));
}
DI bf16x8 lds_tr8(lds_cptr p0, lds_cptr p1) {
  s16x4 a = lds_tr(p0), b = lds_tr(p1);
  return __builtin_shufflevector(a, b, 0, 1, 2, 3, 4, 5, 6, 7);
}
#define STAMP_AT(kid, slot) do {} while (0)
template <int N> DI void wait_vm() { asm volatile("s_waitcnt vmcnt(%0)" ::"n"(N) : "memory"); }
DI void bar() { __builtin_amdgcn_s_barrier(); }

constexpr int MNS = 8;
struct ModTask { u32x4 w[4]; u16 cv[4]; };
DI void mod_load(ModTask& mt, const u16* __restrict__ c, const u16* __restrict__ wmod, int cb, int ks, int t) {
  const int cc = t & 7, rg = t >> 3;
  const int k0 = ks * 128 + rg;
  const u16* wp = wmod + (size_t)k0 * (6 * DM) + cb * 64 + cc * 8;
#pragma unroll
  for (int i = 0; i < 4; ++i) mt.w[i] = *(const u32x4*)(wp + (size_t)i * 32 * (6 * DM));
#pragma unroll
  for (int i = 0; i < 4; ++i) mt.cv[i] = c[k0 + 32 * i];
}
DI void mod_finish(const ModTask& mt, float* __restrict__ part, int cb, int ks, int t, float* s_red  ) {
  float acc[8];
#pragma unroll
  for (int j = 0; j < 8; ++j) acc[j] = 0.f;
#pragma unroll
  for (int i = 0; i < 4; ++i) {
    const float cv = bf2f(mt.cv[i]);
    const float sv = rbf(cv / (1.f + __expf(-cv)));
#pragma unroll
    for (int j = 0; j < 4; ++j) {
      acc[2 * j] += sv * bflo(mt.w[i][j]);
      acc[2 * j + 1] += sv * bfhi(mt.w[i][j]);
    }
  }
#pragma unroll
  for (int j = 0; j < 8; ++j) {
    float v = acc[j];
    v += __shfl_xor(v, 8);
    v += __shfl_xor(v, 16);
    v += __shfl_xor(v, 32);
    acc[j] = v;
  }
  const int lane = t & 63, wv = t >> 6;
  if (lane < 8) {
#pragma unroll
    for (int j = 0; j < 8; ++j) s_red[wv * 64 + lane * 8 + j] = acc[j];
  }
  __syncthreads();
  if (t < 64) part[ks * (6 * DM) + cb * 64 + t] = (s_red[t] + s_red[64 + t]) + (s_red[128 + t] + s_red[192 + t]);
}
DI f32x4 gate4(const float* __restrict__ gate, int col) {
  f32x4 gt = *(const f32x4*)(gate + col);
#pragma unroll
  for (int i = 1; i < MNS; ++i) gt += *(const f32x4*)(gate + i * (6 * DM) + col);
#pragma unroll
  for (int j = 0; j < 4; ++j) gt[j] = rbf(gt[j]);
  return gt;
}

DI void touch16k(const char* p, int t) {
  u32x4 v[4];
#pragma unroll
  for (int i = 0; i < 4; ++i) v[i] = *(const u32x4*)(p + i * 4096 + t * 16);
#pragma unroll
  for (int i = 0; i < 4; ++i) asm volatile("" ::"v"(v[i]));
}

__global__ __launch_bounds__(256) void k_pre(const u16* __restrict__ c, const u16* __restrict__ wmod, float* __restrict__ part,
                                              const char* __restrict__ x, const char* __restrict__ wqkv,
                                              unsigned* __restrict__ flags, float* __restrict__ stats) {
  __shared__ float s_red[256];
  const int t = threadIdx.x, b = blockIdx.x;
  if (b < 256) STAMP_AT(0, 0);
  if (b < 256) {
    ModTask mt;
    mod_load(mt, c, wmod, b >> 3, b & 7, t);
    mod_finish(mt, part, b >> 3, b & 7, t, s_red);
    if (b == 0) flags[t * 16] = 0u;
  } else if (b < 512) {
    const int lane = t & 63, wv = t >> 6;
    u32x4 v[2][2];
#pragma unroll
    for (int r = 0; r < 2; ++r) {
      const char* xr = x + (size_t)((b - 256) * 8 + wv * 2 + r) * (DM * 2);
      v[r][0] = *(const u32x4*)(xr + lane * 16);
      v[r][1] = *(const u32x4*)(xr + 1024 + lane * 16);
    }
#pragma unroll
    for (int r = 0; r < 2; ++r) {
      float f[16];
#pragma unroll
      for (int j = 0; j < 4; ++j) {
        f[2 * j] = bflo(v[r][0][j]); f[2 * j + 1] = bfhi(v[r][0][j]);
        f[8 + 2 * j] = bflo(v[r][1][j]); f[8 + 2 * j + 1] = bfhi(v[r][1][j]);
      }
      float s = 0.f;
#pragma unroll
      for (int j = 0; j < 16; ++j) s += f[j];
#pragma unroll
      for (int o = 1; o < 64; o <<= 1) s += __shfl_xor(s, o);
      const float mean = s * (1.f / DM);
      float q = 0.f;
#pragma unroll
      for (int j = 0; j < 16; ++j) { float d = f[j] - mean; q += d * d; }
#pragma unroll
      for (int o = 1; o < 64; o <<= 1) q += __shfl_xor(q, o);
      const float rden = 1.f / rbf(sqrtf(rbf(rbf(q * (1.f / DM)) + 1e-5f)));
      if (lane == 0) *(f32x2*)(stats + ((b - 256) * 8 + wv * 2 + r) * 2) = f32x2{-rbf(mean) * rden, rden};
    }
  } else {
    touch16k(wqkv + (size_t)(b - 512) * 16384, t);
  }
  if (b < 256) STAMP_AT(0, 3);
}

constexpr int WDP = DM + 64;
template <int KID, int NS, int NPF>
__global__ __launch_bounds__(512) void k_ln(const u16* __restrict__ x, const float* __restrict__ ss  ,
                                             u16* __restrict__ h16, const char* __restrict__ pf,
                                             const u16* __restrict__ wsrc, u16* __restrict__ wdst) {
  __shared__ __attribute__((aligned(16))) float sm[2048];
  const int t = threadIdx.x;
  if (blockIdx.x >= 256 + NPF) {
    const int r0 = (blockIdx.x - 256 - NPF) * 16 + (t >> 7);
    u32x4 v[4];
#pragma unroll
    for (int i = 0; i < 4; ++i) v[i] = *(const u32x4*)(wsrc + (size_t)(r0 + 4 * i) * DM + (t & 127) * 8);
#pragma unroll
    for (int i = 0; i < 4; ++i) *(u32x4*)(wdst + (size_t)(r0 + 4 * i) * WDP + (t & 127) * 8) = v[i];
    return;
  }
  if (blockIdx.x >= 256) {
    touch16k(pf + (size_t)((blockIdx.x - 256) * 2 + (t >> 8)) * 16384, t & 255);
    return;
  }
  const int lane = t & 63, wv = t >> 6;
  const int row = blockIdx.x * 8 + wv;
  STAMP_AT(KID, 0);
  const u16* xr = x + (size_t)row * DM;
  u32x4 v0 = *(const u32x4*)(xr + lane * 8);
  u32x4 v1 = *(const u32x4*)(xr + 512 + lane * 8);
  {
    f32x4 pv[NS];
#pragma unroll
    for (int i = 0; i < NS; ++i) pv[i] = *(const f32x4*)(ss + i * (6 * DM) + t * 4);
    f32x4 a = pv[0];
#pragma unroll
    for (int i = 1; i < NS; ++i) a += pv[i];
#pragma unroll
    for (int j = 0; j < 4; ++j) {
      float v = rbf(a[j]);
      a[j] = t < 256 ? v : rbf(1.f + v);
    }
    *(f32x4*)(sm + t * 4) = a;
  }
  float f[16];
#pragma unroll
  for (int j = 0; j < 4; ++j) {
    f[2 * j] = bflo(v0[j]); f[2 * j + 1] = bfhi(v0[j]);
    f[8 + 2 * j] = bflo(v1[j]); f[8 + 2 * j + 1] = bfhi(v1[j]);
  }
  float s = 0.f;
#pragma unroll
  for (int j = 0; j < 16; ++j) s += f[j];
#pragma unroll
  for (int o = 1; o < 64; o <<= 1) s += __shfl_xor(s, o);
  const float mean = s * (1.f / DM);
  float q = 0.f;
#pragma unroll
  for (int j = 0; j < 16; ++j) { float d = f[j] - mean; q += d * d; }
#pragma unroll
  for (int o = 1; o < 64; o <<= 1) q += __shfl_xor(q, o);
  const float mb = rbf(mean);
  const float vb = rbf(q * (1.f / DM));
  const float rden = 1.f / rbf(sqrtf(rbf(vb + 1e-5f)));
  __syncthreads();
#pragma unroll
  for (int half = 0; half < 2; ++half) {
    const int k0 = half * 512 + lane * 8;
    f32x4 sh0 = *(const f32x4*)(sm + k0), sh1 = *(const f32x4*)(sm + k0 + 4);
    f32x4 sc0 = *(const f32x4*)(sm + 1024 + k0), sc1 = *(const f32x4*)(sm + 1024 + k0 + 4);
    float y[8];
#pragma unroll
    for (int j = 0; j < 8; ++j) {
      float g = j < 4 ? sc0[j] : sc1[j - 4];
      float sh = j < 4 ? sh0[j] : sh1[j - 4];
      float d = rbf(f[half * 8 + j] - mb);
      float n = rbf(d * rden);
      y[j] = rbf(n * g) + sh;
    }
    u32x4 o;
#pragma unroll
    for (int j = 0; j < 4; ++j) o[j] = pack2(y[2 * j], y[2 * j + 1]);
    *(u32x4*)(h16 + ((size_t)(k0 >> 4) * SEQ + row) * 16 + (k0 & 15)) = o;
  }
  STAMP_AT(KID, 3);
}

DI void st16(void* p, u32x4 v) {
  *(u32x4*)p = v;
}
DI u32x4 pair16(u32x2 a, u32x2 b) {
  auto s0 = __builtin_amdgcn_permlane16_swap(a[0], b[0], false, false);
  auto s1 = __builtin_amdgcn_permlane16_swap(a[1], b[1], false, false);
  return u32x4{s0[0], s1[0], s0[1], s1[1]};
}

DI int hA(int r) { return (-r) & 3; }

template <int BM, int NGRP, int KS, int NST, int NLW_ = 8>
struct GCfg {
  static constexpr int NLW = NLW_;
  static constexpr int A_SUB = BM * 64;
  static constexpr int W_SUB = NGRP * 4096;
  static constexpr int STAGE = KS * (A_SUB + W_SUB);
  static constexpr int APW = KS * BM / 16 / NLW;
  static constexpr int WPW = KS * NGRP * 4 / NLW;
  static constexpr int PPW = APW + WPW;
  static constexpr int LDS = STAGE * NST;
};

template <class C>
struct Loader {
  const char* abase;
  const char* wbase[C::WPW];
  unsigned av[C::APW];
  unsigned wv[C::WPW];
  int aoff[C::APW];
  int woff[C::WPW];
};

template <int BM, int NGRP, int KS, int NST, int MT, int NT, int KSPLIT, int NKT, int KID, int IOFS, int NLW = 8, bool LOADS = (NLW == 8), int ARING = 0>
struct Engine {
  typedef GCfg<BM, NGRP, KS, NST, NLW> C;
  static constexpr bool DED = !LOADS;
  static constexpr int SPW = KS / KSPLIT;
  static constexpr int P = C::PPW;
  static constexpr int G = NT * SPW;
  static constexpr int WSTG = ARING ? KS * C::W_SUB : C::STAGE;
  static constexpr int ASTG = KS * C::A_SUB;
  static constexpr int ABASE = NST * WSTG;
  int acur, anxt;
  f32x4 (&acc)[NT][MT];
  lds_ptr smem;
  Loader<C>& ld;
  long a_adv, w_adv;
  const int (&waddr)[NT];
  int a_sub0, w_sub0;
  int cur, nxt, fre;
  bf16x8 wf[NT], af[2][MT];

  DI Engine(f32x4 (&acc_)[NT][MT], lds_ptr smem_, Loader<C>& ld_, long a_adv_, long w_adv_, int aaddr, const int (&waddr_)[NT], int kg)
      : acc(acc_), smem(smem_), ld(ld_), a_adv(a_adv_), w_adv(w_adv_), waddr(waddr_) {
    a_sub0 = kg * C::A_SUB + aaddr;
    w_sub0 = (ARING ? 0 : KS * C::A_SUB) + kg * C::W_SUB;
  }
  template <int I> DI void piece(int stage_off) {
    if constexpr (I < C::APW) {
      glds16(ld.abase, ld.av[I], lds_u32(smem) + stage_off + ld.aoff[I]);
      if constexpr (I == C::APW - 1) ld.abase += a_adv * 2;
    } else {
      constexpr int J = I - C::APW;
      glds16(ld.wbase[J], ld.wv[J], lds_u32(smem) + stage_off + ld.woff[J]);
      ld.wbase[J] += w_adv * 2;
    }
  }
  template <int I> DI void issue_all(int stage_off) {
    if constexpr (I < P) { piece<I>(stage_off); issue_all<I + 1>(stage_off); }
  }
  template <int g, int I> DI void issue_at(int stage_off) {
    if constexpr (I < P) {
      constexpr int span = (G - IOFS) > P ? (G - IOFS) : P;
      constexpr int pos = IOFS + (I * span) / P < G ? IOFS + (I * span) / P : G - 1;
      if constexpr (pos == g) piece<I>(stage_off);
      issue_at<g, I + 1>(stage_off);
    }
  }
  template <int PAR, int S, bool PF, bool DOISSUE, int n>
  DI void groups(lds_cptr ab, lds_cptr wb) {
    if constexpr (n < NT) {
#pragma unroll
      for (int m = 0; m < MT; ++m) acc[n][m] = MFMA16(wf[n], af[PAR][m], acc[n][m]);
      if constexpr (PF) {
        wf[n] = lds_tr8(wb + waddr[n], wb + waddr[n] + 512);
#pragma unroll
        for (int m = 0; m < MT; ++m)
          if ((m * NT) / MT == n) af[PAR ^ 1][m] = lds_read128(ab + m * 512);
      }
      if constexpr (DOISSUE && !DED) issue_at<S * NT + n, 0>(fre);
      __builtin_amdgcn_sched_barrier(0);
      groups<PAR, S, PF, DOISSUE, n + 1>(ab, wb);
    }
  }
  template <int PAR, bool DOISSUE, bool LAST, int S> DI void steps() {
    if constexpr (S < SPW) {
      if constexpr (S + 1 < SPW)
        groups<PAR, S, true, DOISSUE, 0>(smem + (ARING ? ABASE + acur : cur) + a_sub0 + (S + 1) * KSPLIT * C::A_SUB, smem + cur + w_sub0 + (S + 1) * KSPLIT * C::W_SUB);
      else if constexpr (!LAST)
        groups<PAR, S, true, DOISSUE, 0>(smem + (ARING ? ABASE + anxt : nxt) + a_sub0, smem + nxt + w_sub0);
      else
        groups<PAR, S, false, false, 0>(smem, smem);
      steps<PAR ^ 1, DOISSUE, LAST, S + 1>();
    }
  }
  template <int PAR, bool DOISSUE, bool LAST, int WAITN> DI void iter() {
    if constexpr (!LAST) { if constexpr (!DED) wait_vm<WAITN>(); bar(); }
    steps<PAR, DOISSUE, LAST, 0>();
    fre = cur; cur = nxt; nxt = (nxt + WSTG == NST * WSTG) ? 0 : nxt + WSTG;
    if constexpr (ARING > 0) { acur = anxt; anxt = (anxt + ASTG == ARING * ASTG) ? 0 : anxt + ASTG; }
  }
  template <int KT> DI void tail() {
    if constexpr (KT < NKT) {
      constexpr bool DOISSUE = KT + NST - 1 < NKT;
      constexpr bool LAST = KT == NKT - 1;
      constexpr int newest = (KT + NST - 2) < (NKT - 1) ? (KT + NST - 2) : (NKT - 1);
      constexpr int younger = newest - (KT + 1) > 0 ? newest - (KT + 1) : 0;
      iter<(KT * SPW) & 1, DOISSUE, LAST, P * younger>();
      tail<KT + 1>();
    }
  }
  DI void start() {
    static_assert(NKT >= NST + 1, "too few k tiles");
    STAMP_AT(KID, 0);
    if constexpr (!DED) {
#pragma unroll
      for (int s = 0; s < NST - 1; ++s) issue_all<0>(s * C::STAGE);
    }
  }
  template <int KT> DI void ltail() {
    if constexpr (KT < NKT - 1) {
      constexpr int newest = (KT + NST - 2) < (NKT - 1) ? (KT + NST - 2) : (NKT - 1);
      constexpr int younger = newest - (KT + 1) > 0 ? newest - (KT + 1) : 0;
      wait_vm<P * younger>();
      bar();
      ltail<KT + 1>();
    }
  }
  DI void loader_run() {
#pragma unroll
    for (int s = 0; s < NST - 1; ++s) issue_all<0>(s * C::STAGE);
    wait_vm<P*(NST - 2)>();
    bar();
    fre = (NST - 1) * C::STAGE;
    for (int kt = 0; kt < NKT - NST + 1; ++kt) {
      wait_vm<P*(NST - 3)>();
      bar();
      issue_all<0>(fre);
      fre = (fre + C::STAGE == NST * C::STAGE) ? 0 : fre + C::STAGE;
    }
    ltail<NKT - NST + 1>();
  }
  DI void run() { run([] {}); }
  template <class Hook> DI void run(Hook hook) {
    if constexpr (!DED) wait_vm<P*(NST - 2)>();
    bar();
    STAMP_AT(KID, 1);
    {
      lds_cptr ab = smem + (ARING ? ABASE : 0) + a_sub0;
      lds_cptr wb = smem + w_sub0;
#pragma unroll
      for (int m = 0; m < MT; ++m) af[0][m] = lds_read128(ab + m * 512);
#pragma unroll
      for (int n = 0; n < NT; ++n) wf[n] = lds_tr8(wb + waddr[n], wb + waddr[n] + 512);
    }
    hook();
    cur = 0; nxt = WSTG; fre = (NST - 1) * WSTG;
    acur = 0; anxt = ASTG;
    constexpr int STEADY = NKT - NST + 1;
    constexpr int LOOPN = (STEADY / 2) * 2;
    for (int kt = 0; kt < LOOPN; kt += 2) {
      iter<0, true, false, P*(NST - 3)>();
      iter<SPW & 1, true, false, P*(NST - 3)>();
    }
    tail<LOOPN>();
    STAMP_AT(KID, 2);
  }
};

template <class C, int BM, int NGRP, int KS>
DI void setup_loader_A(Loader<C>& ld, const u16* Ablk, int m0, int w, int lane) {
#pragma unroll
  for (int i = 0; i < C::APW; ++i) {
    int a = w + C::NLW * i;
    int blk = a / (BM / 32), j = a % (BM / 32);
    ld.aoff[i] = blk * (BM * 32) + j * 1024;
    ld.av[i] = ((blk * SEQ + m0 + j * 32) * 16) * 2 + lane * 16;
  }
  ld.abase = (const char*)Ablk;
}

DI int wfrag_addr(int t, int lane) {
  int g = lane >> 4, q = (lane & 15) >> 2, p = lane & 3;
  int f = ((q >> 1) & 1) | ((g & 1) << 1);
  return (t >> 2) * 4096 + (8 * g + q) * 128 + (((t & 3) ^ f) * 32) + p * 8;
}
template <int BM>
DI int afrag_addr(int mrow0, int lane) {
  int r = lane & 15, g = lane >> 4;
  return (g >> 1) * (BM * 32) + (mrow0 + r) * 32 + (g & 1) * 16;
}

constexpr float QSCALE = 0.17677669529663687f * 1.4426950408889634f;
typedef GCfg<128, 3, 2, 4, 4> CQ;
constexpr int QW_STG = 2 * CQ::W_SUB, QA_STG = 2 * CQ::A_SUB, QA_BASE = 4 * QW_STG, QT_BASE = QA_BASE + 3 * QA_STG;
constexpr int QLDS = QT_BASE + 8192;
DI void gld16(u32x4& d, const char* base, unsigned voff) {
  asm volatile("global_load_dwordx4 %0, %1, %2" : "=v"(d) : "v"(voff), "s"(base) : "memory");
}
template <int N> DI void wait_vm_pin(u32x4 (&r)[4]) {
  asm volatile("s_waitcnt vmcnt(%4)" : "+v"(r[0]), "+v"(r[1]), "+v"(r[2]), "+v"(r[3]) : "n"(N) : "memory");
}
__global__ __launch_bounds__(768, 3) void k_qkv(const u16* __restrict__ x, const float* __restrict__ stats,
                                                 const float* __restrict__ part, const u16* __restrict__ W,
                                                 u16* __restrict__ out) {
  extern __shared__ __attribute__((aligned(16))) char smem_[];
  lds_ptr smem = (lds_ptr)smem_;
  const int lane = threadIdx.x & 63;
  const int w = __builtin_amdgcn_readfirstlane(threadIdx.x >> 6);
  const int xcd = blockIdx.x & 7, idx = blockIdx.x >> 3;
  const int tm = (xcd & 1) * 8 + (idx & 7), tn = (xcd >> 1) * 4 + (idx >> 3);
  const int m0 = tm * 128, n0 = tn * 192;
  constexpr int LDW = 3 * DM, NKT = DM / 64;
  if (w >= 8) {
    const int lw = w - 8;
    unsigned wv[6]; int woff[6];
#pragma unroll
    for (int i = 0; i < 6; ++i) {
      int b = lw + 4 * i;
      int sub = b / 12, r = b % 12, grp = r >> 2, o = r & 3;
      woff[i] = sub * CQ::W_SUB + grp * 4096 + o * 1024;
      int k = sub * 32 + 8 * o + (lane >> 3);
      int f = ((lane >> 4) & 1) | ((o & 1) << 1);
      int col = n0 + grp * 64 + ((((lane & 7) >> 1) ^ f) * 16) + (lane & 1) * 8;
      wv[i] = (k * LDW + col) * 2;
    }
    const char* wbase = (const char*)W;
    auto wissue = [&](int stg) {
#pragma unroll
      for (int i = 0; i < 6; ++i) glds16(wbase, wv[i], lds_u32(smem) + stg * QW_STG + woff[i]);
      wbase += (size_t)64 * LDW * 2;
    };
    const int c8 = lane & 7, r8 = lane >> 3;
    const char* xbase = (const char*)x;
    const unsigned xv = ((m0 + lw * 32 + r8) * DM + c8 * 8) * 2;
    auto xissue = [&](u32x4 (&d)[4]) {
#pragma unroll
      for (int i = 0; i < 4; ++i) gld16(d[i], xbase + (size_t)i * 8 * DM * 2, xv);
      xbase += 128;
    };
    u32x4 xa[4], xb[4];
    xissue(xa); xissue(xb);
    wissue(0); wissue(1); wissue(2);
    f32x2 st[4];
#pragma unroll
    for (int i = 0; i < 4; ++i) st[i] = *(const f32x2*)(stats + (m0 + lw * 32 + i * 8 + r8) * 2);
    const int awr = QA_BASE + (c8 >> 1) * 4096 + (lw * 32 + r8) * 32 + (c8 & 1) * 16;
    auto xform = [&](const u32x4 (&d)[4], int stage, int abuf) {
      const __attribute__((address_space(3))) f32x4* tb =
          (const __attribute__((address_space(3))) f32x4*)(smem + QT_BASE) + stage * 16 + c8 * 2;
      const f32x4 s0 = tb[0], s1 = tb[1], g0 = tb[256], g1 = tb[257];
#pragma unroll
      for (int i = 0; i < 4; ++i) {
        u32x4 o;
#pragma unroll
        for (int j = 0; j < 4; ++j) {
          const float ga = j < 2 ? g0[2 * j] : g1[2 * j - 4], gb = j < 2 ? g0[2 * j + 1] : g1[2 * j - 3];
          const float sa = j < 2 ? s0[2 * j] : s1[2 * j - 4], sb = j < 2 ? s0[2 * j + 1] : s1[2 * j - 3];
          const float na = __builtin_fmaf(bflo(d[i][j]), st[i][1], st[i][0]);
          const float nb = __builtin_fmaf(bfhi(d[i][j]), st[i][1], st[i][0]);
          o[j] = pack2(__builtin_fmaf(na, ga, sa), __builtin_fmaf(nb, gb, sb));
        }
        *(__attribute__((address_space(3))) u32x4*)(smem + awr + abuf * QA_STG + i * 256) = o;
      }
    };
    bar();
    wait_vm_pin<0>(xa); wait_vm_pin<0>(xb);
    xform(xa, 0, 0); xissue(xa);
    xform(xb, 1, 1); xissue(xb);
    asm volatile("s_waitcnt lgkmcnt(0)" ::: "memory");
    bar();
    int abuf = 2, wstg = 3;
#pragma unroll 1
    for (int kt = 0; kt < NKT - 4; kt += 2) {
      bar();
      if (kt == 0) wait_vm_pin<4>(xa); else wait_vm_pin<16>(xa);
      xform(xa, kt + 2, abuf); xissue(xa); wissue(wstg);
      abuf = abuf == 2 ? 0 : abuf + 1; wstg = (wstg + 1) & 3;
      if (kt == 0) asm volatile("s_waitcnt lgkmcnt(0)" ::: "memory");
      else asm volatile("s_waitcnt vmcnt(10) lgkmcnt(0)" ::: "memory");
      bar();
      if (kt == 0) wait_vm_pin<10>(xb); else wait_vm_pin<16>(xb);
      xform(xb, kt + 3, abuf); xissue(xb); wissue(wstg);
      abuf = abuf == 2 ? 0 : abuf + 1; wstg = (wstg + 1) & 3;
      asm volatile("s_waitcnt vmcnt(10) lgkmcnt(0)" ::: "memory");
    }
    bar();
    wait_vm_pin<0>(xa); wait_vm_pin<0>(xb);
    xform(xa, NKT - 2, abuf); wissue(wstg);
    abuf = abuf == 2 ? 0 : abuf + 1;
    asm volatile("s_waitcnt lgkmcnt(0)" ::: "memory");
    bar();
    xform(xb, NKT - 1, abuf);
    asm volatile("s_waitcnt vmcnt(0) lgkmcnt(0)" ::: "memory");
    bar();
    return;
  }
  {
    const int t = threadIdx.x;
    f32x4 a = *(const f32x4*)(part + t * 4);
#pragma unroll
    for (int i = 1; i < MNS; ++i) a += *(const f32x4*)(part + i * (6 * DM) + t * 4);
#pragma unroll
    for (int j = 0; j < 4; ++j) {
      float v = rbf(a[j]);
      a[j] = t < 256 ? v : rbf(1.f + v);
    }
    *(__attribute__((address_space(3))) f32x4*)(smem + QT_BASE + t * 16) = a;
    asm volatile("s_waitcnt lgkmcnt(0)" ::: "memory");
    bar();
  }
  Loader<CQ> ld;
  const int wm = w & 1, wn = (w >> 1) & 3;
  int waddr[3];
#pragma unroll
  for (int n = 0; n < 3; ++n) waddr[n] = wfrag_addr(wn * 3 + n, lane);
  const int aaddr = afrag_addr<128>(wm * 64, lane);
  f32x4 acc[3][4];
#pragma unroll
  for (int n = 0; n < 3; ++n)
#pragma unroll
    for (int m = 0; m < 4; ++m) acc[n][m] = f32x4{0.f, 0.f, 0.f, 0.f};
  Engine<128, 3, 2, 4, 4, 3, 1, NKT, 1, 0, 4, false, 3> eng(acc, smem, ld, 0, 0, aaddr, waddr, 0);
  eng.start();
  eng.run();
  const int g = lane >> 4;
#pragma unroll
  for (int n = 0; n < 3; ++n) {
    const int cb = (n0 >> 4) + wn * 3 + n;
    const float sc = cb < DM / 16 ? QSCALE : 1.f;
#pragma unroll
    for (int m = 0; m < 4; m += 2) {
      u32x2 a, b;
      a[0] = pack2(acc[n][m][0] * sc, acc[n][m][1] * sc);
      a[1] = pack2(acc[n][m][2] * sc, acc[n][m][3] * sc);
      b[0] = pack2(acc[n][m + 1][0] * sc, acc[n][m + 1][1] * sc);
      b[1] = pack2(acc[n][m + 1][2] * sc, acc[n][m + 1][3] * sc);
      const int row = m0 + wm * 64 + (m + (g & 1)) * 16 + (lane & 15);
      st16(out + ((size_t)cb * SEQ + row) * 16 + (g >> 1) * 8, pair16(a, b));
    }
  }
  STAMP_AT(1, 3);
}

typedef GCfg<128, 1, 2, 6, 4> CP;
template <int KDIM, int LDW>
__global__ __launch_bounds__(768, 3) void k_proj(const u16* __restrict__ Ablk, const u16* __restrict__ W,
                                                  const u16* __restrict__ res, const float* __restrict__ gate,
                                                  u16* __restrict__ out) {
  extern __shared__ __attribute__((aligned(16))) char smem_[];
  lds_ptr smem = (lds_ptr)smem_;
  const int lane = threadIdx.x & 63;
  const int w = __builtin_amdgcn_readfirstlane(threadIdx.x >> 6);
  const int xcd = blockIdx.x & 7, idx = blockIdx.x >> 3;
  const int tm = (xcd & 3) * 4 + (idx & 3), tn = (xcd >> 2) * 8 + (idx >> 2);
  const int m0 = tm * 128, n0 = tn * 64;
  Loader<CP> ld;
  const int lw = w & 3;
  setup_loader_A<CP, 128, 1, 2>(ld, Ablk, m0, lw, lane);
#pragma unroll
  for (int i = 0; i < CP::WPW; ++i) {
    int b = lw + 4 * i;
    int sub = b >> 2, o = b & 3;
    ld.woff[i] = 2 * CP::A_SUB + sub * CP::W_SUB + o * 1024;
    int k = sub * 32 + 8 * o + (lane >> 3);
    int f = ((lane >> 4) & 1) | ((o & 1) << 1);
    int col = n0 + ((((lane & 7) >> 1) ^ f) * 16) + (lane & 1) * 8;
    ld.wv[i] = (k * LDW + col) * 2;
    ld.wbase[i] = (const char*)W;
  }
  const int wm = w & 1, wn = (w >> 1) & 1, kg = (w >> 2) & 1;
  int waddr[2];
#pragma unroll
  for (int n = 0; n < 2; ++n) waddr[n] = wfrag_addr(wn * 2 + n, lane);
  const int aaddr = afrag_addr<128>(wm * 64, lane);
  f32x4 acc[2][4];
#pragma unroll
  for (int n = 0; n < 2; ++n)
#pragma unroll
    for (int m = 0; m < 4; ++m) acc[n][m] = f32x4{0.f, 0.f, 0.f, 0.f};
  Engine<128, 1, 2, 6, 4, 2, 2, KDIM / 64, (KDIM == DM ? 3 : 5), 0, 4> eng(acc, smem, ld, (long)2 * SEQ * 32, (long)64 * LDW, aaddr, waddr, kg);
  if (w >= 8) { eng.loader_run(); return; }
  const int g = lane >> 4;
  const int colw = n0 + (wn * 2 + kg) * 16;
  u32x2 rr[4];
  f32x4 gp[MNS];
  eng.start();
  eng.run([&] {
#pragma unroll
    for (int m = 0; m < 4; ++m)
      rr[m] = *(const u32x2*)(res + (size_t)(m0 + wm * 64 + m * 16 + (lane & 15)) * DM + colw + 4 * g);
#pragma unroll
    for (int i = 0; i < MNS; ++i) gp[i] = *(const f32x4*)(gate + i * (6 * DM) + colw + 4 * g);
  });
  __syncthreads();
  __attribute__((address_space(3))) f32x4* red = (__attribute__((address_space(3))) f32x4*)smem;
  f32x4 kp[4];
  if (kg == 0) {
#pragma unroll
    for (int m = 0; m < 4; ++m) { red[(w * 4 + m) * 64 + lane] = acc[1][m]; kp[m] = acc[0][m]; }
  } else {
#pragma unroll
    for (int m = 0; m < 4; ++m) { red[(w * 4 + m) * 64 + lane] = acc[0][m]; kp[m] = acc[1][m]; }
  }
  __syncthreads();
  {
    f32x4 gt = gp[0];
#pragma unroll
    for (int i = 1; i < MNS; ++i) gt += gp[i];
#pragma unroll
    for (int j = 0; j < 4; ++j) gt[j] = rbf(gt[j]);
#pragma unroll
    for (int m = 0; m < 4; m += 2) {
      u32x2 o[2];
#pragma unroll
      for (int i = 0; i < 2; ++i) {
        f32x4 a = kp[m + i] + red[((w ^ 4) * 4 + m + i) * 64 + lane];
        u32x2 r = rr[m + i];
        o[i][0] = pack2(bflo(r[0]) + rbf(gt[0] * rbf(a[0])), bfhi(r[0]) + rbf(gt[1] * rbf(a[1])));
        o[i][1] = pack2(bflo(r[1]) + rbf(gt[2] * rbf(a[2])), bfhi(r[1]) + rbf(gt[3] * rbf(a[3])));
      }
      const int row = m0 + wm * 64 + (m + (g & 1)) * 16 + (lane & 15);
      *(u32x4*)(out + (size_t)row * DM + colw + (g >> 1) * 8) = pair16(o[0], o[1]);
    }
  }
  STAMP_AT((KDIM == DM ? 3 : 5), 3);
}

typedef GCfg<256, 4, 1, 4, 4> CG;
template <int NH_>
DI void glu_wave(lds_ptr smem, Loader<CG>& ld, int lane, int wm, int t0, int m0, int f0, u16* __restrict__ out) {
  constexpr int NT = 2 * NH_;
  int waddr[NT];
#pragma unroll
  for (int n = 0; n < NH_; ++n) {
    waddr[n] = wfrag_addr(t0 + n, lane);
    waddr[NH_ + n] = waddr[n] + 8192;
  }
  const int aaddr = afrag_addr<256>(wm * 64, lane);
  f32x4 acc[NT][4];
#pragma unroll
  for (int n = 0; n < NT; ++n)
#pragma unroll
    for (int m = 0; m < 4; ++m) acc[n][m] = f32x4{0.f, 0.f, 0.f, 0.f};
  Engine<256, 4, 1, 4, 4, NT, 1, DM / 32, 4, 0, 4, (NH_ == 3)> eng(acc, smem, ld, (long)SEQ * 32, (long)32 * FF, aaddr, waddr, 0);
  eng.start();
  eng.run();
  const int g = lane >> 4;
#pragma unroll
  for (int n = 0; n < NH_; ++n) {
    const int cb = (f0 >> 4) + t0 + n;
#pragma unroll
    for (int m = 0; m < 4; m += 2) {
      u32x2 o[2];
#pragma unroll
      for (int i = 0; i < 2; ++i) {
        f32x4 a = acc[n][m + i], b = acc[NH_ + n][m + i];
        o[i][0] = pack2(rbf(a[0]) * rbf(b[0]), rbf(a[1]) * rbf(b[1]));
        o[i][1] = pack2(rbf(a[2]) * rbf(b[2]), rbf(a[3]) * rbf(b[3]));
      }
      const int row = m0 + wm * 64 + (m + (g & 1)) * 16 + (lane & 15);
      st16(out + ((size_t)cb * SEQ + row) * 16 + (g >> 1) * 8, pair16(o[0], o[1]));
    }
  }
  STAMP_AT(4, 3);
}

__global__ __launch_bounds__(512, 2) void k_glu(const u16* __restrict__ Ablk, const u16* __restrict__ Wg,
                                                 const u16* __restrict__ Wu, u16* __restrict__ out) {
  extern __shared__ __attribute__((aligned(16))) char smem_[];
  lds_ptr smem = (lds_ptr)smem_;
  const int lane = threadIdx.x & 63;
  const int w = __builtin_amdgcn_readfirstlane(threadIdx.x >> 6);
  const int xcd = blockIdx.x & 7, idx = blockIdx.x >> 3;
  const int tm = idx & 7, tn = xcd * 4 + (idx >> 3);
  const int m0 = tm * 256, f0 = tn * 112;
  Loader<CG> ld;
  const int lw = w & 3;
  setup_loader_A<CG, 256, 4, 1>(ld, Ablk, m0, lw, lane);
#pragma unroll
  for (int i = 0; i < CG::WPW; ++i) {
    int b = lw + 4 * i;
    int grp = b >> 2, o = b & 3;
    ld.woff[i] = CG::A_SUB + grp * 4096 + o * 1024;
    int k = 8 * o + (lane >> 3);
    int f = ((lane >> 4) & 1) | ((o & 1) << 1);
    int pr = ((lane & 7) >> 1) ^ f;
    if ((grp & 1) && pr == 3) pr = 0;
    int col = f0 + (grp & 1) * 64 + pr * 16 + (lane & 1) * 8;
    ld.wv[i] = (k * FF + col) * 2;
    ld.wbase[i] = (const char*)(grp >> 1 ? Wu : Wg);
  }
  const int wm = w & 3;
  if (w < 4) glu_wave<4>(smem, ld, lane, wm, 0, m0, f0, out);
  else glu_wave<3>(smem, ld, lane, wm, 4, m0, f0, out);
}

constexpr int ACH = 128, ANST = 4, ASTAGE = ACH * 64 * 2 + 256;
constexpr int ALDS = ANST * ASTAGE + 1024 + 16;

DI float rowmax16(const f32x16& v) {
  float mx = fmaxf(fmaxf(v[0], v[1]), v[2]);
#pragma unroll
  for (int r = 3; r < 15; r += 2) mx = fmaxf(fmaxf(mx, v[r]), v[r + 1]);
  mx = fmaxf(mx, v[15]);
  auto sw = __builtin_amdgcn_permlane32_swap(__float_as_uint(mx), __float_as_uint(mx), false, false);
  return fmaxf(__uint_as_float(sw[0]), __uint_as_float(sw[1]));
}

template <bool SAFE>
DI void attn_pass(lds_ptr smem, int lane, int w, const char* Kp, const char* Vp, const char* pf_wm, const char* pf_wo, const char* pf_wg,
                  const char* pf_wu, const char* pf_wd, const bf16x8& qa0, const bf16x8& qa1, const bf16x8& qb0,
                  const bf16x8& qb1, f32x16& oa, f32x16& ob, float& refa, float& refb, float& la_out, float& lb_out,
                  ModTask& mt, const u16* c) {
  const int r32 = lane & 31, hh = lane >> 5, kh = w >> 2;
  const unsigned kvo = ((32 * (w & 3) + (lane >> 1)) * 16 + (((lane & 1) ^ ((lane >> 4) & 1)) * 8)) * 2;
  const unsigned vvo = ((32 * (w & 3)) * 16) * 2 + lane * 16;
  const char* ksrc = Kp;
  const char* vsrc = Vp;
  auto issue = [&](int buf) {
    if (w < 4) {
      const unsigned l0 = lds_u32(smem) + buf * ASTAGE + w * 1024;
      glds16(ksrc, kvo, l0);
      glds16(ksrc + SEQ * 32, kvo, l0 + 4096);
      glds16(vsrc, vvo, l0 + 8192);
      glds16(vsrc + SEQ * 32, vvo, l0 + 8192 + 4096 + 128);
    }
    ksrc += ACH * 32; vsrc += ACH * 32;
  };
  const unsigned pfo = lane * 16;
  auto prefetch = [&](int ch) {
    if (w >= 4) {
#pragma unroll
      for (int i = 0; i < 3; ++i) {
        int pc = (ch * 3 + i) * 4 + (w - 4);
        const char* base;
        if (pc < 32) base = pf_wm + (size_t)(blockIdx.x * 4 + (pc >> 3)) * (12 * DM) + 4 * DM + (pc & 7) * 1024;
        else if (pc < 40) base = pf_wo + (size_t)blockIdx.x * 8192 + (pc - 32) * 1024;
        else if (pc < 68) base = pf_wd + (size_t)blockIdx.x * 28672 + (pc - 40) * 1024;
        else if (pc < 96) base = pf_wg + (size_t)blockIdx.x * 28672 + (pc - 68) * 1024;
        else if (pc < 124) base = pf_wu + (size_t)blockIdx.x * 28672 + (pc - 96) * 1024;
        else break;
        glds16(base, pfo, lds_u32(smem) + ANST * ASTAGE);
      }
    }
  };
  issue(0); issue(1); issue(2);
  const int kaddr0 = kh * 2048 + r32 * 32 + ((hh ^ ((r32 >> 3) & 1)) * 16);
  const int kaddr1 = kaddr0 + 4096;
  const int vaddr = 8192 + kh * 2048 + ((lane >> 4) & 1) * (4096 + 128) + (4 * hh + ((lane & 15) >> 2)) * 32 + (lane & 3) * 8;
  bf16x8 sel;
  {
    const short one = ((lane & 15) == ((lane >> 4) & 1)) ? (short)0x3F80 : (short)0;
#pragma unroll
    for (int j = 0; j < 8; ++j) sel[j] = one;
  }
  f32x16 nma, nmb, sa, sb;
#pragma unroll
  for (int r = 0; r < 16; ++r) { oa[r] = 0.f; ob[r] = 0.f; nma[r] = 0.f; nmb[r] = 0.f; }
  f32x4 lsa = {0.f, 0.f, 0.f, 0.f}, lsb = {0.f, 0.f, 0.f, 0.f};
  float la = 0.f, lb = 0.f;
  constexpr int NCH = SEQ / ACH;
  if (w < 4) wait_vm<8>();
  bar();
  const bf16x8 kf0_ = lds_read128(smem + kaddr0);
  const bf16x8 kf1_ = lds_read128(smem + kaddr1);
  {
    sa = MFMA32(kf0_, qa0, nma); sa = MFMA32(kf1_, qa1, sa);
    sb = MFMA32(kf0_, qb0, nmb); sb = MFMA32(kf1_, qb1, sb);
    const float ma = rowmax16(sa), mb = rowmax16(sb);
#pragma unroll
    for (int r = 0; r < 16; ++r) { nma[r] = -ma; sa[r] -= ma; nmb[r] = -mb; sb[r] -= mb; }
  }
  if constexpr (SAFE) {
    auto half = [&](f32x16& s, f32x16& o, f32x16& nm, float& l, const bf16x8& q0f, const bf16x8& q1f,
                    const bf16x8& kn0, const bf16x8& kn1, const bf16x8& vf0, const bf16x8& vf1, bool has_next) {
      f32x16 p;
      const float dl = fmaxf(rowmax16(s), 0.f);
      const float f = __builtin_amdgcn_exp2f(-dl);
      float t = 0.f;
#pragma unroll
      for (int r = 0; r < 16; ++r) { nm[r] -= dl; o[r] *= f; p[r] = __builtin_amdgcn_exp2f(s[r] - dl); t += p[r]; }
      l = l * f + t;
      if (has_next) { s = MFMA32(kn0, q0f, nm); s = MFMA32(kn1, q1f, s); }
      u32x4 pa, pb;
#pragma unroll
      for (int j = 0; j < 4; ++j) { pa[j] = pack2(p[2 * j], p[2 * j + 1]); pb[j] = pack2(p[8 + 2 * j], p[8 + 2 * j + 1]); }
      o = MFMA32(vf0, __builtin_bit_cast(bf16x8, pa), o);
      o = MFMA32(vf1, __builtin_bit_cast(bf16x8, pb), o);
    };
    auto step = [&](lds_cptr vb, lds_cptr knext, bool has_next) {
      bf16x8 kn0, kn1;
      if (has_next) { kn0 = lds_read128(knext + kaddr0); kn1 = lds_read128(knext + kaddr1); }
      bf16x8 vf0 = lds_tr8(vb, vb + 256);
      bf16x8 vf1 = lds_tr8(vb + 512, vb + 768);
      half(sa, oa, nma, la, qa0, qa1, kn0, kn1, vf0, vf1, has_next);
      half(sb, ob, nmb, lb, qb0, qb1, kn0, kn1, vf0, vf1, has_next);
    };
    for (int ch = 0; ch < NCH; ++ch) {
      if (w < 4) { if (ch + 2 < NCH) wait_vm<4>(); else wait_vm<0>(); }
      bar();
      if (ch + 3 < NCH) issue((ch + 3) & 3);
      lds_cptr sbuf = smem + (ch & 3) * ASTAGE;
      lds_cptr nbuf = smem + ((ch + 1) & 3) * ASTAGE;
      step(sbuf + vaddr, sbuf + 1024, true);
      step(sbuf + 1024 + vaddr, nbuf, ch + 1 < NCH);
    }
  } else {
    u32x4 pa0, pa1, pb0, pb1;
    bf16x8 kc0 = kf0_, kc1 = kf1_;
    bf16x8 vp0, vp1;
#pragma unroll
    for (int j = 0; j < 4; ++j) { pb0[j] = 0u; pb1[j] = 0u; }
#pragma unroll
    for (int j = 0; j < 8; ++j) { vp0[j] = 0; vp1[j] = 0; }
#define SB_ __builtin_amdgcn_sched_barrier(0)
#define EX(i) e[i] = __builtin_amdgcn_exp2f(S[i])
#define PHASE(S, P0, P1, M0, M1, M2, M3, M4, M5, R0, R1) do { float e[16]; \
      M0; SB_; R0; EX(0); EX(1); EX(2); EX(3); SB_; \
      M1; SB_; R1; EX(4); EX(5); EX(6); EX(7); SB_; \
      M2; SB_; EX(8); EX(9); P0[0] = pack2(e[0], e[1]); P0[1] = pack2(e[2], e[3]); SB_; \
      M3; SB_; EX(10); EX(11); P0[2] = pack2(e[4], e[5]); P0[3] = pack2(e[6], e[7]); SB_; \
      M4; SB_; EX(12); EX(13); EX(14); EX(15); SB_; \
      M5; SB_; P1[0] = pack2(e[8], e[9]); P1[1] = pack2(e[10], e[11]); P1[2] = pack2(e[12], e[13]); P1[3] = pack2(e[14], e[15]); SB_; \
    } while (0)
#define BF(x) __builtin_bit_cast(bf16x8, x)
    auto step = [&](lds_cptr vb, lds_cptr knext, auto has_next_t) {
      constexpr bool HN = decltype(has_next_t)::value;
      bf16x8 kn0, kn1, vc0, vc1;
      { const f32x16& S = sa;
        PHASE(S, pa0, pa1,
              sb = MFMA32(kc0, qb0, nmb), sb = MFMA32(kc1, qb1, sb),
              ob = MFMA32(vp0, BF(pb0), ob), ob = MFMA32(vp1, BF(pb1), ob),
              lsb = MFMA16(sel, BF(pb0), lsb), lsb = MFMA16(sel, BF(pb1), lsb),
              if (HN) { kn0 = lds_read128(knext + kaddr0); kn1 = lds_read128(knext + kaddr1); },
              { vc0 = lds_tr8(vb, vb + 256); vc1 = lds_tr8(vb + 512, vb + 768); }); }
      { const f32x16& S = sb;
        PHASE(S, pb0, pb1,
              if (HN) sa = MFMA32(kn0, qa0, nma), if (HN) sa = MFMA32(kn1, qa1, sa),
              oa = MFMA32(vc0, BF(pa0), oa), oa = MFMA32(vc1, BF(pa1), oa),
              lsa = MFMA16(sel, BF(pa0), lsa), lsa = MFMA16(sel, BF(pa1), lsa),
              , ); }
      if (HN) { kc0 = kn0; kc1 = kn1; }
      vp0 = vc0; vp1 = vc1;
    };
    typedef std::integral_constant<bool, true> T_;
    typedef std::integral_constant<bool, false> F_;
    for (int ch = 0; ch < NCH - 1; ++ch) {
      if (w < 4) { if (ch + 2 < NCH) wait_vm<4>(); else wait_vm<0>(); }
      bar();
      if (ch + 3 < NCH) issue((ch + 3) & 3);
      prefetch(ch);
      lds_cptr sbuf = smem + (ch & 3) * ASTAGE;
      lds_cptr nbuf = smem + ((ch + 1) & 3) * ASTAGE;
      step(sbuf + vaddr, sbuf + 1024, T_{});
      step(sbuf + 1024 + vaddr, nbuf, T_{});
    }
    {
      const int task = blockIdx.x * 2 + (threadIdx.x >> 8);
      mod_load(mt, c, (const u16*)pf_wm, 32 + (task >> 3), task & 7, threadIdx.x & 255);
      lds_cptr sbuf = smem + ((NCH - 1) & 3) * ASTAGE;
      step(sbuf + vaddr, sbuf + 1024, T_{});
      step(sbuf + 1024 + vaddr, sbuf, F_{});
    }
    ob = MFMA32(vp0, BF(pb0), ob); ob = MFMA32(vp1, BF(pb1), ob);
    lsb = MFMA16(sel, BF(pb0), lsb); lsb = MFMA16(sel, BF(pb1), lsb);
#undef PHASE
#undef EX
#undef BF
  }
  refa = nma[0]; refb = nmb[0];
  if constexpr (SAFE) {
    auto s1 = __builtin_amdgcn_permlane32_swap(__float_as_uint(la), __float_as_uint(la), false, false);
    la_out = __uint_as_float(s1[0]) + __uint_as_float(s1[1]);
    auto s2 = __builtin_amdgcn_permlane32_swap(__float_as_uint(lb), __float_as_uint(lb), false, false);
    lb_out = __uint_as_float(s2[0]) + __uint_as_float(s2[1]);
  } else {
    const float a0 = __shfl(lsa[0], lane & 15), a1 = __shfl(lsa[1], lane & 15);
    const float b0 = __shfl(lsb[0], lane & 15), b1 = __shfl(lsb[1], lane & 15);
    la_out = (lane & 16) ? a1 : a0;
    lb_out = (lane & 16) ? b1 : b0;
  }
}

__global__ __launch_bounds__(512, 2) void k_attn(const u16* __restrict__ qkv, u16* __restrict__ oblk,
                                                  const u16* __restrict__ c, const u16* __restrict__ wmod, float* __restrict__ part,
                                                  const char* __restrict__ pf_wo, const char* __restrict__ pf_wg,
                                                  const char* __restrict__ pf_wu, const char* __restrict__ pf_wd) {
  extern __shared__ __attribute__((aligned(16))) char smem_[];
  lds_ptr smem = (lds_ptr)smem_;
  const int lane = threadIdx.x & 63;
  const int w = __builtin_amdgcn_readfirstlane(threadIdx.x >> 6);
  const int xcd = blockIdx.x & 7, idx = blockIdx.x >> 3;
  const int head = xcd * 4 + (idx >> 3), qb = idx & 7;
  const int r32 = lane & 31, hh = lane >> 5;
  const int qg = w & 3, kh = w >> 2;
  const u16* Qp = qkv + (size_t)head * SEQ * 32;
  const char* Kp = (const char*)(qkv + (size_t)(32 + head) * SEQ * 32);
  const char* Vp = (const char*)(qkv + (size_t)(64 + head) * SEQ * 32);
  const int q0 = qb * 256 + qg * 64;
  STAMP_AT(2, 0);
  __attribute__((address_space(3))) int* flag = (__attribute__((address_space(3))) int*)(smem + ANST * ASTAGE + 1024);
  if (threadIdx.x == 0) *flag = 0;
  bf16x8 qa0 = *(const bf16x8*)(Qp + (q0 + r32) * 16 + hh * 8);
  bf16x8 qa1 = *(const bf16x8*)(Qp + (SEQ + q0 + r32) * 16 + hh * 8);
  bf16x8 qb0 = *(const bf16x8*)(Qp + (q0 + 32 + r32) * 16 + hh * 8);
  bf16x8 qb1 = *(const bf16x8*)(Qp + (SEQ + q0 + 32 + r32) * 16 + hh * 8);
  f32x16 oa, ob;
  float nma, nmb, la, lb;
  ModTask mt;
  attn_pass<false>(smem, lane, w, Kp, Vp, (const char*)wmod, pf_wo, pf_wg, pf_wu, pf_wd, qa0, qa1, qb0, qb1, oa, ob, nma, nmb, la, lb, mt, c);
  {
    float big = fmaxf(fabsf(la), fabsf(lb));
#pragma unroll
    for (int r = 0; r < 16; ++r) big = fmaxf(big, fmaxf(fabsf(oa[r]), fabsf(ob[r])));
    bool bad = !(big < 1e30f);
#pragma unroll
    for (int r = 0; r < 16; ++r) bad = bad || (oa[r] != oa[r]) || (ob[r] != ob[r]);
    bad = bad || (la != la) || (lb != lb);
    if (__any(bad) && lane == 0) *flag = 1;
  }
  __syncthreads();
  if (__builtin_amdgcn_readfirstlane(*flag) != 0) {
    attn_pass<true>(smem, lane, w, Kp, Vp, (const char*)wmod, pf_wo, pf_wg, pf_wu, pf_wd, qa0, qa1, qb0, qb1, oa, ob, nma, nmb, la, lb, mt, c);
    __syncthreads();
  }
  STAMP_AT(2, 2);
  const int task = blockIdx.x * 2 + (threadIdx.x >> 8);
  {
    __attribute__((address_space(3))) float* xb = (__attribute__((address_space(3))) float*)smem;
    const f32x16& og = kh == 0 ? ob : oa;
    __attribute__((address_space(3))) float* dst = xb + w * (18 * 64) + lane;
#pragma unroll
    for (int r = 0; r < 16; ++r) dst[r * 64] = og[r];
    dst[16 * 64] = kh == 0 ? lb : la;
    dst[17 * 64] = kh == 0 ? nmb : nma;
    __syncthreads();
    const __attribute__((address_space(3))) float* src = xb + (w ^ 4) * (18 * 64) + lane;
    f32x16 om = kh == 0 ? oa : ob;
    const float nm_me = kh == 0 ? nma : nmb;
    const float nm_ot = src[17 * 64];
    const float nmin = fminf(nm_me, nm_ot);
    const float f_me = __builtin_amdgcn_exp2f(nmin - nm_me), f_ot = __builtin_amdgcn_exp2f(nmin - nm_ot);
    const float rl = 1.f / ((kh == 0 ? la : lb) * f_me + src[16 * 64] * f_ot);
    const float g_me = f_me * rl, g_ot = f_ot * rl;
    u16* op = oblk + ((size_t)head * 2 * SEQ + q0 + kh * 32 + r32) * 16 + 4 * hh;
#pragma unroll
    for (int g = 0; g < 4; ++g) {
      float v0 = om[4 * g] * g_me + src[(4 * g) * 64] * g_ot;
      float v1 = om[4 * g + 1] * g_me + src[(4 * g + 1) * 64] * g_ot;
      float v2 = om[4 * g + 2] * g_me + src[(4 * g + 2) * 64] * g_ot;
      float v3 = om[4 * g + 3] * g_me + src[(4 * g + 3) * 64] * g_ot;
      u32x2 v;
      v[0] = pack2(v0, v1);
      v[1] = pack2(v2, v3);
      *(u32x2*)(op + (g >> 1) * (SEQ * 16) + (g & 1) * 8) = v;
    }
  }
  mod_finish(mt, part, 32 + (task >> 3), task & 7, threadIdx.x & 255, (float*)(smem_ + 40960) + (threadIdx.x >> 8) * 256);
  wait_vm<0>();
  STAMP_AT(2, 3);
}

extern "C" void kernel_launch(void* const* d_in, const int* in_sizes, int n_in, void* d_out, int out_size,
                              void* d_ws, size_t ws_size, hipStream_t stream) {
  const u16* x = (const u16*)d_in[0];
  const u16* wqkv = (const u16*)d_in[1];
  const u16* wo = (const u16*)d_in[2];
  const u16* wg = (const u16*)d_in[4];
  const u16* wu = (const u16*)d_in[5];
  const u16* wd = (const u16*)d_in[6];
  const u16* c = (const u16*)d_in[7];
  const u16* wmod = (const u16*)d_in[8];
  u16* out = (u16*)d_out;
  char* ws = (char*)d_ws;
  float* part = (float*)ws;
  float* modf = (float*)(ws + (256 << 10));
  unsigned* flags = (unsigned*)(ws + (512 << 10));
  float* stats = (float*)(ws + (768 << 10));
  u16* h1 = (u16*)(ws + (1 << 20));
  u16* qkv = (u16*)(ws + (8 << 20));
  u16* ob = (u16*)(ws + (24 << 20));
  u16* x2 = (u16*)(ws + (32 << 20));
  u16* h2 = (u16*)(ws + (40 << 20));
  u16* hh = (u16*)(ws + (48 << 20));
  u16* wdp = (u16*)(ws + (64 << 20));

  hipFuncSetAttribute((const void*)k_qkv, hipFuncAttributeMaxDynamicSharedMemorySize, QLDS);
  hipFuncSetAttribute((const void*)k_proj<DM, DM>, hipFuncAttributeMaxDynamicSharedMemorySize, CP::LDS);
  hipFuncSetAttribute((const void*)k_proj<FF, WDP>, hipFuncAttributeMaxDynamicSharedMemorySize, CP::LDS);
  hipFuncSetAttribute((const void*)k_glu, hipFuncAttributeMaxDynamicSharedMemorySize, CG::LDS);
  hipFuncSetAttribute((const void*)k_attn, hipFuncAttributeMaxDynamicSharedMemorySize, ALDS);

  k_pre<<<256 + 256 + 384, 256, 0, stream>>>(c, wmod, part, (const char*)x, (const char*)wqkv, flags, stats);
  k_qkv<<<256, 768, QLDS, stream>>>(x, stats, part, wqkv, qkv);
  k_attn<<<256, 512, ALDS, stream>>>(qkv, ob, c, wmod, part, (const char*)wo, (const char*)wg, (const char*)wu, (const char*)wd);
  k_proj<DM, DM><<<256, 768, CP::LDS, stream>>>(ob, wo, x, part + 2 * DM, x2);
  k_ln<7, MNS, 0><<<256 + 224, 512, 0, stream>>>(x2, part + 3 * DM, h2, (const char*)wqkv, wd, wdp);
  k_glu<<<256, 512, CG::LDS, stream>>>(h2, wg, wu, hh);
  k_proj<FF, WDP><<<256, 768, CP::LDS, stream>>>(hh, wdp, x2, part + 5 * DM, out);
}
```

```cpp
#include <hip/hip_runtime.h>
#include <hip/hip_bf16.h>
#include <stdint.h>

typedef __attribute__((ext_vector_type(8))) short bf16x8;
typedef __attribute__((ext_vector_type(4))) short s16x4;
typedef __attribute__((ext_vector_type(4))) float f32x4;
typedef __attribute__((ext_vector_type(2))) float f32x2;
typedef __attribute__((ext_vector_type(16))) float f32x16;
typedef __attribute__((ext_vector_type(4))) unsigned u32x4;
typedef __attribute__((ext_vector_type(2))) unsigned u32x2;
typedef __attribute__((ext_vector_type(2))) __bf16 bf16x2_t;
typedef unsigned short u16;
typedef __attribute__((address_space(3))) char* lds_ptr;
typedef __attribute__((address_space(3))) const char* lds_cptr;

#define DI __device__ __forceinline__

constexpr int SEQ = 2048, DM = 1024, FF = 3584, NH = 32;

DI float bf2f(u16 x) { return __uint_as_float(((unsigned)x) << 16); }
DI float bflo(unsigned x) { return __uint_as_float(x << 16); }
DI float bfhi(unsigned x) { return __uint_as_float(x & 0xffff0000u); }
DI unsigned pack2(float a, float b) {
  f32x2 v = {a, b};
  bf16x2_t r = __builtin_convertvector(v, bf16x2_t);
  return __builtin_bit_cast(unsigned, r);
}
DI float rbf(float a) { return bflo(pack2(a, 0.f) & 0xffffu); }

#define MFMA16(a, b, c) __builtin_amdgcn_mfma_f32_16x16x32_bf16((a), (b), (c), 0, 0, 0)
#define MFMA32(a, b, c) __builtin_amdgcn_mfma_f32_32x32x16_bf16((a), (b), (c), 0, 0, 0)

DI void glds16(const void* base, unsigned voff, unsigned lds_addr) {
  unsigned sv;
  asm volatile("s_mov_b32 %0, m0\n\ts_mov_b32 m0, %3\n\ts_nop 0\n\tglobal_load_lds_dwordx4 %1, %2\n\ts_mov_b32 m0, %0"
               : "=&s"(sv) : "v"(voff), "s"(base), "s"(lds_addr) : "memory");
}
DI unsigned lds_u32(lds_ptr p) { return (unsigned)(uintptr_t)p; }
DI bf16x8 lds_read128(lds_cptr p) { return *(const __attribute__((address_space(3))) bf16x8*)p; }
DI s16x4 lds_tr(lds_cptr p) {
  typedef short v4i16_t __attribute__((ext_vector_type(4)));
  return __builtin_bit_cast(s16x4, __builtin_amdgcn_ds_read_tr16_b64_v4i16((__attribute__((address_space(3))) v4i16_t*)p));
}
DI bf16x8 lds_tr8(lds_cptr p0, lds_cptr p1) {
  s16x4 a = lds_tr(p0), b = lds_tr(p1);
  return __builtin_shufflevector(a, b, 0, 1, 2, 3, 4, 5, 6, 7);
}
#define STAMP_AT(kid, slot) do {} while (0)
template <int N> DI void wait_vm() { asm volatile("s_waitcnt vmcnt(%0)" ::"n"(N) : "memory"); }
DI void bar() { __builtin_amdgcn_s_barrier(); }

constexpr int MNS = 8;
struct ModTask { u32x4 w[4]; u16 cv[4]; };
DI void mod_load(ModTask& mt, const u16* __restrict__ c, const u16* __restrict__ wmod, int cb, int ks, int t) {
  const int cc = t & 7, rg = t >> 3;
  const int k0 = ks * 128 + rg;
  const u16* wp = wmod + (size_t)k0 * (6 * DM) + cb * 64 + cc * 8;
#pragma unroll
  for (int i = 0; i < 4; ++i) mt.w[i] = *(const u32x4*)(wp + (size_t)i * 32 * (6 * DM));
#pragma unroll
  for (int i = 0; i < 4; ++i) mt.cv[i] = c[k0 + 32 * i];
}
DI void mod_finish(const ModTask& mt, float* __restrict__ part, int cb, int ks, int t, float* s_red  ) {
  float acc[8];
#pragma unroll
  for (int j = 0; j < 8; ++j) acc[j] = 0.f;
#pragma unroll
  for (int i = 0; i < 4; ++i) {
    const float cv = bf2f(mt.cv[i]);
    const float sv = rbf(cv / (1.f + __expf(-cv)));
#pragma unroll
    for (int j = 0; j < 4; ++j) {
      acc[2 * j] += sv * bflo(mt.w[i][j]);
      acc[2 * j + 1] += sv * bfhi(mt.w[i][j]);
    }
  }
#pragma unroll
  for (int j = 0; j < 8; ++j) {
    float v = acc[j];
    v += __shfl_xor(v, 8);
    v += __shfl_xor(v, 16);
    v += __shfl_xor(v, 32);
    acc[j] = v;
  }
  const int lane = t & 63, wv = t >> 6;
  if (lane < 8) {
#pragma unroll
    for (int j = 0; j < 8; ++j) s_red[wv * 64 + lane * 8 + j] = acc[j];
  }
  __syncthreads();
  if (t < 64) part[ks * (6 * DM) + cb * 64 + t] = (s_red[t] + s_red[64 + t]) + (s_red[128 + t] + s_red[192 + t]);
}
DI f32x4 gate4(const float* __restrict__ gate, int col) {
  f32x4 gt = *(const f32x4*)(gate + col);
#pragma unroll
  for (int i = 1; i < MNS; ++i) gt += *(const f32x4*)(gate + i * (6 * DM) + col);
#pragma unroll
  for (int j = 0; j < 4; ++j) gt[j] = rbf(gt[j]);
  return gt;
}

DI void touch16k(const char* p, int t) {
  u32x4 v[4];
#pragma unroll
  for (int i = 0; i < 4; ++i) v[i] = *(const u32x4*)(p + i * 4096 + t * 16);
#pragma unroll
  for (int i = 0; i < 4; ++i) asm volatile("" ::"v"(v[i]));
}

__global__ __launch_bounds__(256) void k_pre(const u16* __restrict__ c, const u16* __restrict__ wmod, float* __restrict__ part,
                                              const char* __restrict__ x, const char* __restrict__ wqkv,
                                              unsigned* __restrict__ flags) {
  __shared__ float s_red[256];
  const int t = threadIdx.x, b = blockIdx.x;
  if (b < 256) STAMP_AT(0, 0);
  if (b < 256) {
    ModTask mt;
    mod_load(mt, c, wmod, b >> 3, b & 7, t);
    mod_finish(mt, part, b >> 3, b & 7, t, s_red);
    if (b == 0) flags[t * 16] = 0u;
  } else {
    touch16k(x + (size_t)(b - 256) * 16384, t);
  }
  if (b < 256) STAMP_AT(0, 3);
}

constexpr int WDP = DM + 64;
template <int KID, int NS, int NPF>
__global__ __launch_bounds__(512) void k_ln(const u16* __restrict__ x, const float* __restrict__ ss  ,
                                             u16* __restrict__ h16, const char* __restrict__ pf,
                                             const u16* __restrict__ wsrc, u16* __restrict__ wdst) {
  __shared__ __attribute__((aligned(16))) float sm[2048];
  const int t = threadIdx.x;
  if (blockIdx.x >= 256 + NPF) {
    const int r0 = (blockIdx.x - 256 - NPF) * 16 + (t >> 7);
    u32x4 v[4];
#pragma unroll
    for (int i = 0; i < 4; ++i) v[i] = *(const u32x4*)(wsrc + (size_t)(r0 + 4 * i) * DM + (t & 127) * 8);
#pragma unroll
    for (int i = 0; i < 4; ++i) *(u32x4*)(wdst + (size_t)(r0 + 4 * i) * WDP + (t & 127) * 8) = v[i];
    return;
  }
  if (blockIdx.x >= 256) {
    touch16k(pf + (size_t)((blockIdx.x - 256) * 2 + (t >> 8)) * 16384, t & 255);
    return;
  }
  const int lane = t & 63, wv = t >> 6;
  const int row = blockIdx.x * 8 + wv;
  STAMP_AT(KID, 0);
  const u16* xr = x + (size_t)row * DM;
  u32x4 v0 = *(const u32x4*)(xr + lane * 8);
  u32x4 v1 = *(const u32x4*)(xr + 512 + lane * 8);
  {
    f32x4 pv[NS];
#pragma unroll
    for (int i = 0; i < NS; ++i) pv[i] = *(const f32x4*)(ss + i * (6 * DM) + t * 4);
    f32x4 a = pv[0];
#pragma unroll
    for (int i = 1; i < NS; ++i) a += pv[i];
#pragma unroll
    for (int j = 0; j < 4; ++j) {
      float v = rbf(a[j]);
      a[j] = t < 256 ? v : rbf(1.f + v);
    }
    *(f32x4*)(sm + t * 4) = a;
  }
  float f[16];
#pragma unroll
  for (int j = 0; j < 4; ++j) {
    f[2 * j] = bflo(v0[j]); f[2 * j + 1] = bfhi(v0[j]);
    f[8 + 2 * j] = bflo(v1[j]); f[8 + 2 * j + 1] = bfhi(v1[j]);
  }
  float s = 0.f;
#pragma unroll
  for (int j = 0; j < 16; ++j) s += f[j];
#pragma unroll
  for (int o = 1; o < 64; o <<= 1) s += __shfl_xor(s, o);
  const float mean = s * (1.f / DM);
  float q = 0.f;
#pragma unroll
  for (int j = 0; j < 16; ++j) { float d = f[j] - mean; q += d * d; }
#pragma unroll
  for (int o = 1; o < 64; o <<= 1) q += __shfl_xor(q, o);
  const float mb = rbf(mean);
  const float vb = rbf(q * (1.f / DM));
  const float rden = 1.f / rbf(sqrtf(rbf(vb + 1e-5f)));
  __syncthreads();
#pragma unroll
  for (int half = 0; half < 2; ++half) {
    const int k0 = half * 512 + lane * 8;
    f32x4 sh0 = *(const f32x4*)(sm + k0), sh1 = *(const f32x4*)(sm + k0 + 4);
    f32x4 sc0 = *(const f32x4*)(sm + 1024 + k0), sc1 = *(const f32x4*)(sm + 1024 + k0 + 4);
    float y[8];
#pragma unroll
    for (int j = 0; j < 8; ++j) {
      float g = j < 4 ? sc0[j] : sc1[j - 4];
      float sh = j < 4 ? sh0[j] : sh1[j - 4];
      float d = rbf(f[half * 8 + j] - mb);
      float n = rbf(d * rden);
      y[j] = rbf(n * g) + sh;
    }
    u32x4 o;
#pragma unroll
    for (int j = 0; j < 4; ++j) o[j] = pack2(y[2 * j], y[2 * j + 1]);
    *(u32x4*)(h16 + ((size_t)(k0 >> 4) * SEQ + row) * 16 + (k0 & 15)) = o;
  }
  STAMP_AT(KID, 3);
}

DI void st16(void* p, u32x4 v) {
  *(u32x4*)p = v;
}
DI u32x4 pair16(u32x2 a, u32x2 b) {
  auto s0 = __builtin_amdgcn_permlane16_swap(a[0], b[0], false, false);
  auto s1 = __builtin_amdgcn_permlane16_swap(a[1], b[1], false, false);
  return u32x4{s0[0], s1[0], s0[1], s1[1]};
}

DI int hA(int r) { return (-r) & 3; }

template <int BM, int NGRP, int KS, int NST, int NLW_ = 8>
struct GCfg {
  static constexpr int NLW = NLW_;
  static constexpr int A_SUB = BM * 64;
  static constexpr int W_SUB = NGRP * 4096;
  static constexpr int STAGE = KS * (A_SUB + W_SUB);
  static constexpr int APW = KS * BM / 16 / NLW;
  static constexpr int WPW = KS * NGRP * 4 / NLW;
  static constexpr int PPW = APW + WPW;
  static constexpr int LDS = STAGE * NST;
};

template <class C>
struct Loader {
  const char* abase;
  const char* wbase[C::WPW];
  unsigned av[C::APW];
  unsigned wv[C::WPW];
  int aoff[C::APW];
  int woff[C::WPW];
};

template <int BM, int NGRP, int KS, int NST, int MT, int NT, int KSPLIT, int NKT, int KID, int IOFS, int NLW = 8, bool LOADS = (NLW == 8)>
struct Engine {
  typedef GCfg<BM, NGRP, KS, NST, NLW> C;
  static constexpr bool DED = !LOADS;
  static constexpr int SPW = KS / KSPLIT;
  static constexpr int P = C::PPW;
  static constexpr int G = NT * SPW;
  f32x4 (&acc)[NT][MT];
  lds_ptr smem;
  Loader<C>& ld;
  long a_adv, w_adv;
  const int (&waddr)[NT];
  int a_sub0, w_sub0;
  int cur, nxt, fre;
  bf16x8 wf[NT], af[2][MT];

  DI Engine(f32x4 (&acc_)[NT][MT], lds_ptr smem_, Loader<C>& ld_, long a_adv_, long w_adv_, int aaddr, const int (&waddr_)[NT], int kg)
      : acc(acc_), smem(smem_), ld(ld_), a_adv(a_adv_), w_adv(w_adv_), waddr(waddr_) {
    a_sub0 = kg * C::A_SUB + aaddr;
    w_sub0 = KS * C::A_SUB + kg * C::W_SUB;
  }
  template <int I> DI void piece(int stage_off) {
    if constexpr (I < C::APW) {
      glds16(ld.abase, ld.av[I], lds_u32(smem) + stage_off + ld.aoff[I]);
      if constexpr (I == C::APW - 1) ld.abase += a_adv * 2;
    } else {
      constexpr int J = I - C::APW;
      glds16(ld.wbase[J], ld.wv[J], lds_u32(smem) + stage_off + ld.woff[J]);
      ld.wbase[J] += w_adv * 2;
    }
  }
  template <int I> DI void issue_all(int stage_off) {
    if constexpr (I < P) { piece<I>(stage_off); issue_all<I + 1>(stage_off); }
  }
  template <int g, int I> DI void issue_at(int stage_off) {
    if constexpr (I < P) {
      constexpr int span = (G - IOFS) > P ? (G - IOFS) : P;
      constexpr int pos = IOFS + (I * span) / P < G ? IOFS + (I * span) / P : G - 1;
      if constexpr (pos == g) piece<I>(stage_off);
      issue_at<g, I + 1>(stage_off);
    }
  }
  template <int PAR, int S, bool PF, bool DOISSUE, int n>
  DI void groups(lds_cptr ab, lds_cptr wb) {
    if constexpr (n < NT) {
#pragma unroll
      for (int m = 0; m < MT; ++m) acc[n][m] = MFMA16(wf[n], af[PAR][m], acc[n][m]);
      if constexpr (PF) {
        wf[n] = lds_tr8(wb + waddr[n], wb + waddr[n] + 512);
#pragma unroll
        for (int m = 0; m < MT; ++m)
          if ((m * NT) / MT == n) af[PAR ^ 1][m] = lds_read128(ab + m * 512);
      }
      if constexpr (DOISSUE && !DED) issue_at<S * NT + n, 0>(fre);
      __builtin_amdgcn_sched_barrier(0);
      groups<PAR, S, PF, DOISSUE, n + 1>(ab, wb);
    }
  }
  template <int PAR, bool DOISSUE, bool LAST, int S> DI void steps() {
    if constexpr (S < SPW) {
      if constexpr (S + 1 < SPW)
        groups<PAR, S, true, DOISSUE, 0>(smem + cur + a_sub0 + (S + 1) * KSPLIT * C::A_SUB, smem + cur + w_sub0 + (S + 1) * KSPLIT * C::W_SUB);
      else if constexpr (!LAST)
        groups<PAR, S, true, DOISSUE, 0>(smem + nxt + a_sub0, smem + nxt + w_sub0);
      else
        groups<PAR, S, false, false, 0>(smem, smem);
      steps<PAR ^ 1, DOISSUE, LAST, S + 1>();
    }
  }
  template <int PAR, bool DOISSUE, bool LAST, int WAITN> DI void iter() {
    if constexpr (!LAST) { if constexpr (!DED) wait_vm<WAITN>(); bar(); }
    steps<PAR, DOISSUE, LAST, 0>();
    fre = cur; cur = nxt; nxt = (nxt + C::STAGE == NST * C::STAGE) ? 0 : nxt + C::STAGE;
  }
  template <int KT> DI void tail() {
    if constexpr (KT < NKT) {
      constexpr bool DOISSUE = KT + NST - 1 < NKT;
      constexpr bool LAST = KT == NKT - 1;
      constexpr int newest = (KT + NST - 2) < (NKT - 1) ? (KT + NST - 2) : (NKT - 1);
      constexpr int younger = newest - (KT + 1) > 0 ? newest - (KT + 1) : 0;
      iter<(KT * SPW) & 1, DOISSUE, LAST, P * younger>();
      tail<KT + 1>();
    }
  }
  DI void start() {
    static_assert(NKT >= NST + 1, "too few k tiles");
    STAMP_AT(KID, 0);
    if constexpr (!DED) {
#pragma unroll
      for (int s = 0; s < NST - 1; ++s) issue_all<0>(s * C::STAGE);
    }
  }
  template <int KT> DI void ltail() {
    if constexpr (KT < NKT - 1) {
      constexpr int newest = (KT + NST - 2) < (NKT - 1) ? (KT + NST - 2) : (NKT - 1);
      constexpr int younger = newest - (KT + 1) > 0 ? newest - (KT + 1) : 0;
      wait_vm<P * younger>();
      bar();
      ltail<KT + 1>();
    }
  }
  DI void loader_run() {
#pragma unroll
    for (int s = 0; s < NST - 1; ++s) issue_all<0>(s * C::STAGE);
    wait_vm<P*(NST - 2)>();
    bar();
    fre = (NST - 1) * C::STAGE;
    for (int kt = 0; kt < NKT - NST + 1; ++kt) {
      wait_vm<P*(NST - 3)>();
      bar();
      issue_all<0>(fre);
      fre = (fre + C::STAGE == NST * C::STAGE) ? 0 : fre + C::STAGE;
    }
    ltail<NKT - NST + 1>();
  }
  DI void run() { run([] {}); }
  template <class Hook> DI void run(Hook hook) {
    if constexpr (!DED) wait_vm<P*(NST - 2)>();
    bar();
    STAMP_AT(KID, 1);
    {
      lds_cptr ab = smem + a_sub0;
      lds_cptr wb = smem + w_sub0;
#pragma unroll
      for (int m = 0; m < MT; ++m) af[0][m] = lds_read128(ab + m * 512);
#pragma unroll
      for (int n = 0; n < NT; ++n) wf[n] = lds_tr8(wb + waddr[n], wb + waddr[n] + 512);
    }
    hook();
    cur = 0; nxt = C::STAGE; fre = (NST - 1) * C::STAGE;
    constexpr int STEADY = NKT - NST + 1;
    constexpr int LOOPN = (STEADY / 2) * 2;
    for (int kt = 0; kt < LOOPN; kt += 2) {
      iter<0, true, false, P*(NST - 3)>();
      iter<SPW & 1, true, false, P*(NST - 3)>();
    }
    tail<LOOPN>();
    STAMP_AT(KID, 2);
  }
};

template <class C, int BM, int NGRP, int KS>
DI void setup_loader_A(Loader<C>& ld, const u16* Ablk, int m0, int w, int lane) {
#pragma unroll
  for (int i = 0; i < C::APW; ++i) {
    int a = w + C::NLW * i;
    int blk = a / (BM / 32), j = a % (BM / 32);
    ld.aoff[i] = blk * (BM * 32) + j * 1024;
    ld.av[i] = ((blk * SEQ + m0 + j * 32) * 16) * 2 + lane * 16;
  }
  ld.abase = (const char*)Ablk;
}

DI int wfrag_addr(int t, int lane) {
  int g = lane >> 4, q = (lane & 15) >> 2, p = lane & 3;
  int f = ((q >> 1) & 1) | ((g & 1) << 1);
  return (t >> 2) * 4096 + (8 * g + q) * 128 + (((t & 3) ^ f) * 32) + p * 8;
}
template <int BM>
DI int afrag_addr(int mrow0, int lane) {
  int r = lane & 15, g = lane >> 4;
  return (g >> 1) * (BM * 32) + (mrow0 + r) * 32 + (g & 1) * 16;
}

constexpr float QSCALE = 0.17677669529663687f * 1.4426950408889634f;
typedef GCfg<128, 3, 2, 4, 4> CQ;
__global__ __launch_bounds__(768, 3) void k_qkv(const u16* __restrict__ Ablk, const u16* __restrict__ W,
                                                 u16* __restrict__ out) {
  extern __shared__ __attribute__((aligned(16))) char smem_[];
  lds_ptr smem = (lds_ptr)smem_;
  const int lane = threadIdx.x & 63;
  const int w = __builtin_amdgcn_readfirstlane(threadIdx.x >> 6);
  const int xcd = blockIdx.x & 7, idx = blockIdx.x >> 3;
  const int tm = (xcd & 1) * 8 + (idx & 7), tn = (xcd >> 1) * 4 + (idx >> 3);
  const int m0 = tm * 128, n0 = tn * 192;
  constexpr int LDW = 3 * DM;
  Loader<CQ> ld;
  const int lw = w & 3;
  setup_loader_A<CQ, 128, 3, 2>(ld, Ablk, m0, lw, lane);
#pragma unroll
  for (int i = 0; i < CQ::WPW; ++i) {
    int b = lw + 4 * i;
    int sub = b / 12, r = b % 12, grp = r >> 2, o = r & 3;
    ld.woff[i] = 2 * CQ::A_SUB + sub * CQ::W_SUB + grp * 4096 + o * 1024;
    int k = sub * 32 + 8 * o + (lane >> 3);
    int f = ((lane >> 4) & 1) | ((o & 1) << 1);
    int col = n0 + grp * 64 + ((((lane & 7) >> 1) ^ f) * 16) + (lane & 1) * 8;
    ld.wv[i] = (k * LDW + col) * 2;
    ld.wbase[i] = (const char*)W;
  }
  const int wm = w & 1, wn = (w >> 1) & 3;
  int waddr[3];
#pragma unroll
  for (int n = 0; n < 3; ++n) waddr[n] = wfrag_addr(wn * 3 + n, lane);
  const int aaddr = afrag_addr<128>(wm * 64, lane);
  f32x4 acc[3][4];
#pragma unroll
  for (int n = 0; n < 3; ++n)
#pragma unroll
    for (int m = 0; m < 4; ++m) acc[n][m] = f32x4{0.f, 0.f, 0.f, 0.f};
  Engine<128, 3, 2, 4, 4, 3, 1, DM / 64, 1, 0, 4> eng(acc, smem, ld, (long)2 * SEQ * 32, (long)64 * LDW, aaddr, waddr, 0);
  if (w >= 8) { eng.loader_run(); return; }
  eng.start();
  eng.run();
  const int g = lane >> 4;
#pragma unroll
  for (int n = 0; n < 3; ++n) {
    const int cb = (n0 >> 4) + wn * 3 + n;
    const float sc = cb < DM / 16 ? QSCALE : 1.f;
#pragma unroll
    for (int m = 0; m < 4; m += 2) {
      u32x2 a, b;
      a[0] = pack2(acc[n][m][0] * sc, acc[n][m][1] * sc);
      a[1] = pack2(acc[n][m][2] * sc, acc[n][m][3] * sc);
      b[0] = pack2(acc[n][m + 1][0] * sc, acc[n][m + 1][1] * sc);
      b[1] = pack2(acc[n][m + 1][2] * sc, acc[n][m + 1][3] * sc);
      const int row = m0 + wm * 64 + (m + (g & 1)) * 16 + (lane & 15);
      st16(out + ((size_t)cb * SEQ + row) * 16 + (g >> 1) * 8, pair16(a, b));
    }
  }
  STAMP_AT(1, 3);
}

typedef GCfg<128, 1, 2, 6, 4> CP;
template <int KDIM, int LDW>
__global__ __launch_bounds__(768, 3) void k_proj(const u16* __restrict__ Ablk, const u16* __restrict__ W,
                                                  const u16* __restrict__ res, const float* __restrict__ gate,
                                                  u16* __restrict__ out) {
  extern __shared__ __attribute__((aligned(16))) char smem_[];
  lds_ptr smem = (lds_ptr)smem_;
  const int lane = threadIdx.x & 63;
  const int w = __builtin_amdgcn_readfirstlane(threadIdx.x >> 6);
  const int xcd = blockIdx.x & 7, idx = blockIdx.x >> 3;
  const int tm = (xcd & 3) * 4 + (idx & 3), tn = (xcd >> 2) * 8 + (idx >> 2);
  const int m0 = tm * 128, n0 = tn * 64;
  Loader<CP> ld;
  const int lw = w & 3;
  setup_loader_A<CP, 128, 1, 2>(ld, Ablk, m0, lw, lane);
#pragma unroll
  for (int i = 0; i < CP::WPW; ++i) {
    int b = lw + 4 * i;
    int sub = b >> 2, o = b & 3;
    ld.woff[i] = 2 * CP::A_SUB + sub * CP::W_SUB + o * 1024;
    int k = sub * 32 + 8 * o + (lane >> 3);
    int f = ((lane >> 4) & 1) | ((o & 1) << 1);
    int col = n0 + ((((lane & 7) >> 1) ^ f) * 16) + (lane & 1) * 8;
    ld.wv[i] = (k * LDW + col) * 2;
    ld.wbase[i] = (const char*)W;
  }
  const int wm = w & 1, wn = (w >> 1) & 1, kg = (w >> 2) & 1;
  int waddr[2];
#pragma unroll
  for (int n = 0; n < 2; ++n) waddr[n] = wfrag_addr(wn * 2 + n, lane);
  const int aaddr = afrag_addr<128>(wm * 64, lane);
  f32x4 acc[2][4];
#pragma unroll
  for (int n = 0; n < 2; ++n)
#pragma unroll
    for (int m = 0; m < 4; ++m) acc[n][m] = f32x4{0.f, 0.f, 0.f, 0.f};
  Engine<128, 1, 2, 6, 4, 2, 2, KDIM / 64, (KDIM == DM ? 3 : 5), 0, 4> eng(acc, smem, ld, (long)2 * SEQ * 32, (long)64 * LDW, aaddr, waddr, kg);
  if (w >= 8) { eng.loader_run(); return; }
  const int g = lane >> 4;
  const int colw = n0 + (wn * 2 + kg) * 16;
  u32x2 rr[4];
  f32x4 gp[MNS];
  eng.start();
  eng.run([&] {
#pragma unroll
    for (int m = 0; m < 4; ++m)
      rr[m] = *(const u32x2*)(res + (size_t)(m0 + wm * 64 + m * 16 + (lane & 15)) * DM + colw + 4 * g);
#pragma unroll
    for (int i = 0; i < MNS; ++i) gp[i] = *(const f32x4*)(gate + i * (6 * DM) + colw + 4 * g);
  });
  __syncthreads();
  __attribute__((address_space(3))) f32x4* red = (__attribute__((address_space(3))) f32x4*)smem;
  f32x4 kp[4];
  if (kg == 0) {
#pragma unroll
    for (int m = 0; m < 4; ++m) { red[(w * 4 + m) * 64 + lane] = acc[1][m]; kp[m] = acc[0][m]; }
  } else {
#pragma unroll
    for (int m = 0; m < 4; ++m) { red[(w * 4 + m) * 64 + lane] = acc[0][m]; kp[m] = acc[1][m]; }
  }
  __syncthreads();
  {
    f32x4 gt = gp[0];
#pragma unroll
    for (int i = 1; i < MNS; ++i) gt += gp[i];
#pragma unroll
    for (int j = 0; j < 4; ++j) gt[j] = rbf(gt[j]);
#pragma unroll
    for (int m = 0; m < 4; m += 2) {
      u32x2 o[2];
#pragma unroll
      for (int i = 0; i < 2; ++i) {
        f32x4 a = kp[m + i] + red[((w ^ 4) * 4 + m + i) * 64 + lane];
        u32x2 r = rr[m + i];
        o[i][0] = pack2(bflo(r[0]) + rbf(gt[0] * rbf(a[0])), bfhi(r[0]) + rbf(gt[1] * rbf(a[1])));
        o[i][1] = pack2(bflo(r[1]) + rbf(gt[2] * rbf(a[2])), bfhi(r[1]) + rbf(gt[3] * rbf(a[3])));
      }
      const int row = m0 + wm * 64 + (m + (g & 1)) * 16 + (lane & 15);
      *(u32x4*)(out + (size_t)row * DM + colw + (g >> 1) * 8) = pair16(o[0], o[1]);
    }
  }
  STAMP_AT((KDIM == DM ? 3 : 5), 3);
}

typedef GCfg<256, 4, 1, 4, 4> CG;
template <int NH_>
DI void glu_wave(lds_ptr smem, Loader<CG>& ld, int lane, int wm, int t0, int m0, int f0, u16* __restrict__ out) {
  constexpr int NT = 2 * NH_;
  int waddr[NT];
#pragma unroll
  for (int n = 0; n < NH_; ++n) {
    waddr[n] = wfrag_addr(t0 + n, lane);
    waddr[NH_ + n] = waddr[n] + 8192;
  }
  const int aaddr = afrag_addr<256>(wm * 64, lane);
  f32x4 acc[NT][4];
#pragma unroll
  for (int n = 0; n < NT; ++n)
#pragma unroll
    for (int m = 0; m < 4; ++m) acc[n][m] = f32x4{0.f, 0.f, 0.f, 0.f};
  Engine<256, 4, 1, 4, 4, NT, 1, DM / 32, 4, 0, 4, (NH_ == 3)> eng(acc, smem, ld, (long)SEQ * 32, (long)32 * FF, aaddr, waddr, 0);
  eng.start();
  eng.run();
  const int g = lane >> 4;
#pragma unroll
  for (int n = 0; n < NH_; ++n) {
    const int cb = (f0 >> 4) + t0 + n;
#pragma unroll
    for (int m = 0; m < 4; m += 2) {
      u32x2 o[2];
#pragma unroll
      for (int i = 0; i < 2; ++i) {
        f32x4 a = acc[n][m + i], b = acc[NH_ + n][m + i];
        o[i][0] = pack2(rbf(a[0]) * rbf(b[0]), rbf(a[1]) * rbf(b[1]));
        o[i][1] = pack2(rbf(a[2]) * rbf(b[2]), rbf(a[3]) * rbf(b[3]));
      }
      const int row = m0 + wm * 64 + (m + (g & 1)) * 16 + (lane & 15);
      st16(out + ((size_t)cb * SEQ + row) * 16 + (g >> 1) * 8, pair16(o[0], o[1]));
    }
  }
  STAMP_AT(4, 3);
}

__global__ __launch_bounds__(512, 2) void k_glu(const u16* __restrict__ Ablk, const u16* __restrict__ Wg,
                                                 const u16* __restrict__ Wu, u16* __restrict__ out) {
  extern __shared__ __attribute__((aligned(16))) char smem_[];
  lds_ptr smem = (lds_ptr)smem_;
  const int lane = threadIdx.x & 63;
  const int w = __builtin_amdgcn_readfirstlane(threadIdx.x >> 6);
  const int xcd = blockIdx.x & 7, idx = blockIdx.x >> 3;
  const int tm = idx & 7, tn = xcd * 4 + (idx >> 3);
  const int m0 = tm * 256, f0 = tn * 112;
  Loader<CG> ld;
  const int lw = w & 3;
  setup_loader_A<CG, 256, 4, 1>(ld, Ablk, m0, lw, lane);
#pragma unroll
  for (int i = 0; i < CG::WPW; ++i) {
    int b = lw + 4 * i;
    int grp = b >> 2, o = b & 3;
    ld.woff[i] = CG::A_SUB + grp * 4096 + o * 1024;
    int k = 8 * o + (lane >> 3);
    int f = ((lane >> 4) & 1) | ((o & 1) << 1);
    int pr = ((lane & 7) >> 1) ^ f;
    if ((grp & 1) && pr == 3) pr = 0;
    int col = f0 + (grp & 1) * 64 + pr * 16 + (lane & 1) * 8;
    ld.wv[i] = (k * FF + col) * 2;
    ld.wbase[i] = (const char*)(grp >> 1 ? Wu : Wg);
  }
  const int wm = w & 3;
  if (w < 4) glu_wave<4>(smem, ld, lane, wm, 0, m0, f0, out);
  else glu_wave<3>(smem, ld, lane, wm, 4, m0, f0, out);
}

constexpr int ACH = 128, ANST = 4, ASTAGE = ACH * 64 * 2 + 256;
constexpr int ALDS = ANST * ASTAGE + 1024 + 16;

DI float rowmax16(const f32x16& v) {
  float mx = fmaxf(fmaxf(v[0], v[1]), v[2]);
#pragma unroll
  for (int r = 3; r < 15; r += 2) mx = fmaxf(fmaxf(mx, v[r]), v[r + 1]);
  mx = fmaxf(mx, v[15]);
  auto sw = __builtin_amdgcn_permlane32_swap(__float_as_uint(mx), __float_as_uint(mx), false, false);
  return fmaxf(__uint_as_float(sw[0]), __uint_as_float(sw[1]));
}

template <bool SAFE>
DI void attn_pass(lds_ptr smem, int lane, int w, const char* Kp, const char* Vp, const char* pf_wm, const char* pf_wo, const char* pf_wg,
                  const char* pf_wu, const char* pf_wd, const bf16x8& qa0, const bf16x8& qa1, const bf16x8& qb0,
                  const bf16x8& qb1, f32x16& oa, f32x16& ob, float& refa, float& refb, float& la_out, float& lb_out,
                  ModTask& mt, const u16* c) {
  const int r32 = lane & 31, hh = lane >> 5, kh = w >> 2;
  const unsigned kvo = ((32 * (w & 3) + (lane >> 1)) * 16 + (((lane & 1) ^ ((lane >> 4) & 1)) * 8)) * 2;
  const unsigned vvo = ((32 * (w & 3)) * 16) * 2 + lane * 16;
  const char* ksrc = Kp;
  const char* vsrc = Vp;
  auto issue = [&](int buf) {
    if (w < 4) {
      const unsigned l0 = lds_u32(smem) + buf * ASTAGE + w * 1024;
      glds16(ksrc, kvo, l0);
      glds16(ksrc + SEQ * 32, kvo, l0 + 4096);
      glds16(vsrc, vvo, l0 + 8192);
      glds16(vsrc + SEQ * 32, vvo, l0 + 8192 + 4096 + 128);
    }
    ksrc += ACH * 32; vsrc += ACH * 32;
  };
  const unsigned pfo = lane * 16;
  auto prefetch = [&](int ch) {
    if (w >= 4) {
#pragma unroll
      for (int i = 0; i < 3; ++i) {
        int pc = (ch * 3 + i) * 4 + (w - 4);
        const char* base;
        if (pc < 32) base = pf_wm + (size_t)(blockIdx.x * 4 + (pc >> 3)) * (12 * DM) + 4 * DM + (pc & 7) * 1024;
        else if (pc < 40) base = pf_wo + (size_t)blockIdx.x * 8192 + (pc - 32) * 1024;
        else if (pc < 68) base = pf_wd + (size_t)blockIdx.x * 28672 + (pc - 40) * 1024;
        else if (pc < 96) base = pf_wg + (size_t)blockIdx.x * 28672 + (pc - 68) * 1024;
        else if (pc < 124) base = pf_wu + (size_t)blockIdx.x * 28672 + (pc - 96) * 1024;
        else break;
        glds16(base, pfo, lds_u32(smem) + ANST * ASTAGE);
      }
    }
  };
  issue(0); issue(1); issue(2);
  const int kaddr0 = kh * 2048 + r32 * 32 + ((hh ^ ((r32 >> 3) & 1)) * 16);
  const int kaddr1 = kaddr0 + 4096;
  const int vaddr = 8192 + kh * 2048 + ((lane >> 4) & 1) * (4096 + 128) + (4 * hh + ((lane & 15) >> 2)) * 32 + (lane & 3) * 8;
  bf16x8 sel;
  {
    const short one = ((lane & 15) == ((lane >> 4) & 1)) ? (short)0x3F80 : (short)0;
#pragma unroll
    for (int j = 0; j < 8; ++j) sel[j] = one;
  }
  f32x16 nma, nmb, sa, sb;
#pragma unroll
  for (int r = 0; r < 16; ++r) { oa[r] = 0.f; ob[r] = 0.f; nma[r] = 0.f; nmb[r] = 0.f; }
  f32x4 lsa = {0.f, 0.f, 0.f, 0.f}, lsb = {0.f, 0.f, 0.f, 0.f};
  float la = 0.f, lb = 0.f;
  constexpr int NCH = SEQ / ACH;
  if (w < 4) wait_vm<8>();
  bar();
  const bf16x8 kf0_ = lds_read128(smem + kaddr0);
  const bf16x8 kf1_ = lds_read128(smem + kaddr1);
  {
    sa = MFMA32(kf0_, qa0, nma); sa = MFMA32(kf1_, qa1, sa);
    sb = MFMA32(kf0_, qb0, nmb); sb = MFMA32(kf1_, qb1, sb);
    const float ma = rowmax16(sa), mb = rowmax16(sb);
#pragma unroll
    for (int r = 0; r < 16; ++r) { nma[r] = -ma; sa[r] -= ma; nmb[r] = -mb; sb[r] -= mb; }
  }
  if constexpr (SAFE) {
    auto half = [&](f32x16& s, f32x16& o, f32x16& nm, float& l, const bf16x8& q0f, const bf16x8& q1f,
                    const bf16x8& kn0, const bf16x8& kn1, const bf16x8& vf0, const bf16x8& vf1, bool has_next) {
      f32x16 p;
      const float dl = fmaxf(rowmax16(s), 0.f);
      const float f = __builtin_amdgcn_exp2f(-dl);
      float t = 0.f;
#pragma unroll
      for (int r = 0; r < 16; ++r) { nm[r] -= dl; o[r] *= f; p[r] = __builtin_amdgcn_exp2f(s[r] - dl); t += p[r]; }
      l = l * f + t;
      if (has_next) { s = MFMA32(kn0, q0f, nm); s = MFMA32(kn1, q1f, s); }
      u32x4 pa, pb;
#pragma unroll
      for (int j = 0; j < 4; ++j) { pa[j] = pack2(p[2 * j], p[2 * j + 1]); pb[j] = pack2(p[8 + 2 * j], p[8 + 2 * j + 1]); }
      o = MFMA32(vf0, __builtin_bit_cast(bf16x8, pa), o);
      o = MFMA32(vf1, __builtin_bit_cast(bf16x8, pb), o);
    };
    auto step = [&](lds_cptr vb, lds_cptr knext, bool has_next) {
      bf16x8 kn0, kn1;
      if (has_next) { kn0 = lds_read128(knext + kaddr0); kn1 = lds_read128(knext + kaddr1); }
      bf16x8 vf0 = lds_tr8(vb, vb + 256);
      bf16x8 vf1 = lds_tr8(vb + 512, vb + 768);
      half(sa, oa, nma, la, qa0, qa1, kn0, kn1, vf0, vf1, has_next);
      half(sb, ob, nmb, lb, qb0, qb1, kn0, kn1, vf0, vf1, has_next);
    };
    for (int ch = 0; ch < NCH; ++ch) {
      if (w < 4) { if (ch + 2 < NCH) wait_vm<4>(); else wait_vm<0>(); }
      bar();
      if (ch + 3 < NCH) issue((ch + 3) & 3);
      lds_cptr sbuf = smem + (ch & 3) * ASTAGE;
      lds_cptr nbuf = smem + ((ch + 1) & 3) * ASTAGE;
      step(sbuf + vaddr, sbuf + 1024, true);
      step(sbuf + 1024 + vaddr, nbuf, ch + 1 < NCH);
    }
  } else {
    u32x4 pa0, pa1, pb0, pb1;
    bf16x8 kc0 = kf0_, kc1 = kf1_;
    bf16x8 vp0, vp1;
#pragma unroll
    for (int j = 0; j < 4; ++j) { pb0[j] = 0u; pb1[j] = 0u; }
#pragma unroll
    for (int j = 0; j < 8; ++j) { vp0[j] = 0; vp1[j] = 0; }
#define SB_ __builtin_amdgcn_sched_barrier(0)
#define EX(i) e[i] = __builtin_amdgcn_exp2f(S[i])
#define PHASE(S, P0, P1, M0, M1, M2, M3, M4, M5, R0, R1) do { float e[16]; \
      M0; SB_; R0; EX(0); EX(1); EX(2); EX(3); SB_; \
      M1; SB_; R1; EX(4); EX(5); EX(6); EX(7); SB_; \
      M2; SB_; EX(8); EX(9); P0[0] = pack2(e[0], e[1]); P0[1] = pack2(e[2], e[3]); SB_; \
      M3; SB_; EX(10); EX(11); P0[2] = pack2(e[4], e[5]); P0[3] = pack2(e[6], e[7]); SB_; \
      M4; SB_; EX(12); EX(13); EX(14); EX(15); SB_; \
      M5; SB_; P1[0] = pack2(e[8], e[9]); P1[1] = pack2(e[10], e[11]); P1[2] = pack2(e[12], e[13]); P1[3] = pack2(e[14], e[15]); SB_; \
    } while (0)
#define BF(x) __builtin_bit_cast(bf16x8, x)
    auto step = [&](lds_cptr vb, lds_cptr knext, auto has_next_t) {
      constexpr bool HN = decltype(has_next_t)::value;
      bf16x8 kn0, kn1, vc0, vc1;
      { const f32x16& S = sa;
        PHASE(S, pa0, pa1,
              sb = MFMA32(kc0, qb0, nmb), sb = MFMA32(kc1, qb1, sb),
              ob = MFMA32(vp0, BF(pb0), ob), ob = MFMA32(vp1, BF(pb1), ob),
              lsb = MFMA16(sel, BF(pb0), lsb), lsb = MFMA16(sel, BF(pb1), lsb),
              if (HN) { kn0 = lds_read128(knext + kaddr0); kn1 = lds_read128(knext + kaddr1); },
              { vc0 = lds_tr8(vb, vb + 256); vc1 = lds_tr8(vb + 512, vb + 768); }); }
      { const f32x16& S = sb;
        PHASE(S, pb0, pb1,
              if (HN) sa = MFMA32(kn0, qa0, nma), if (HN) sa = MFMA32(kn1, qa1, sa),
              oa = MFMA32(vc0, BF(pa0), oa), oa = MFMA32(vc1, BF(pa1), oa),
              lsa = MFMA16(sel, BF(pa0), lsa), lsa = MFMA16(sel, BF(pa1), lsa),
              , ); }
      if (HN) { kc0 = kn0; kc1 = kn1; }
      vp0 = vc0; vp1 = vc1;
    };
    typedef std::integral_constant<bool, true> T_;
    typedef std::integral_constant<bool, false> F_;
    for (int ch = 0; ch < NCH - 1; ++ch) {
      if (w < 4) { if (ch + 2 < NCH) wait_vm<4>(); else wait_vm<0>(); }
      bar();
      if (ch + 3 < NCH) issue((ch + 3) & 3);
      prefetch(ch);
      lds_cptr sbuf = smem + (ch & 3) * ASTAGE;
      lds_cptr nbuf = smem + ((ch + 1) & 3) * ASTAGE;
      step(sbuf + vaddr, sbuf + 1024, T_{});
      step(sbuf + 1024 + vaddr, nbuf, T_{});
    }
    {
      const int task = blockIdx.x * 2 + (threadIdx.x >> 8);
      mod_load(mt, c, (const u16*)pf_wm, 32 + (task >> 3), task & 7, threadIdx.x & 255);
      lds_cptr sbuf = smem + ((NCH - 1) & 3) * ASTAGE;
      step(sbuf + vaddr, sbuf + 1024, T_{});
      step(sbuf + 1024 + vaddr, sbuf, F_{});
    }
    ob = MFMA32(vp0, BF(pb0), ob); ob = MFMA32(vp1, BF(pb1), ob);
    lsb = MFMA16(sel, BF(pb0), lsb); lsb = MFMA16(sel, BF(pb1), lsb);
#undef PHASE
#undef EX
#undef BF
  }
  refa = nma[0]; refb = nmb[0];
  if constexpr (SAFE) {
    auto s1 = __builtin_amdgcn_permlane32_swap(__float_as_uint(la), __float_as_uint(la), false, false);
    la_out = __uint_as_float(s1[0]) + __uint_as_float(s1[1]);
    auto s2 = __builtin_amdgcn_permlane32_swap(__float_as_uint(lb), __float_as_uint(lb), false, false);
    lb_out = __uint_as_float(s2[0]) + __uint_as_float(s2[1]);
  } else {
    const float a0 = __shfl(lsa[0], lane & 15), a1 = __shfl(lsa[1], lane & 15);
    const float b0 = __shfl(lsb[0], lane & 15), b1 = __shfl(lsb[1], lane & 15);
    la_out = (lane & 16) ? a1 : a0;
    lb_out = (lane & 16) ? b1 : b0;
  }
}

__global__ __launch_bounds__(512, 2) void k_attn(const u16* __restrict__ qkv, u16* __restrict__ oblk,
                                                  const u16* __restrict__ c, const u16* __restrict__ wmod, float* __restrict__ part,
                                                  const char* __restrict__ pf_wo, const char* __restrict__ pf_wg,
                                                  const char* __restrict__ pf_wu, const char* __restrict__ pf_wd) {
  extern __shared__ __attribute__((aligned(16))) char smem_[];
  lds_ptr smem = (lds_ptr)smem_;
  const int lane = threadIdx.x & 63;
  const int w = __builtin_amdgcn_readfirstlane(threadIdx.x >> 6);
  const int xcd = blockIdx.x & 7, idx = blockIdx.x >> 3;
  const int head = xcd * 4 + (idx >> 3), qb = idx & 7;
  const int r32 = lane & 31, hh = lane >> 5;
  const int qg = w & 3, kh = w >> 2;
  const u16* Qp = qkv + (size_t)head * SEQ * 32;
  const char* Kp = (const char*)(qkv + (size_t)(32 + head) * SEQ * 32);
  const char* Vp = (const char*)(qkv + (size_t)(64 + head) * SEQ * 32);
  const int q0 = qb * 256 + qg * 64;
  STAMP_AT(2, 0);
  __attribute__((address_space(3))) int* flag = (__attribute__((address_space(3))) int*)(smem + ANST * ASTAGE + 1024);
  if (threadIdx.x == 0) *flag = 0;
  bf16x8 qa0 = *(const bf16x8*)(Qp + (q0 + r32) * 16 + hh * 8);
  bf16x8 qa1 = *(const bf16x8*)(Qp + (SEQ + q0 + r32) * 16 + hh * 8);
  bf16x8 qb0 = *(const bf16x8*)(Qp + (q0 + 32 + r32) * 16 + hh * 8);
  bf16x8 qb1 = *(const bf16x8*)(Qp + (SEQ + q0 + 32 + r32) * 16 + hh * 8);
  f32x16 oa, ob;
  float nma, nmb, la, lb;
  ModTask mt;
  attn_pass<false>(smem, lane, w, Kp, Vp, (const char*)wmod, pf_wo, pf_wg, pf_wu, pf_wd, qa0, qa1, qb0, qb1, oa, ob, nma, nmb, la, lb, mt, c);
  {
    float big = fmaxf(fabsf(la), fabsf(lb));
#pragma unroll
    for (int r = 0; r < 16; ++r) big = fmaxf(big, fmaxf(fabsf(oa[r]), fabsf(ob[r])));
    bool bad = !(big < 1e30f);
#pragma unroll
    for (int r = 0; r < 16; ++r) bad = bad || (oa[r] != oa[r]) || (ob[r] != ob[r]);
    bad = bad || (la != la) || (lb != lb);
    if (__any(bad) && lane == 0) *flag = 1;
  }
  __syncthreads();
  if (__builtin_amdgcn_readfirstlane(*flag) != 0) {
    attn_pass<true>(smem, lane, w, Kp, Vp, (const char*)wmod, pf_wo, pf_wg, pf_wu, pf_wd, qa0, qa1, qb0, qb1, oa, ob, nma, nmb, la, lb, mt, c);
    __syncthreads();
  }
  STAMP_AT(2, 2);
  const int task = blockIdx.x * 2 + (threadIdx.x >> 8);
  {
    __attribute__((address_space(3))) float* xb = (__attribute__((address_space(3))) float*)smem;
    const f32x16& og = kh == 0 ? ob : oa;
    __attribute__((address_space(3))) float* dst = xb + w * (18 * 64) + lane;
#pragma unroll
    for (int r = 0; r < 16; ++r) dst[r * 64] = og[r];
    dst[16 * 64] = kh == 0 ? lb : la;
    dst[17 * 64] = kh == 0 ? nmb : nma;
    __syncthreads();
    const __attribute__((address_space(3))) float* src = xb + (w ^ 4) * (18 * 64) + lane;
    f32x16 om = kh == 0 ? oa : ob;
    const float nm_me = kh == 0 ? nma : nmb;
    const float nm_ot = src[17 * 64];
    const float nmin = fminf(nm_me, nm_ot);
    const float f_me = __builtin_amdgcn_exp2f(nmin - nm_me), f_ot = __builtin_amdgcn_exp2f(nmin - nm_ot);
    const float rl = 1.f / ((kh == 0 ? la : lb) * f_me + src[16 * 64] * f_ot);
    const float g_me = f_me * rl, g_ot = f_ot * rl;
    u16* op = oblk + ((size_t)head * 2 * SEQ + q0 + kh * 32 + r32) * 16 + 4 * hh;
#pragma unroll
    for (int g = 0; g < 4; ++g) {
      float v0 = om[4 * g] * g_me + src[(4 * g) * 64] * g_ot;
      float v1 = om[4 * g + 1] * g_me + src[(4 * g + 1) * 64] * g_ot;
      float v2 = om[4 * g + 2] * g_me + src[(4 * g + 2) * 64] * g_ot;
      float v3 = om[4 * g + 3] * g_me + src[(4 * g + 3) * 64] * g_ot;
      u32x2 v;
      v[0] = pack2(v0, v1);
      v[1] = pack2(v2, v3);
      *(u32x2*)(op + (g >> 1) * (SEQ * 16) + (g & 1) * 8) = v;
    }
  }
  mod_finish(mt, part, 32 + (task >> 3), task & 7, threadIdx.x & 255, (float*)(smem_ + 40960) + (threadIdx.x >> 8) * 256);
  wait_vm<0>();
  STAMP_AT(2, 3);
}

extern "C" void kernel_launch(void* const* d_in, const int* in_sizes, int n_in, void* d_out, int out_size,
                              void* d_ws, size_t ws_size, hipStream_t stream) {
  const u16* x = (const u16*)d_in[0];
  const u16* wqkv = (const u16*)d_in[1];
  const u16* wo = (const u16*)d_in[2];
  const u16* wg = (const u16*)d_in[4];
  const u16* wu = (const u16*)d_in[5];
  const u16* wd = (const u16*)d_in[6];
  const u16* c = (const u16*)d_in[7];
  const u16* wmod = (const u16*)d_in[8];
  u16* out = (u16*)d_out;
  char* ws = (char*)d_ws;
  float* part = (float*)ws;
  float* modf = (float*)(ws + (256 << 10));
  unsigned* flags = (unsigned*)(ws + (512 << 10));
  u16* h1 = (u16*)(ws + (1 << 20));
  u16* qkv = (u16*)(ws + (8 << 20));
  u16* ob = (u16*)(ws + (24 << 20));
  u16* x2 = (u16*)(ws + (32 << 20));
  u16* h2 = (u16*)(ws + (40 << 20));
  u16* hh = (u16*)(ws + (48 << 20));
  u16* wdp = (u16*)(ws + (64 << 20));

  hipFuncSetAttribute((const void*)k_qkv, hipFuncAttributeMaxDynamicSharedMemorySize, CQ::LDS);
  hipFuncSetAttribute((const void*)k_proj<DM, DM>, hipFuncAttributeMaxDynamicSharedMemorySize, CP::LDS);
  hipFuncSetAttribute((const void*)k_proj<FF, WDP>, hipFuncAttributeMaxDynamicSharedMemorySize, CP::LDS);
  hipFuncSetAttribute((const void*)k_glu, hipFuncAttributeMaxDynamicSharedMemorySize, CG::LDS);
  hipFuncSetAttribute((const void*)k_attn, hipFuncAttributeMaxDynamicSharedMemorySize, ALDS);

  k_pre<<<256 + 256, 256, 0, stream>>>(c, wmod, part, (const char*)x, (const char*)wqkv, flags);
  k_ln<6, MNS, 192><<<256 + 192, 512, 0, stream>>>(x, part, h1, (const char*)wqkv, wd, wdp);
  k_qkv<<<256, 768, CQ::LDS, stream>>>(h1, wqkv, qkv);
  k_attn<<<256, 512, ALDS, stream>>>(qkv, ob, c, wmod, part, (const char*)wo, (const char*)wg, (const char*)wu, (const char*)wd);
  k_proj<DM, DM><<<256, 768, CP::LDS, stream>>>(ob, wo, x, part + 2 * DM, x2);
  k_ln<7, MNS, 0><<<256 + 224, 512, 0, stream>>>(x2, part + 3 * DM, h2, (const char*)wqkv, wd, wdp);
  k_glu<<<256, 512, CG::LDS, stream>>>(h2, wg, wu, hh);
  k_proj<FF, WDP><<<256, 768, CP::LDS, stream>>>(hh, wdp, x2, part + 5 * DM, out);
}
```

```cpp
#include <hip/hip_runtime.h>
#include <hip/hip_bf16.h>
#include <stdint.h>

typedef __attribute__((ext_vector_type(8))) short bf16x8;
typedef __attribute__((ext_vector_type(4))) short s16x4;
typedef __attribute__((ext_vector_type(4))) float f32x4;
typedef __attribute__((ext_vector_type(2))) float f32x2;
typedef __attribute__((ext_vector_type(16))) float f32x16;
typedef __attribute__((ext_vector_type(4))) unsigned u32x4;
typedef __attribute__((ext_vector_type(2))) unsigned u32x2;
typedef __attribute__((ext_vector_type(2))) __bf16 bf16x2_t;
typedef unsigned short u16;
typedef __attribute__((address_space(3))) char* lds_ptr;
typedef __attribute__((address_space(3))) const char* lds_cptr;

#define DI __device__ __forceinline__

constexpr int SEQ = 2048, DM = 1024, FF = 3584, NH = 32;

DI float bf2f(u16 x) { return __uint_as_float(((unsigned)x) << 16); }
DI float bflo(unsigned x) { return __uint_as_float(x << 16); }
DI float bfhi(unsigned x) { return __uint_as_float(x & 0xffff0000u); }
DI unsigned pack2(float a, float b) {
  f32x2 v = {a, b};
  bf16x2_t r = __builtin_convertvector(v, bf16x2_t);
  return __builtin_bit_cast(unsigned, r);
}
DI float rbf(float a) { return bflo(pack2(a, 0.f) & 0xffffu); }

#define MFMA16(a, b, c) __builtin_amdgcn_mfma_f32_16x16x32_bf16((a), (b), (c), 0, 0, 0)
#define MFMA32(a, b, c) __builtin_amdgcn_mfma_f32_32x32x16_bf16((a), (b), (c), 0, 0, 0)

DI void glds16(const void* base, unsigned voff, unsigned lds_addr) {
  unsigned sv;
  asm volatile("s_mov_b32 %0, m0\n\ts_mov_b32 m0, %3\n\ts_nop 0\n\tglobal_load_lds_dwordx4 %1, %2\n\ts_mov_b32 m0, %0"
               : "=&s"(sv) : "v"(voff), "s"(base), "s"(lds_addr) : "memory");
}
DI unsigned lds_u32(lds_ptr p) { return (unsigned)(uintptr_t)p; }
DI bf16x8 lds_read128(lds_cptr p) { return *(const __attribute__((address_space(3))) bf16x8*)p; }
DI s16x4 lds_tr(lds_cptr p) {
  typedef short v4i16_t __attribute__((ext_vector_type(4)));
  return __builtin_bit_cast(s16x4, __builtin_amdgcn_ds_read_tr16_b64_v4i16((__attribute__((address_space(3))) v4i16_t*)p));
}
DI bf16x8 lds_tr8(lds_cptr p0, lds_cptr p1) {
  s16x4 a = lds_tr(p0), b = lds_tr(p1);
  return __builtin_shufflevector(a, b, 0, 1, 2, 3, 4, 5, 6, 7);
}
#define STAMP_AT(kid, slot) do {} while (0)
template <int N> DI void wait_vm() { asm volatile("s_waitcnt vmcnt(%0)" ::"n"(N) : "memory"); }
DI void bar() { __builtin_amdgcn_s_barrier(); }

constexpr int MNS = 8;
struct ModTask { u32x4 w[4]; u16 cv[4]; };
DI void mod_load(ModTask& mt, const u16* __restrict__ c, const u16* __restrict__ wmod, int cb, int ks, int t) {
  const int cc = t & 7, rg = t >> 3;
  const int k0 = ks * 128 + rg;
  const u16* wp = wmod + (size_t)k0 * (6 * DM) + cb * 64 + cc * 8;
#pragma unroll
  for (int i = 0; i < 4; ++i) mt.w[i] = *(const u32x4*)(wp + (size_t)i * 32 * (6 * DM));
#pragma unroll
  for (int i = 0; i < 4; ++i) mt.cv[i] = c[k0 + 32 * i];
}
DI void mod_finish(const ModTask& mt, float* __restrict__ part, int cb, int ks, int t, float* s_red  ) {
  float acc[8];
#pragma unroll
  for (int j = 0; j < 8; ++j) acc[j] = 0.f;
#pragma unroll
  for (int i = 0; i < 4; ++i) {
    const float cv = bf2f(mt.cv[i]);
    const float sv = rbf(cv / (1.f + __expf(-cv)));
#pragma unroll
    for (int j = 0; j < 4; ++j) {
      acc[2 * j] += sv * bflo(mt.w[i][j]);
      acc[2 * j + 1] += sv * bfhi(mt.w[i][j]);
    }
  }
#pragma unroll
  for (int j = 0; j < 8; ++j) {
    float v = acc[j];
    v += __shfl_xor(v, 8);
    v += __shfl_xor(v, 16);
    v += __shfl_xor(v, 32);
    acc[j] = v;
  }
  const int lane = t & 63, wv = t >> 6;
  if (lane < 8) {
#pragma unroll
    for (int j = 0; j < 8; ++j) s_red[wv * 64 + lane * 8 + j] = acc[j];
  }
  __syncthreads();
  if (t < 64) part[ks * (6 * DM) + cb * 64 + t] = (s_red[t] + s_red[64 + t]) + (s_red[128 + t] + s_red[192 + t]);
}
DI f32x4 gate4(const float* __restrict__ gate, int col) {
  f32x4 gt = *(const f32x4*)(gate + col);
#pragma unroll
  for (int i = 1; i < MNS; ++i) gt += *(const f32x4*)(gate + i * (6 * DM) + col);
#pragma unroll
  for (int j = 0; j < 4; ++j) gt[j] = rbf(gt[j]);
  return gt;
}

DI void touch16k(const char* p, int t) {
  u32x4 v[4];
#pragma unroll
  for (int i = 0; i < 4; ++i) v[i] = *(const u32x4*)(p + i * 4096 + t * 16);
#pragma unroll
  for (int i = 0; i < 4; ++i) asm volatile("" ::"v"(v[i]));
}

__global__ __launch_bounds__(256) void k_pre(const u16* __restrict__ c, const u16* __restrict__ wmod, float* __restrict__ part,
                                              const char* __restrict__ x, const char* __restrict__ wqkv,
                                              unsigned* __restrict__ flags, float* __restrict__ stats) {
  __shared__ float s_red[256];
  const int t = threadIdx.x, b = blockIdx.x;
  if (b < 256) STAMP_AT(0, 0);
  if (b < 256) {
    ModTask mt;
    mod_load(mt, c, wmod, b >> 3, b & 7, t);
    mod_finish(mt, part, b >> 3, b & 7, t, s_red);
    if (b == 0) flags[t * 16] = 0u;
  } else if (b < 512) {
    const int lane = t & 63, wv = t >> 6;
    u32x4 v[2][2];
#pragma unroll
    for (int r = 0; r < 2; ++r) {
      const char* xr = x + (size_t)((b - 256) * 8 + wv * 2 + r) * (DM * 2);
      v[r][0] = *(const u32x4*)(xr + lane * 16);
      v[r][1] = *(const u32x4*)(xr + 1024 + lane * 16);
    }
#pragma unroll
    for (int r = 0; r < 2; ++r) {
      float f[16];
#pragma unroll
      for (int j = 0; j < 4; ++j) {
        f[2 * j] = bflo(v[r][0][j]); f[2 * j + 1] = bfhi(v[r][0][j]);
        f[8 + 2 * j] = bflo(v[r][1][j]); f[8 + 2 * j + 1] = bfhi(v[r][1][j]);
      }
      float s = 0.f;
#pragma unroll
      for (int j = 0; j < 16; ++j) s += f[j];
#pragma unroll
      for (int o = 1; o < 64; o <<= 1) s += __shfl_xor(s, o);
      const float mean = s * (1.f / DM);
      float q = 0.f;
#pragma unroll
      for (int j = 0; j < 16; ++j) { float d = f[j] - mean; q += d * d; }
#pragma unroll
      for (int o = 1; o < 64; o <<= 1) q += __shfl_xor(q, o);
      const float rden = 1.f / rbf(sqrtf(rbf(rbf(q * (1.f / DM)) + 1e-5f)));
      if (lane == 0) *(f32x2*)(stats + ((b - 256) * 8 + wv * 2 + r) * 2) = f32x2{-rbf(mean) * rden, rden};
    }
  } else {
    touch16k(wqkv + (size_t)(b - 512) * 16384, t);
  }
  if (b < 256) STAMP_AT(0, 3);
}

constexpr int WDP = DM + 64;
template <int KID, int NS, int NPF>
__global__ __launch_bounds__(512) void k_ln(const u16* __restrict__ x, const float* __restrict__ ss  ,
                                             u16* __restrict__ h16, const char* __restrict__ pf,
                                             const u16* __restrict__ wsrc, u16* __restrict__ wdst) {
  __shared__ __attribute__((aligned(16))) float sm[2048];
  const int t = threadIdx.x;
  if (blockIdx.x >= 256 + NPF) {
    const int r0 = (blockIdx.x - 256 - NPF) * 16 + (t >> 7);
    u32x4 v[4];
#pragma unroll
    for (int i = 0; i < 4; ++i) v[i] = *(const u32x4*)(wsrc + (size_t)(r0 + 4 * i) * DM + (t & 127) * 8);
#pragma unroll
    for (int i = 0; i < 4; ++i) *(u32x4*)(wdst + (size_t)(r0 + 4 * i) * WDP + (t & 127) * 8) = v[i];
    return;
  }
  if (blockIdx.x >= 256) {
    touch16k(pf + (size_t)((blockIdx.x - 256) * 2 + (t >> 8)) * 16384, t & 255);
    return;
  }
  const int lane = t & 63, wv = t >> 6;
  const int row = blockIdx.x * 8 + wv;
  STAMP_AT(KID, 0);
  const u16* xr = x + (size_t)row * DM;
  u32x4 v0 = *(const u32x4*)(xr + lane * 8);
  u32x4 v1 = *(const u32x4*)(xr + 512 + lane * 8);
  {
    f32x4 pv[NS];
#pragma unroll
    for (int i = 0; i < NS; ++i) pv[i] = *(const f32x4*)(ss + i * (6 * DM) + t * 4);
    f32x4 a = pv[0];
#pragma unroll
    for (int i = 1; i < NS; ++i) a += pv[i];
#pragma unroll
    for (int j = 0; j < 4; ++j) {
      float v = rbf(a[j]);
      a[j] = t < 256 ? v : rbf(1.f + v);
    }
    *(f32x4*)(sm + t * 4) = a;
  }
  float f[16];
#pragma unroll
  for (int j = 0; j < 4; ++j) {
    f[2 * j] = bflo(v0[j]); f[2 * j + 1] = bfhi(v0[j]);
    f[8 + 2 * j] = bflo(v1[j]); f[8 + 2 * j + 1] = bfhi(v1[j]);
  }
  float s = 0.f;
#pragma unroll
  for (int j = 0; j < 16; ++j) s += f[j];
#pragma unroll
  for (int o = 1; o < 64; o <<= 1) s += __shfl_xor(s, o);
  const float mean = s * (1.f / DM);
  float q = 0.f;
#pragma unroll
  for (int j = 0; j < 16; ++j) { float d = f[j] - mean; q += d * d; }
#pragma unroll
  for (int o = 1; o < 64; o <<= 1) q += __shfl_xor(q, o);
  const float mb = rbf(mean);
  const float vb = rbf(q * (1.f / DM));
  const float rden = 1.f / rbf(sqrtf(rbf(vb + 1e-5f)));
  __syncthreads();
#pragma unroll
  for (int half = 0; half < 2; ++half) {
    const int k0 = half * 512 + lane * 8;
    f32x4 sh0 = *(const f32x4*)(sm + k0), sh1 = *(const f32x4*)(sm + k0 + 4);
    f32x4 sc0 = *(const f32x4*)(sm + 1024 + k0), sc1 = *(const f32x4*)(sm + 1024 + k0 + 4);
    float y[8];
#pragma unroll
    for (int j = 0; j < 8; ++j) {
      float g = j < 4 ? sc0[j] : sc1[j - 4];
      float sh = j < 4 ? sh0[j] : sh1[j - 4];
      float d = rbf(f[half * 8 + j] - mb);
      float n = rbf(d * rden);
      y[j] = rbf(n * g) + sh;
    }
    u32x4 o;
#pragma unroll
    for (int j = 0; j < 4; ++j) o[j] = pack2(y[2 * j], y[2 * j + 1]);
    *(u32x4*)(h16 + ((size_t)(k0 >> 4) * SEQ + row) * 16 + (k0 & 15)) = o;
  }
  STAMP_AT(KID, 3);
}

DI void st16(void* p, u32x4 v) {
  *(u32x4*)p = v;
}
DI u32x4 pair16(u32x2 a, u32x2 b) {
  auto s0 = __builtin_amdgcn_permlane16_swap(a[0], b[0], false, false);
  auto s1 = __builtin_amdgcn_permlane16_swap(a[1], b[1], false, false);
  return u32x4{s0[0], s1[0], s0[1], s1[1]};
}

DI int hA(int r) { return (-r) & 3; }

template <int BM, int NGRP, int KS, int NST, int NLW_ = 8, int APAD = 0>
struct GCfg {
  static constexpr int NLW = NLW_;
  static constexpr int A_BLK = BM * 32 + APAD;
  static constexpr int A_SUB = 2 * A_BLK;
  static constexpr int W_SUB = NGRP * 4096;
  static constexpr int STAGE = KS * (A_SUB + W_SUB);
  static constexpr int APW = KS * BM / 16 / NLW;
  static constexpr int WPW = KS * NGRP * 4 / NLW;
  static constexpr int PPW = APW + WPW;
  static constexpr int LDS = STAGE * NST;
};

template <class C>
struct Loader {
  const char* abase;
  const char* wbase[C::WPW];
  unsigned av[C::APW];
  unsigned wv[C::WPW];
  int aoff[C::APW];
  int woff[C::WPW];
};

template <int BM, int NGRP, int KS, int NST, int MT, int NT, int KSPLIT, int NKT, int KID, int IOFS, int NLW = 8, bool LOADS = (NLW == 8), int ARING = 0>
struct Engine {
  typedef GCfg<BM, NGRP, KS, NST, NLW, (ARING ? 32 : 0)> C;
  static constexpr bool DED = !LOADS;
  static constexpr int SPW = KS / KSPLIT;
  static constexpr int P = C::PPW;
  static constexpr int G = NT * SPW;
  static constexpr int WSTG = ARING ? KS * C::W_SUB : C::STAGE;
  static constexpr int ASTG = KS * C::A_SUB;
  static constexpr int ABASE = NST * WSTG;
  int acur, anxt;
  f32x4 (&acc)[NT][MT];
  lds_ptr smem;
  Loader<C>& ld;
  long a_adv, w_adv;
  const int (&waddr)[NT];
  int a_sub0, w_sub0;
  int cur, nxt, fre;
  bf16x8 wf[NT], af[2][MT];

  DI Engine(f32x4 (&acc_)[NT][MT], lds_ptr smem_, Loader<C>& ld_, long a_adv_, long w_adv_, int aaddr, const int (&waddr_)[NT], int kg)
      : acc(acc_), smem(smem_), ld(ld_), a_adv(a_adv_), w_adv(w_adv_), waddr(waddr_) {
    a_sub0 = kg * C::A_SUB + aaddr;
    w_sub0 = (ARING ? 0 : KS * C::A_SUB) + kg * C::W_SUB;
  }
  template <int I> DI void piece(int stage_off) {
    if constexpr (I < C::APW) {
      glds16(ld.abase, ld.av[I], lds_u32(smem) + stage_off + ld.aoff[I]);
      if constexpr (I == C::APW - 1) ld.abase += a_adv * 2;
    } else {
      constexpr int J = I - C::APW;
      glds16(ld.wbase[J], ld.wv[J], lds_u32(smem) + stage_off + ld.woff[J]);
      ld.wbase[J] += w_adv * 2;
    }
  }
  template <int I> DI void issue_all(int stage_off) {
    if constexpr (I < P) { piece<I>(stage_off); issue_all<I + 1>(stage_off); }
  }
  template <int g, int I> DI void issue_at(int stage_off) {
    if constexpr (I < P) {
      constexpr int span = (G - IOFS) > P ? (G - IOFS) : P;
      constexpr int pos = IOFS + (I * span) / P < G ? IOFS + (I * span) / P : G - 1;
      if constexpr (pos == g) piece<I>(stage_off);
      issue_at<g, I + 1>(stage_off);
    }
  }
  template <int PAR, int S, bool PF, bool DOISSUE, int n>
  DI void groups(lds_cptr ab, lds_cptr wb) {
    if constexpr (n < NT) {
#pragma unroll
      for (int m = 0; m < MT; ++m) acc[n][m] = MFMA16(wf[n], af[PAR][m], acc[n][m]);
      if constexpr (PF) {
        wf[n] = lds_tr8(wb + waddr[n], wb + waddr[n] + 512);
#pragma unroll
        for (int m = 0; m < MT; ++m)
          if ((m * NT) / MT == n) af[PAR ^ 1][m] = lds_read128(ab + m * 512);
      }
      if constexpr (DOISSUE && !DED) issue_at<S * NT + n, 0>(fre);
      __builtin_amdgcn_sched_barrier(0);
      groups<PAR, S, PF, DOISSUE, n + 1>(ab, wb);
    }
  }
  template <int PAR, bool DOISSUE, bool LAST, int S> DI void steps() {
    if constexpr (S < SPW) {
      if constexpr (S + 1 < SPW)
        groups<PAR, S, true, DOISSUE, 0>(smem + (ARING ? ABASE + acur : cur) + a_sub0 + (S + 1) * KSPLIT * C::A_SUB, smem + cur + w_sub0 + (S + 1) * KSPLIT * C::W_SUB);
      else if constexpr (!LAST)
        groups<PAR, S, true, DOISSUE, 0>(smem + (ARING ? ABASE + anxt : nxt) + a_sub0, smem + nxt + w_sub0);
      else
        groups<PAR, S, false, false, 0>(smem, smem);
      steps<PAR ^ 1, DOISSUE, LAST, S + 1>();
    }
  }
  template <int PAR, bool DOISSUE, bool LAST, int WAITN> DI void iter() {
    if constexpr (!LAST) { if constexpr (!DED) wait_vm<WAITN>(); bar(); }
    steps<PAR, DOISSUE, LAST, 0>();
    fre = cur; cur = nxt; nxt = (nxt + WSTG == NST * WSTG) ? 0 : nxt + WSTG;
    if constexpr (ARING > 0) { acur = anxt; anxt = (anxt + ASTG == ARING * ASTG) ? 0 : anxt + ASTG; }
  }
  template <int KT> DI void tail() {
    if constexpr (KT < NKT) {
      constexpr bool DOISSUE = KT + NST - 1 < NKT;
      constexpr bool LAST = KT == NKT - 1;
      constexpr int newest = (KT + NST - 2) < (NKT - 1) ? (KT + NST - 2) : (NKT - 1);
      constexpr int younger = newest - (KT + 1) > 0 ? newest - (KT + 1) : 0;
      iter<(KT * SPW) & 1, DOISSUE, LAST, P * younger>();
      tail<KT + 1>();
    }
  }
  DI void start() {
    static_assert(NKT >= NST + 1, "too few k tiles");
    STAMP_AT(KID, 0);
    if constexpr (!DED) {
#pragma unroll
      for (int s = 0; s < NST - 1; ++s) issue_all<0>(s * C::STAGE);
    }
  }
  template <int KT> DI void ltail() {
    if constexpr (KT < NKT - 1) {
      constexpr int newest = (KT + NST - 2) < (NKT - 1) ? (KT + NST - 2) : (NKT - 1);
      constexpr int younger = newest - (KT + 1) > 0 ? newest - (KT + 1) : 0;
      wait_vm<P * younger>();
      bar();
      ltail<KT + 1>();
    }
  }
  DI void loader_run() {
#pragma unroll
    for (int s = 0; s < NST - 1; ++s) issue_all<0>(s * C::STAGE);
    wait_vm<P*(NST - 2)>();
    bar();
    fre = (NST - 1) * C::STAGE;
    for (int kt = 0; kt < NKT - NST + 1; ++kt) {
      wait_vm<P*(NST - 3)>();
      bar();
      issue_all<0>(fre);
      fre = (fre + C::STAGE == NST * C::STAGE) ? 0 : fre + C::STAGE;
    }
    ltail<NKT - NST + 1>();
  }
  DI void run() { run([] {}); }
  template <class Hook> DI void run(Hook hook) {
    if constexpr (!DED) wait_vm<P*(NST - 2)>();
    bar();
    STAMP_AT(KID, 1);
    {
      lds_cptr ab = smem + (ARING ? ABASE : 0) + a_sub0;
      lds_cptr wb = smem + w_sub0;
#pragma unroll
      for (int m = 0; m < MT; ++m) af[0][m] = lds_read128(ab + m * 512);
#pragma unroll
      for (int n = 0; n < NT; ++n) wf[n] = lds_tr8(wb + waddr[n], wb + waddr[n] + 512);
    }
    hook();
    cur = 0; nxt = WSTG; fre = (NST - 1) * WSTG;
    acur = 0; anxt = ASTG;
    constexpr int STEADY = NKT - NST + 1;
    constexpr int LOOPN = (STEADY / 2) * 2;
    for (int kt = 0; kt < LOOPN; kt += 2) {
      iter<0, true, false, P*(NST - 3)>();
      iter<SPW & 1, true, false, P*(NST - 3)>();
    }
    tail<LOOPN>();
    STAMP_AT(KID, 2);
  }
};

template <class C, int BM, int NGRP, int KS>
DI void setup_loader_A(Loader<C>& ld, const u16* Ablk, int m0, int w, int lane) {
#pragma unroll
  for (int i = 0; i < C::APW; ++i) {
    int a = w + C::NLW * i;
    int blk = a / (BM / 32), j = a % (BM / 32);
    ld.aoff[i] = blk * (BM * 32) + j * 1024;
    ld.av[i] = ((blk * SEQ + m0 + j * 32) * 16) * 2 + lane * 16;
  }
  ld.abase = (const char*)Ablk;
}

DI int wfrag_addr(int t, int lane) {
  int g = lane >> 4, q = (lane & 15) >> 2, p = lane & 3;
  int f = ((q >> 1) & 1) | ((g & 1) << 1);
  return (t >> 2) * 4096 + (8 * g + q) * 128 + (((t & 3) ^ f) * 32) + p * 8;
}
template <int BM>
DI int afrag_addr(int mrow0, int lane) {
  int r = lane & 15, g = lane >> 4;
  return (g >> 1) * (BM * 32) + (mrow0 + r) * 32 + (g & 1) * 16;
}

constexpr float QSCALE = 0.17677669529663687f * 1.4426950408889634f;
typedef GCfg<128, 3, 2, 4, 4, 32> CQ;
constexpr int QW_STG = 2 * CQ::W_SUB, QA_STG = 2 * CQ::A_SUB, QA_BASE = 4 * QW_STG, QT_BASE = QA_BASE + 3 * QA_STG;
constexpr int QLDS = QT_BASE + 8192;
DI float fma1(float a, float b, float c) { float r; asm("v_fma_f32 %0, %1, %2, %3" : "=v"(r) : "v"(a), "v"(b), "v"(c)); return r; }
DI void gld16(u32x4& d, const char* base, unsigned voff) {
  asm volatile("global_load_dwordx4 %0, %1, %2" : "=v"(d) : "v"(voff), "s"(base) : "memory");
}
template <int N> DI void wait_vm_pin(u32x4 (&r)[4]) {
  asm volatile("s_waitcnt vmcnt(%4)" : "+v"(r[0]), "+v"(r[1]), "+v"(r[2]), "+v"(r[3]) : "n"(N) : "memory");
}
__global__ __launch_bounds__(1024, 4) void k_qkv(const u16* __restrict__ x, const float* __restrict__ stats,
                                                  const float* __restrict__ part, const u16* __restrict__ W,
                                                  u16* __restrict__ out) {
  extern __shared__ __attribute__((aligned(16))) char smem_[];
  lds_ptr smem = (lds_ptr)smem_;
  const int lane = threadIdx.x & 63;
  const int w = __builtin_amdgcn_readfirstlane(threadIdx.x >> 6);
  const int xcd = blockIdx.x & 7, idx = blockIdx.x >> 3;
  const int tm = (xcd & 1) * 8 + (idx & 7), tn = (xcd >> 1) * 4 + (idx >> 3);
  const int m0 = tm * 128, n0 = tn * 192;
  constexpr int LDW = 3 * DM, NKT = DM / 64;
  if (w >= 12) {
    const int lw = w - 12;
    const int c8 = lane & 7, r8 = lane >> 3;
    const char* xbase = (const char*)x;
    const unsigned xv = ((m0 + lw * 32 + r8) * DM + c8 * 8) * 2;
    auto xissue = [&](u32x4 (&d)[4]) {
#pragma unroll
      for (int i = 0; i < 4; ++i) gld16(d[i], xbase + (size_t)i * 8 * DM * 2, xv);
      xbase += 128;
    };
    u32x4 xr[4][4];
    xissue(xr[0]); xissue(xr[1]); xissue(xr[2]); xissue(xr[3]);
    f32x2 st[4];
#pragma unroll
    for (int i = 0; i < 4; ++i) st[i] = *(const f32x2*)(stats + (m0 + lw * 32 + i * 8 + r8) * 2);
    const int awr = QA_BASE + (c8 >> 1) * CQ::A_BLK + (lw * 32 + r8) * 32 + (c8 & 1) * 16;
    auto xform = [&](const u32x4 (&d)[4], int stage, int abuf) {
      const __attribute__((address_space(3))) f32x4* tb =
          (const __attribute__((address_space(3))) f32x4*)(smem + QT_BASE) + stage * 16 + c8 * 2;
      const f32x4 s0 = tb[0], s1 = tb[1], g0 = tb[256], g1 = tb[257];
#pragma unroll
      for (int h = 0; h < 2; ++h) {
        float t[2][8];
#pragma unroll
        for (int i = 0; i < 2; ++i)
#pragma unroll
          for (int j = 0; j < 4; ++j) { t[i][2 * j] = bflo(d[2 * h + i][j]); t[i][2 * j + 1] = bfhi(d[2 * h + i][j]); }
        __builtin_amdgcn_sched_barrier(0);
#pragma unroll
        for (int i = 0; i < 2; ++i)
#pragma unroll
          for (int j = 0; j < 8; ++j) t[i][j] = fma1(t[i][j], st[2 * h + i][1], st[2 * h + i][0]);
        __builtin_amdgcn_sched_barrier(0);
#pragma unroll
        for (int i = 0; i < 2; ++i)
#pragma unroll
          for (int j = 0; j < 8; ++j) t[i][j] = fma1(t[i][j], j < 4 ? g0[j] : g1[j - 4], j < 4 ? s0[j] : s1[j - 4]);
        __builtin_amdgcn_sched_barrier(0);
#pragma unroll
        for (int i = 0; i < 2; ++i) {
          u32x4 o;
#pragma unroll
          for (int j = 0; j < 4; ++j) o[j] = pack2(t[i][2 * j], t[i][2 * j + 1]);
          *(__attribute__((address_space(3))) u32x4*)(smem + awr + abuf * QA_STG + (2 * h + i) * 256) = o;
        }
      }
    };
    bar();
    wait_vm_pin<0>(xr[0]); wait_vm_pin<0>(xr[1]); wait_vm_pin<0>(xr[2]); wait_vm_pin<0>(xr[3]);
    xform(xr[0], 0, 0); xissue(xr[0]);
    xform(xr[1], 1, 1); xissue(xr[1]);
    asm volatile("s_waitcnt lgkmcnt(0)" ::: "memory");
    bar();
    auto it = [&](auto self, auto kt_) -> void {
      constexpr int kt = decltype(kt_)::value;
      if constexpr (kt < NKT - 1) {
        bar();
        if constexpr (kt + 2 < NKT) {
          constexpr int last = kt + 5 < NKT ? kt + 5 : NKT - 1;
          wait_vm_pin<4 * (last - (kt + 2))>(xr[(kt + 2) & 3]);
          xform(xr[(kt + 2) & 3], kt + 2, (kt + 2) % 3);
          if constexpr (kt + 6 < NKT) xissue(xr[(kt + 2) & 3]);
          asm volatile("s_waitcnt lgkmcnt(0)" ::: "memory");
        }
        self(self, std::integral_constant<int, kt + 1>{});
      }
    };
    it(it, std::integral_constant<int, 0>{});
    return;
  }
  if (w >= 8) {
    const int lw = w - 8;
    unsigned wv[6]; int woff[6];
#pragma unroll
    for (int i = 0; i < 6; ++i) {
      int b = lw + 4 * i;
      int sub = b / 12, r = b % 12, grp = r >> 2, o = r & 3;
      woff[i] = sub * CQ::W_SUB + grp * 4096 + o * 1024;
      int k = sub * 32 + 8 * o + (lane >> 3);
      int f = ((lane >> 4) & 1) | ((o & 1) << 1);
      int col = n0 + grp * 64 + ((((lane & 7) >> 1) ^ f) * 16) + (lane & 1) * 8;
      wv[i] = (k * LDW + col) * 2;
    }
    const char* wbase = (const char*)W;
    auto wissue = [&](int stg) {
#pragma unroll
      for (int i = 0; i < 6; ++i) glds16(wbase, wv[i], lds_u32(smem) + stg * QW_STG + woff[i]);
      wbase += (size_t)64 * LDW * 2;
    };
    wissue(0); wissue(1); wissue(2);
    bar();
    wait_vm<12>();
    bar();
    for (int kt = 0; kt < NKT - 3; ++kt) {
      wait_vm<6>();
      bar();
      wissue((kt + 3) & 3);
    }
    wait_vm<6>(); bar();
    wait_vm<0>(); bar();
    return;
  }
  {
    const int t = threadIdx.x;
    f32x4 a = *(const f32x4*)(part + t * 4);
#pragma unroll
    for (int i = 1; i < MNS; ++i) a += *(const f32x4*)(part + i * (6 * DM) + t * 4);
#pragma unroll
    for (int j = 0; j < 4; ++j) {
      float v = rbf(a[j]);
      a[j] = t < 256 ? v : rbf(1.f + v);
    }
    *(__attribute__((address_space(3))) f32x4*)(smem + QT_BASE + t * 16) = a;
    asm volatile("s_waitcnt lgkmcnt(0)" ::: "memory");
    bar();
  }
  Loader<CQ> ld;
  const int wm = w & 1, wn = (w >> 1) & 3;
  int waddr[3];
#pragma unroll
  for (int n = 0; n < 3; ++n) waddr[n] = wfrag_addr(wn * 3 + n, lane);
  const int aaddr = (lane >> 5) * CQ::A_BLK + (wm * 64 + (lane & 15)) * 32 + ((lane >> 4) & 1) * 16;
  f32x4 acc[3][4];
#pragma unroll
  for (int n = 0; n < 3; ++n)
#pragma unroll
    for (int m = 0; m < 4; ++m) acc[n][m] = f32x4{0.f, 0.f, 0.f, 0.f};
  Engine<128, 3, 2, 4, 4, 3, 1, NKT, 1, 0, 4, false, 3> eng(acc, smem, ld, 0, 0, aaddr, waddr, 0);
  eng.start();
  eng.run();
  const int g = lane >> 4;
#pragma unroll
  for (int n = 0; n < 3; ++n) {
    const int cb = (n0 >> 4) + wn * 3 + n;
    const float sc = cb < DM / 16 ? QSCALE : 1.f;
#pragma unroll
    for (int m = 0; m < 4; m += 2) {
      u32x2 a, b;
      a[0] = pack2(acc[n][m][0] * sc, acc[n][m][1] * sc);
      a[1] = pack2(acc[n][m][2] * sc, acc[n][m][3] * sc);
      b[0] = pack2(acc[n][m + 1][0] * sc, acc[n][m + 1][1] * sc);
      b[1] = pack2(acc[n][m + 1][2] * sc, acc[n][m + 1][3] * sc);
      const int row = m0 + wm * 64 + (m + (g & 1)) * 16 + (lane & 15);
      st16(out + ((size_t)cb * SEQ + row) * 16 + (g >> 1) * 8, pair16(a, b));
    }
  }
  STAMP_AT(1, 3);
}

typedef GCfg<128, 1, 2, 6, 4> CP;
template <int KDIM, int LDW>
__global__ __launch_bounds__(768, 3) void k_proj(const u16* __restrict__ Ablk, const u16* __restrict__ W,
                                                  const u16* __restrict__ res, const float* __restrict__ gate,
                                                  u16* __restrict__ out) {
  extern __shared__ __attribute__((aligned(16))) char smem_[];
  lds_ptr smem = (lds_ptr)smem_;
  const int lane = threadIdx.x & 63;
  const int w = __builtin_amdgcn_readfirstlane(threadIdx.x >> 6);
  const int xcd = blockIdx.x & 7, idx = blockIdx.x >> 3;
  const int tm = (xcd & 3) * 4 + (idx & 3), tn = (xcd >> 2) * 8 + (idx >> 2);
  const int m0 = tm * 128, n0 = tn * 64;
  Loader<CP> ld;
  const int lw = w & 3;
  setup_loader_A<CP, 128, 1, 2>(ld, Ablk, m0, lw, lane);
#pragma unroll
  for (int i = 0; i < CP::WPW; ++i) {
    int b = lw + 4 * i;
    int sub = b >> 2, o = b & 3;
    ld.woff[i] = 2 * CP::A_SUB + sub * CP::W_SUB + o * 1024;
    int k = sub * 32 + 8 * o + (lane >> 3);
    int f = ((lane >> 4) & 1) | ((o & 1) << 1);
    int col = n0 + ((((lane & 7) >> 1) ^ f) * 16) + (lane & 1) * 8;
    ld.wv[i] = (k * LDW + col) * 2;
    ld.wbase[i] = (const char*)W;
  }
  const int wm = w & 1, wn = (w >> 1) & 1, kg = (w >> 2) & 1;
  int waddr[2];
#pragma unroll
  for (int n = 0; n < 2; ++n) waddr[n] = wfrag_addr(wn * 2 + n, lane);
  const int aaddr = afrag_addr<128>(wm * 64, lane);
  f32x4 acc[2][4];
#pragma unroll
  for (int n = 0; n < 2; ++n)
#pragma unroll
    for (int m = 0; m < 4; ++m) acc[n][m] = f32x4{0.f, 0.f, 0.f, 0.f};
  Engine<128, 1, 2, 6, 4, 2, 2, KDIM / 64, (KDIM == DM ? 3 : 5), 0, 4> eng(acc, smem, ld, (long)2 * SEQ * 32, (long)64 * LDW, aaddr, waddr, kg);
  if (w >= 8) { eng.loader_run(); return; }
  const int g = lane >> 4;
  const int colw = n0 + (wn * 2 + kg) * 16;
  u32x2 rr[4];
  f32x4 gp[MNS];
  eng.start();
  eng.run([&] {
#pragma unroll
    for (int m = 0; m < 4; ++m)
      rr[m] = *(const u32x2*)(res + (size_t)(m0 + wm * 64 + m * 16 + (lane & 15)) * DM + colw + 4 * g);
#pragma unroll
    for (int i = 0; i < MNS; ++i) gp[i] = *(const f32x4*)(gate + i * (6 * DM) + colw + 4 * g);
  });
  __syncthreads();
  __attribute__((address_space(3))) f32x4* red = (__attribute__((address_space(3))) f32x4*)smem;
  f32x4 kp[4];
  if (kg == 0) {
#pragma unroll
    for (int m = 0; m < 4; ++m) { red[(w * 4 + m) * 64 + lane] = acc[1][m]; kp[m] = acc[0][m]; }
  } else {
#pragma unroll
    for (int m = 0; m < 4; ++m) { red[(w * 4 + m) * 64 + lane] = acc[0][m]; kp[m] = acc[1][m]; }
  }
  __syncthreads();
  {
    f32x4 gt = gp[0];
#pragma unroll
    for (int i = 1; i < MNS; ++i) gt += gp[i];
#pragma unroll
    for (int j = 0; j < 4; ++j) gt[j] = rbf(gt[j]);
#pragma unroll
    for (int m = 0; m < 4; m += 2) {
      u32x2 o[2];
#pragma unroll
      for (int i = 0; i < 2; ++i) {
        f32x4 a = kp[m + i] + red[((w ^ 4) * 4 + m + i) * 64 + lane];
        u32x2 r = rr[m + i];
        o[i][0] = pack2(bflo(r[0]) + rbf(gt[0] * rbf(a[0])), bfhi(r[0]) + rbf(gt[1] * rbf(a[1])));
        o[i][1] = pack2(bflo(r[1]) + rbf(gt[2] * rbf(a[2])), bfhi(r[1]) + rbf(gt[3] * rbf(a[3])));
      }
      const int row = m0 + wm * 64 + (m + (g & 1)) * 16 + (lane & 15);
      *(u32x4*)(out + (size_t)row * DM + colw + (g >> 1) * 8) = pair16(o[0], o[1]);
    }
  }
  STAMP_AT((KDIM == DM ? 3 : 5), 3);
}

typedef GCfg<256, 4, 1, 4, 4> CG;
template <int NH_>
DI void glu_wave(lds_ptr smem, Loader<CG>& ld, int lane, int wm, int t0, int m0, int f0, u16* __restrict__ out) {
  constexpr int NT = 2 * NH_;
  int waddr[NT];
#pragma unroll
  for (int n = 0; n < NH_; ++n) {
    waddr[n] = wfrag_addr(t0 + n, lane);
    waddr[NH_ + n] = waddr[n] + 8192;
  }
  const int aaddr = afrag_addr<256>(wm * 64, lane);
  f32x4 acc[NT][4];
#pragma unroll
  for (int n = 0; n < NT; ++n)
#pragma unroll
    for (int m = 0; m < 4; ++m) acc[n][m] = f32x4{0.f, 0.f, 0.f, 0.f};
  Engine<256, 4, 1, 4, 4, NT, 1, DM / 32, 4, 0, 4, (NH_ == 3)> eng(acc, smem, ld, (long)SEQ * 32, (long)32 * FF, aaddr, waddr, 0);
  eng.start();
  eng.run();
  const int g = lane >> 4;
#pragma unroll
  for (int n = 0; n < NH_; ++n) {
    const int cb = (f0 >> 4) + t0 + n;
#pragma unroll
    for (int m = 0; m < 4; m += 2) {
      u32x2 o[2];
#pragma unroll
      for (int i = 0; i < 2; ++i) {
        f32x4 a = acc[n][m + i], b = acc[NH_ + n][m + i];
        o[i][0] = pack2(rbf(a[0]) * rbf(b[0]), rbf(a[1]) * rbf(b[1]));
        o[i][1] = pack2(rbf(a[2]) * rbf(b[2]), rbf(a[3]) * rbf(b[3]));
      }
      const int row = m0 + wm * 64 + (m + (g & 1)) * 16 + (lane & 15);
      st16(out + ((size_t)cb * SEQ + row) * 16 + (g >> 1) * 8, pair16(o[0], o[1]));
    }
  }
  STAMP_AT(4, 3);
}

__global__ __launch_bounds__(512, 2) void k_glu(const u16* __restrict__ Ablk, const u16* __restrict__ Wg,
                                                 const u16* __restrict__ Wu, u16* __restrict__ out) {
  extern __shared__ __attribute__((aligned(16))) char smem_[];
  lds_ptr smem = (lds_ptr)smem_;
  const int lane = threadIdx.x & 63;
  const int w = __builtin_amdgcn_readfirstlane(threadIdx.x >> 6);
  const int xcd = blockIdx.x & 7, idx = blockIdx.x >> 3;
  const int tm = idx & 7, tn = xcd * 4 + (idx >> 3);
  const int m0 = tm * 256, f0 = tn * 112;
  Loader<CG> ld;
  const int lw = w & 3;
  setup_loader_A<CG, 256, 4, 1>(ld, Ablk, m0, lw, lane);
#pragma unroll
  for (int i = 0; i < CG::WPW; ++i) {
    int b = lw + 4 * i;
    int grp = b >> 2, o = b & 3;
    ld.woff[i] = CG::A_SUB + grp * 4096 + o * 1024;
    int k = 8 * o + (lane >> 3);
    int f = ((lane >> 4) & 1) | ((o & 1) << 1);
    int pr = ((lane & 7) >> 1) ^ f;
    if ((grp & 1) && pr == 3) pr = 0;
    int col = f0 + (grp & 1) * 64 + pr * 16 + (lane & 1) * 8;
    ld.wv[i] = (k * FF + col) * 2;
    ld.wbase[i] = (const char*)(grp >> 1 ? Wu : Wg);
  }
  const int wm = w & 3;
  if (w < 4) glu_wave<4>(smem, ld, lane, wm, 0, m0, f0, out);
  else glu_wave<3>(smem, ld, lane, wm, 4, m0, f0, out);
}

constexpr int ACH = 128, ANST = 4, ASTAGE = ACH * 64 * 2 + 256;
constexpr int ALDS = ANST * ASTAGE + 1024 + 16;

DI float rowmax16(const f32x16& v) {
  float mx = fmaxf(fmaxf(v[0], v[1]), v[2]);
#pragma unroll
  for (int r = 3; r < 15; r += 2) mx = fmaxf(fmaxf(mx, v[r]), v[r + 1]);
  mx = fmaxf(mx, v[15]);
  auto sw = __builtin_amdgcn_permlane32_swap(__float_as_uint(mx), __float_as_uint(mx), false, false);
  return fmaxf(__uint_as_float(sw[0]), __uint_as_float(sw[1]));
}

template <bool SAFE>
DI void attn_pass(lds_ptr smem, int lane, int w, const char* Kp, const char* Vp, const char* pf_wm, const char* pf_wo, const char* pf_wg,
                  const char* pf_wu, const char* pf_wd, const bf16x8& qa0, const bf16x8& qa1, const bf16x8& qb0,
                  const bf16x8& qb1, f32x16& oa, f32x16& ob, float& refa, float& refb, float& la_out, float& lb_out,
                  ModTask& mt, const u16* c) {
  const int r32 = lane & 31, hh = lane >> 5, kh = w >> 2;
  const unsigned kvo = ((32 * (w & 3) + (lane >> 1)) * 16 + (((lane & 1) ^ ((lane >> 4) & 1)) * 8)) * 2;
  const unsigned vvo = ((32 * (w & 3)) * 16) * 2 + lane * 16;
  const char* ksrc = Kp;
  const char* vsrc = Vp;
  auto issue = [&](int buf) {
    if (w < 4) {
      const unsigned l0 = lds_u32(smem) + buf * ASTAGE + w * 1024;
      glds16(ksrc, kvo, l0);
      glds16(ksrc + SEQ * 32, kvo, l0 + 4096);
      glds16(vsrc, vvo, l0 + 8192);
      glds16(vsrc + SEQ * 32, vvo, l0 + 8192 + 4096 + 128);
    }
    ksrc += ACH * 32; vsrc += ACH * 32;
  };
  const unsigned pfo = lane * 16;
  auto prefetch = [&](int ch) {
    if (w >= 4) {
#pragma unroll
      for (int i = 0; i < 3; ++i) {
        int pc = (ch * 3 + i) * 4 + (w - 4);
        const char* base;
        if (pc < 32) base = pf_wm + (size_t)(blockIdx.x * 4 + (pc >> 3)) * (12 * DM) + 4 * DM + (pc & 7) * 1024;
        else if (pc < 40) base = pf_wo + (size_t)blockIdx.x * 8192 + (pc - 32) * 1024;
        else if (pc < 68) base = pf_wd + (size_t)blockIdx.x * 28672 + (pc - 40) * 1024;
        else if (pc < 96) base = pf_wg + (size_t)blockIdx.x * 28672 + (pc - 68) * 1024;
        else if (pc < 124) base = pf_wu + (size_t)blockIdx.x * 28672 + (pc - 96) * 1024;
        else break;
        glds16(base, pfo, lds_u32(smem) + ANST * ASTAGE);
      }
    }
  };
  issue(0); issue(1); issue(2);
  const int kaddr0 = kh * 2048 + r32 * 32 + ((hh ^ ((r32 >> 3) & 1)) * 16);
  const int kaddr1 = kaddr0 + 4096;
  const int vaddr = 8192 + kh * 2048 + ((lane >> 4) & 1) * (4096 + 128) + (4 * hh + ((lane & 15) >> 2)) * 32 + (lane & 3) * 8;
  bf16x8 sel;
  {
    const short one = ((lane & 15) == ((lane >> 4) & 1)) ? (short)0x3F80 : (short)0;
#pragma unroll
    for (int j = 0; j < 8; ++j) sel[j] = one;
  }
  f32x16 nma, nmb, sa, sb;
#pragma unroll
  for (int r = 0; r < 16; ++r) { oa[r] = 0.f; ob[r] = 0.f; nma[r] = 0.f; nmb[r] = 0.f; }
  f32x4 lsa = {0.f, 0.f, 0.f, 0.f}, lsb = {0.f, 0.f, 0.f, 0.f};
  float la = 0.f, lb = 0.f;
  constexpr int NCH = SEQ / ACH;
  if (w < 4) wait_vm<8>();
  bar();
  const bf16x8 kf0_ = lds_read128(smem + kaddr0);
  const bf16x8 kf1_ = lds_read128(smem + kaddr1);
  {
    sa = MFMA32(kf0_, qa0, nma); sa = MFMA32(kf1_, qa1, sa);
    sb = MFMA32(kf0_, qb0, nmb); sb = MFMA32(kf1_, qb1, sb);
    const float ma = rowmax16(sa), mb = rowmax16(sb);
#pragma unroll
    for (int r = 0; r < 16; ++r) { nma[r] = -ma; sa[r] -= ma; nmb[r] = -mb; sb[r] -= mb; }
  }
  if constexpr (SAFE) {
    auto half = [&](f32x16& s, f32x16& o, f32x16& nm, float& l, const bf16x8& q0f, const bf16x8& q1f,
                    const bf16x8& kn0, const bf16x8& kn1, const bf16x8& vf0, const bf16x8& vf1, bool has_next) {
      f32x16 p;
      const float dl = fmaxf(rowmax16(s), 0.f);
      const float f = __builtin_amdgcn_exp2f(-dl);
      float t = 0.f;
#pragma unroll
      for (int r = 0; r < 16; ++r) { nm[r] -= dl; o[r] *= f; p[r] = __builtin_amdgcn_exp2f(s[r] - dl); t += p[r]; }
      l = l * f + t;
      if (has_next) { s = MFMA32(kn0, q0f, nm); s = MFMA32(kn1, q1f, s); }
      u32x4 pa, pb;
#pragma unroll
      for (int j = 0; j < 4; ++j) { pa[j] = pack2(p[2 * j], p[2 * j + 1]); pb[j] = pack2(p[8 + 2 * j], p[8 + 2 * j + 1]); }
      o = MFMA32(vf0, __builtin_bit_cast(bf16x8, pa), o);
      o = MFMA32(vf1, __builtin_bit_cast(bf16x8, pb), o);
    };
    auto step = [&](lds_cptr vb, lds_cptr knext, bool has_next) {
      bf16x8 kn0, kn1;
      if (has_next) { kn0 = lds_read128(knext + kaddr0); kn1 = lds_read128(knext + kaddr1); }
      bf16x8 vf0 = lds_tr8(vb, vb + 256);
      bf16x8 vf1 = lds_tr8(vb + 512, vb + 768);
      half(sa, oa, nma, la, qa0, qa1, kn0, kn1, vf0, vf1, has_next);
      half(sb, ob, nmb, lb, qb0, qb1, kn0, kn1, vf0, vf1, has_next);
    };
    for (int ch = 0; ch < NCH; ++ch) {
      if (w < 4) { if (ch + 2 < NCH) wait_vm<4>(); else wait_vm<0>(); }
      bar();
      if (ch + 3 < NCH) issue((ch + 3) & 3);
      lds_cptr sbuf = smem + (ch & 3) * ASTAGE;
      lds_cptr nbuf = smem + ((ch + 1) & 3) * ASTAGE;
      step(sbuf + vaddr, sbuf + 1024, true);
      step(sbuf + 1024 + vaddr, nbuf, ch + 1 < NCH);
    }
  } else {
    u32x4 pa0, pa1, pb0, pb1;
    bf16x8 kc0 = kf0_, kc1 = kf1_;
    bf16x8 vp0, vp1;
#pragma unroll
    for (int j = 0; j < 4; ++j) { pb0[j] = 0u; pb1[j] = 0u; }
#pragma unroll
    for (int j = 0; j < 8; ++j) { vp0[j] = 0; vp1[j] = 0; }
#define SB_ __builtin_amdgcn_sched_barrier(0)
#define EX(i) e[i] = __builtin_amdgcn_exp2f(S[i])
#define PHASE(S, P0, P1, M0, M1, M2, M3, M4, M5, R0, R1) do { float e[16]; \
      M0; SB_; R0; EX(0); EX(1); EX(2); EX(3); SB_; \
      M1; SB_; R1; EX(4); EX(5); EX(6); EX(7); SB_; \
      M2; SB_; EX(8); EX(9); P0[0] = pack2(e[0], e[1]); P0[1] = pack2(e[2], e[3]); SB_; \
      M3; SB_; EX(10); EX(11); P0[2] = pack2(e[4], e[5]); P0[3] = pack2(e[6], e[7]); SB_; \
      M4; SB_; EX(12); EX(13); EX(14); EX(15); SB_; \
      M5; SB_; P1[0] = pack2(e[8], e[9]); P1[1] = pack2(e[10], e[11]); P1[2] = pack2(e[12], e[13]); P1[3] = pack2(e[14], e[15]); SB_; \
    } while (0)
#define BF(x) __builtin_bit_cast(bf16x8, x)
    auto step = [&](lds_cptr vb, lds_cptr knext, auto has_next_t) {
      constexpr bool HN = decltype(has_next_t)::value;
      bf16x8 kn0, kn1, vc0, vc1;
      { const f32x16& S = sa;
        PHASE(S, pa0, pa1,
              sb = MFMA32(kc0, qb0, nmb), sb = MFMA32(kc1, qb1, sb),
              ob = MFMA32(vp0, BF(pb0), ob), ob = MFMA32(vp1, BF(pb1), ob),
              lsb = MFMA16(sel, BF(pb0), lsb), lsb = MFMA16(sel, BF(pb1), lsb),
              if (HN) { kn0 = lds_read128(knext + kaddr0); kn1 = lds_read128(knext + kaddr1); },
              { vc0 = lds_tr8(vb, vb + 256); vc1 = lds_tr8(vb + 512, vb + 768); }); }
      { const f32x16& S = sb;
        PHASE(S, pb0, pb1,
              if (HN) sa = MFMA32(kn0, qa0, nma), if (HN) sa = MFMA32(kn1, qa1, sa),
              oa = MFMA32(vc0, BF(pa0), oa), oa = MFMA32(vc1, BF(pa1), oa),
              lsa = MFMA16(sel, BF(pa0), lsa), lsa = MFMA16(sel, BF(pa1), lsa),
              , ); }
      if (HN) { kc0 = kn0; kc1 = kn1; }
      vp0 = vc0; vp1 = vc1;
    };
    typedef std::integral_constant<bool, true> T_;
    typedef std::integral_constant<bool, false> F_;
    for (int ch = 0; ch < NCH - 1; ++ch) {
      if (w < 4) { if (ch + 2 < NCH) wait_vm<4>(); else wait_vm<0>(); }
      bar();
      if (ch + 3 < NCH) issue((ch + 3) & 3);
      prefetch(ch);
      lds_cptr sbuf = smem + (ch & 3) * ASTAGE;
      lds_cptr nbuf = smem + ((ch + 1) & 3) * ASTAGE;
      step(sbuf + vaddr, sbuf + 1024, T_{});
      step(sbuf + 1024 + vaddr, nbuf, T_{});
    }
    {
      const int task = blockIdx.x * 2 + (threadIdx.x >> 8);
      mod_load(mt, c, (const u16*)pf_wm, 32 + (task >> 3), task & 7, threadIdx.x & 255);
      lds_cptr sbuf = smem + ((NCH - 1) & 3) * ASTAGE;
      step(sbuf + vaddr, sbuf + 1024, T_{});
      step(sbuf + 1024 + vaddr, sbuf, F_{});
    }
    ob = MFMA32(vp0, BF(pb0), ob); ob = MFMA32(vp1, BF(pb1), ob);
    lsb = MFMA16(sel, BF(pb0), lsb); lsb = MFMA16(sel, BF(pb1), lsb);
#undef PHASE
#undef EX
#undef BF
  }
  refa = nma[0]; refb = nmb[0];
  if constexpr (SAFE) {
    auto s1 = __builtin_amdgcn_permlane32_swap(__float_as_uint(la), __float_as_uint(la), false, false);
    la_out = __uint_as_float(s1[0]) + __uint_as_float(s1[1]);
    auto s2 = __builtin_amdgcn_permlane32_swap(__float_as_uint(lb), __float_as_uint(lb), false, false);
    lb_out = __uint_as_float(s2[0]) + __uint_as_float(s2[1]);
  } else {
    const float a0 = __shfl(lsa[0], lane & 15), a1 = __shfl(lsa[1], lane & 15);
    const float b0 = __shfl(lsb[0], lane & 15), b1 = __shfl(lsb[1], lane & 15);
    la_out = (lane & 16) ? a1 : a0;
    lb_out = (lane & 16) ? b1 : b0;
  }
}

__global__ __launch_bounds__(512, 2) void k_attn(const u16* __restrict__ qkv, u16* __restrict__ oblk,
                                                  const u16* __restrict__ c, const u16* __restrict__ wmod, float* __restrict__ part,
                                                  const char* __restrict__ pf_wo, const char* __restrict__ pf_wg,
                                                  const char* __restrict__ pf_wu, const char* __restrict__ pf_wd) {
  extern __shared__ __attribute__((aligned(16))) char smem_[];
  lds_ptr smem = (lds_ptr)smem_;
  const int lane = threadIdx.x & 63;
  const int w = __builtin_amdgcn_readfirstlane(threadIdx.x >> 6);
  const int xcd = blockIdx.x & 7, idx = blockIdx.x >> 3;
  const int head = xcd * 4 + (idx >> 3), qb = idx & 7;
  const int r32 = lane & 31, hh = lane >> 5;
  const int qg = w & 3, kh = w >> 2;
  const u16* Qp = qkv + (size_t)head * SEQ * 32;
  const char* Kp = (const char*)(qkv + (size_t)(32 + head) * SEQ * 32);
  const char* Vp = (const char*)(qkv + (size_t)(64 + head) * SEQ * 32);
  const int q0 = qb * 256 + qg * 64;
  STAMP_AT(2, 0);
  __attribute__((address_space(3))) int* flag = (__attribute__((address_space(3))) int*)(smem + ANST * ASTAGE + 1024);
  if (threadIdx.x == 0) *flag = 0;
  bf16x8 qa0 = *(const bf16x8*)(Qp + (q0 + r32) * 16 + hh * 8);
  bf16x8 qa1 = *(const bf16x8*)(Qp + (SEQ + q0 + r32) * 16 + hh * 8);
  bf16x8 qb0 = *(const bf16x8*)(Qp + (q0 + 32 + r32) * 16 + hh * 8);
  bf16x8 qb1 = *(const bf16x8*)(Qp + (SEQ + q0 + 32 + r32) * 16 + hh * 8);
  f32x16 oa, ob;
  float nma, nmb, la, lb;
  ModTask mt;
  attn_pass<false>(smem, lane, w, Kp, Vp, (const char*)wmod, pf_wo, pf_wg, pf_wu, pf_wd, qa0, qa1, qb0, qb1, oa, ob, nma, nmb, la, lb, mt, c);
  {
    float big = fmaxf(fabsf(la), fabsf(lb));
#pragma unroll
    for (int r = 0; r < 16; ++r) big = fmaxf(big, fmaxf(fabsf(oa[r]), fabsf(ob[r])));
    bool bad = !(big < 1e30f);
#pragma unroll
    for (int r = 0; r < 16; ++r) bad = bad || (oa[r] != oa[r]) || (ob[r] != ob[r]);
    bad = bad || (la != la) || (lb != lb);
    if (__any(bad) && lane == 0) *flag = 1;
  }
  __syncthreads();
  if (__builtin_amdgcn_readfirstlane(*flag) != 0) {
    attn_pass<true>(smem, lane, w, Kp, Vp, (const char*)wmod, pf_wo, pf_wg, pf_wu, pf_wd, qa0, qa1, qb0, qb1, oa, ob, nma, nmb, la, lb, mt, c);
    __syncthreads();
  }
  STAMP_AT(2, 2);
  const int task = blockIdx.x * 2 + (threadIdx.x >> 8);
  {
    __attribute__((address_space(3))) float* xb = (__attribute__((address_space(3))) float*)smem;
    const f32x16& og = kh == 0 ? ob : oa;
    __attribute__((address_space(3))) float* dst = xb + w * (18 * 64) + lane;
#pragma unroll
    for (int r = 0; r < 16; ++r) dst[r * 64] = og[r];
    dst[16 * 64] = kh == 0 ? lb : la;
    dst[17 * 64] = kh == 0 ? nmb : nma;
    __syncthreads();
    const __attribute__((address_space(3))) float* src = xb + (w ^ 4) * (18 * 64) + lane;
    f32x16 om = kh == 0 ? oa : ob;
    const float nm_me = kh == 0 ? nma : nmb;
    const float nm_ot = src[17 * 64];
    const float nmin = fminf(nm_me, nm_ot);
    const float f_me = __builtin_amdgcn_exp2f(nmin - nm_me), f_ot = __builtin_amdgcn_exp2f(nmin - nm_ot);
    const float rl = 1.f / ((kh == 0 ? la : lb) * f_me + src[16 * 64] * f_ot);
    const float g_me = f_me * rl, g_ot = f_ot * rl;
    u16* op = oblk + ((size_t)head * 2 * SEQ + q0 + kh * 32 + r32) * 16 + 4 * hh;
#pragma unroll
    for (int g = 0; g < 4; ++g) {
      float v0 = om[4 * g] * g_me + src[(4 * g) * 64] * g_ot;
      float v1 = om[4 * g + 1] * g_me + src[(4 * g + 1) * 64] * g_ot;
      float v2 = om[4 * g + 2] * g_me + src[(4 * g + 2) * 64] * g_ot;
      float v3 = om[4 * g + 3] * g_me + src[(4 * g + 3) * 64] * g_ot;
      u32x2 v;
      v[0] = pack2(v0, v1);
      v[1] = pack2(v2, v3);
      *(u32x2*)(op + (g >> 1) * (SEQ * 16) + (g & 1) * 8) = v;
    }
  }
  mod_finish(mt, part, 32 + (task >> 3), task & 7, threadIdx.x & 255, (float*)(smem_ + 40960) + (threadIdx.x >> 8) * 256);
  wait_vm<0>();
  STAMP_AT(2, 3);
}

extern "C" void kernel_launch(void* const* d_in, const int* in_sizes, int n_in, void* d_out, int out_size,
                              void* d_ws, size_t ws_size, hipStream_t stream) {
  const u16* x = (const u16*)d_in[0];
  const u16* wqkv = (const u16*)d_in[1];
  const u16* wo = (const u16*)d_in[2];
  const u16* wg = (const u16*)d_in[4];
  const u16* wu = (const u16*)d_in[5];
  const u16* wd = (const u16*)d_in[6];
  const u16* c = (const u16*)d_in[7];
  const u16* wmod = (const u16*)d_in[8];
  u16* out = (u16*)d_out;
  char* ws = (char*)d_ws;
  float* part = (float*)ws;
  float* modf = (float*)(ws + (256 << 10));
  unsigned* flags = (unsigned*)(ws + (512 << 10));
  float* stats = (float*)(ws + (768 << 10));
  u16* h1 = (u16*)(ws + (1 << 20));
  u16* qkv = (u16*)(ws + (8 << 20));
  u16* ob = (u16*)(ws + (24 << 20));
  u16* x2 = (u16*)(ws + (32 << 20));
  u16* h2 = (u16*)(ws + (40 << 20));
  u16* hh = (u16*)(ws + (48 << 20));
  u16* wdp = (u16*)(ws + (64 << 20));

  hipFuncSetAttribute((const void*)k_qkv, hipFuncAttributeMaxDynamicSharedMemorySize, QLDS);
  hipFuncSetAttribute((const void*)k_proj<DM, DM>, hipFuncAttributeMaxDynamicSharedMemorySize, CP::LDS);
  hipFuncSetAttribute((const void*)k_proj<FF, WDP>, hipFuncAttributeMaxDynamicSharedMemorySize, CP::LDS);
  hipFuncSetAttribute((const void*)k_glu, hipFuncAttributeMaxDynamicSharedMemorySize, CG::LDS);
  hipFuncSetAttribute((const void*)k_attn, hipFuncAttributeMaxDynamicSharedMemorySize, ALDS);

  k_pre<<<256 + 256 + 384, 256, 0, stream>>>(c, wmod, part, (const char*)x, (const char*)wqkv, flags, stats);
  k_qkv<<<256, 1024, QLDS, stream>>>(x, stats, part, wqkv, qkv);
  k_attn<<<256, 512, ALDS, stream>>>(qkv, ob, c, wmod, part, (const char*)wo, (const char*)wg, (const char*)wu, (const char*)wd);
  k_proj<DM, DM><<<256, 768, CP::LDS, stream>>>(ob, wo, x, part + 2 * DM, x2);
  k_ln<7, MNS, 0><<<256 + 224, 512, 0, stream>>>(x2, part + 3 * DM, h2, (const char*)wqkv, wd, wdp);
  k_glu<<<256, 512, CG::LDS, stream>>>(h2, wg, wu, hh);
  k_proj<FF, WDP><<<256, 768, CP::LDS, stream>>>(hh, wdp, x2, part + 5 * DM, out);
}
```

```cpp
#define NTST
#include <hip/hip_runtime.h>
#include <hip/hip_bf16.h>
#include <stdint.h>

typedef __attribute__((ext_vector_type(8))) short bf16x8;
typedef __attribute__((ext_vector_type(4))) short s16x4;
typedef __attribute__((ext_vector_type(4))) float f32x4;
typedef __attribute__((ext_vector_type(2))) float f32x2;
typedef __attribute__((ext_vector_type(16))) float f32x16;
typedef __attribute__((ext_vector_type(4))) unsigned u32x4;
typedef __attribute__((ext_vector_type(2))) unsigned u32x2;
typedef __attribute__((ext_vector_type(2))) __bf16 bf16x2_t;
typedef unsigned short u16;
typedef __attribute__((address_space(3))) char* lds_ptr;
typedef __attribute__((address_space(3))) const char* lds_cptr;

#define DI __device__ __forceinline__

constexpr int SEQ = 2048, DM = 1024, FF = 3584, NH = 32;

DI float bf2f(u16 x) { return __uint_as_float(((unsigned)x) << 16); }
DI float bflo(unsigned x) { return __uint_as_float(x << 16); }
DI float bfhi(unsigned x) { return __uint_as_float(x & 0xffff0000u); }
DI unsigned pack2(float a, float b) {
  f32x2 v = {a, b};
  bf16x2_t r = __builtin_convertvector(v, bf16x2_t);
  return __builtin_bit_cast(unsigned, r);
}
DI float rbf(float a) { return bflo(pack2(a, 0.f) & 0xffffu); }

#define MFMA16(a, b, c) __builtin_amdgcn_mfma_f32_16x16x32_bf16((a), (b), (c), 0, 0, 0)
#define MFMA32(a, b, c) __builtin_amdgcn_mfma_f32_32x32x16_bf16((a), (b), (c), 0, 0, 0)

DI void glds16(const void* base, unsigned voff, unsigned lds_addr) {
  unsigned sv;
  asm volatile("s_mov_b32 %0, m0\n\ts_mov_b32 m0, %3\n\ts_nop 0\n\tglobal_load_lds_dwordx4 %1, %2\n\ts_mov_b32 m0, %0"
               : "=&s"(sv) : "v"(voff), "s"(base), "s"(lds_addr) : "memory");
}
DI unsigned lds_u32(lds_ptr p) { return (unsigned)(uintptr_t)p; }
DI bf16x8 lds_read128(lds_cptr p) { return *(const __attribute__((address_space(3))) bf16x8*)p; }
DI s16x4 lds_tr(lds_cptr p) {
  typedef short v4i16_t __attribute__((ext_vector_type(4)));
  return __builtin_bit_cast(s16x4, __builtin_amdgcn_ds_read_tr16_b64_v4i16((__attribute__((address_space(3))) v4i16_t*)p));
}
DI bf16x8 lds_tr8(lds_cptr p0, lds_cptr p1) {
  s16x4 a = lds_tr(p0), b = lds_tr(p1);
  return __builtin_shufflevector(a, b, 0, 1, 2, 3, 4, 5, 6, 7);
}
#define STAMP_AT(kid, slot) do {} while (0)
template <int N> DI void wait_vm() { asm volatile("s_waitcnt vmcnt(%0)" ::"n"(N) : "memory"); }
DI void bar() { __builtin_amdgcn_s_barrier(); }

constexpr int MNS = 8;
struct ModTask { u32x4 w[4]; u16 cv[4]; };
DI void mod_load(ModTask& mt, const u16* __restrict__ c, const u16* __restrict__ wmod, int cb, int ks, int t) {
  const int cc = t & 7, rg = t >> 3;
  const int k0 = ks * 128 + rg;
  const u16* wp = wmod + (size_t)k0 * (6 * DM) + cb * 64 + cc * 8;
#pragma unroll
  for (int i = 0; i < 4; ++i) mt.w[i] = *(const u32x4*)(wp + (size_t)i * 32 * (6 * DM));
#pragma unroll
  for (int i = 0; i < 4; ++i) mt.cv[i] = c[k0 + 32 * i];
}
DI void mod_finish(const ModTask& mt, float* __restrict__ part, int cb, int ks, int t, float* s_red  ) {
  float acc[8];
#pragma unroll
  for (int j = 0; j < 8; ++j) acc[j] = 0.f;
#pragma unroll
  for (int i = 0; i < 4; ++i) {
    const float cv = bf2f(mt.cv[i]);
    const float sv = rbf(cv / (1.f + __expf(-cv)));
#pragma unroll
    for (int j = 0; j < 4; ++j) {
      acc[2 * j] += sv * bflo(mt.w[i][j]);
      acc[2 * j + 1] += sv * bfhi(mt.w[i][j]);
    }
  }
#pragma unroll
  for (int j = 0; j < 8; ++j) {
    float v = acc[j];
    v += __shfl_xor(v, 8);
    v += __shfl_xor(v, 16);
    v += __shfl_xor(v, 32);
    acc[j] = v;
  }
  const int lane = t & 63, wv = t >> 6;
  if (lane < 8) {
#pragma unroll
    for (int j = 0; j < 8; ++j) s_red[wv * 64 + lane * 8 + j] = acc[j];
  }
  __syncthreads();
  if (t < 64) part[ks * (6 * DM) + cb * 64 + t] = (s_red[t] + s_red[64 + t]) + (s_red[128 + t] + s_red[192 + t]);
}
DI f32x4 gate4(const float* __restrict__ gate, int col) {
  f32x4 gt = *(const f32x4*)(gate + col);
#pragma unroll
  for (int i = 1; i < MNS; ++i) gt += *(const f32x4*)(gate + i * (6 * DM) + col);
#pragma unroll
  for (int j = 0; j < 4; ++j) gt[j] = rbf(gt[j]);
  return gt;
}

DI void touch16k(const char* p, int t) {
  u32x4 v[4];
#pragma unroll
  for (int i = 0; i < 4; ++i) v[i] = *(const u32x4*)(p + i * 4096 + t * 16);
#pragma unroll
  for (int i = 0; i < 4; ++i) asm volatile("" ::"v"(v[i]));
}

__global__ __launch_bounds__(256) void k_pre(const u16* __restrict__ c, const u16* __restrict__ wmod, float* __restrict__ part,
                                              const char* __restrict__ x, const char* __restrict__ wqkv,
                                              unsigned* __restrict__ flags) {
  __shared__ float s_red[256];
  const int t = threadIdx.x, b = blockIdx.x;
  if (b < 256) STAMP_AT(0, 0);
  if (b < 256) {
    ModTask mt;
    mod_load(mt, c, wmod, b >> 3, b & 7, t);
    mod_finish(mt, part, b >> 3, b & 7, t, s_red);
    if (b == 0) flags[t * 16] = 0u;
  } else {
    touch16k(x + (size_t)(b - 256) * 16384, t);
  }
  if (b < 256) STAMP_AT(0, 3);
}

constexpr int WDP = DM + 64;
template <int KID, int NS, int NPF>
__global__ __launch_bounds__(512) void k_ln(const u16* __restrict__ x, const float* __restrict__ ss  ,
                                             u16* __restrict__ h16, const char* __restrict__ pf,
                                             const u16* __restrict__ wsrc, u16* __restrict__ wdst) {
  __shared__ __attribute__((aligned(16))) float sm[2048];
  const int t = threadIdx.x;
  if (blockIdx.x >= 256 + NPF) {
    const int r0 = (blockIdx.x - 256 - NPF) * 16 + (t >> 7);
    u32x4 v[4];
#pragma unroll
    for (int i = 0; i < 4; ++i) v[i] = *(const u32x4*)(wsrc + (size_t)(r0 + 4 * i) * DM + (t & 127) * 8);
#pragma unroll
    for (int i = 0; i < 4; ++i) *(u32x4*)(wdst + (size_t)(r0 + 4 * i) * WDP + (t & 127) * 8) = v[i];
    return;
  }
  if (blockIdx.x >= 256) {
    touch16k(pf + (size_t)((blockIdx.x - 256) * 2 + (t >> 8)) * 16384, t & 255);
    return;
  }
  const int lane = t & 63, wv = t >> 6;
  const int row = blockIdx.x * 8 + wv;
  STAMP_AT(KID, 0);
  const u16* xr = x + (size_t)row * DM;
  u32x4 v0 = *(const u32x4*)(xr + lane * 8);
  u32x4 v1 = *(const u32x4*)(xr + 512 + lane * 8);
  {
    f32x4 pv[NS];
#pragma unroll
    for (int i = 0; i < NS; ++i) pv[i] = *(const f32x4*)(ss + i * (6 * DM) + t * 4);
    f32x4 a = pv[0];
#pragma unroll
    for (int i = 1; i < NS; ++i) a += pv[i];
#pragma unroll
    for (int j = 0; j < 4; ++j) {
      float v = rbf(a[j]);
      a[j] = t < 256 ? v : rbf(1.f + v);
    }
    *(f32x4*)(sm + t * 4) = a;
  }
  float f[16];
#pragma unroll
  for (int j = 0; j < 4; ++j) {
    f[2 * j] = bflo(v0[j]); f[2 * j + 1] = bfhi(v0[j]);
    f[8 + 2 * j] = bflo(v1[j]); f[8 + 2 * j + 1] = bfhi(v1[j]);
  }
  float s = 0.f;
#pragma unroll
  for (int j = 0; j < 16; ++j) s += f[j];
#pragma unroll
  for (int o = 1; o < 64; o <<= 1) s += __shfl_xor(s, o);
  const float mean = s * (1.f / DM);
  float q = 0.f;
#pragma unroll
  for (int j = 0; j < 16; ++j) { float d = f[j] - mean; q += d * d; }
#pragma unroll
  for (int o = 1; o < 64; o <<= 1) q += __shfl_xor(q, o);
  const float mb = rbf(mean);
  const float vb = rbf(q * (1.f / DM));
  const float rden = 1.f / rbf(sqrtf(rbf(vb + 1e-5f)));
  __syncthreads();
#pragma unroll
  for (int half = 0; half < 2; ++half) {
    const int k0 = half * 512 + lane * 8;
    f32x4 sh0 = *(const f32x4*)(sm + k0), sh1 = *(const f32x4*)(sm + k0 + 4);
    f32x4 sc0 = *(const f32x4*)(sm + 1024 + k0), sc1 = *(const f32x4*)(sm + 1024 + k0 + 4);
    float y[8];
#pragma unroll
    for (int j = 0; j < 8; ++j) {
      float g = j < 4 ? sc0[j] : sc1[j - 4];
      float sh = j < 4 ? sh0[j] : sh1[j - 4];
      float d = rbf(f[half * 8 + j] - mb);
      float n = rbf(d * rden);
      y[j] = rbf(n * g) + sh;
    }
    u32x4 o;
#pragma unroll
    for (int j = 0; j < 4; ++j) o[j] = pack2(y[2 * j], y[2 * j + 1]);
    *(u32x4*)(h16 + ((size_t)(k0 >> 4) * SEQ + row) * 16 + (k0 & 15)) = o;
  }
  STAMP_AT(KID, 3);
}

DI void st16(void* p, u32x4 v) {
  asm volatile("global_store_dwordx4 %0, %1, off sc1\n\ts_nop 1" ::"v"(p), "v"(v) : "memory");
}
DI u32x4 pair16(u32x2 a, u32x2 b) {
  auto s0 = __builtin_amdgcn_permlane16_swap(a[0], b[0], false, false);
  auto s1 = __builtin_amdgcn_permlane16_swap(a[1], b[1], false, false);
  return u32x4{s0[0], s1[0], s0[1], s1[1]};
}

DI int hA(int r) { return (-r) & 3; }

template <int BM, int NGRP, int KS, int NST, int NLW_ = 8>
struct GCfg {
  static constexpr int NLW = NLW_;
  static constexpr int A_SUB = BM * 64;
  static constexpr int W_SUB = NGRP * 4096;
  static constexpr int STAGE = KS * (A_SUB + W_SUB);
  static constexpr int APW = KS * BM / 16 / NLW;
  static constexpr int WPW = KS * NGRP * 4 / NLW;
  static constexpr int PPW = APW + WPW;
  static constexpr int LDS = STAGE * NST;
};

template <class C>
struct Loader {
  const char* abase;
  const char* wbase[C::WPW];
  unsigned av[C::APW];
  unsigned wv[C::WPW];
  int aoff[C::APW];
  int woff[C::WPW];
};

template <int BM, int NGRP, int KS, int NST, int MT, int NT, int KSPLIT, int NKT, int KID, int IOFS, int NLW = 8, bool LOADS = (NLW == 8)>
struct Engine {
  typedef GCfg<BM, NGRP, KS, NST, NLW> C;
  static constexpr bool DED = !LOADS;
  static constexpr int SPW = KS / KSPLIT;
  static constexpr int P = C::PPW;
  static constexpr int G = NT * SPW;
  f32x4 (&acc)[NT][MT];
  lds_ptr smem;
  Loader<C>& ld;
  long a_adv, w_adv;
  const int (&waddr)[NT];
  int a_sub0, w_sub0;
  int cur, nxt, fre;
  bf16x8 wf[NT], af[2][MT];

  DI Engine(f32x4 (&acc_)[NT][MT], lds_ptr smem_, Loader<C>& ld_, long a_adv_, long w_adv_, int aaddr, const int (&waddr_)[NT], int kg)
      : acc(acc_), smem(smem_), ld(ld_), a_adv(a_adv_), w_adv(w_adv_), waddr(waddr_) {
    a_sub0 = kg * C::A_SUB + aaddr;
    w_sub0 = KS * C::A_SUB + kg * C::W_SUB;
  }
  template <int I> DI void piece(int stage_off) {
    if constexpr (I < C::APW) {
      glds16(ld.abase, ld.av[I], lds_u32(smem) + stage_off + ld.aoff[I]);
      if constexpr (I == C::APW - 1) ld.abase += a_adv * 2;
    } else {
      constexpr int J = I - C::APW;
      glds16(ld.wbase[J], ld.wv[J], lds_u32(smem) + stage_off + ld.woff[J]);
      ld.wbase[J] += w_adv * 2;
    }
  }
  template <int I> DI void issue_all(int stage_off) {
    if constexpr (I < P) { piece<I>(stage_off); issue_all<I + 1>(stage_off); }
  }
  template <int g, int I> DI void issue_at(int stage_off) {
    if constexpr (I < P) {
      constexpr int span = (G - IOFS) > P ? (G - IOFS) : P;
      constexpr int pos = IOFS + (I * span) / P < G ? IOFS + (I * span) / P : G - 1;
      if constexpr (pos == g) piece<I>(stage_off);
      issue_at<g, I + 1>(stage_off);
    }
  }
  template <int PAR, int S, bool PF, bool DOISSUE, int n>
  DI void groups(lds_cptr ab, lds_cptr wb) {
    if constexpr (n < NT) {
#pragma unroll
      for (int m = 0; m < MT; ++m) acc[n][m] = MFMA16(wf[n], af[PAR][m], acc[n][m]);
      if constexpr (PF) {
        wf[n] = lds_tr8(wb + waddr[n], wb + waddr[n] + 512);
#pragma unroll
        for (int m = 0; m < MT; ++m)
          if ((m * NT) / MT == n) af[PAR ^ 1][m] = lds_read128(ab + m * 512);
      }
      if constexpr (DOISSUE && !DED) issue_at<S * NT + n, 0>(fre);
      __builtin_amdgcn_sched_barrier(0);
      groups<PAR, S, PF, DOISSUE, n + 1>(ab, wb);
    }
  }
  template <int PAR, bool DOISSUE, bool LAST, int S> DI void steps() {
    if constexpr (S < SPW) {
      if constexpr (S + 1 < SPW)
        groups<PAR, S, true, DOISSUE, 0>(smem + cur + a_sub0 + (S + 1) * KSPLIT * C::A_SUB, smem + cur + w_sub0 + (S + 1) * KSPLIT * C::W_SUB);
      else if constexpr (!LAST)
        groups<PAR, S, true, DOISSUE, 0>(smem + nxt + a_sub0, smem + nxt + w_sub0);
      else
        groups<PAR, S, false, false, 0>(smem, smem);
      steps<PAR ^ 1, DOISSUE, LAST, S + 1>();
    }
  }
  template <int PAR, bool DOISSUE, bool LAST, int WAITN> DI void iter() {
    if constexpr (!LAST) { if constexpr (!DED) wait_vm<WAITN>(); bar(); }
    steps<PAR, DOISSUE, LAST, 0>();
    fre = cur; cur = nxt; nxt = (nxt + C::STAGE == NST * C::STAGE) ? 0 : nxt + C::STAGE;
  }
  template <int KT> DI void tail() {
    if constexpr (KT < NKT) {
      constexpr bool DOISSUE = KT + NST - 1 < NKT;
      constexpr bool LAST = KT == NKT - 1;
      constexpr int newest = (KT + NST - 2) < (NKT - 1) ? (KT + NST - 2) : (NKT - 1);
      constexpr int younger = newest - (KT + 1) > 0 ? newest - (KT + 1) : 0;
      iter<(KT * SPW) & 1, DOISSUE, LAST, P * younger>();
      tail<KT + 1>();
    }
  }
  DI void start() {
    static_assert(NKT >= NST + 1, "too few k tiles");
    STAMP_AT(KID, 0);
    if constexpr (!DED) {
#pragma unroll
      for (int s = 0; s < NST - 1; ++s) issue_all<0>(s * C::STAGE);
    }
  }
  template <int KT> DI void ltail() {
    if constexpr (KT < NKT - 1) {
      constexpr int newest = (KT + NST - 2) < (NKT - 1) ? (KT + NST - 2) : (NKT - 1);
      constexpr int younger = newest - (KT + 1) > 0 ? newest - (KT + 1) : 0;
      wait_vm<P * younger>();
      bar();
      ltail<KT + 1>();
    }
  }
  DI void loader_run() {
#pragma unroll
    for (int s = 0; s < NST - 1; ++s) issue_all<0>(s * C::STAGE);
    wait_vm<P*(NST - 2)>();
    bar();
    fre = (NST - 1) * C::STAGE;
    for (int kt = 0; kt < NKT - NST + 1; ++kt) {
      wait_vm<P*(NST - 3)>();
      bar();
      issue_all<0>(fre);
      fre = (fre + C::STAGE == NST * C::STAGE) ? 0 : fre + C::STAGE;
    }
    ltail<NKT - NST + 1>();
  }
  DI void run() { run([] {}); }
  template <class Hook> DI void run(Hook hook) {
    if constexpr (!DED) wait_vm<P*(NST - 2)>();
    bar();
    STAMP_AT(KID, 1);
    {
      lds_cptr ab = smem + a_sub0;
      lds_cptr wb = smem + w_sub0;
#pragma unroll
      for (int m = 0; m < MT; ++m) af[0][m] = lds_read128(ab + m * 512);
#pragma unroll
      for (int n = 0; n < NT; ++n) wf[n] = lds_tr8(wb + waddr[n], wb + waddr[n] + 512);
    }
    hook();
    cur = 0; nxt = C::STAGE; fre = (NST - 1) * C::STAGE;
    constexpr int STEADY = NKT - NST + 1;
    constexpr int LOOPN = (STEADY / 2) * 2;
    for (int kt = 0; kt < LOOPN; kt += 2) {
      iter<0, true, false, P*(NST - 3)>();
      iter<SPW & 1, true, false, P*(NST - 3)>();
    }
    tail<LOOPN>();
    STAMP_AT(KID, 2);
  }
};

template <class C, int BM, int NGRP, int KS>
DI void setup_loader_A(Loader<C>& ld, const u16* Ablk, int m0, int w, int lane) {
#pragma unroll
  for (int i = 0; i < C::APW; ++i) {
    int a = w + C::NLW * i;
    int blk = a / (BM / 32), j = a % (BM / 32);
    ld.aoff[i] = blk * (BM * 32) + j * 1024;
    ld.av[i] = ((blk * SEQ + m0 + j * 32) * 16) * 2 + lane * 16;
  }
  ld.abase = (const char*)Ablk;
}

DI int wfrag_addr(int t, int lane) {
  int g = lane >> 4, q = (lane & 15) >> 2, p = lane & 3;
  int f = ((q >> 1) & 1) | ((g & 1) << 1);
  return (t >> 2) * 4096 + (8 * g + q) * 128 + (((t & 3) ^ f) * 32) + p * 8;
}
template <int BM>
DI int afrag_addr(int mrow0, int lane) {
  int r = lane & 15, g = lane >> 4;
  return (g >> 1) * (BM * 32) + (mrow0 + r) * 32 + (g & 1) * 16;
}

constexpr float QSCALE = 0.17677669529663687f * 1.4426950408889634f;
typedef GCfg<128, 3, 2, 4, 4> CQ;
__global__ __launch_bounds__(768, 3) void k_qkv(const u16* __restrict__ Ablk, const u16* __restrict__ W,
                                                 u16* __restrict__ out) {
  extern __shared__ __attribute__((aligned(16))) char smem_[];
  lds_ptr smem = (lds_ptr)smem_;
  const int lane = threadIdx.x & 63;
  const int w = __builtin_amdgcn_readfirstlane(threadIdx.x >> 6);
  const int xcd = blockIdx.x & 7, idx = blockIdx.x >> 3;
  const int tm = (xcd & 1) * 8 + (idx & 7), tn = (xcd >> 1) * 4 + (idx >> 3);
  const int m0 = tm * 128, n0 = tn * 192;
  constexpr int LDW = 3 * DM;
  Loader<CQ> ld;
  const int lw = w & 3;
  setup_loader_A<CQ, 128, 3, 2>(ld, Ablk, m0, lw, lane);
#pragma unroll
  for (int i = 0; i < CQ::WPW; ++i) {
    int b = lw + 4 * i;
    int sub = b / 12, r = b % 12, grp = r >> 2, o = r & 3;
    ld.woff[i] = 2 * CQ::A_SUB + sub * CQ::W_SUB + grp * 4096 + o * 1024;
    int k = sub * 32 + 8 * o + (lane >> 3);
    int f = ((lane >> 4) & 1) | ((o & 1) << 1);
    int col = n0 + grp * 64 + ((((lane & 7) >> 1) ^ f) * 16) + (lane & 1) * 8;
    ld.wv[i] = (k * LDW + col) * 2;
    ld.wbase[i] = (const char*)W;
  }
  const int wm = w & 1, wn = (w >> 1) & 3;
  int waddr[3];
#pragma unroll
  for (int n = 0; n < 3; ++n) waddr[n] = wfrag_addr(wn * 3 + n, lane);
  const int aaddr = afrag_addr<128>(wm * 64, lane);
  f32x4 acc[3][4];
#pragma unroll
  for (int n = 0; n < 3; ++n)
#pragma unroll
    for (int m = 0; m < 4; ++m) acc[n][m] = f32x4{0.f, 0.f, 0.f, 0.f};
  Engine<128, 3, 2, 4, 4, 3, 1, DM / 64, 1, 0, 4> eng(acc, smem, ld, (long)2 * SEQ * 32, (long)64 * LDW, aaddr, waddr, 0);
  if (w >= 8) { eng.loader_run(); return; }
  eng.start();
  eng.run();
  const int g = lane >> 4;
#pragma unroll
  for (int n = 0; n < 3; ++n) {
    const int cb = (n0 >> 4) + wn * 3 + n;
    const float sc = cb < DM / 16 ? QSCALE : 1.f;
#pragma unroll
    for (int m = 0; m < 4; m += 2) {
      u32x2 a, b;
      a[0] = pack2(acc[n][m][0] * sc, acc[n][m][1] * sc);
      a[1] = pack2(acc[n][m][2] * sc, acc[n][m][3] * sc);
      b[0] = pack2(acc[n][m + 1][0] * sc, acc[n][m + 1][1] * sc);
      b[1] = pack2(acc[n][m + 1][2] * sc, acc[n][m + 1][3] * sc);
      const int row = m0 + wm * 64 + (m + (g & 1)) * 16 + (lane & 15);
      st16(out + ((size_t)cb * SEQ + row) * 16 + (g >> 1) * 8, pair16(a, b));
    }
  }
  STAMP_AT(1, 3);
}

typedef GCfg<128, 1, 2, 6, 4> CP;
template <int KDIM, int LDW>
__global__ __launch_bounds__(768, 3) void k_proj(const u16* __restrict__ Ablk, const u16* __restrict__ W,
                                                  const u16* __restrict__ res, const float* __restrict__ gate,
                                                  u16* __restrict__ out) {
  extern __shared__ __attribute__((aligned(16))) char smem_[];
  lds_ptr smem = (lds_ptr)smem_;
  const int lane = threadIdx.x & 63;
  const int w = __builtin_amdgcn_readfirstlane(threadIdx.x >> 6);
  const int xcd = blockIdx.x & 7, idx = blockIdx.x >> 3;
  const int tm = (xcd & 3) * 4 + (idx & 3), tn = (xcd >> 2) * 8 + (idx >> 2);
  const int m0 = tm * 128, n0 = tn * 64;
  Loader<CP> ld;
  const int lw = w & 3;
  setup_loader_A<CP, 128, 1, 2>(ld, Ablk, m0, lw, lane);
#pragma unroll
  for (int i = 0; i < CP::WPW; ++i) {
    int b = lw + 4 * i;
    int sub = b >> 2, o = b & 3;
    ld.woff[i] = 2 * CP::A_SUB + sub * CP::W_SUB + o * 1024;
    int k = sub * 32 + 8 * o + (lane >> 3);
    int f = ((lane >> 4) & 1) | ((o & 1) << 1);
    int col = n0 + ((((lane & 7) >> 1) ^ f) * 16) + (lane & 1) * 8;
    ld.wv[i] = (k * LDW + col) * 2;
    ld.wbase[i] = (const char*)W;
  }
  const int wm = w & 1, wn = (w >> 1) & 1, kg = (w >> 2) & 1;
  int waddr[2];
#pragma unroll
  for (int n = 0; n < 2; ++n) waddr[n] = wfrag_addr(wn * 2 + n, lane);
  const int aaddr = afrag_addr<128>(wm * 64, lane);
  f32x4 acc[2][4];
#pragma unroll
  for (int n = 0; n < 2; ++n)
#pragma unroll
    for (int m = 0; m < 4; ++m) acc[n][m] = f32x4{0.f, 0.f, 0.f, 0.f};
  Engine<128, 1, 2, 6, 4, 2, 2, KDIM / 64, (KDIM == DM ? 3 : 5), 0, 4> eng(acc, smem, ld, (long)2 * SEQ * 32, (long)64 * LDW, aaddr, waddr, kg);
  if (w >= 8) { eng.loader_run(); return; }
  const int g = lane >> 4;
  const int colw = n0 + (wn * 2 + kg) * 16;
  u32x2 rr[4];
  f32x4 gp[MNS];
  eng.start();
  eng.run([&] {
#pragma unroll
    for (int m = 0; m < 4; ++m)
      rr[m] = *(const u32x2*)(res + (size_t)(m0 + wm * 64 + m * 16 + (lane & 15)) * DM + colw + 4 * g);
#pragma unroll
    for (int i = 0; i < MNS; ++i) gp[i] = *(const f32x4*)(gate + i * (6 * DM) + colw + 4 * g);
  });
  __syncthreads();
  __attribute__((address_space(3))) f32x4* red = (__attribute__((address_space(3))) f32x4*)smem;
  f32x4 kp[4];
  if (kg == 0) {
#pragma unroll
    for (int m = 0; m < 4; ++m) { red[(w * 4 + m) * 64 + lane] = acc[1][m]; kp[m] = acc[0][m]; }
  } else {
#pragma unroll
    for (int m = 0; m < 4; ++m) { red[(w * 4 + m) * 64 + lane] = acc[0][m]; kp[m] = acc[1][m]; }
  }
  __syncthreads();
  {
    f32x4 gt = gp[0];
#pragma unroll
    for (int i = 1; i < MNS; ++i) gt += gp[i];
#pragma unroll
    for (int j = 0; j < 4; ++j) gt[j] = rbf(gt[j]);
#pragma unroll
    for (int m = 0; m < 4; m += 2) {
      u32x2 o[2];
#pragma unroll
      for (int i = 0; i < 2; ++i) {
        f32x4 a = kp[m + i] + red[((w ^ 4) * 4 + m + i) * 64 + lane];
        u32x2 r = rr[m + i];
        o[i][0] = pack2(bflo(r[0]) + rbf(gt[0] * rbf(a[0])), bfhi(r[0]) + rbf(gt[1] * rbf(a[1])));
        o[i][1] = pack2(bflo(r[1]) + rbf(gt[2] * rbf(a[2])), bfhi(r[1]) + rbf(gt[3] * rbf(a[3])));
      }
      const int row = m0 + wm * 64 + (m + (g & 1)) * 16 + (lane & 15);
      *(u32x4*)(out + (size_t)row * DM + colw + (g >> 1) * 8) = pair16(o[0], o[1]);
    }
  }
  STAMP_AT((KDIM == DM ? 3 : 5), 3);
}

typedef GCfg<256, 4, 1, 4, 4> CG;
template <int NH_>
DI void glu_wave(lds_ptr smem, Loader<CG>& ld, int lane, int wm, int t0, int m0, int f0, u16* __restrict__ out) {
  constexpr int NT = 2 * NH_;
  int waddr[NT];
#pragma unroll
  for (int n = 0; n < NH_; ++n) {
    waddr[n] = wfrag_addr(t0 + n, lane);
    waddr[NH_ + n] = waddr[n] + 8192;
  }
  const int aaddr = afrag_addr<256>(wm * 64, lane);
  f32x4 acc[NT][4];
#pragma unroll
  for (int n = 0; n < NT; ++n)
#pragma unroll
    for (int m = 0; m < 4; ++m) acc[n][m] = f32x4{0.f, 0.f, 0.f, 0.f};
  Engine<256, 4, 1, 4, 4, NT, 1, DM / 32, 4, 0, 4, (NH_ == 3)> eng(acc, smem, ld, (long)SEQ * 32, (long)32 * FF, aaddr, waddr, 0);
  eng.start();
  eng.run();
  const int g = lane >> 4;
#pragma unroll
  for (int n = 0; n < NH_; ++n) {
    const int cb = (f0 >> 4) + t0 + n;
#pragma unroll
    for (int m = 0; m < 4; m += 2) {
      u32x2 o[2];
#pragma unroll
      for (int i = 0; i < 2; ++i) {
        f32x4 a = acc[n][m + i], b = acc[NH_ + n][m + i];
        o[i][0] = pack2(rbf(a[0]) * rbf(b[0]), rbf(a[1]) * rbf(b[1]));
        o[i][1] = pack2(rbf(a[2]) * rbf(b[2]), rbf(a[3]) * rbf(b[3]));
      }
      const int row = m0 + wm * 64 + (m + (g & 1)) * 16 + (lane & 15);
      st16(out + ((size_t)cb * SEQ + row) * 16 + (g >> 1) * 8, pair16(o[0], o[1]));
    }
  }
  STAMP_AT(4, 3);
}

__global__ __launch_bounds__(512, 2) void k_glu(const u16* __restrict__ Ablk, const u16* __restrict__ Wg,
                                                 const u16* __restrict__ Wu, u16* __restrict__ out) {
  extern __shared__ __attribute__((aligned(16))) char smem_[];
  lds_ptr smem = (lds_ptr)smem_;
  const int lane = threadIdx.x & 63;
  const int w = __builtin_amdgcn_readfirstlane(threadIdx.x >> 6);
  const int xcd = blockIdx.x & 7, idx = blockIdx.x >> 3;
  const int tm = idx & 7, tn = xcd * 4 + (idx >> 3);
  const int m0 = tm * 256, f0 = tn * 112;
  Loader<CG> ld;
  const int lw = w & 3;
  setup_loader_A<CG, 256, 4, 1>(ld, Ablk, m0, lw, lane);
#pragma unroll
  for (int i = 0; i < CG::WPW; ++i) {
    int b = lw + 4 * i;
    int grp = b >> 2, o = b & 3;
    ld.woff[i] = CG::A_SUB + grp * 4096 + o * 1024;
    int k = 8 * o + (lane >> 3);
    int f = ((lane >> 4) & 1) | ((o & 1) << 1);
    int pr = ((lane & 7) >> 1) ^ f;
    if ((grp & 1) && pr == 3) pr = 0;
    int col = f0 + (grp & 1) * 64 + pr * 16 + (lane & 1) * 8;
    ld.wv[i] = (k * FF + col) * 2;
    ld.wbase[i] = (const char*)(grp >> 1 ? Wu : Wg);
  }
  const int wm = w & 3;
  if (w < 4) glu_wave<4>(smem, ld, lane, wm, 0, m0, f0, out);
  else glu_wave<3>(smem, ld, lane, wm, 4, m0, f0, out);
}

constexpr int ACH = 128, ANST = 4, ASTAGE = ACH * 64 * 2 + 256;
constexpr int ALDS = ANST * ASTAGE + 1024 + 16;

DI float rowmax16(const f32x16& v) {
  float mx = fmaxf(fmaxf(v[0], v[1]), v[2]);
#pragma unroll
  for (int r = 3; r < 15; r += 2) mx = fmaxf(fmaxf(mx, v[r]), v[r + 1]);
  mx = fmaxf(mx, v[15]);
  auto sw = __builtin_amdgcn_permlane32_swap(__float_as_uint(mx), __float_as_uint(mx), false, false);
  return fmaxf(__uint_as_float(sw[0]), __uint_as_float(sw[1]));
}

template <bool SAFE>
DI void attn_pass(lds_ptr smem, int lane, int w, const char* Kp, const char* Vp, const char* pf_wm, const char* pf_wo, const char* pf_wg,
                  const char* pf_wu, const char* pf_wd, const bf16x8& qa0, const bf16x8& qa1, const bf16x8& qb0,
                  const bf16x8& qb1, f32x16& oa, f32x16& ob, float& refa, float& refb, float& la_out, float& lb_out,
                  ModTask& mt, const u16* c) {
  const int r32 = lane & 31, hh = lane >> 5, kh = w >> 2;
  const unsigned kvo = ((32 * (w & 3) + (lane >> 1)) * 16 + (((lane & 1) ^ ((lane >> 4) & 1)) * 8)) * 2;
  const unsigned vvo = ((32 * (w & 3)) * 16) * 2 + lane * 16;
  const char* ksrc = Kp;
  const char* vsrc = Vp;
  auto issue = [&](int buf) {
    if (w < 4) {
      const unsigned l0 = lds_u32(smem) + buf * ASTAGE + w * 1024;
      glds16(ksrc, kvo, l0);
      glds16(ksrc + SEQ * 32, kvo, l0 + 4096);
      glds16(vsrc, vvo, l0 + 8192);
      glds16(vsrc + SEQ * 32, vvo, l0 + 8192 + 4096 + 128);
    }
    ksrc += ACH * 32; vsrc += ACH * 32;
  };
  const unsigned pfo = lane * 16;
  auto prefetch = [&](int ch) {
    if (w >= 4) {
#pragma unroll
      for (int i = 0; i < 3; ++i) {
        int pc = (ch * 3 + i) * 4 + (w - 4);
        const char* base;
        if (pc < 32) base = pf_wm + (size_t)(blockIdx.x * 4 + (pc >> 3)) * (12 * DM) + 4 * DM + (pc & 7) * 1024;
        else if (pc < 40) base = pf_wo + (size_t)blockIdx.x * 8192 + (pc - 32) * 1024;
        else if (pc < 68) base = pf_wd + (size_t)blockIdx.x * 28672 + (pc - 40) * 1024;
        else if (pc < 96) base = pf_wg + (size_t)blockIdx.x * 28672 + (pc - 68) * 1024;
        else if (pc < 124) base = pf_wu + (size_t)blockIdx.x * 28672 + (pc - 96) * 1024;
        else break;
        glds16(base, pfo, lds_u32(smem) + ANST * ASTAGE);
      }
    }
  };
  issue(0); issue(1); issue(2);
  const int kaddr0 = kh * 2048 + r32 * 32 + ((hh ^ ((r32 >> 3) & 1)) * 16);
  const int kaddr1 = kaddr0 + 4096;
  const int vaddr = 8192 + kh * 2048 + ((lane >> 4) & 1) * (4096 + 128) + (4 * hh + ((lane & 15) >> 2)) * 32 + (lane & 3) * 8;
  bf16x8 sel;
  {
    const short one = ((lane & 15) == ((lane >> 4) & 1)) ? (short)0x3F80 : (short)0;
#pragma unroll
    for (int j = 0; j < 8; ++j) sel[j] = one;
  }
  f32x16 nma, nmb, sa, sb;
#pragma unroll
  for (int r = 0; r < 16; ++r) { oa[r] = 0.f; ob[r] = 0.f; nma[r] = 0.f; nmb[r] = 0.f; }
  f32x4 lsa = {0.f, 0.f, 0.f, 0.f}, lsb = {0.f, 0.f, 0.f, 0.f};
  float la = 0.f, lb = 0.f;
  constexpr int NCH = SEQ / ACH;
  if (w < 4) wait_vm<8>();
  bar();
  const bf16x8 kf0_ = lds_read128(smem + kaddr0);
  const bf16x8 kf1_ = lds_read128(smem + kaddr1);
  {
    sa = MFMA32(kf0_, qa0, nma); sa = MFMA32(kf1_, qa1, sa);
    sb = MFMA32(kf0_, qb0, nmb); sb = MFMA32(kf1_, qb1, sb);
    const float ma = rowmax16(sa), mb = rowmax16(sb);
#pragma unroll
    for (int r = 0; r < 16; ++r) { nma[r] = -ma; sa[r] -= ma; nmb[r] = -mb; sb[r] -= mb; }
  }
  if constexpr (SAFE) {
    auto half = [&](f32x16& s, f32x16& o, f32x16& nm, float& l, const bf16x8& q0f, const bf16x8& q1f,
                    const bf16x8& kn0, const bf16x8& kn1, const bf16x8& vf0, const bf16x8& vf1, bool has_next) {
      f32x16 p;
      const float dl = fmaxf(rowmax16(s), 0.f);
      const float f = __builtin_amdgcn_exp2f(-dl);
      float t = 0.f;
#pragma unroll
      for (int r = 0; r < 16; ++r) { nm[r] -= dl; o[r] *= f; p[r] = __builtin_amdgcn_exp2f(s[r] - dl); t += p[r]; }
      l = l * f + t;
      if (has_next) { s = MFMA32(kn0, q0f, nm); s = MFMA32(kn1, q1f, s); }
      u32x4 pa, pb;
#pragma unroll
      for (int j = 0; j < 4; ++j) { pa[j] = pack2(p[2 * j], p[2 * j + 1]); pb[j] = pack2(p[8 + 2 * j], p[8 + 2 * j + 1]); }
      o = MFMA32(vf0, __builtin_bit_cast(bf16x8, pa), o);
      o = MFMA32(vf1, __builtin_bit_cast(bf16x8, pb), o);
    };
    auto step = [&](lds_cptr vb, lds_cptr knext, bool has_next) {
      bf16x8 kn0, kn1;
      if (has_next) { kn0 = lds_read128(knext + kaddr0); kn1 = lds_read128(knext + kaddr1); }
      bf16x8 vf0 = lds_tr8(vb, vb + 256);
      bf16x8 vf1 = lds_tr8(vb + 512, vb + 768);
      half(sa, oa, nma, la, qa0, qa1, kn0, kn1, vf0, vf1, has_next);
      half(sb, ob, nmb, lb, qb0, qb1, kn0, kn1, vf0, vf1, has_next);
    };
    for (int ch = 0; ch < NCH; ++ch) {
      if (w < 4) { if (ch + 2 < NCH) wait_vm<4>(); else wait_vm<0>(); }
      bar();
      if (ch + 3 < NCH) issue((ch + 3) & 3);
      lds_cptr sbuf = smem + (ch & 3) * ASTAGE;
      lds_cptr nbuf = smem + ((ch + 1) & 3) * ASTAGE;
      step(sbuf + vaddr, sbuf + 1024, true);
      step(sbuf + 1024 + vaddr, nbuf, ch + 1 < NCH);
    }
  } else {
    u32x4 pa0, pa1, pb0, pb1;
    bf16x8 kc0 = kf0_, kc1 = kf1_;
    bf16x8 vp0, vp1;
#pragma unroll
    for (int j = 0; j < 4; ++j) { pb0[j] = 0u; pb1[j] = 0u; }
#pragma unroll
    for (int j = 0; j < 8; ++j) { vp0[j] = 0; vp1[j] = 0; }
#define SB_ __builtin_amdgcn_sched_barrier(0)
#define EX(i) e[i] = __builtin_amdgcn_exp2f(S[i])
#define PHASE(S, P0, P1, M0, M1, M2, M3, M4, M5, R0, R1) do { float e[16]; \
      M0; SB_; R0; EX(0); EX(1); EX(2); EX(3); SB_; \
      M1; SB_; R1; EX(4); EX(5); EX(6); EX(7); SB_; \
      M2; SB_; EX(8); EX(9); P0[0] = pack2(e[0], e[1]); P0[1] = pack2(e[2], e[3]); SB_; \
      M3; SB_; EX(10); EX(11); P0[2] = pack2(e[4], e[5]); P0[3] = pack2(e[6], e[7]); SB_; \
      M4; SB_; EX(12); EX(13); EX(14); EX(15); SB_; \
      M5; SB_; P1[0] = pack2(e[8], e[9]); P1[1] = pack2(e[10], e[11]); P1[2] = pack2(e[12], e[13]); P1[3] = pack2(e[14], e[15]); SB_; \
    } while (0)
#define BF(x) __builtin_bit_cast(bf16x8, x)
    auto step = [&](lds_cptr vb, lds_cptr knext, auto has_next_t) {
      constexpr bool HN = decltype(has_next_t)::value;
      bf16x8 kn0, kn1, vc0, vc1;
      { const f32x16& S = sa;
        PHASE(S, pa0, pa1,
              sb = MFMA32(kc0, qb0, nmb), sb = MFMA32(kc1, qb1, sb),
              ob = MFMA32(vp0, BF(pb0), ob), ob = MFMA32(vp1, BF(pb1), ob),
              lsb = MFMA16(sel, BF(pb0), lsb), lsb = MFMA16(sel, BF(pb1), lsb),
              if (HN) { kn0 = lds_read128(knext + kaddr0); kn1 = lds_read128(knext + kaddr1); },
              { vc0 = lds_tr8(vb, vb + 256); vc1 = lds_tr8(vb + 512, vb + 768); }); }
      { const f32x16& S = sb;
        PHASE(S, pb0, pb1,
              if (HN) sa = MFMA32(kn0, qa0, nma), if (HN) sa = MFMA32(kn1, qa1, sa),
              oa = MFMA32(vc0, BF(pa0), oa), oa = MFMA32(vc1, BF(pa1), oa),
              lsa = MFMA16(sel, BF(pa0), lsa), lsa = MFMA16(sel, BF(pa1), lsa),
              , ); }
      if (HN) { kc0 = kn0; kc1 = kn1; }
      vp0 = vc0; vp1 = vc1;
    };
    typedef std::integral_constant<bool, true> T_;
    typedef std::integral_constant<bool, false> F_;
    for (int ch = 0; ch < NCH - 1; ++ch) {
      if (w < 4) { if (ch + 2 < NCH) wait_vm<4>(); else wait_vm<0>(); }
      bar();
      if (ch + 3 < NCH) issue((ch + 3) & 3);
      prefetch(ch);
      lds_cptr sbuf = smem + (ch & 3) * ASTAGE;
      lds_cptr nbuf = smem + ((ch + 1) & 3) * ASTAGE;
      step(sbuf + vaddr, sbuf + 1024, T_{});
      step(sbuf + 1024 + vaddr, nbuf, T_{});
    }
    {
      const int task = blockIdx.x * 2 + (threadIdx.x >> 8);
      mod_load(mt, c, (const u16*)pf_wm, 32 + (task >> 3), task & 7, threadIdx.x & 255);
      lds_cptr sbuf = smem + ((NCH - 1) & 3) * ASTAGE;
      step(sbuf + vaddr, sbuf + 1024, T_{});
      step(sbuf + 1024 + vaddr, sbuf, F_{});
    }
    ob = MFMA32(vp0, BF(pb0), ob); ob = MFMA32(vp1, BF(pb1), ob);
    lsb = MFMA16(sel, BF(pb0), lsb); lsb = MFMA16(sel, BF(pb1), lsb);
#undef PHASE
#undef EX
#undef BF
  }
  refa = nma[0]; refb = nmb[0];
  if constexpr (SAFE) {
    auto s1 = __builtin_amdgcn_permlane32_swap(__float_as_uint(la), __float_as_uint(la), false, false);
    la_out = __uint_as_float(s1[0]) + __uint_as_float(s1[1]);
    auto s2 = __builtin_amdgcn_permlane32_swap(__float_as_uint(lb), __float_as_uint(lb), false, false);
    lb_out = __uint_as_float(s2[0]) + __uint_as_float(s2[1]);
  } else {
    const float a0 = __shfl(lsa[0], lane & 15), a1 = __shfl(lsa[1], lane & 15);
    const float b0 = __shfl(lsb[0], lane & 15), b1 = __shfl(lsb[1], lane & 15);
    la_out = (lane & 16) ? a1 : a0;
    lb_out = (lane & 16) ? b1 : b0;
  }
}

__global__ __launch_bounds__(512, 2) void k_attn(const u16* __restrict__ qkv, u16* __restrict__ oblk,
                                                  const u16* __restrict__ c, const u16* __restrict__ wmod, float* __restrict__ part,
                                                  const char* __restrict__ pf_wo, const char* __restrict__ pf_wg,
                                                  const char* __restrict__ pf_wu, const char* __restrict__ pf_wd) {
  extern __shared__ __attribute__((aligned(16))) char smem_[];
  lds_ptr smem = (lds_ptr)smem_;
  const int lane = threadIdx.x & 63;
  const int w = __builtin_amdgcn_readfirstlane(threadIdx.x >> 6);
  const int xcd = blockIdx.x & 7, idx = blockIdx.x >> 3;
  const int head = xcd * 4 + (idx >> 3), qb = idx & 7;
  const int r32 = lane & 31, hh = lane >> 5;
  const int qg = w & 3, kh = w >> 2;
  const u16* Qp = qkv + (size_t)head * SEQ * 32;
  const char* Kp = (const char*)(qkv + (size_t)(32 + head) * SEQ * 32);
  const char* Vp = (const char*)(qkv + (size_t)(64 + head) * SEQ * 32);
  const int q0 = qb * 256 + qg * 64;
  STAMP_AT(2, 0);
  __attribute__((address_space(3))) int* flag = (__attribute__((address_space(3))) int*)(smem + ANST * ASTAGE + 1024);
  if (threadIdx.x == 0) *flag = 0;
  bf16x8 qa0 = *(const bf16x8*)(Qp + (q0 + r32) * 16 + hh * 8);
  bf16x8 qa1 = *(const bf16x8*)(Qp + (SEQ + q0 + r32) * 16 + hh * 8);
  bf16x8 qb0 = *(const bf16x8*)(Qp + (q0 + 32 + r32) * 16 + hh * 8);
  bf16x8 qb1 = *(const bf16x8*)(Qp + (SEQ + q0 + 32 + r32) * 16 + hh * 8);
  f32x16 oa, ob;
  float nma, nmb, la, lb;
  ModTask mt;
  attn_pass<false>(smem, lane, w, Kp, Vp, (const char*)wmod, pf_wo, pf_wg, pf_wu, pf_wd, qa0, qa1, qb0, qb1, oa, ob, nma, nmb, la, lb, mt, c);
  {
    float big = fmaxf(fabsf(la), fabsf(lb));
#pragma unroll
    for (int r = 0; r < 16; ++r) big = fmaxf(big, fmaxf(fabsf(oa[r]), fabsf(ob[r])));
    bool bad = !(big < 1e30f);
#pragma unroll
    for (int r = 0; r < 16; ++r) bad = bad || (oa[r] != oa[r]) || (ob[r] != ob[r]);
    bad = bad || (la != la) || (lb != lb);
    if (__any(bad) && lane == 0) *flag = 1;
  }
  __syncthreads();
  if (__builtin_amdgcn_readfirstlane(*flag) != 0) {
    attn_pass<true>(smem, lane, w, Kp, Vp, (const char*)wmod, pf_wo, pf_wg, pf_wu, pf_wd, qa0, qa1, qb0, qb1, oa, ob, nma, nmb, la, lb, mt, c);
    __syncthreads();
  }
  STAMP_AT(2, 2);
  const int task = blockIdx.x * 2 + (threadIdx.x >> 8);
  {
    __attribute__((address_space(3))) float* xb = (__attribute__((address_space(3))) float*)smem;
    const f32x16& og = kh == 0 ? ob : oa;
    __attribute__((address_space(3))) float* dst = xb + w * (18 * 64) + lane;
#pragma unroll
    for (int r = 0; r < 16; ++r) dst[r * 64] = og[r];
    dst[16 * 64] = kh == 0 ? lb : la;
    dst[17 * 64] = kh == 0 ? nmb : nma;
    __syncthreads();
    const __attribute__((address_space(3))) float* src = xb + (w ^ 4) * (18 * 64) + lane;
    f32x16 om = kh == 0 ? oa : ob;
    const float nm_me = kh == 0 ? nma : nmb;
    const float nm_ot = src[17 * 64];
    const float nmin = fminf(nm_me, nm_ot);
    const float f_me = __builtin_amdgcn_exp2f(nmin - nm_me), f_ot = __builtin_amdgcn_exp2f(nmin - nm_ot);
    const float rl = 1.f / ((kh == 0 ? la : lb) * f_me + src[16 * 64] * f_ot);
    const float g_me = f_me * rl, g_ot = f_ot * rl;
    u16* op = oblk + ((size_t)head * 2 * SEQ + q0 + kh * 32 + r32) * 16 + 4 * hh;
#pragma unroll
    for (int g = 0; g < 4; ++g) {
      float v0 = om[4 * g] * g_me + src[(4 * g) * 64] * g_ot;
      float v1 = om[4 * g + 1] * g_me + src[(4 * g + 1) * 64] * g_ot;
      float v2 = om[4 * g + 2] * g_me + src[(4 * g + 2) * 64] * g_ot;
      float v3 = om[4 * g + 3] * g_me + src[(4 * g + 3) * 64] * g_ot;
      u32x2 v;
      v[0] = pack2(v0, v1);
      v[1] = pack2(v2, v3);
      *(u32x2*)(op + (g >> 1) * (SEQ * 16) + (g & 1) * 8) = v;
    }
  }
  mod_finish(mt, part, 32 + (task >> 3), task & 7, threadIdx.x & 255, (float*)(smem_ + 40960) + (threadIdx.x >> 8) * 256);
  wait_vm<0>();
  STAMP_AT(2, 3);
}

extern "C" void kernel_launch(void* const* d_in, const int* in_sizes, int n_in, void* d_out, int out_size,
                              void* d_ws, size_t ws_size, hipStream_t stream) {
  const u16* x = (const u16*)d_in[0];
  const u16* wqkv = (const u16*)d_in[1];
  const u16* wo = (const u16*)d_in[2];
  const u16* wg = (const u16*)d_in[4];
  const u16* wu = (const u16*)d_in[5];
  const u16* wd = (const u16*)d_in[6];
  const u16* c = (const u16*)d_in[7];
  const u16* wmod = (const u16*)d_in[8];
  u16* out = (u16*)d_out;
  char* ws = (char*)d_ws;
  float* part = (float*)ws;
  float* modf = (float*)(ws + (256 << 10));
  unsigned* flags = (unsigned*)(ws + (512 << 10));
  u16* h1 = (u16*)(ws + (1 << 20));
  u16* qkv = (u16*)(ws + (8 << 20));
  u16* ob = (u16*)(ws + (24 << 20));
  u16* x2 = (u16*)(ws + (32 << 20));
  u16* h2 = (u16*)(ws + (40 << 20));
  u16* hh = (u16*)(ws + (48 << 20));
  u16* wdp = (u16*)(ws + (64 << 20));

  hipFuncSetAttribute((const void*)k_qkv, hipFuncAttributeMaxDynamicSharedMemorySize, CQ::LDS);
  hipFuncSetAttribute((const void*)k_proj<DM, DM>, hipFuncAttributeMaxDynamicSharedMemorySize, CP::LDS);
  hipFuncSetAttribute((const void*)k_proj<FF, WDP>, hipFuncAttributeMaxDynamicSharedMemorySize, CP::LDS);
  hipFuncSetAttribute((const void*)k_glu, hipFuncAttributeMaxDynamicSharedMemorySize, CG::LDS);
  hipFuncSetAttribute((const void*)k_attn, hipFuncAttributeMaxDynamicSharedMemorySize, ALDS);

  k_pre<<<256 + 256, 256, 0, stream>>>(c, wmod, part, (const char*)x, (const char*)wqkv, flags);
  k_ln<6, MNS, 192><<<256 + 192, 512, 0, stream>>>(x, part, h1, (const char*)wqkv, wd, wdp);
  k_qkv<<<256, 768, CQ::LDS, stream>>>(h1, wqkv, qkv);
  k_attn<<<256, 512, ALDS, stream>>>(qkv, ob, c, wmod, part, (const char*)wo, (const char*)wg, (const char*)wu, (const char*)wd);
  k_proj<DM, DM><<<256, 768, CP::LDS, stream>>>(ob, wo, x, part + 2 * DM, x2);
  k_ln<7, MNS, 0><<<256 + 224, 512, 0, stream>>>(x2, part + 3 * DM, h2, (const char*)wqkv, wd, wdp);
  k_glu<<<256, 512, CG::LDS, stream>>>(h2, wg, wu, hh);
  k_proj<FF, WDP><<<256, 768, CP::LDS, stream>>>(hh, wdp, x2, part + 5 * DM, out);
}
```

```cpp
#include <hip/hip_runtime.h>
#include <hip/hip_bf16.h>
#include <stdint.h>

typedef __attribute__((ext_vector_type(8))) short bf16x8;
typedef __attribute__((ext_vector_type(4))) short s16x4;
typedef __attribute__((ext_vector_type(4))) float f32x4;
typedef __attribute__((ext_vector_type(2))) float f32x2;
typedef __attribute__((ext_vector_type(16))) float f32x16;
typedef __attribute__((ext_vector_type(4))) unsigned u32x4;
typedef __attribute__((ext_vector_type(2))) unsigned u32x2;
typedef __attribute__((ext_vector_type(2))) __bf16 bf16x2_t;
typedef unsigned short u16;
typedef __attribute__((address_space(3))) char* lds_ptr;
typedef __attribute__((address_space(3))) const char* lds_cptr;

#define DI __device__ __forceinline__

constexpr int SEQ = 2048, DM = 1024, FF = 3584, NH = 32;

DI float bf2f(u16 x) { return __uint_as_float(((unsigned)x) << 16); }
DI float bflo(unsigned x) { return __uint_as_float(x << 16); }
DI float bfhi(unsigned x) { return __uint_as_float(x & 0xffff0000u); }
DI unsigned pack2(float a, float b) {
  f32x2 v = {a, b};
  bf16x2_t r = __builtin_convertvector(v, bf16x2_t);
  return __builtin_bit_cast(unsigned, r);
}
DI float rbf(float a) { return bflo(pack2(a, 0.f) & 0xffffu); }

#define MFMA16(a, b, c) __builtin_amdgcn_mfma_f32_16x16x32_bf16((a), (b), (c), 0, 0, 0)
#define MFMA32(a, b, c) __builtin_amdgcn_mfma_f32_32x32x16_bf16((a), (b), (c), 0, 0, 0)

DI void glds16(const void* base, unsigned voff, unsigned lds_addr) {
  unsigned sv;
  asm volatile("s_mov_b32 %0, m0\n\ts_mov_b32 m0, %3\n\ts_nop 0\n\tglobal_load_lds_dwordx4 %1, %2\n\ts_mov_b32 m0, %0"
               : "=&s"(sv) : "v"(voff), "s"(base), "s"(lds_addr) : "memory");
}
DI unsigned lds_u32(lds_ptr p) { return (unsigned)(uintptr_t)p; }
DI bf16x8 lds_read128(lds_cptr p) { return *(const __attribute__((address_space(3))) bf16x8*)p; }
DI s16x4 lds_tr(lds_cptr p) {
  typedef short v4i16_t __attribute__((ext_vector_type(4)));
  return __builtin_bit_cast(s16x4, __builtin_amdgcn_ds_read_tr16_b64_v4i16((__attribute__((address_space(3))) v4i16_t*)p));
}
DI bf16x8 lds_tr8(lds_cptr p0, lds_cptr p1) {
  s16x4 a = lds_tr(p0), b = lds_tr(p1);
  return __builtin_shufflevector(a, b, 0, 1, 2, 3, 4, 5, 6, 7);
}
#define STAMP_AT(kid, slot) do {} while (0)
DI void st16(void* p, u32x4 v) { asm volatile("global_store_dwordx4 %0, %1, off sc1\n\ts_nop 1" ::"v"(p), "v"(v) : "memory"); }
template <int N> DI void wait_vm() { asm volatile("s_waitcnt vmcnt(%0)" ::"n"(N) : "memory"); }
DI void bar() { __builtin_amdgcn_s_barrier(); }

constexpr int MNS = 8;
struct ModTask { u32x4 w[4]; u16 cv[4]; };
DI void mod_load(ModTask& mt, const u16* __restrict__ c, const u16* __restrict__ wmod, int cb, int ks, int t) {
  const int cc = t & 7, rg = t >> 3;
  const int k0 = ks * 128 + rg;
  const u16* wp = wmod + (size_t)k0 * (6 * DM) + cb * 64 + cc * 8;
#pragma unroll
  for (int i = 0; i < 4; ++i) mt.w[i] = *(const u32x4*)(wp + (size_t)i * 32 * (6 * DM));
#pragma unroll
  for (int i = 0; i < 4; ++i) mt.cv[i] = c[k0 + 32 * i];
}
DI void mod_finish(const ModTask& mt, float* __restrict__ part, int cb, int ks, int t, float* s_red  ) {
  float acc[8];
#pragma unroll
  for (int j = 0; j < 8; ++j) acc[j] = 0.f;
#pragma unroll
  for (int i = 0; i < 4; ++i) {
    const float cv = bf2f(mt.cv[i]);
    const float sv = rbf(cv / (1.f + __expf(-cv)));
#pragma unroll
    for (int j = 0; j < 4; ++j) {
      acc[2 * j] += sv * bflo(mt.w[i][j]);
      acc[2 * j + 1] += sv * bfhi(mt.w[i][j]);
    }
  }
#pragma unroll
  for (int j = 0; j < 8; ++j) {
    float v = acc[j];
    v += __shfl_xor(v, 8);
    v += __shfl_xor(v, 16);
    v += __shfl_xor(v, 32);
    acc[j] = v;
  }
  const int lane = t & 63, wv = t >> 6;
  if (lane < 8) {
#pragma unroll
    for (int j = 0; j < 8; ++j) s_red[wv * 64 + lane * 8 + j] = acc[j];
  }
  __syncthreads();
  if (t < 64) part[ks * (6 * DM) + cb * 64 + t] = (s_red[t] + s_red[64 + t]) + (s_red[128 + t] + s_red[192 + t]);
}
DI f32x4 gate4(const float* __restrict__ gate, int col) {
  f32x4 gt = *(const f32x4*)(gate + col);
#pragma unroll
  for (int i = 1; i < MNS; ++i) gt += *(const f32x4*)(gate + i * (6 * DM) + col);
#pragma unroll
  for (int j = 0; j < 4; ++j) gt[j] = rbf(gt[j]);
  return gt;
}

DI void touch16k(const char* p, int t) {
  u32x4 v[4];
#pragma unroll
  for (int i = 0; i < 4; ++i) v[i] = *(const u32x4*)(p + i * 4096 + t * 16);
#pragma unroll
  for (int i = 0; i < 4; ++i) asm volatile("" ::"v"(v[i]));
}

__global__ __launch_bounds__(256) void k_pre(const u16* __restrict__ c, const u16* __restrict__ wmod, float* __restrict__ part,
                                              const char* __restrict__ x, const char* __restrict__ wqkv,
                                              unsigned* __restrict__ flags) {
  __shared__ float s_red[256];
  const int t = threadIdx.x, b = blockIdx.x;
  if (b < 256) STAMP_AT(0, 0);
  if (b < 256) {
    ModTask mt;
    mod_load(mt, c, wmod, b >> 3, b & 7, t);
    mod_finish(mt, part, b >> 3, b & 7, t, s_red);
    if (b == 0) flags[t * 16] = 0u;
  } else {
    touch16k(x + (size_t)(b - 256) * 16384, t);
  }
  if (b < 256) STAMP_AT(0, 3);
}

constexpr int WDP = DM + 64;
template <int KID, int NS, int NPF>
__global__ __launch_bounds__(512) void k_ln(const u16* __restrict__ x, const float* __restrict__ ss  ,
                                             u16* __restrict__ h16, const char* __restrict__ pf,
                                             const u16* __restrict__ wsrc, u16* __restrict__ wdst) {
  __shared__ __attribute__((aligned(16))) float sm[2048];
  const int t = threadIdx.x;
  if (blockIdx.x >= 256 + NPF) {
    const int r0 = (blockIdx.x - 256 - NPF) * 16 + (t >> 7);
    u32x4 v[4];
#pragma unroll
    for (int i = 0; i < 4; ++i) v[i] = *(const u32x4*)(wsrc + (size_t)(r0 + 4 * i) * DM + (t & 127) * 8);
#pragma unroll
    for (int i = 0; i < 4; ++i) st16(wdst + (size_t)(r0 + 4 * i) * WDP + (t & 127) * 8, v[i]);
    return;
  }
  if (blockIdx.x >= 256) {
    touch16k(pf + (size_t)((blockIdx.x - 256) * 2 + (t >> 8)) * 16384, t & 255);
    return;
  }
  const int lane = t & 63, wv = t >> 6;
  const int row = blockIdx.x * 8 + wv;
  STAMP_AT(KID, 0);
  const u16* xr = x + (size_t)row * DM;
  u32x4 v0 = *(const u32x4*)(xr + lane * 8);
  u32x4 v1 = *(const u32x4*)(xr + 512 + lane * 8);
  {
    f32x4 pv[NS];
#pragma unroll
    for (int i = 0; i < NS; ++i) pv[i] = *(const f32x4*)(ss + i * (6 * DM) + t * 4);
    f32x4 a = pv[0];
#pragma unroll
    for (int i = 1; i < NS; ++i) a += pv[i];
#pragma unroll
    for (int j = 0; j < 4; ++j) {
      float v = rbf(a[j]);
      a[j] = t < 256 ? v : rbf(1.f + v);
    }
    *(f32x4*)(sm + t * 4) = a;
  }
  float f[16];
#pragma unroll
  for (int j = 0; j < 4; ++j) {
    f[2 * j] = bflo(v0[j]); f[2 * j + 1] = bfhi(v0[j]);
    f[8 + 2 * j] = bflo(v1[j]); f[8 + 2 * j + 1] = bfhi(v1[j]);
  }
  float s = 0.f;
#pragma unroll
  for (int j = 0; j < 16; ++j) s += f[j];
#pragma unroll
  for (int o = 1; o < 64; o <<= 1) s += __shfl_xor(s, o);
  const float mean = s * (1.f / DM);
  float q = 0.f;
#pragma unroll
  for (int j = 0; j < 16; ++j) { float d = f[j] - mean; q += d * d; }
#pragma unroll
  for (int o = 1; o < 64; o <<= 1) q += __shfl_xor(q, o);
  const float mb = rbf(mean);
  const float vb = rbf(q * (1.f / DM));
  const float rden = 1.f / rbf(sqrtf(rbf(vb + 1e-5f)));
  __syncthreads();
#pragma unroll
  for (int half = 0; half < 2; ++half) {
    const int k0 = half * 512 + lane * 8;
    f32x4 sh0 = *(const f32x4*)(sm + k0), sh1 = *(const f32x4*)(sm + k0 + 4);
    f32x4 sc0 = *(const f32x4*)(sm + 1024 + k0), sc1 = *(const f32x4*)(sm + 1024 + k0 + 4);
    float y[8];
#pragma unroll
    for (int j = 0; j < 8; ++j) {
      float g = j < 4 ? sc0[j] : sc1[j - 4];
      float sh = j < 4 ? sh0[j] : sh1[j - 4];
      float d = rbf(f[half * 8 + j] - mb);
      float n = rbf(d * rden);
      y[j] = rbf(n * g) + sh;
    }
    u32x4 o;
#pragma unroll
    for (int j = 0; j < 4; ++j) o[j] = pack2(y[2 * j], y[2 * j + 1]);
    *(u32x4*)(h16 + ((size_t)(k0 >> 4) * SEQ + row) * 16 + (k0 & 15)) = o;
  }
  STAMP_AT(KID, 3);
}

DI u32x4 pair16(u32x2 a, u32x2 b) {
  auto s0 = __builtin_amdgcn_permlane16_swap(a[0], b[0], false, false);
  auto s1 = __builtin_amdgcn_permlane16_swap(a[1], b[1], false, false);
  return u32x4{s0[0], s1[0], s0[1], s1[1]};
}

DI int hA(int r) { return (-r) & 3; }

template <int BM, int NGRP, int KS, int NST, int NLW_ = 8>
struct GCfg {
  static constexpr int NLW = NLW_;
  static constexpr int A_SUB = BM * 64;
  static constexpr int W_SUB = NGRP * 4096;
  static constexpr int STAGE = KS * (A_SUB + W_SUB);
  static constexpr int APW = KS * BM / 16 / NLW;
  static constexpr int WPW = KS * NGRP * 4 / NLW;
  static constexpr int PPW = APW + WPW;
  static constexpr int LDS = STAGE * NST;
};

template <class C>
struct Loader {
  const char* abase;
  const char* wbase[C::WPW];
  unsigned av[C::APW];
  unsigned wv[C::WPW];
  int aoff[C::APW];
  int woff[C::WPW];
};

template <int BM, int NGRP, int KS, int NST, int MT, int NT, int KSPLIT, int NKT, int KID, int IOFS, int NLW = 8, bool LOADS = (NLW == 8)>
struct Engine {
  typedef GCfg<BM, NGRP, KS, NST, NLW> C;
  static constexpr bool DED = !LOADS;
  static constexpr int SPW = KS / KSPLIT;
  static constexpr int P = C::PPW;
  static constexpr int G = NT * SPW;
  f32x4 (&acc)[NT][MT];
  lds_ptr smem;
  Loader<C>& ld;
  long a_adv, w_adv;
  const int (&waddr)[NT];
  int a_sub0, w_sub0;
  int cur, nxt, fre;
  bf16x8 wf[NT], af[2][MT];

  DI Engine(f32x4 (&acc_)[NT][MT], lds_ptr smem_, Loader<C>& ld_, long a_adv_, long w_adv_, int aaddr, const int (&waddr_)[NT], int kg)
      : acc(acc_), smem(smem_), ld(ld_), a_adv(a_adv_), w_adv(w_adv_), waddr(waddr_) {
    a_sub0 = kg * C::A_SUB + aaddr;
    w_sub0 = KS * C::A_SUB + kg * C::W_SUB;
  }
  template <int I> DI void piece(int stage_off) {
    if constexpr (I < C::APW) {
      glds16(ld.abase, ld.av[I], lds_u32(smem) + stage_off + ld.aoff[I]);
      if constexpr (I == C::APW - 1) ld.abase += a_adv * 2;
    } else {
      constexpr int J = I - C::APW;
      glds16(ld.wbase[J], ld.wv[J], lds_u32(smem) + stage_off + ld.woff[J]);
      ld.wbase[J] += w_adv * 2;
    }
  }
  template <int I> DI void issue_all(int stage_off) {
    if constexpr (I < P) { piece<I>(stage_off); issue_all<I + 1>(stage_off); }
  }
  template <int g, int I> DI void issue_at(int stage_off) {
    if constexpr (I < P) {
      constexpr int span = (G - IOFS) > P ? (G - IOFS) : P;
      constexpr int pos = IOFS + (I * span) / P < G ? IOFS + (I * span) / P : G - 1;
      if constexpr (pos == g) piece<I>(stage_off);
      issue_at<g, I + 1>(stage_off);
    }
  }
  template <int PAR, int S, bool PF, bool DOISSUE, int n>
  DI void groups(lds_cptr ab, lds_cptr wb) {
    if constexpr (n < NT) {
#pragma unroll
      for (int m = 0; m < MT; ++m) acc[n][m] = MFMA16(wf[n], af[PAR][m], acc[n][m]);
      if constexpr (PF) {
        wf[n] = lds_tr8(wb + waddr[n], wb + waddr[n] + 512);
#pragma unroll
        for (int m = 0; m < MT; ++m)
          if ((m * NT) / MT == n) af[PAR ^ 1][m] = lds_read128(ab + m * 512);
      }
      if constexpr (DOISSUE && !DED) issue_at<S * NT + n, 0>(fre);
      __builtin_amdgcn_sched_barrier(0);
      groups<PAR, S, PF, DOISSUE, n + 1>(ab, wb);
    }
  }
  template <int PAR, bool DOISSUE, bool LAST, int S> DI void steps() {
    if constexpr (S < SPW) {
      if constexpr (S + 1 < SPW)
        groups<PAR, S, true, DOISSUE, 0>(smem + cur + a_sub0 + (S + 1) * KSPLIT * C::A_SUB, smem + cur + w_sub0 + (S + 1) * KSPLIT * C::W_SUB);
      else if constexpr (!LAST)
        groups<PAR, S, true, DOISSUE, 0>(smem + nxt + a_sub0, smem + nxt + w_sub0);
      else
        groups<PAR, S, false, false, 0>(smem, smem);
      steps<PAR ^ 1, DOISSUE, LAST, S + 1>();
    }
  }
  template <int PAR, bool DOISSUE, bool LAST, int WAITN> DI void iter() {
    if constexpr (!LAST) { if constexpr (!DED) wait_vm<WAITN>(); bar(); }
    steps<PAR, DOISSUE, LAST, 0>();
    fre = cur; cur = nxt; nxt = (nxt + C::STAGE == NST * C::STAGE) ? 0 : nxt + C::STAGE;
  }
  template <int KT> DI void tail() {
    if constexpr (KT < NKT) {
      constexpr bool DOISSUE = KT + NST - 1 < NKT;
      constexpr bool LAST = KT == NKT - 1;
      constexpr int newest = (KT + NST - 2) < (NKT - 1) ? (KT + NST - 2) : (NKT - 1);
      constexpr int younger = newest - (KT + 1) > 0 ? newest - (KT + 1) : 0;
      iter<(KT * SPW) & 1, DOISSUE, LAST, P * younger>();
      tail<KT + 1>();
    }
  }
  DI void start() {
    static_assert(NKT >= NST + 1, "too few k tiles");
    STAMP_AT(KID, 0);
    if constexpr (!DED) {
#pragma unroll
      for (int s = 0; s < NST - 1; ++s) issue_all<0>(s * C::STAGE);
    }
  }
  template <int KT, class Hook> DI void ltail(Hook& hook) {
    if constexpr (KT < NKT - 1) {
      constexpr int newest = (KT + NST - 2) < (NKT - 1) ? (KT + NST - 2) : (NKT - 1);
      constexpr int younger = newest - (KT + 1) > 0 ? newest - (KT + 1) : 0;
      wait_vm<P * younger>();
      if constexpr (KT == NKT - 2) { hook(1); asm volatile("s_waitcnt lgkmcnt(0)" ::: "memory"); }
      bar();
      if constexpr (KT == NKT - NST + 1) hook(0);
      ltail<KT + 1>(hook);
    }
  }
  static constexpr int FREE_OFF = ((NKT - NST) % NST) * C::STAGE;
  DI void loader_run() { loader_run([](int) {}); }
  template <class Hook> DI void loader_run(Hook hook) {
    __builtin_amdgcn_s_setprio(1);
#pragma unroll
    for (int s = 0; s < NST - 1; ++s) issue_all<0>(s * C::STAGE);
    wait_vm<P*(NST - 2)>();
    bar();
    fre = (NST - 1) * C::STAGE;
    for (int kt = 0; kt < NKT - NST + 1; ++kt) {
      wait_vm<P*(NST - 3)>();
      bar();
      issue_all<0>(fre);
      fre = (fre + C::STAGE == NST * C::STAGE) ? 0 : fre + C::STAGE;
    }
    ltail<NKT - NST + 1>(hook);
  }
  DI void run() { run([] {}); }
  template <class Hook> DI void run(Hook hook) {
    if constexpr (!DED) wait_vm<P*(NST - 2)>();
    bar();
    STAMP_AT(KID, 1);
    {
      lds_cptr ab = smem + a_sub0;
      lds_cptr wb = smem + w_sub0;
#pragma unroll
      for (int m = 0; m < MT; ++m) af[0][m] = lds_read128(ab + m * 512);
#pragma unroll
      for (int n = 0; n < NT; ++n) wf[n] = lds_tr8(wb + waddr[n], wb + waddr[n] + 512);
    }
    hook();
    cur = 0; nxt = C::STAGE; fre = (NST - 1) * C::STAGE;
    constexpr int STEADY = NKT - NST + 1;
    constexpr int LOOPN = (STEADY / 2) * 2;
    for (int kt = 0; kt < LOOPN; kt += 2) {
      iter<0, true, false, P*(NST - 3)>();
      iter<SPW & 1, true, false, P*(NST - 3)>();
    }
    tail<LOOPN>();
    STAMP_AT(KID, 2);
  }
};

template <class C, int BM, int NGRP, int KS>
DI void setup_loader_A(Loader<C>& ld, const u16* Ablk, int m0, int w, int lane) {
#pragma unroll
  for (int i = 0; i < C::APW; ++i) {
    int a = w + C::NLW * i;
    int blk = a / (BM / 32), j = a % (BM / 32);
    ld.aoff[i] = blk * (BM * 32) + j * 1024;
    ld.av[i] = ((blk * SEQ + m0 + j * 32) * 16) * 2 + lane * 16;
  }
  ld.abase = (const char*)Ablk;
}

DI int wfrag_addr(int t, int lane) {
  int g = lane >> 4, q = (lane & 15) >> 2, p = lane & 3;
  int f = ((q >> 1) & 1) | ((g & 1) << 1);
  return (t >> 2) * 4096 + (8 * g + q) * 128 + (((t & 3) ^ f) * 32) + p * 8;
}
template <int BM>
DI int afrag_addr(int mrow0, int lane) {
  int r = lane & 15, g = lane >> 4;
  return (g >> 1) * (BM * 32) + (mrow0 + r) * 32 + (g & 1) * 16;
}

constexpr float QSCALE = 0.17677669529663687f * 1.4426950408889634f;
typedef GCfg<128, 3, 2, 4, 4> CQ;
__global__ __launch_bounds__(768, 3) void k_qkv(const u16* __restrict__ Ablk, const u16* __restrict__ W,
                                                 u16* __restrict__ out) {
  extern __shared__ __attribute__((aligned(16))) char smem_[];
  lds_ptr smem = (lds_ptr)smem_;
  const int lane = threadIdx.x & 63;
  const int w = __builtin_amdgcn_readfirstlane(threadIdx.x >> 6);
  const int xcd = blockIdx.x & 7, idx = blockIdx.x >> 3;
  const int tm = (xcd & 1) * 8 + (idx & 7), tn = (xcd >> 1) * 4 + (idx >> 3);
  const int m0 = tm * 128, n0 = tn * 192;
  constexpr int LDW = 3 * DM;
  Loader<CQ> ld;
  const int lw = w & 3;
  setup_loader_A<CQ, 128, 3, 2>(ld, Ablk, m0, lw, lane);
#pragma unroll
  for (int i = 0; i < CQ::WPW; ++i) {
    int b = lw + 4 * i;
    int sub = b / 12, r = b % 12, grp = r >> 2, o = r & 3;
    ld.woff[i] = 2 * CQ::A_SUB + sub * CQ::W_SUB + grp * 4096 + o * 1024;
    int k = sub * 32 + 8 * o + (lane >> 3);
    int f = ((lane >> 4) & 1) | ((o & 1) << 1);
    int col = n0 + grp * 64 + ((((lane & 7) >> 1) ^ f) * 16) + (lane & 1) * 8;
    ld.wv[i] = (k * LDW + col) * 2;
    ld.wbase[i] = (const char*)W;
  }
  const int wm = w & 1, wn = (w >> 1) & 3;
  int waddr[3];
#pragma unroll
  for (int n = 0; n < 3; ++n) waddr[n] = wfrag_addr(wn * 3 + n, lane);
  const int aaddr = afrag_addr<128>(wm * 64, lane);
  f32x4 acc[3][4];
#pragma unroll
  for (int n = 0; n < 3; ++n)
#pragma unroll
    for (int m = 0; m < 4; ++m) acc[n][m] = f32x4{0.f, 0.f, 0.f, 0.f};
  Engine<128, 3, 2, 4, 4, 3, 1, DM / 64, 1, 0, 4> eng(acc, smem, ld, (long)2 * SEQ * 32, (long)64 * LDW, aaddr, waddr, 0);
  if (w >= 8) { eng.loader_run(); return; }
  eng.start();
  eng.run();
  const int g = lane >> 4;
#pragma unroll
  for (int n = 0; n < 3; ++n) {
    const int cb = (n0 >> 4) + wn * 3 + n;
    const float sc = cb < DM / 16 ? QSCALE : 1.f;
#pragma unroll
    for (int m = 0; m < 4; m += 2) {
      u32x2 a, b;
      a[0] = pack2(acc[n][m][0] * sc, acc[n][m][1] * sc);
      a[1] = pack2(acc[n][m][2] * sc, acc[n][m][3] * sc);
      b[0] = pack2(acc[n][m + 1][0] * sc, acc[n][m + 1][1] * sc);
      b[1] = pack2(acc[n][m + 1][2] * sc, acc[n][m + 1][3] * sc);
      const int row = m0 + wm * 64 + (m + (g & 1)) * 16 + (lane & 15);
      st16(out + ((size_t)cb * SEQ + row) * 16 + (g >> 1) * 8, pair16(a, b));
    }
  }
  STAMP_AT(1, 3);
}

typedef GCfg<128, 1, 2, 6, 4> CP;
template <int KDIM, int LDW>
__global__ __launch_bounds__(768, 3) void k_proj(const u16* __restrict__ Ablk, const u16* __restrict__ W,
                                                  const u16* __restrict__ res, const float* __restrict__ gate,
                                                  u16* __restrict__ out) {
  extern __shared__ __attribute__((aligned(16))) char smem_[];
  lds_ptr smem = (lds_ptr)smem_;
  const int lane = threadIdx.x & 63;
  const int w = __builtin_amdgcn_readfirstlane(threadIdx.x >> 6);
  const int xcd = blockIdx.x & 7, idx = blockIdx.x >> 3;
  const int tm = (xcd & 3) * 4 + (idx & 3), tn = (xcd >> 2) * 8 + (idx >> 2);
  const int m0 = tm * 128, n0 = tn * 64;
  Loader<CP> ld;
  const int lw = w & 3;
  setup_loader_A<CP, 128, 1, 2>(ld, Ablk, m0, lw, lane);
#pragma unroll
  for (int i = 0; i < CP::WPW; ++i) {
    int b = lw + 4 * i;
    int sub = b >> 2, o = b & 3;
    ld.woff[i] = 2 * CP::A_SUB + sub * CP::W_SUB + o * 1024;
    int k = sub * 32 + 8 * o + (lane >> 3);
    int f = ((lane >> 4) & 1) | ((o & 1) << 1);
    int col = n0 + ((((lane & 7) >> 1) ^ f) * 16) + (lane & 1) * 8;
    ld.wv[i] = (k * LDW + col) * 2;
    ld.wbase[i] = (const char*)W;
  }
  const int wm = w & 1, wn = (w >> 1) & 1, kg = (w >> 2) & 1;
  int waddr[2];
#pragma unroll
  for (int n = 0; n < 2; ++n) waddr[n] = wfrag_addr(wn * 2 + n, lane);
  const int aaddr = afrag_addr<128>(wm * 64, lane);
  f32x4 acc[2][4];
#pragma unroll
  for (int n = 0; n < 2; ++n)
#pragma unroll
    for (int m = 0; m < 4; ++m) acc[n][m] = f32x4{0.f, 0.f, 0.f, 0.f};
  Engine<128, 1, 2, 6, 4, 2, 2, KDIM / 64, (KDIM == DM ? 3 : 5), 0, 4> eng(acc, smem, ld, (long)2 * SEQ * 32, (long)64 * LDW, aaddr, waddr, kg);
  constexpr int EOFF = decltype(eng)::FREE_OFF;
  static_assert(EOFF >= 32768, "overlaps the K-group exchange area");
  if (w >= 8) {
    float gp[MNS];
    eng.loader_run([&](int phase) {
      if (phase == 0) {
#pragma unroll
        for (int i = 0; i < 4; ++i) {
          const int pc = lw + 4 * i;
          glds16(res + (size_t)(m0 + pc * 8) * DM + n0, ((lane >> 3) * DM + (lane & 7) * 8) * 2, lds_u32(smem) + EOFF + pc * 1024);
        }
#pragma unroll
        for (int i = 0; i < MNS; ++i) gp[i] = gate[i * (6 * DM) + n0 + lane];
      } else if (w == 8) {
        float v = 0.f;
#pragma unroll
        for (int i = 0; i < MNS; ++i) v += gp[i];
        *(__attribute__((address_space(3))) float*)(smem + EOFF + 16384 + lane * 4) = rbf(v);
      }
    });
    return;
  }
  const int g = lane >> 4;
  const int colw = n0 + (wn * 2 + kg) * 16;
  eng.start();
  eng.run();
  __builtin_amdgcn_s_barrier();
  asm volatile("" ::: "memory");
  __attribute__((address_space(3))) f32x4* red = (__attribute__((address_space(3))) f32x4*)smem;
  f32x4 kp[4];
  if (kg == 0) {
#pragma unroll
    for (int m = 0; m < 4; ++m) { red[(w * 4 + m) * 64 + lane] = acc[1][m]; kp[m] = acc[0][m]; }
  } else {
#pragma unroll
    for (int m = 0; m < 4; ++m) { red[(w * 4 + m) * 64 + lane] = acc[0][m]; kp[m] = acc[1][m]; }
  }
  asm volatile("s_waitcnt lgkmcnt(0)" ::: "memory");
  __builtin_amdgcn_s_barrier();
  asm volatile("" ::: "memory");
  {
    const f32x4 gt = *(const __attribute__((address_space(3))) f32x4*)(smem + EOFF + 16384 + ((wn * 2 + kg) * 16 + 4 * g) * 4);
#pragma unroll
    for (int m = 0; m < 4; m += 2) {
      u32x2 o[2];
#pragma unroll
      for (int i = 0; i < 2; ++i) {
        f32x4 a = kp[m + i] + red[((w ^ 4) * 4 + m + i) * 64 + lane];
        const u32x2 r = *(const __attribute__((address_space(3))) u32x2*)(smem + EOFF + (wm * 64 + (m + i) * 16 + (lane & 15)) * 128 + ((wn * 2 + kg) * 16 + 4 * g) * 2);
        o[i][0] = pack2(bflo(r[0]) + rbf(gt[0] * rbf(a[0])), bfhi(r[0]) + rbf(gt[1] * rbf(a[1])));
        o[i][1] = pack2(bflo(r[1]) + rbf(gt[2] * rbf(a[2])), bfhi(r[1]) + rbf(gt[3] * rbf(a[3])));
      }
      const int row = m0 + wm * 64 + (m + (g & 1)) * 16 + (lane & 15);
      *(u32x4*)(out + (size_t)row * DM + colw + (g >> 1) * 8) = pair16(o[0], o[1]);
    }
  }
  STAMP_AT((KDIM == DM ? 3 : 5), 3);
}

typedef GCfg<256, 4, 1, 4, 4> CG;
template <int NH_>
DI void glu_wave(lds_ptr smem, Loader<CG>& ld, int lane, int wm, int t0, int m0, int f0, u16* __restrict__ out) {
  constexpr int NT = 2 * NH_;
  int waddr[NT];
#pragma unroll
  for (int n = 0; n < NH_; ++n) {
    waddr[n] = wfrag_addr(t0 + n, lane);
    waddr[NH_ + n] = waddr[n] + 8192;
  }
  const int aaddr = afrag_addr<256>(wm * 64, lane);
  f32x4 acc[NT][4];
#pragma unroll
  for (int n = 0; n < NT; ++n)
#pragma unroll
    for (int m = 0; m < 4; ++m) acc[n][m] = f32x4{0.f, 0.f, 0.f, 0.f};
  Engine<256, 4, 1, 4, 4, NT, 1, DM / 32, 4, 0, 4, (NH_ == 3)> eng(acc, smem, ld, (long)SEQ * 32, (long)32 * FF, aaddr, waddr, 0);
  eng.start();
  eng.run();
  const int g = lane >> 4;
#pragma unroll
  for (int n = 0; n < NH_; ++n) {
    const int cb = (f0 >> 4) + t0 + n;
#pragma unroll
    for (int m = 0; m < 4; m += 2) {
      u32x2 o[2];
#pragma unroll
      for (int i = 0; i < 2; ++i) {
        f32x4 a = acc[n][m + i], b = acc[NH_ + n][m + i];
        o[i][0] = pack2(rbf(a[0]) * rbf(b[0]), rbf(a[1]) * rbf(b[1]));
        o[i][1] = pack2(rbf(a[2]) * rbf(b[2]), rbf(a[3]) * rbf(b[3]));
      }
      const int row = m0 + wm * 64 + (m + (g & 1)) * 16 + (lane & 15);
      st16(out + ((size_t)cb * SEQ + row) * 16 + (g >> 1) * 8, pair16(o[0], o[1]));
    }
  }
  STAMP_AT(4, 3);
}

__global__ __launch_bounds__(512, 2) void k_glu(const u16* __restrict__ Ablk, const u16* __restrict__ Wg,
                                                 const u16* __restrict__ Wu, u16* __restrict__ out) {
  extern __shared__ __attribute__((aligned(16))) char smem_[];
  lds_ptr smem = (lds_ptr)smem_;
  const int lane = threadIdx.x & 63;
  const int w = __builtin_amdgcn_readfirstlane(threadIdx.x >> 6);
  const int xcd = blockIdx.x & 7, idx = blockIdx.x >> 3;
  const int tm = idx & 7, tn = xcd * 4 + (idx >> 3);
  const int m0 = tm * 256, f0 = tn * 112;
  Loader<CG> ld;
  const int lw = w & 3;
  setup_loader_A<CG, 256, 4, 1>(ld, Ablk, m0, lw, lane);
#pragma unroll
  for (int i = 0; i < CG::WPW; ++i) {
    int b = lw + 4 * i;
    int grp = b >> 2, o = b & 3;
    ld.woff[i] = CG::A_SUB + grp * 4096 + o * 1024;
    int k = 8 * o + (lane >> 3);
    int f = ((lane >> 4) & 1) | ((o & 1) << 1);
    int pr = ((lane & 7) >> 1) ^ f;
    if ((grp & 1) && pr == 3) pr = 0;
    int col = f0 + (grp & 1) * 64 + pr * 16 + (lane & 1) * 8;
    ld.wv[i] = (k * FF + col) * 2;
    ld.wbase[i] = (const char*)(grp >> 1 ? Wu : Wg);
  }
  if (w >= 4) __builtin_amdgcn_s_setprio(1);
  const int wm = w & 3;
  if (w < 4) glu_wave<4>(smem, ld, lane, wm, 0, m0, f0, out);
  else glu_wave<3>(smem, ld, lane, wm, 4, m0, f0, out);
}

constexpr int ACH = 128, ANST = 4, ASTAGE = ACH * 64 * 2 + 256;
constexpr int ALDS = ANST * ASTAGE + 1024 + 16;

DI float rowmax16(const f32x16& v) {
  float mx = fmaxf(fmaxf(v[0], v[1]), v[2]);
#pragma unroll
  for (int r = 3; r < 15; r += 2) mx = fmaxf(fmaxf(mx, v[r]), v[r + 1]);
  mx = fmaxf(mx, v[15]);
  auto sw = __builtin_amdgcn_permlane32_swap(__float_as_uint(mx), __float_as_uint(mx), false, false);
  return fmaxf(__uint_as_float(sw[0]), __uint_as_float(sw[1]));
}

template <bool SAFE>
DI void attn_pass(lds_ptr smem, int lane, int w, const char* Kp, const char* Vp, const char* pf_wm, const char* pf_wo, const char* pf_wg,
                  const char* pf_wu, const char* pf_wd, const bf16x8& qa0, const bf16x8& qa1, const bf16x8& qb0,
                  const bf16x8& qb1, f32x16& oa, f32x16& ob, float& refa, float& refb, float& la_out, float& lb_out,
                  ModTask& mt, const u16* c) {
  const int r32 = lane & 31, hh = lane >> 5, kh = w >> 2;
  const unsigned kvo = ((32 * (w & 3) + (lane >> 1)) * 16 + (((lane & 1) ^ ((lane >> 4) & 1)) * 8)) * 2;
  const unsigned vvo = ((32 * (w & 3)) * 16) * 2 + lane * 16;
  const char* ksrc = Kp;
  const char* vsrc = Vp;
  auto issue = [&](int buf) {
    if (w < 4) {
      const unsigned l0 = lds_u32(smem) + buf * ASTAGE + w * 1024;
      glds16(ksrc, kvo, l0);
      glds16(ksrc + SEQ * 32, kvo, l0 + 4096);
      glds16(vsrc, vvo, l0 + 8192);
      glds16(vsrc + SEQ * 32, vvo, l0 + 8192 + 4096 + 128);
    }
    ksrc += ACH * 32; vsrc += ACH * 32;
  };
  const unsigned pfo = lane * 16;
  auto prefetch = [&](int ch) {
    if (w >= 4) {
#pragma unroll
      for (int i = 0; i < 3; ++i) {
        int pc = (ch * 3 + i) * 4 + (w - 4);
        const char* base;
        if (pc < 32) base = pf_wm + (size_t)(blockIdx.x * 4 + (pc >> 3)) * (12 * DM) + 4 * DM + (pc & 7) * 1024;
        else if (pc < 40) base = pf_wo + (size_t)blockIdx.x * 8192 + (pc - 32) * 1024;
        else if (pc < 68) base = pf_wd + (size_t)blockIdx.x * 28672 + (pc - 40) * 1024;
        else if (pc < 96) base = pf_wg + (size_t)blockIdx.x * 28672 + (pc - 68) * 1024;
        else if (pc < 124) base = pf_wu + (size_t)blockIdx.x * 28672 + (pc - 96) * 1024;
        else break;
        glds16(base, pfo, lds_u32(smem) + ANST * ASTAGE);
      }
    }
  };
  issue(0); issue(1); issue(2);
  const int kaddr0 = kh * 2048 + r32 * 32 + ((hh ^ ((r32 >> 3) & 1)) * 16);
  const int kaddr1 = kaddr0 + 4096;
  const int vaddr = 8192 + kh * 2048 + ((lane >> 4) & 1) * (4096 + 128) + (4 * hh + ((lane & 15) >> 2)) * 32 + (lane & 3) * 8;
  bf16x8 sel;
  {
    const short one = ((lane & 15) == ((lane >> 4) & 1)) ? (short)0x3F80 : (short)0;
#pragma unroll
    for (int j = 0; j < 8; ++j) sel[j] = one;
  }
  f32x16 nma, nmb, sa, sb;
#pragma unroll
  for (int r = 0; r < 16; ++r) { oa[r] = 0.f; ob[r] = 0.f; nma[r] = 0.f; nmb[r] = 0.f; }
  f32x4 lsa = {0.f, 0.f, 0.f, 0.f}, lsb = {0.f, 0.f, 0.f, 0.f};
  float la = 0.f, lb = 0.f;
  constexpr int NCH = SEQ / ACH;
  if (w < 4) wait_vm<8>();
  bar();
  const bf16x8 kf0_ = lds_read128(smem + kaddr0);
  const bf16x8 kf1_ = lds_read128(smem + kaddr1);
  {
    sa = MFMA32(kf0_, qa0, nma); sa = MFMA32(kf1_, qa1, sa);
    sb = MFMA32(kf0_, qb0, nmb); sb = MFMA32(kf1_, qb1, sb);
    const float ma = rowmax16(sa), mb = rowmax16(sb);
#pragma unroll
    for (int r = 0; r < 16; ++r) { nma[r] = -ma; sa[r] -= ma; nmb[r] = -mb; sb[r] -= mb; }
  }
  if constexpr (SAFE) {
    auto half = [&](f32x16& s, f32x16& o, f32x16& nm, float& l, const bf16x8& q0f, const bf16x8& q1f,
                    const bf16x8& kn0, const bf16x8& kn1, const bf16x8& vf0, const bf16x8& vf1, bool has_next) {
      f32x16 p;
      const float dl = fmaxf(rowmax16(s), 0.f);
      const float f = __builtin_amdgcn_exp2f(-dl);
      float t = 0.f;
#pragma unroll
      for (int r = 0; r < 16; ++r) { nm[r] -= dl; o[r] *= f; p[r] = __builtin_amdgcn_exp2f(s[r] - dl); t += p[r]; }
      l = l * f + t;
      if (has_next) { s = MFMA32(kn0, q0f, nm); s = MFMA32(kn1, q1f, s); }
      u32x4 pa, pb;
#pragma unroll
      for (int j = 0; j < 4; ++j) { pa[j] = pack2(p[2 * j], p[2 * j + 1]); pb[j] = pack2(p[8 + 2 * j], p[8 + 2 * j + 1]); }
      o = MFMA32(vf0, __builtin_bit_cast(bf16x8, pa), o);
      o = MFMA32(vf1, __builtin_bit_cast(bf16x8, pb), o);
    };
    auto step = [&](lds_cptr vb, lds_cptr knext, bool has_next) {
      bf16x8 kn0, kn1;
      if (has_next) { kn0 = lds_read128(knext + kaddr0); kn1 = lds_read128(knext + kaddr1); }
      bf16x8 vf0 = lds_tr8(vb, vb + 256);
      bf16x8 vf1 = lds_tr8(vb + 512, vb + 768);
      half(sa, oa, nma, la, qa0, qa1, kn0, kn1, vf0, vf1, has_next);
      half(sb, ob, nmb, lb, qb0, qb1, kn0, kn1, vf0, vf1, has_next);
    };
    for (int ch = 0; ch < NCH; ++ch) {
      if (w < 4) { if (ch + 2 < NCH) wait_vm<4>(); else wait_vm<0>(); }
      bar();
      if (ch + 3 < NCH) issue((ch + 3) & 3);
      lds_cptr sbuf = smem + (ch & 3) * ASTAGE;
      lds_cptr nbuf = smem + ((ch + 1) & 3) * ASTAGE;
      step(sbuf + vaddr, sbuf + 1024, true);
      step(sbuf + 1024 + vaddr, nbuf, ch + 1 < NCH);
    }
  } else {
    u32x4 pa0, pa1, pb0, pb1;
    bf16x8 kc0 = kf0_, kc1 = kf1_;
    bf16x8 vp0, vp1;
#pragma unroll
    for (int j = 0; j < 4; ++j) { pb0[j] = 0u; pb1[j] = 0u; }
#pragma unroll
    for (int j = 0; j < 8; ++j) { vp0[j] = 0; vp1[j] = 0; }
#define SB_ __builtin_amdgcn_sched_barrier(0)
#define EX(i) e[i] = __builtin_amdgcn_exp2f(S[i])
#define PHASE(S, P0, P1, M0, M1, M2, M3, M4, M5, R0, R1) do { float e[16]; \
      M0; SB_; R0; EX(0); EX(1); EX(2); EX(3); SB_; \
      M1; SB_; R1; EX(4); EX(5); EX(6); EX(7); SB_; \
      M2; SB_; EX(8); EX(9); P0[0] = pack2(e[0], e[1]); P0[1] = pack2(e[2], e[3]); SB_; \
      M3; SB_; EX(10); EX(11); P0[2] = pack2(e[4], e[5]); P0[3] = pack2(e[6], e[7]); SB_; \
      M4; SB_; EX(12); EX(13); EX(14); EX(15); SB_; \
      M5; SB_; P1[0] = pack2(e[8], e[9]); P1[1] = pack2(e[10], e[11]); P1[2] = pack2(e[12], e[13]); P1[3] = pack2(e[14], e[15]); SB_; \
    } while (0)
#define BF(x) __builtin_bit_cast(bf16x8, x)
    auto step = [&](lds_cptr vb, lds_cptr knext, auto has_next_t) {
      constexpr bool HN = decltype(has_next_t)::value;
      bf16x8 kn0, kn1, vc0, vc1;
      { const f32x16& S = sa;
        PHASE(S, pa0, pa1,
              sb = MFMA32(kc0, qb0, nmb), sb = MFMA32(kc1, qb1, sb),
              ob = MFMA32(vp0, BF(pb0), ob), ob = MFMA32(vp1, BF(pb1), ob),
              lsb = MFMA16(sel, BF(pb0), lsb), lsb = MFMA16(sel, BF(pb1), lsb),
              if (HN) { kn0 = lds_read128(knext + kaddr0); kn1 = lds_read128(knext + kaddr1); },
              { vc0 = lds_tr8(vb, vb + 256); vc1 = lds_tr8(vb + 512, vb + 768); }); }
      { const f32x16& S = sb;
        PHASE(S, pb0, pb1,
              if (HN) sa = MFMA32(kn0, qa0, nma), if (HN) sa = MFMA32(kn1, qa1, sa),
              oa = MFMA32(vc0, BF(pa0), oa), oa = MFMA32(vc1, BF(pa1), oa),
              lsa = MFMA16(sel, BF(pa0), lsa), lsa = MFMA16(sel, BF(pa1), lsa),
              , ); }
      if (HN) { kc0 = kn0; kc1 = kn1; }
      vp0 = vc0; vp1 = vc1;
    };
    typedef std::integral_constant<bool, true> T_;
    typedef std::integral_constant<bool, false> F_;
    for (int ch = 0; ch < NCH - 1; ++ch) {
      if (w < 4) { if (ch + 2 < NCH) wait_vm<4>(); else wait_vm<0>(); }
      bar();
      if (ch + 3 < NCH) issue((ch + 3) & 3);
      prefetch(ch);
      lds_cptr sbuf = smem + (ch & 3) * ASTAGE;
      lds_cptr nbuf = smem + ((ch + 1) & 3) * ASTAGE;
      step(sbuf + vaddr, sbuf + 1024, T_{});
      step(sbuf + 1024 + vaddr, nbuf, T_{});
    }
    {
      const int task = blockIdx.x * 2 + (threadIdx.x >> 8);
      mod_load(mt, c, (const u16*)pf_wm, 32 + (task >> 3), task & 7, threadIdx.x & 255);
      lds_cptr sbuf = smem + ((NCH - 1) & 3) * ASTAGE;
      step(sbuf + vaddr, sbuf + 1024, T_{});
      step(sbuf + 1024 + vaddr, sbuf, F_{});
    }
    ob = MFMA32(vp0, BF(pb0), ob); ob = MFMA32(vp1, BF(pb1), ob);
    lsb = MFMA16(sel, BF(pb0), lsb); lsb = MFMA16(sel, BF(pb1), lsb);
#undef PHASE
#undef EX
#undef BF
  }
  refa = nma[0]; refb = nmb[0];
  if constexpr (SAFE) {
    auto s1 = __builtin_amdgcn_permlane32_swap(__float_as_uint(la), __float_as_uint(la), false, false);
    la_out = __uint_as_float(s1[0]) + __uint_as_float(s1[1]);
    auto s2 = __builtin_amdgcn_permlane32_swap(__float_as_uint(lb), __float_as_uint(lb), false, false);
    lb_out = __uint_as_float(s2[0]) + __uint_as_float(s2[1]);
  } else {
    const float a0 = __shfl(lsa[0], lane & 15), a1 = __shfl(lsa[1], lane & 15);
    const float b0 = __shfl(lsb[0], lane & 15), b1 = __shfl(lsb[1], lane & 15);
    la_out = (lane & 16) ? a1 : a0;
    lb_out = (lane & 16) ? b1 : b0;
  }
}

__global__ __launch_bounds__(512, 2) void k_attn(const u16* __restrict__ qkv, u16* __restrict__ oblk,
                                                  const u16* __restrict__ c, const u16* __restrict__ wmod, float* __restrict__ part,
                                                  const char* __restrict__ pf_wo, const char* __restrict__ pf_wg,
                                                  const char* __restrict__ pf_wu, const char* __restrict__ pf_wd) {
  extern __shared__ __attribute__((aligned(16))) char smem_[];
  lds_ptr smem = (lds_ptr)smem_;
  const int lane = threadIdx.x & 63;
  const int w = __builtin_amdgcn_readfirstlane(threadIdx.x >> 6);
  const int xcd = blockIdx.x & 7, idx = blockIdx.x >> 3;
  const int head = xcd * 4 + (idx >> 3), qb = idx & 7;
  const int r32 = lane & 31, hh = lane >> 5;
  const int qg = w & 3, kh = w >> 2;
  const u16* Qp = qkv + (size_t)head * SEQ * 32;
  const char* Kp = (const char*)(qkv + (size_t)(32 + head) * SEQ * 32);
  const char* Vp = (const char*)(qkv + (size_t)(64 + head) * SEQ * 32);
  const int q0 = qb * 256 + qg * 64;
  STAMP_AT(2, 0);
  __attribute__((address_space(3))) int* flag = (__attribute__((address_space(3))) int*)(smem + ANST * ASTAGE + 1024);
  if (threadIdx.x == 0) *flag = 0;
  bf16x8 qa0 = *(const bf16x8*)(Qp + (q0 + r32) * 16 + hh * 8);
  bf16x8 qa1 = *(const bf16x8*)(Qp + (SEQ + q0 + r32) * 16 + hh * 8);
  bf16x8 qb0 = *(const bf16x8*)(Qp + (q0 + 32 + r32) * 16 + hh * 8);
  bf16x8 qb1 = *(const bf16x8*)(Qp + (SEQ + q0 + 32 + r32) * 16 + hh * 8);
  f32x16 oa, ob;
  float nma, nmb, la, lb;
  ModTask mt;
  attn_pass<false>(smem, lane, w, Kp, Vp, (const char*)wmod, pf_wo, pf_wg, pf_wu, pf_wd, qa0, qa1, qb0, qb1, oa, ob, nma, nmb, la, lb, mt, c);
  {
    float big = fmaxf(fabsf(la), fabsf(lb));
#pragma unroll
    for (int r = 0; r < 16; ++r) big = fmaxf(big, fmaxf(fabsf(oa[r]), fabsf(ob[r])));
    bool bad = !(big < 1e30f);
#pragma unroll
    for (int r = 0; r < 16; ++r) bad = bad || (oa[r] != oa[r]) || (ob[r] != ob[r]);
    bad = bad || (la != la) || (lb != lb);
    if (__any(bad) && lane == 0) *flag = 1;
  }
  __syncthreads();
  if (__builtin_amdgcn_readfirstlane(*flag) != 0) {
    attn_pass<true>(smem, lane, w, Kp, Vp, (const char*)wmod, pf_wo, pf_wg, pf_wu, pf_wd, qa0, qa1, qb0, qb1, oa, ob, nma, nmb, la, lb, mt, c);
    __syncthreads();
  }
  STAMP_AT(2, 2);
  const int task = blockIdx.x * 2 + (threadIdx.x >> 8);
  {
    __attribute__((address_space(3))) float* xb = (__attribute__((address_space(3))) float*)smem;
    const f32x16& og = kh == 0 ? ob : oa;
    __attribute__((address_space(3))) float* dst = xb + w * (18 * 64) + lane;
#pragma unroll
    for (int r = 0; r < 16; ++r) dst[r * 64] = og[r];
    dst[16 * 64] = kh == 0 ? lb : la;
    dst[17 * 64] = kh == 0 ? nmb : nma;
    __syncthreads();
    const __attribute__((address_space(3))) float* src = xb + (w ^ 4) * (18 * 64) + lane;
    f32x16 om = kh == 0 ? oa : ob;
    const float nm_me = kh == 0 ? nma : nmb;
    const float nm_ot = src[17 * 64];
    const float nmin = fminf(nm_me, nm_ot);
    const float f_me = __builtin_amdgcn_exp2f(nmin - nm_me), f_ot = __builtin_amdgcn_exp2f(nmin - nm_ot);
    const float rl = 1.f / ((kh == 0 ? la : lb) * f_me + src[16 * 64] * f_ot);
    const float g_me = f_me * rl, g_ot = f_ot * rl;
    u16* op = oblk + ((size_t)head * 2 * SEQ + q0 + kh * 32 + r32) * 16 + 4 * hh;
#pragma unroll
    for (int g = 0; g < 4; ++g) {
      float v0 = om[4 * g] * g_me + src[(4 * g) * 64] * g_ot;
      float v1 = om[4 * g + 1] * g_me + src[(4 * g + 1) * 64] * g_ot;
      float v2 = om[4 * g + 2] * g_me + src[(4 * g + 2) * 64] * g_ot;
      float v3 = om[4 * g + 3] * g_me + src[(4 * g + 3) * 64] * g_ot;
      u32x2 v;
      v[0] = pack2(v0, v1);
      v[1] = pack2(v2, v3);
      *(u32x2*)(op + (g >> 1) * (SEQ * 16) + (g & 1) * 8) = v;
    }
  }
  mod_finish(mt, part, 32 + (task >> 3), task & 7, threadIdx.x & 255, (float*)(smem_ + 40960) + (threadIdx.x >> 8) * 256);
  wait_vm<0>();
  STAMP_AT(2, 3);
}

extern "C" void kernel_launch(void* const* d_in, const int* in_sizes, int n_in, void* d_out, int out_size,
                              void* d_ws, size_t ws_size, hipStream_t stream) {
  const u16* x = (const u16*)d_in[0];
  const u16* wqkv = (const u16*)d_in[1];
  const u16* wo = (const u16*)d_in[2];
  const u16* wg = (const u16*)d_in[4];
  const u16* wu = (const u16*)d_in[5];
  const u16* wd = (const u16*)d_in[6];
  const u16* c = (const u16*)d_in[7];
  const u16* wmod = (const u16*)d_in[8];
  u16* out = (u16*)d_out;
  char* ws = (char*)d_ws;
  float* part = (float*)ws;
  float* modf = (float*)(ws + (256 << 10));
  unsigned* flags = (unsigned*)(ws + (512 << 10));
  u16* h1 = (u16*)(ws + (1 << 20));
  u16* qkv = (u16*)(ws + (8 << 20));
  u16* ob = (u16*)(ws + (24 << 20));
  u16* x2 = (u16*)(ws + (32 << 20));
  u16* h2 = (u16*)(ws + (40 << 20));
  u16* hh = (u16*)(ws + (48 << 20));
  u16* wdp = (u16*)(ws + (64 << 20));

  hipFuncSetAttribute((const void*)k_qkv, hipFuncAttributeMaxDynamicSharedMemorySize, CQ::LDS);
  hipFuncSetAttribute((const void*)k_proj<DM, DM>, hipFuncAttributeMaxDynamicSharedMemorySize, CP::LDS);
  hipFuncSetAttribute((const void*)k_proj<FF, WDP>, hipFuncAttributeMaxDynamicSharedMemorySize, CP::LDS);
  hipFuncSetAttribute((const void*)k_glu, hipFuncAttributeMaxDynamicSharedMemorySize, CG::LDS);
  hipFuncSetAttribute((const void*)k_attn, hipFuncAttributeMaxDynamicSharedMemorySize, ALDS);

  k_pre<<<256 + 256, 256, 0, stream>>>(c, wmod, part, (const char*)x, (const char*)wqkv, flags);
  k_ln<6, MNS, 192><<<256 + 192, 512, 0, stream>>>(x, part, h1, (const char*)wqkv, wd, wdp);
  k_qkv<<<256, 768, CQ::LDS, stream>>>(h1, wqkv, qkv);
  k_attn<<<256, 512, ALDS, stream>>>(qkv, ob, c, wmod, part, (const char*)wo, (const char*)wg, (const char*)wu, (const char*)wd);
  k_proj<DM, DM><<<256, 768, CP::LDS, stream>>>(ob, wo, x, part + 2 * DM, x2);
  k_ln<7, MNS, 0><<<256 + 224, 512, 0, stream>>>(x2, part + 3 * DM, h2, (const char*)wqkv, wd, wdp);
  k_glu<<<256, 512, CG::LDS, stream>>>(h2, wg, wu, hh);
  k_proj<FF, WDP><<<256, 768, CP::LDS, stream>>>(hh, wdp, x2, part + 5 * DM, out);
}
```

```cpp
#include <hip/hip_runtime.h>
#include <hip/hip_bf16.h>
#include <stdint.h>

typedef __attribute__((ext_vector_type(8))) short bf16x8;
typedef __attribute__((ext_vector_type(4))) short s16x4;
typedef __attribute__((ext_vector_type(4))) float f32x4;
typedef __attribute__((ext_vector_type(2))) float f32x2;
typedef __attribute__((ext_vector_type(16))) float f32x16;
typedef __attribute__((ext_vector_type(4))) unsigned u32x4;
typedef __attribute__((ext_vector_type(2))) unsigned u32x2;
typedef __attribute__((ext_vector_type(2))) __bf16 bf16x2_t;
typedef unsigned short u16;
typedef __attribute__((address_space(3))) char* lds_ptr;
typedef __attribute__((address_space(3))) const char* lds_cptr;

#define DI __device__ __forceinline__

constexpr int SEQ = 2048, DM = 1024, FF = 3584, NH = 32;

DI float bf2f(u16 x) { return __uint_as_float(((unsigned)x) << 16); }
DI float bflo(unsigned x) { return __uint_as_float(x << 16); }
DI float bfhi(unsigned x) { return __uint_as_float(x & 0xffff0000u); }
DI unsigned pack2(float a, float b) {
  f32x2 v = {a, b};
  bf16x2_t r = __builtin_convertvector(v, bf16x2_t);
  return __builtin_bit_cast(unsigned, r);
}
DI float rbf(float a) { return bflo(pack2(a, 0.f) & 0xffffu); }

#define MFMA16(a, b, c) __builtin_amdgcn_mfma_f32_16x16x32_bf16((a), (b), (c), 0, 0, 0)
#define MFMA32(a, b, c) __builtin_amdgcn_mfma_f32_32x32x16_bf16((a), (b), (c), 0, 0, 0)

DI void glds16(const void* base, unsigned voff, unsigned lds_addr) {
  unsigned sv;
  asm volatile("s_mov_b32 %0, m0\n\ts_mov_b32 m0, %3\n\ts_nop 0\n\tglobal_load_lds_dwordx4 %1, %2\n\ts_mov_b32 m0, %0"
               : "=&s"(sv) : "v"(voff), "s"(base), "s"(lds_addr) : "memory");
}
DI unsigned lds_u32(lds_ptr p) { return (unsigned)(uintptr_t)p; }
DI bf16x8 lds_read128(lds_cptr p) { return *(const __attribute__((address_space(3))) bf16x8*)p; }
DI s16x4 lds_tr(lds_cptr p) {
  typedef short v4i16_t __attribute__((ext_vector_type(4)));
  return __builtin_bit_cast(s16x4, __builtin_amdgcn_ds_read_tr16_b64_v4i16((__attribute__((address_space(3))) v4i16_t*)p));
}
DI bf16x8 lds_tr8(lds_cptr p0, lds_cptr p1) {
  s16x4 a = lds_tr(p0), b = lds_tr(p1);
  return __builtin_shufflevector(a, b, 0, 1, 2, 3, 4, 5, 6, 7);
}
#define STAMP_AT(kid, slot) do {} while (0)
DI void st16(void* p, u32x4 v) { asm volatile("global_store_dwordx4 %0, %1, off sc1\n\ts_nop 1" ::"v"(p), "v"(v) : "memory"); }
template <int N> DI void wait_vm() { asm volatile("s_waitcnt vmcnt(%0)" ::"n"(N) : "memory"); }
DI void bar() { __builtin_amdgcn_s_barrier(); }

constexpr int MNS = 8;
struct ModTask { u32x4 w[4]; u16 cv[4]; };
DI void mod_load(ModTask& mt, const u16* __restrict__ c, const u16* __restrict__ wmod, int cb, int ks, int t) {
  const int cc = t & 7, rg = t >> 3;
  const int k0 = ks * 128 + rg;
  const u16* wp = wmod + (size_t)k0 * (6 * DM) + cb * 64 + cc * 8;
#pragma unroll
  for (int i = 0; i < 4; ++i) mt.w[i] = *(const u32x4*)(wp + (size_t)i * 32 * (6 * DM));
#pragma unroll
  for (int i = 0; i < 4; ++i) mt.cv[i] = c[k0 + 32 * i];
}
DI void mod_finish(const ModTask& mt, float* __restrict__ part, int cb, int ks, int t, float* s_red  ) {
  float acc[8];
#pragma unroll
  for (int j = 0; j < 8; ++j) acc[j] = 0.f;
#pragma unroll
  for (int i = 0; i < 4; ++i) {
    const float cv = bf2f(mt.cv[i]);
    const float sv = rbf(cv / (1.f + __expf(-cv)));
#pragma unroll
    for (int j = 0; j < 4; ++j) {
      acc[2 * j] += sv * bflo(mt.w[i][j]);
      acc[2 * j + 1] += sv * bfhi(mt.w[i][j]);
    }
  }
#pragma unroll
  for (int j = 0; j < 8; ++j) {
    float v = acc[j];
    v += __shfl_xor(v, 8);
    v += __shfl_xor(v, 16);
    v += __shfl_xor(v, 32);
    acc[j] = v;
  }
  const int lane = t & 63, wv = t >> 6;
  if (lane < 8) {
#pragma unroll
    for (int j = 0; j < 8; ++j) s_red[wv * 64 + lane * 8 + j] = acc[j];
  }
  __syncthreads();
  if (t < 64) part[ks * (6 * DM) + cb * 64 + t] = (s_red[t] + s_red[64 + t]) + (s_red[128 + t] + s_red[192 + t]);
}
DI f32x4 gate4(const float* __restrict__ gate, int col) {
  f32x4 gt = *(const f32x4*)(gate + col);
#pragma unroll
  for (int i = 1; i < MNS; ++i) gt += *(const f32x4*)(gate + i * (6 * DM) + col);
#pragma unroll
  for (int j = 0; j < 4; ++j) gt[j] = rbf(gt[j]);
  return gt;
}

DI void touch16k(const char* p, int t) {
  u32x4 v[4];
#pragma unroll
  for (int i = 0; i < 4; ++i) v[i] = *(const u32x4*)(p + i * 4096 + t * 16);
#pragma unroll
  for (int i = 0; i < 4; ++i) asm volatile("" ::"v"(v[i]));
}

__global__ __launch_bounds__(256) void k_pre(const u16* __restrict__ c, const u16* __restrict__ wmod, float* __restrict__ part,
                                              const char* __restrict__ x) {
  __shared__ float s_red[256];
  const int t = threadIdx.x, b = blockIdx.x;
  if (b < 256) STAMP_AT(0, 0);
  if (b < 256) {
    ModTask mt;
    mod_load(mt, c, wmod, b >> 3, b & 7, t);
    mod_finish(mt, part, b >> 3, b & 7, t, s_red);
  } else {
    touch16k(x + (size_t)(b - 256) * 16384, t);
  }
  if (b < 256) STAMP_AT(0, 3);
}

constexpr int WDP = DM + 64;
template <int KID, int NS, int NPF>
__global__ __launch_bounds__(512) void k_ln(const u16* __restrict__ x, const float* __restrict__ ss  ,
                                             u16* __restrict__ h16, const char* __restrict__ pf,
                                             const u16* __restrict__ wsrc, u16* __restrict__ wdst) {
  __shared__ __attribute__((aligned(16))) float sm[2048];
  const int t = threadIdx.x;
  if (blockIdx.x >= 256 + NPF) {
    const int r0 = (blockIdx.x - 256 - NPF) * 16 + (t >> 7);
    u32x4 v[4];
#pragma unroll
    for (int i = 0; i < 4; ++i) v[i] = *(const u32x4*)(wsrc + (size_t)(r0 + 4 * i) * DM + (t & 127) * 8);
#pragma unroll
    for (int i = 0; i < 4; ++i) st16(wdst + (size_t)(r0 + 4 * i) * WDP + (t & 127) * 8, v[i]);
    return;
  }
  if (blockIdx.x >= 256) {
    touch16k(pf + (size_t)((blockIdx.x - 256) * 2 + (t >> 8)) * 16384, t & 255);
    return;
  }
  const int lane = t & 63, wv = t >> 6;
  const int row = blockIdx.x * 8 + wv;
  STAMP_AT(KID, 0);
  const u16* xr = x + (size_t)row * DM;
  u32x4 v0 = *(const u32x4*)(xr + lane * 8);
  u32x4 v1 = *(const u32x4*)(xr + 512 + lane * 8);
  {
    f32x4 pv[NS];
#pragma unroll
    for (int i = 0; i < NS; ++i) pv[i] = *(const f32x4*)(ss + i * (6 * DM) + t * 4);
    f32x4 a = pv[0];
#pragma unroll
    for (int i = 1; i < NS; ++i) a += pv[i];
#pragma unroll
    for (int j = 0; j < 4; ++j) {
      float v = rbf(a[j]);
      a[j] = t < 256 ? v : rbf(1.f + v);
    }
    *(f32x4*)(sm + t * 4) = a;
  }
  float f[16];
#pragma unroll
  for (int j = 0; j < 4; ++j) {
    f[2 * j] = bflo(v0[j]); f[2 * j + 1] = bfhi(v0[j]);
    f[8 + 2 * j] = bflo(v1[j]); f[8 + 2 * j + 1] = bfhi(v1[j]);
  }
  float s = 0.f;
#pragma unroll
  for (int j = 0; j < 16; ++j) s += f[j];
#pragma unroll
  for (int o = 1; o < 64; o <<= 1) s += __shfl_xor(s, o);
  const float mean = s * (1.f / DM);
  float q = 0.f;
#pragma unroll
  for (int j = 0; j < 16; ++j) { float d = f[j] - mean; q += d * d; }
#pragma unroll
  for (int o = 1; o < 64; o <<= 1) q += __shfl_xor(q, o);
  const float mb = rbf(mean);
  const float vb = rbf(q * (1.f / DM));
  const float rden = 1.f / rbf(sqrtf(rbf(vb + 1e-5f)));
  __syncthreads();
#pragma unroll
  for (int half = 0; half < 2; ++half) {
    const int k0 = half * 512 + lane * 8;
    f32x4 sh0 = *(const f32x4*)(sm + k0), sh1 = *(const f32x4*)(sm + k0 + 4);
    f32x4 sc0 = *(const f32x4*)(sm + 1024 + k0), sc1 = *(const f32x4*)(sm + 1024 + k0 + 4);
    float y[8];
#pragma unroll
    for (int j = 0; j < 8; ++j) {
      float g = j < 4 ? sc0[j] : sc1[j - 4];
      float sh = j < 4 ? sh0[j] : sh1[j - 4];
      float d = rbf(f[half * 8 + j] - mb);
      float n = rbf(d * rden);
      y[j] = rbf(n * g) + sh;
    }
    u32x4 o;
#pragma unroll
    for (int j = 0; j < 4; ++j) o[j] = pack2(y[2 * j], y[2 * j + 1]);
    *(u32x4*)(h16 + ((size_t)(k0 >> 4) * SEQ + row) * 16 + (k0 & 15)) = o;
  }
  STAMP_AT(KID, 3);
}

DI u32x4 pair16(u32x2 a, u32x2 b) {
  auto s0 = __builtin_amdgcn_permlane16_swap(a[0], b[0], false, false);
  auto s1 = __builtin_amdgcn_permlane16_swap(a[1], b[1], false, false);
  return u32x4{s0[0], s1[0], s0[1], s1[1]};
}

template <int BM, int NGRP, int KS, int NST, int NLW_ = 8>
struct GCfg {
  static constexpr int NLW = NLW_;
  static constexpr int A_SUB = BM * 64;
  static constexpr int W_SUB = NGRP * 4096;
  static constexpr int STAGE = KS * (A_SUB + W_SUB);
  static constexpr int APW = KS * BM / 16 / NLW;
  static constexpr int WPW = KS * NGRP * 4 / NLW;
  static constexpr int PPW = APW + WPW;
  static constexpr int LDS = STAGE * NST;
};

template <class C>
struct Loader {
  const char* abase;
  const char* wbase[C::WPW];
  unsigned av[C::APW];
  unsigned wv[C::WPW];
  int aoff[C::APW];
  int woff[C::WPW];
};

template <int BM, int NGRP, int KS, int NST, int MT, int NT, int KSPLIT, int NKT, int KID, int IOFS, int NLW = 8, bool LOADS = (NLW == 8)>
struct Engine {
  typedef GCfg<BM, NGRP, KS, NST, NLW> C;
  static constexpr bool DED = !LOADS;
  static constexpr int SPW = KS / KSPLIT;
  static constexpr int P = C::PPW;
  static constexpr int G = NT * SPW;
  f32x4 (&acc)[NT][MT];
  lds_ptr smem;
  Loader<C>& ld;
  long a_adv, w_adv;
  const int (&waddr)[NT];
  int a_sub0, w_sub0;
  int cur, nxt, fre;
  bf16x8 wf[NT], af[2][MT];

  DI Engine(f32x4 (&acc_)[NT][MT], lds_ptr smem_, Loader<C>& ld_, long a_adv_, long w_adv_, int aaddr, const int (&waddr_)[NT], int kg)
      : acc(acc_), smem(smem_), ld(ld_), a_adv(a_adv_), w_adv(w_adv_), waddr(waddr_) {
    a_sub0 = kg * C::A_SUB + aaddr;
    w_sub0 = KS * C::A_SUB + kg * C::W_SUB;
  }
  template <int I> DI void piece(int stage_off) {
    if constexpr (I < C::APW) {
      glds16(ld.abase, ld.av[I], lds_u32(smem) + stage_off + ld.aoff[I]);
      if constexpr (I == C::APW - 1) ld.abase += a_adv * 2;
    } else {
      constexpr int J = I - C::APW;
      glds16(ld.wbase[J], ld.wv[J], lds_u32(smem) + stage_off + ld.woff[J]);
      ld.wbase[J] += w_adv * 2;
    }
  }
  template <int I> DI void issue_all(int stage_off) {
    if constexpr (I < P) { piece<I>(stage_off); issue_all<I + 1>(stage_off); }
  }
  template <int g, int I> DI void issue_at(int stage_off) {
    if constexpr (I < P) {
      constexpr int span = (G - IOFS) > P ? (G - IOFS) : P;
      constexpr int pos = IOFS + (I * span) / P < G ? IOFS + (I * span) / P : G - 1;
      if constexpr (pos == g) piece<I>(stage_off);
      issue_at<g, I + 1>(stage_off);
    }
  }
  template <int PAR, int S, bool PF, bool DOISSUE, int n>
  DI void groups(lds_cptr ab, lds_cptr wb) {
    if constexpr (n < NT) {
#pragma unroll
      for (int m = 0; m < MT; ++m) acc[n][m] = MFMA16(wf[n], af[PAR][m], acc[n][m]);
      if constexpr (PF) {
        wf[n] = lds_tr8(wb + waddr[n], wb + waddr[n] + 512);
#pragma unroll
        for (int m = 0; m < MT; ++m)
          if ((m * NT) / MT == n) af[PAR ^ 1][m] = lds_read128(ab + m * 512);
      }
      if constexpr (DOISSUE && !DED) issue_at<S * NT + n, 0>(fre);
      __builtin_amdgcn_sched_barrier(0);
      groups<PAR, S, PF, DOISSUE, n + 1>(ab, wb);
    }
  }
  template <int PAR, bool DOISSUE, bool LAST, int S> DI void steps() {
    if constexpr (S < SPW) {
      if constexpr (S + 1 < SPW)
        groups<PAR, S, true, DOISSUE, 0>(smem + cur + a_sub0 + (S + 1) * KSPLIT * C::A_SUB, smem + cur + w_sub0 + (S + 1) * KSPLIT * C::W_SUB);
      else if constexpr (!LAST)
        groups<PAR, S, true, DOISSUE, 0>(smem + nxt + a_sub0, smem + nxt + w_sub0);
      else
        groups<PAR, S, false, false, 0>(smem, smem);
      steps<PAR ^ 1, DOISSUE, LAST, S + 1>();
    }
  }
  template <int PAR, bool DOISSUE, bool LAST, int WAITN> DI void iter() {
    if constexpr (!LAST) { if constexpr (!DED) wait_vm<WAITN>(); bar(); }
    steps<PAR, DOISSUE, LAST, 0>();
    fre = cur; cur = nxt; nxt = (nxt + C::STAGE == NST * C::STAGE) ? 0 : nxt + C::STAGE;
  }
  template <int KT> DI void tail() {
    if constexpr (KT < NKT) {
      constexpr bool DOISSUE = KT + NST - 1 < NKT;
      constexpr bool LAST = KT == NKT - 1;
      constexpr int newest = (KT + NST - 2) < (NKT - 1) ? (KT + NST - 2) : (NKT - 1);
      constexpr int younger = newest - (KT + 1) > 0 ? newest - (KT + 1) : 0;
      iter<(KT * SPW) & 1, DOISSUE, LAST, P * younger>();
      tail<KT + 1>();
    }
  }
  DI void start() {
    static_assert(NKT >= NST + 1, "too few k tiles");
    STAMP_AT(KID, 0);
    if constexpr (!DED) {
#pragma unroll
      for (int s = 0; s < NST - 1; ++s) issue_all<0>(s * C::STAGE);
    }
  }
  template <int KT, class Hook> DI void ltail(Hook& hook) {
    if constexpr (KT < NKT - 1) {
      constexpr int newest = (KT + NST - 2) < (NKT - 1) ? (KT + NST - 2) : (NKT - 1);
      constexpr int younger = newest - (KT + 1) > 0 ? newest - (KT + 1) : 0;
      wait_vm<P * younger>();
      if constexpr (KT == NKT - 2) { hook(1); asm volatile("s_waitcnt lgkmcnt(0)" ::: "memory"); }
      bar();
      if constexpr (KT == NKT - NST + 1) hook(0);
      ltail<KT + 1>(hook);
    }
  }
  static constexpr int FREE_OFF = ((NKT - NST) % NST) * C::STAGE;
  DI void loader_run() { loader_run([](int) {}); }
  template <class Hook> DI void loader_run(Hook hook) {
    __builtin_amdgcn_s_setprio(1);
#pragma unroll
    for (int s = 0; s < NST - 1; ++s) issue_all<0>(s * C::STAGE);
    wait_vm<P*(NST - 2)>();
    bar();
    fre = (NST - 1) * C::STAGE;
    for (int kt = 0; kt < NKT - NST + 1; ++kt) {
      wait_vm<P*(NST - 3)>();
      bar();
      issue_all<0>(fre);
      fre = (fre + C::STAGE == NST * C::STAGE) ? 0 : fre + C::STAGE;
    }
    ltail<NKT - NST + 1>(hook);
  }
  DI void run() { run([] {}); }
  template <class Hook> DI void run(Hook hook) {
    if constexpr (!DED) wait_vm<P*(NST - 2)>();
    bar();
    STAMP_AT(KID, 1);
    {
      lds_cptr ab = smem + a_sub0;
      lds_cptr wb = smem + w_sub0;
#pragma unroll
      for (int m = 0; m < MT; ++m) af[0][m] = lds_read128(ab + m * 512);
#pragma unroll
      for (int n = 0; n < NT; ++n) wf[n] = lds_tr8(wb + waddr[n], wb + waddr[n] + 512);
    }
    hook();
    cur = 0; nxt = C::STAGE; fre = (NST - 1) * C::STAGE;
    constexpr int STEADY = NKT - NST + 1;
    constexpr int LOOPN = (STEADY / 2) * 2;
    for (int kt = 0; kt < LOOPN; kt += 2) {
      iter<0, true, false, P*(NST - 3)>();
      iter<SPW & 1, true, false, P*(NST - 3)>();
    }
    tail<LOOPN>();
    STAMP_AT(KID, 2);
  }
};

template <class C, int BM, int NGRP, int KS>
DI void setup_loader_A(Loader<C>& ld, const u16* Ablk, int m0, int w, int lane) {
#pragma unroll
  for (int i = 0; i < C::APW; ++i) {
    int a = w + C::NLW * i;
    int blk = a / (BM / 32), j = a % (BM / 32);
    ld.aoff[i] = blk * (BM * 32) + j * 1024;
    ld.av[i] = ((blk * SEQ + m0 + j * 32) * 16) * 2 + lane * 16;
  }
  ld.abase = (const char*)Ablk;
}

DI int wfrag_addr(int t, int lane) {
  int g = lane >> 4, q = (lane & 15) >> 2, p = lane & 3;
  int f = ((q >> 1) & 1) | ((g & 1) << 1);
  return (t >> 2) * 4096 + (8 * g + q) * 128 + (((t & 3) ^ f) * 32) + p * 8;
}
template <int BM>
DI int afrag_addr(int mrow0, int lane) {
  int r = lane & 15, g = lane >> 4;
  return (g >> 1) * (BM * 32) + (mrow0 + r) * 32 + (g & 1) * 16;
}

constexpr float QSCALE = 0.17677669529663687f * 1.4426950408889634f;
typedef GCfg<128, 3, 2, 4, 4> CQ;
__global__ __launch_bounds__(768, 3) void k_qkv(const u16* __restrict__ Ablk, const u16* __restrict__ W,
                                                 u16* __restrict__ out) {
  extern __shared__ __attribute__((aligned(16))) char smem_[];
  lds_ptr smem = (lds_ptr)smem_;
  const int lane = threadIdx.x & 63;
  const int w = __builtin_amdgcn_readfirstlane(threadIdx.x >> 6);
  const int xcd = blockIdx.x & 7, idx = blockIdx.x >> 3;
  const int tm = (xcd & 1) * 8 + (idx & 7), tn = (xcd >> 1) * 4 + (idx >> 3);
  const int m0 = tm * 128, n0 = tn * 192;
  constexpr int LDW = 3 * DM;
  Loader<CQ> ld;
  const int lw = w & 3;
  setup_loader_A<CQ, 128, 3, 2>(ld, Ablk, m0, lw, lane);
#pragma unroll
  for (int i = 0; i < CQ::WPW; ++i) {
    int b = lw + 4 * i;
    int sub = b / 12, r = b % 12, grp = r >> 2, o = r & 3;
    ld.woff[i] = 2 * CQ::A_SUB + sub * CQ::W_SUB + grp * 4096 + o * 1024;
    int k = sub * 32 + 8 * o + (lane >> 3);
    int f = ((lane >> 4) & 1) | ((o & 1) << 1);
    int col = n0 + grp * 64 + ((((lane & 7) >> 1) ^ f) * 16) + (lane & 1) * 8;
    ld.wv[i] = (k * LDW + col) * 2;
    ld.wbase[i] = (const char*)W;
  }
  const int wm = w & 1, wn = (w >> 1) & 3;
  int waddr[3];
#pragma unroll
  for (int n = 0; n < 3; ++n) waddr[n] = wfrag_addr(wn * 3 + n, lane);
  const int aaddr = afrag_addr<128>(wm * 64, lane);
  f32x4 acc[3][4];
#pragma unroll
  for (int n = 0; n < 3; ++n)
#pragma unroll
    for (int m = 0; m < 4; ++m) acc[n][m] = f32x4{0.f, 0.f, 0.f, 0.f};
  Engine<128, 3, 2, 4, 4, 3, 1, DM / 64, 1, 0, 4> eng(acc, smem, ld, (long)2 * SEQ * 32, (long)64 * LDW, aaddr, waddr, 0);
  if (w >= 8) { eng.loader_run(); return; }
  eng.start();
  eng.run();
  const int g = lane >> 4;
#pragma unroll
  for (int n = 0; n < 3; ++n) {
    const int cb = (n0 >> 4) + wn * 3 + n;
    const float sc = cb < DM / 16 ? QSCALE : 1.f;
#pragma unroll
    for (int m = 0; m < 4; m += 2) {
      u32x2 a, b;
      a[0] = pack2(acc[n][m][0] * sc, acc[n][m][1] * sc);
      a[1] = pack2(acc[n][m][2] * sc, acc[n][m][3] * sc);
      b[0] = pack2(acc[n][m + 1][0] * sc, acc[n][m + 1][1] * sc);
      b[1] = pack2(acc[n][m + 1][2] * sc, acc[n][m + 1][3] * sc);
      const int row = m0 + wm * 64 + (m + (g & 1)) * 16 + (lane & 15);
      st16(out + ((size_t)cb * SEQ + row) * 16 + (g >> 1) * 8, pair16(a, b));
    }
  }
  STAMP_AT(1, 3);
}

typedef GCfg<128, 1, 2, 6, 4> CP;
template <int KDIM, int LDW>
__global__ __launch_bounds__(768, 3) void k_proj(const u16* __restrict__ Ablk, const u16* __restrict__ W,
                                                  const u16* __restrict__ res, const float* __restrict__ gate,
                                                  u16* __restrict__ out) {
  extern __shared__ __attribute__((aligned(16))) char smem_[];
  lds_ptr smem = (lds_ptr)smem_;
  const int lane = threadIdx.x & 63;
  const int w = __builtin_amdgcn_readfirstlane(threadIdx.x >> 6);
  const int xcd = blockIdx.x & 7, idx = blockIdx.x >> 3;
  const int tm = (xcd & 3) * 4 + (idx & 3), tn = (xcd >> 2) * 8 + (idx >> 2);
  const int m0 = tm * 128, n0 = tn * 64;
  Loader<CP> ld;
  const int lw = w & 3;
  setup_loader_A<CP, 128, 1, 2>(ld, Ablk, m0, lw, lane);
#pragma unroll
  for (int i = 0; i < CP::WPW; ++i) {
    int b = lw + 4 * i;
    int sub = b >> 2, o = b & 3;
    ld.woff[i] = 2 * CP::A_SUB + sub * CP::W_SUB + o * 1024;
    int k = sub * 32 + 8 * o + (lane >> 3);
    int f = ((lane >> 4) & 1) | ((o & 1) << 1);
    int col = n0 + ((((lane & 7) >> 1) ^ f) * 16) + (lane & 1) * 8;
    ld.wv[i] = (k * LDW + col) * 2;
    ld.wbase[i] = (const char*)W;
  }
  const int wm = w & 1, wn = (w >> 1) & 1, kg = (w >> 2) & 1;
  int waddr[2];
#pragma unroll
  for (int n = 0; n < 2; ++n) waddr[n] = wfrag_addr(wn * 2 + n, lane);
  const int aaddr = afrag_addr<128>(wm * 64, lane);
  f32x4 acc[2][4];
#pragma unroll
  for (int n = 0; n < 2; ++n)
#pragma unroll
    for (int m = 0; m < 4; ++m) acc[n][m] = f32x4{0.f, 0.f, 0.f, 0.f};
  Engine<128, 1, 2, 6, 4, 2, 2, KDIM / 64, (KDIM == DM ? 3 : 5), 0, 4> eng(acc, smem, ld, (long)2 * SEQ * 32, (long)64 * LDW, aaddr, waddr, kg);
  constexpr int EOFF = decltype(eng)::FREE_OFF;
  static_assert(EOFF >= 32768, "overlaps the K-group exchange area");
  if (w >= 8) {
    float gp[MNS];
    eng.loader_run([&](int phase) {
      if (phase == 0) {
#pragma unroll
        for (int i = 0; i < 4; ++i) {
          const int pc = lw + 4 * i;
          glds16(res + (size_t)(m0 + pc * 8) * DM + n0, ((lane >> 3) * DM + (lane & 7) * 8) * 2, lds_u32(smem) + EOFF + pc * 1024);
        }
#pragma unroll
        for (int i = 0; i < MNS; ++i) gp[i] = gate[i * (6 * DM) + n0 + lane];
      } else if (w == 8) {
        float v = 0.f;
#pragma unroll
        for (int i = 0; i < MNS; ++i) v += gp[i];
        *(__attribute__((address_space(3))) float*)(smem + EOFF + 16384 + lane * 4) = rbf(v);
      }
    });
    return;
  }
  const int g = lane >> 4;
  const int colw = n0 + (wn * 2 + kg) * 16;
  eng.start();
  eng.run();
  __builtin_amdgcn_s_barrier();
  asm volatile("" ::: "memory");
  __attribute__((address_space(3))) f32x4* red = (__attribute__((address_space(3))) f32x4*)smem;
  f32x4 kp[4];
  if (kg == 0) {
#pragma unroll
    for (int m = 0; m < 4; ++m) { red[(w * 4 + m) * 64 + lane] = acc[1][m]; kp[m] = acc[0][m]; }
  } else {
#pragma unroll
    for (int m = 0; m < 4; ++m) { red[(w * 4 + m) * 64 + lane] = acc[0][m]; kp[m] = acc[1][m]; }
  }
  asm volatile("s_waitcnt lgkmcnt(0)" ::: "memory");
  __builtin_amdgcn_s_barrier();
  asm volatile("" ::: "memory");
  {
    const f32x4 gt = *(const __attribute__((address_space(3))) f32x4*)(smem + EOFF + 16384 + ((wn * 2 + kg) * 16 + 4 * g) * 4);
#pragma unroll
    for (int m = 0; m < 4; m += 2) {
      u32x2 o[2];
#pragma unroll
      for (int i = 0; i < 2; ++i) {
        f32x4 a = kp[m + i] + red[((w ^ 4) * 4 + m + i) * 64 + lane];
        const u32x2 r = *(const __attribute__((address_space(3))) u32x2*)(smem + EOFF + (wm * 64 + (m + i) * 16 + (lane & 15)) * 128 + ((wn * 2 + kg) * 16 + 4 * g) * 2);
        o[i][0] = pack2(bflo(r[0]) + rbf(gt[0] * rbf(a[0])), bfhi(r[0]) + rbf(gt[1] * rbf(a[1])));
        o[i][1] = pack2(bflo(r[1]) + rbf(gt[2] * rbf(a[2])), bfhi(r[1]) + rbf(gt[3] * rbf(a[3])));
      }
      const int row = m0 + wm * 64 + (m + (g & 1)) * 16 + (lane & 15);
      *(u32x4*)(out + (size_t)row * DM + colw + (g >> 1) * 8) = pair16(o[0], o[1]);
    }
  }
  STAMP_AT((KDIM == DM ? 3 : 5), 3);
}

typedef GCfg<256, 4, 1, 4, 4> CG;
template <int NH_>
DI void glu_wave(lds_ptr smem, Loader<CG>& ld, int lane, int wm, int t0, int m0, int f0, u16* __restrict__ out) {
  constexpr int NT = 2 * NH_;
  int waddr[NT];
#pragma unroll
  for (int n = 0; n < NH_; ++n) {
    waddr[n] = wfrag_addr(t0 + n, lane);
    waddr[NH_ + n] = waddr[n] + 8192;
  }
  const int aaddr = afrag_addr<256>(wm * 64, lane);
  f32x4 acc[NT][4];
#pragma unroll
  for (int n = 0; n < NT; ++n)
#pragma unroll
    for (int m = 0; m < 4; ++m) acc[n][m] = f32x4{0.f, 0.f, 0.f, 0.f};
  Engine<256, 4, 1, 4, 4, NT, 1, DM / 32, 4, 0, 4, (NH_ == 3)> eng(acc, smem, ld, (long)SEQ * 32, (long)32 * FF, aaddr, waddr, 0);
  eng.start();
  eng.run();
  const int g = lane >> 4;
#pragma unroll
  for (int n = 0; n < NH_; ++n) {
    const int cb = (f0 >> 4) + t0 + n;
#pragma unroll
    for (int m = 0; m < 4; m += 2) {
      u32x2 o[2];
#pragma unroll
      for (int i = 0; i < 2; ++i) {
        f32x4 a = acc[n][m + i], b = acc[NH_ + n][m + i];
        o[i][0] = pack2(rbf(a[0]) * rbf(b[0]), rbf(a[1]) * rbf(b[1]));
        o[i][1] = pack2(rbf(a[2]) * rbf(b[2]), rbf(a[3]) * rbf(b[3]));
      }
      const int row = m0 + wm * 64 + (m + (g & 1)) * 16 + (lane & 15);
      st16(out + ((size_t)cb * SEQ + row) * 16 + (g >> 1) * 8, pair16(o[0], o[1]));
    }
  }
  STAMP_AT(4, 3);
}

__global__ __launch_bounds__(512, 2) void k_glu(const u16* __restrict__ Ablk, const u16* __restrict__ Wg,
                                                 const u16* __restrict__ Wu, u16* __restrict__ out) {
  extern __shared__ __attribute__((aligned(16))) char smem_[];
  lds_ptr smem = (lds_ptr)smem_;
  const int lane = threadIdx.x & 63;
  const int w = __builtin_amdgcn_readfirstlane(threadIdx.x >> 6);
  const int xcd = blockIdx.x & 7, idx = blockIdx.x >> 3;
  const int tm = idx & 7, tn = xcd * 4 + (idx >> 3);
  const int m0 = tm * 256, f0 = tn * 112;
  Loader<CG> ld;
  const int lw = w & 3;
  setup_loader_A<CG, 256, 4, 1>(ld, Ablk, m0, lw, lane);
#pragma unroll
  for (int i = 0; i < CG::WPW; ++i) {
    int b = lw + 4 * i;
    int grp = b >> 2, o = b & 3;
    ld.woff[i] = CG::A_SUB + grp * 4096 + o * 1024;
    int k = 8 * o + (lane >> 3);
    int f = ((lane >> 4) & 1) | ((o & 1) << 1);
    int pr = ((lane & 7) >> 1) ^ f;
    if ((grp & 1) && pr == 3) pr = 0;
    int col = f0 + (grp & 1) * 64 + pr * 16 + (lane & 1) * 8;
    ld.wv[i] = (k * FF + col) * 2;
    ld.wbase[i] = (const char*)(grp >> 1 ? Wu : Wg);
  }
  if (w >= 4) __builtin_amdgcn_s_setprio(1);
  const int wm = w & 3;
  if (w < 4) glu_wave<4>(smem, ld, lane, wm, 0, m0, f0, out);
  else glu_wave<3>(smem, ld, lane, wm, 4, m0, f0, out);
}

constexpr int ACH = 128, ANST = 4, ASTAGE = ACH * 64 * 2 + 256;
constexpr int ALDS = ANST * ASTAGE + 1024 + 16;

DI float rowmax16(const f32x16& v) {
  float mx = fmaxf(fmaxf(v[0], v[1]), v[2]);
#pragma unroll
  for (int r = 3; r < 15; r += 2) mx = fmaxf(fmaxf(mx, v[r]), v[r + 1]);
  mx = fmaxf(mx, v[15]);
  auto sw = __builtin_amdgcn_permlane32_swap(__float_as_uint(mx), __float_as_uint(mx), false, false);
  return fmaxf(__uint_as_float(sw[0]), __uint_as_float(sw[1]));
}

template <bool SAFE>
DI void attn_pass(lds_ptr smem, int lane, int w, const char* Kp, const char* Vp, const char* pf_wm, const char* pf_wo, const char* pf_wg,
                  const char* pf_wu, const char* pf_wd, const bf16x8& qa0, const bf16x8& qa1, const bf16x8& qb0,
                  const bf16x8& qb1, f32x16& oa, f32x16& ob, float& refa, float& refb, float& la_out, float& lb_out,
                  ModTask& mt, const u16* c) {
  const int r32 = lane & 31, hh = lane >> 5, kh = w >> 2;
  const unsigned kvo = ((32 * (w & 3) + (lane >> 1)) * 16 + (((lane & 1) ^ ((lane >> 4) & 1)) * 8)) * 2;
  const unsigned vvo = ((32 * (w & 3)) * 16) * 2 + lane * 16;
  const char* ksrc = Kp;
  const char* vsrc = Vp;
  auto issue = [&](int buf) {
    if (w < 4) {
      const unsigned l0 = lds_u32(smem) + buf * ASTAGE + w * 1024;
      glds16(ksrc, kvo, l0);
      glds16(ksrc + SEQ * 32, kvo, l0 + 4096);
      glds16(vsrc, vvo, l0 + 8192);
      glds16(vsrc + SEQ * 32, vvo, l0 + 8192 + 4096 + 128);
    }
    ksrc += ACH * 32; vsrc += ACH * 32;
  };
  const unsigned pfo = lane * 16;
  auto prefetch = [&](int ch) {
    if (w >= 4) {
#pragma unroll
      for (int i = 0; i < 3; ++i) {
        int pc = (ch * 3 + i) * 4 + (w - 4);
        const char* base;
        if (pc < 32) base = pf_wm + (size_t)(blockIdx.x * 4 + (pc >> 3)) * (12 * DM) + 4 * DM + (pc & 7) * 1024;
        else if (pc < 40) base = pf_wo + (size_t)blockIdx.x * 8192 + (pc - 32) * 1024;
        else if (pc < 68) base = pf_wd + (size_t)blockIdx.x * 28672 + (pc - 40) * 1024;
        else if (pc < 96) base = pf_wg + (size_t)blockIdx.x * 28672 + (pc - 68) * 1024;
        else if (pc < 124) base = pf_wu + (size_t)blockIdx.x * 28672 + (pc - 96) * 1024;
        else break;
        glds16(base, pfo, lds_u32(smem) + ANST * ASTAGE);
      }
    }
  };
  issue(0); issue(1); issue(2);
  const int kaddr0 = kh * 2048 + r32 * 32 + ((hh ^ ((r32 >> 3) & 1)) * 16);
  const int kaddr1 = kaddr0 + 4096;
  const int vaddr = 8192 + kh * 2048 + ((lane >> 4) & 1) * (4096 + 128) + (4 * hh + ((lane & 15) >> 2)) * 32 + (lane & 3) * 8;
  bf16x8 sel;
  {
    const short one = ((lane & 15) == ((lane >> 4) & 1)) ? (short)0x3F80 : (short)0;
#pragma unroll
    for (int j = 0; j < 8; ++j) sel[j] = one;
  }
  f32x16 nma, nmb, sa, sb;
#pragma unroll
  for (int r = 0; r < 16; ++r) { oa[r] = 0.f; ob[r] = 0.f; nma[r] = 0.f; nmb[r] = 0.f; }
  f32x4 lsa = {0.f, 0.f, 0.f, 0.f}, lsb = {0.f, 0.f, 0.f, 0.f};
  float la = 0.f, lb = 0.f;
  constexpr int NCH = SEQ / ACH;
  if (w < 4) wait_vm<8>();
  bar();
  const bf16x8 kf0_ = lds_read128(smem + kaddr0);
  const bf16x8 kf1_ = lds_read128(smem + kaddr1);
  {
    sa = MFMA32(kf0_, qa0, nma); sa = MFMA32(kf1_, qa1, sa);
    sb = MFMA32(kf0_, qb0, nmb); sb = MFMA32(kf1_, qb1, sb);
    const float ma = rowmax16(sa), mb = rowmax16(sb);
#pragma unroll
    for (int r = 0; r < 16; ++r) { nma[r] = -ma; sa[r] -= ma; nmb[r] = -mb; sb[r] -= mb; }
  }
  if constexpr (SAFE) {
    auto half = [&](f32x16& s, f32x16& o, f32x16& nm, float& l, const bf16x8& q0f, const bf16x8& q1f,
                    const bf16x8& kn0, const bf16x8& kn1, const bf16x8& vf0, const bf16x8& vf1, bool has_next) {
      f32x16 p;
      const float dl = fmaxf(rowmax16(s), 0.f);
      const float f = __builtin_amdgcn_exp2f(-dl);
      float t = 0.f;
#pragma unroll
      for (int r = 0; r < 16; ++r) { nm[r] -= dl; o[r] *= f; p[r] = __builtin_amdgcn_exp2f(s[r] - dl); t += p[r]; }
      l = l * f + t;
      if (has_next) { s = MFMA32(kn0, q0f, nm); s = MFMA32(kn1, q1f, s); }
      u32x4 pa, pb;
#pragma unroll
      for (int j = 0; j < 4; ++j) { pa[j] = pack2(p[2 * j], p[2 * j + 1]); pb[j] = pack2(p[8 + 2 * j], p[8 + 2 * j + 1]); }
      o = MFMA32(vf0, __builtin_bit_cast(bf16x8, pa), o);
      o = MFMA32(vf1, __builtin_bit_cast(bf16x8, pb), o);
    };
    auto step = [&](lds_cptr vb, lds_cptr knext, bool has_next) {
      bf16x8 kn0, kn1;
      if (has_next) { kn0 = lds_read128(knext + kaddr0); kn1 = lds_read128(knext + kaddr1); }
      bf16x8 vf0 = lds_tr8(vb, vb + 256);
      bf16x8 vf1 = lds_tr8(vb + 512, vb + 768);
      half(sa, oa, nma, la, qa0, qa1, kn0, kn1, vf0, vf1, has_next);
      half(sb, ob, nmb, lb, qb0, qb1, kn0, kn1, vf0, vf1, has_next);
    };
    for (int ch = 0; ch < NCH; ++ch) {
      if (w < 4) { if (ch + 2 < NCH) wait_vm<4>(); else wait_vm<0>(); }
      bar();
      if (ch + 3 < NCH) issue((ch + 3) & 3);
      lds_cptr sbuf = smem + (ch & 3) * ASTAGE;
      lds_cptr nbuf = smem + ((ch + 1) & 3) * ASTAGE;
      step(sbuf + vaddr, sbuf + 1024, true);
      step(sbuf + 1024 + vaddr, nbuf, ch + 1 < NCH);
    }
  } else {
    u32x4 pa0, pa1, pb0, pb1;
    bf16x8 kc0 = kf0_, kc1 = kf1_;
    bf16x8 vp0, vp1;
#pragma unroll
    for (int j = 0; j < 4; ++j) { pb0[j] = 0u; pb1[j] = 0u; }
#pragma unroll
    for (int j = 0; j < 8; ++j) { vp0[j] = 0; vp1[j] = 0; }
#define SB_ __builtin_amdgcn_sched_barrier(0)
#define EX(i) e[i] = __builtin_amdgcn_exp2f(S[i])
#define PHASE(S, P0, P1, M0, M1, M2, M3, M4, M5, R0, R1) do { float e[16]; \
      M0; SB_; R0; EX(0); EX(1); EX(2); EX(3); SB_; \
      M1; SB_; R1; EX(4); EX(5); EX(6); EX(7); SB_; \
      M2; SB_; EX(8); EX(9); P0[0] = pack2(e[0], e[1]); P0[1] = pack2(e[2], e[3]); SB_; \
      M3; SB_; EX(10); EX(11); P0[2] = pack2(e[4], e[5]); P0[3] = pack2(e[6], e[7]); SB_; \
      M4; SB_; EX(12); EX(13); EX(14); EX(15); SB_; \
      M5; SB_; P1[0] = pack2(e[8], e[9]); P1[1] = pack2(e[10], e[11]); P1[2] = pack2(e[12], e[13]); P1[3] = pack2(e[14], e[15]); SB_; \
    } while (0)
#define BF(x) __builtin_bit_cast(bf16x8, x)
    auto step = [&](lds_cptr vb, lds_cptr knext, auto has_next_t) {
      constexpr bool HN = decltype(has_next_t)::value;
      bf16x8 kn0, kn1, vc0, vc1;
      { const f32x16& S = sa;
        PHASE(S, pa0, pa1,
              sb = MFMA32(kc0, qb0, nmb), sb = MFMA32(kc1, qb1, sb),
              ob = MFMA32(vp0, BF(pb0), ob), ob = MFMA32(vp1, BF(pb1), ob),
              lsb = MFMA16(sel, BF(pb0), lsb), lsb = MFMA16(sel, BF(pb1), lsb),
              if (HN) { kn0 = lds_read128(knext + kaddr0); kn1 = lds_read128(knext + kaddr1); },
              { vc0 = lds_tr8(vb, vb + 256); vc1 = lds_tr8(vb + 512, vb + 768); }); }
      { const f32x16& S = sb;
        PHASE(S, pb0, pb1,
              if (HN) sa = MFMA32(kn0, qa0, nma), if (HN) sa = MFMA32(kn1, qa1, sa),
              oa = MFMA32(vc0, BF(pa0), oa), oa = MFMA32(vc1, BF(pa1), oa),
              lsa = MFMA16(sel, BF(pa0), lsa), lsa = MFMA16(sel, BF(pa1), lsa),
              , ); }
      if (HN) { kc0 = kn0; kc1 = kn1; }
      vp0 = vc0; vp1 = vc1;
    };
    typedef std::integral_constant<bool, true> T_;
    typedef std::integral_constant<bool, false> F_;
    for (int ch = 0; ch < NCH - 1; ++ch) {
      if (w < 4) { if (ch + 2 < NCH) wait_vm<4>(); else wait_vm<0>(); }
      bar();
      if (ch + 3 < NCH) issue((ch + 3) & 3);
      prefetch(ch);
      lds_cptr sbuf = smem + (ch & 3) * ASTAGE;
      lds_cptr nbuf = smem + ((ch + 1) & 3) * ASTAGE;
      step(sbuf + vaddr, sbuf + 1024, T_{});
      step(sbuf + 1024 + vaddr, nbuf, T_{});
    }
    {
      const int task = blockIdx.x * 2 + (threadIdx.x >> 8);
      mod_load(mt, c, (const u16*)pf_wm, 32 + (task >> 3), task & 7, threadIdx.x & 255);
      lds_cptr sbuf = smem + ((NCH - 1) & 3) * ASTAGE;
      step(sbuf + vaddr, sbuf + 1024, T_{});
      step(sbuf + 1024 + vaddr, sbuf, F_{});
    }
    ob = MFMA32(vp0, BF(pb0), ob); ob = MFMA32(vp1, BF(pb1), ob);
    lsb = MFMA16(sel, BF(pb0), lsb); lsb = MFMA16(sel, BF(pb1), lsb);
#undef PHASE
#undef EX
#undef BF
  }
  refa = nma[0]; refb = nmb[0];
  if constexpr (SAFE) {
    auto s1 = __builtin_amdgcn_permlane32_swap(__float_as_uint(la), __float_as_uint(la), false, false);
    la_out = __uint_as_float(s1[0]) + __uint_as_float(s1[1]);
    auto s2 = __builtin_amdgcn_permlane32_swap(__float_as_uint(lb), __float_as_uint(lb), false, false);
    lb_out = __uint_as_float(s2[0]) + __uint_as_float(s2[1]);
  } else {
    const float a0 = __shfl(lsa[0], lane & 15), a1 = __shfl(lsa[1], lane & 15);
    const float b0 = __shfl(lsb[0], lane & 15), b1 = __shfl(lsb[1], lane & 15);
    la_out = (lane & 16) ? a1 : a0;
    lb_out = (lane & 16) ? b1 : b0;
  }
}

__global__ __launch_bounds__(512, 2) void k_attn(const u16* __restrict__ qkv, u16* __restrict__ oblk,
                                                  const u16* __restrict__ c, const u16* __restrict__ wmod, float* __restrict__ part,
                                                  const char* __restrict__ pf_wo, const char* __restrict__ pf_wg,
                                                  const char* __restrict__ pf_wu, const char* __restrict__ pf_wd) {
  extern __shared__ __attribute__((aligned(16))) char smem_[];
  lds_ptr smem = (lds_ptr)smem_;
  const int lane = threadIdx.x & 63;
  const int w = __builtin_amdgcn_readfirstlane(threadIdx.x >> 6);
  const int xcd = blockIdx.x & 7, idx = blockIdx.x >> 3;
  const int head = xcd * 4 + (idx >> 3), qb = idx & 7;
  const int r32 = lane & 31, hh = lane >> 5;
  const int qg = w & 3, kh = w >> 2;
  const u16* Qp = qkv + (size_t)head * SEQ * 32;
  const char* Kp = (const char*)(qkv + (size_t)(32 + head) * SEQ * 32);
  const char* Vp = (const char*)(qkv + (size_t)(64 + head) * SEQ * 32);
  const int q0 = qb * 256 + qg * 64;
  STAMP_AT(2, 0);
  __attribute__((address_space(3))) int* flag = (__attribute__((address_space(3))) int*)(smem + ANST * ASTAGE + 1024);
  if (threadIdx.x == 0) *flag = 0;
  bf16x8 qa0 = *(const bf16x8*)(Qp + (q0 + r32) * 16 + hh * 8);
  bf16x8 qa1 = *(const bf16x8*)(Qp + (SEQ + q0 + r32) * 16 + hh * 8);
  bf16x8 qb0 = *(const bf16x8*)(Qp + (q0 + 32 + r32) * 16 + hh * 8);
  bf16x8 qb1 = *(const bf16x8*)(Qp + (SEQ + q0 + 32 + r32) * 16 + hh * 8);
  f32x16 oa, ob;
  float nma, nmb, la, lb;
  ModTask mt;
  attn_pass<false>(smem, lane, w, Kp, Vp, (const char*)wmod, pf_wo, pf_wg, pf_wu, pf_wd, qa0, qa1, qb0, qb1, oa, ob, nma, nmb, la, lb, mt, c);
  {
    float big = fmaxf(fabsf(la), fabsf(lb));
#pragma unroll
    for (int r = 0; r < 16; ++r) big = fmaxf(big, fmaxf(fabsf(oa[r]), fabsf(ob[r])));
    bool bad = !(big < 1e30f);
#pragma unroll
    for (int r = 0; r < 16; ++r) bad = bad || (oa[r] != oa[r]) || (ob[r] != ob[r]);
    bad = bad || (la != la) || (lb != lb);
    if (__any(bad) && lane == 0) *flag = 1;
  }
  __syncthreads();
  if (__builtin_amdgcn_readfirstlane(*flag) != 0) {
    attn_pass<true>(smem, lane, w, Kp, Vp, (const char*)wmod, pf_wo, pf_wg, pf_wu, pf_wd, qa0, qa1, qb0, qb1, oa, ob, nma, nmb, la, lb, mt, c);
    __syncthreads();
  }
  STAMP_AT(2, 2);
  const int task = blockIdx.x * 2 + (threadIdx.x >> 8);
  {
    __attribute__((address_space(3))) float* xb = (__attribute__((address_space(3))) float*)smem;
    const f32x16& og = kh == 0 ? ob : oa;
    __attribute__((address_space(3))) float* dst = xb + w * (18 * 64) + lane;
#pragma unroll
    for (int r = 0; r < 16; ++r) dst[r * 64] = og[r];
    dst[16 * 64] = kh == 0 ? lb : la;
    dst[17 * 64] = kh == 0 ? nmb : nma;
    __syncthreads();
    const __attribute__((address_space(3))) float* src = xb + (w ^ 4) * (18 * 64) + lane;
    f32x16 om = kh == 0 ? oa : ob;
    const float nm_me = kh == 0 ? nma : nmb;
    const float nm_ot = src[17 * 64];
    const float nmin = fminf(nm_me, nm_ot);
    const float f_me = __builtin_amdgcn_exp2f(nmin - nm_me), f_ot = __builtin_amdgcn_exp2f(nmin - nm_ot);
    const float rl = 1.f / ((kh == 0 ? la : lb) * f_me + src[16 * 64] * f_ot);
    const float g_me = f_me * rl, g_ot = f_ot * rl;
    u16* op = oblk + ((size_t)head * 2 * SEQ + q0 + kh * 32 + r32) * 16 + 4 * hh;
#pragma unroll
    for (int g = 0; g < 4; ++g) {
      float v0 = om[4 * g] * g_me + src[(4 * g) * 64] * g_ot;
      float v1 = om[4 * g + 1] * g_me + src[(4 * g + 1) * 64] * g_ot;
      float v2 = om[4 * g + 2] * g_me + src[(4 * g + 2) * 64] * g_ot;
      float v3 = om[4 * g + 3] * g_me + src[(4 * g + 3) * 64] * g_ot;
      u32x2 v;
      v[0] = pack2(v0, v1);
      v[1] = pack2(v2, v3);
      *(u32x2*)(op + (g >> 1) * (SEQ * 16) + (g & 1) * 8) = v;
    }
  }
  mod_finish(mt, part, 32 + (task >> 3), task & 7, threadIdx.x & 255, (float*)(smem_ + 40960) + (threadIdx.x >> 8) * 256);
  wait_vm<0>();
  STAMP_AT(2, 3);
}

extern "C" void kernel_launch(void* const* d_in, const int* in_sizes, int n_in, void* d_out, int out_size,
                              void* d_ws, size_t ws_size, hipStream_t stream) {
  const u16* x = (const u16*)d_in[0];
  const u16* wqkv = (const u16*)d_in[1];
  const u16* wo = (const u16*)d_in[2];
  const u16* wg = (const u16*)d_in[4];
  const u16* wu = (const u16*)d_in[5];
  const u16* wd = (const u16*)d_in[6];
  const u16* c = (const u16*)d_in[7];
  const u16* wmod = (const u16*)d_in[8];
  u16* out = (u16*)d_out;
  char* ws = (char*)d_ws;
  float* part = (float*)ws;
  u16* h1 = (u16*)(ws + (1 << 20));
  u16* qkv = (u16*)(ws + (8 << 20));
  u16* ob = (u16*)(ws + (24 << 20));
  u16* x2 = (u16*)(ws + (32 << 20));
  u16* h2 = (u16*)(ws + (40 << 20));
  u16* hh = (u16*)(ws + (48 << 20));
  u16* wdp = (u16*)(ws + (64 << 20));

  hipFuncSetAttribute((const void*)k_qkv, hipFuncAttributeMaxDynamicSharedMemorySize, CQ::LDS);
  hipFuncSetAttribute((const void*)k_proj<DM, DM>, hipFuncAttributeMaxDynamicSharedMemorySize, CP::LDS);
  hipFuncSetAttribute((const void*)k_proj<FF, WDP>, hipFuncAttributeMaxDynamicSharedMemorySize, CP::LDS);
  hipFuncSetAttribute((const void*)k_glu, hipFuncAttributeMaxDynamicSharedMemorySize, CG::LDS);
  hipFuncSetAttribute((const void*)k_attn, hipFuncAttributeMaxDynamicSharedMemorySize, ALDS);

  k_pre<<<256 + 256, 256, 0, stream>>>(c, wmod, part, (const char*)x);
  k_ln<6, MNS, 192><<<256 + 192, 512, 0, stream>>>(x, part, h1, (const char*)wqkv, wd, wdp);
  k_qkv<<<256, 768, CQ::LDS, stream>>>(h1, wqkv, qkv);
  k_attn<<<256, 512, ALDS, stream>>>(qkv, ob, c, wmod, part, (const char*)wo, (const char*)wg, (const char*)wu, (const char*)wd);
  k_proj<DM, DM><<<256, 768, CP::LDS, stream>>>(ob, wo, x, part + 2 * DM, x2);
  k_ln<7, MNS, 0><<<256 + 224, 512, 0, stream>>>(x2, part + 3 * DM, h2, (const char*)wqkv, wd, wdp);
  k_glu<<<256, 512, CG::LDS, stream>>>(h2, wg, wu, hh);
  k_proj<FF, WDP><<<256, 768, CP::LDS, stream>>>(hh, wdp, x2, part + 5 * DM, out);
}
```

```cpp
#include <hip/hip_runtime.h>
#include <hip/hip_bf16.h>
#include <stdint.h>

typedef __attribute__((ext_vector_type(8))) short bf16x8;
typedef __attribute__((ext_vector_type(4))) short s16x4;
typedef __attribute__((ext_vector_type(4))) float f32x4;
typedef __attribute__((ext_vector_type(2))) float f32x2;
typedef __attribute__((ext_vector_type(16))) float f32x16;
typedef __attribute__((ext_vector_type(4))) unsigned u32x4;
typedef __attribute__((ext_vector_type(2))) unsigned u32x2;
typedef __attribute__((ext_vector_type(2))) __bf16 bf16x2_t;
typedef unsigned short u16;
typedef __attribute__((address_space(3))) char* lds_ptr;
typedef __attribute__((address_space(3))) const char* lds_cptr;

#define DI __device__ __forceinline__

constexpr int SEQ = 2048, DM = 1024, FF = 3584, NH = 32;

DI float bf2f(u16 x) { return __uint_as_float(((unsigned)x) << 16); }
DI float bflo(unsigned x) { return __uint_as_float(x << 16); }
DI float bfhi(unsigned x) { return __uint_as_float(x & 0xffff0000u); }
DI unsigned pack2(float a, float b) {
  f32x2 v = {a, b};
  bf16x2_t r = __builtin_convertvector(v, bf16x2_t);
  return __builtin_bit_cast(unsigned, r);
}
DI float rbf(float a) { return bflo(pack2(a, 0.f) & 0xffffu); }

#define MFMA16(a, b, c) __builtin_amdgcn_mfma_f32_16x16x32_bf16((a), (b), (c), 0, 0, 0)
#define MFMA32(a, b, c) __builtin_amdgcn_mfma_f32_32x32x16_bf16((a), (b), (c), 0, 0, 0)

DI void glds16(const void* base, unsigned voff, unsigned lds_addr) {
  unsigned sv;
  asm volatile("s_mov_b32 %0, m0\n\ts_mov_b32 m0, %3\n\ts_nop 2\n\tglobal_load_lds_dwordx4 %1, %2\n\ts_mov_b32 m0, %0"
               : "=&s"(sv) : "v"(voff), "s"(base), "s"(lds_addr) : "memory");
}
DI unsigned lds_u32(lds_ptr p) { return (unsigned)(uintptr_t)p; }
DI bf16x8 lds_read128(lds_cptr p) { return *(const __attribute__((address_space(3))) bf16x8*)p; }
DI s16x4 lds_tr(lds_cptr p) {
  typedef short v4i16_t __attribute__((ext_vector_type(4)));
  return __builtin_bit_cast(s16x4, __builtin_amdgcn_ds_read_tr16_b64_v4i16((__attribute__((address_space(3))) v4i16_t*)p));
}
DI bf16x8 lds_tr8(lds_cptr p0, lds_cptr p1) {
  s16x4 a = lds_tr(p0), b = lds_tr(p1);
  return __builtin_shufflevector(a, b, 0, 1, 2, 3, 4, 5, 6, 7);
}
DI void st16(void* p, u32x4 v) { asm volatile("global_store_dwordx4 %0, %1, off sc1\n\ts_nop 1" ::"v"(p), "v"(v) : "memory"); }
template <int N> DI void wait_vm() { asm volatile("s_waitcnt vmcnt(%0)" ::"n"(N) : "memory"); }
DI void bar() { asm volatile("" ::: "memory"); __builtin_amdgcn_s_barrier(); asm volatile("" ::: "memory"); }

constexpr int MNS = 8;
struct ModTask { u32x4 w[4]; u16 cv[4]; };
DI void mod_load(ModTask& mt, const u16* __restrict__ c, const u16* __restrict__ wmod, int cb, int ks, int t) {
  const int cc = t & 7, rg = t >> 3;
  const int k0 = ks * 128 + rg;
  const u16* wp = wmod + (size_t)k0 * (6 * DM) + cb * 64 + cc * 8;
#pragma unroll
  for (int i = 0; i < 4; ++i) mt.w[i] = *(const u32x4*)(wp + (size_t)i * 32 * (6 * DM));
#pragma unroll
  for (int i = 0; i < 4; ++i) mt.cv[i] = c[k0 + 32 * i];
}
DI void mod_finish(const ModTask& mt, float* __restrict__ part, int cb, int ks, int t, float* s_red  ) {
  float acc[8];
#pragma unroll
  for (int j = 0; j < 8; ++j) acc[j] = 0.f;
#pragma unroll
  for (int i = 0; i < 4; ++i) {
    const float cv = bf2f(mt.cv[i]);
    const float sv = rbf(cv / (1.f + __expf(-cv)));
#pragma unroll
    for (int j = 0; j < 4; ++j) {
      acc[2 * j] += sv * bflo(mt.w[i][j]);
      acc[2 * j + 1] += sv * bfhi(mt.w[i][j]);
    }
  }
#pragma unroll
  for (int j = 0; j < 8; ++j) {
    float v = acc[j];
    v += __shfl_xor(v, 8);
    v += __shfl_xor(v, 16);
    v += __shfl_xor(v, 32);
    acc[j] = v;
  }
  const int lane = t & 63, wv = t >> 6;
  if (lane < 8) {
#pragma unroll
    for (int j = 0; j < 8; ++j) s_red[wv * 64 + lane * 8 + j] = acc[j];
  }
  __syncthreads();
  if (t < 64) part[ks * (6 * DM) + cb * 64 + t] = (s_red[t] + s_red[64 + t]) + (s_red[128 + t] + s_red[192 + t]);
}

DI void touch16k(const char* p, int t) {
  u32x4 v[4];
#pragma unroll
  for (int i = 0; i < 4; ++i) v[i] = *(const u32x4*)(p + i * 4096 + t * 16);
#pragma unroll
  for (int i = 0; i < 4; ++i) asm volatile("" ::"v"(v[i]));
}

__global__ __launch_bounds__(256) void k_pre(const u16* __restrict__ c, const u16* __restrict__ wmod, float* __restrict__ part,
                                              const char* __restrict__ x) {
  __shared__ float s_red[256];
  const int t = threadIdx.x, b = blockIdx.x;
  if (b < 256) {
    ModTask mt;
    mod_load(mt, c, wmod, b >> 3, b & 7, t);
    mod_finish(mt, part, b >> 3, b & 7, t, s_red);
  } else {
    touch16k(x + (size_t)(b - 256) * 16384, t);
  }
}

constexpr int WDP = DM + 64;
template <int KID, int NS, int NPF>
__global__ __launch_bounds__(512) void k_ln(const u16* __restrict__ x, const float* __restrict__ ss  ,
                                             u16* __restrict__ h16, const char* __restrict__ pf,
                                             const u16* __restrict__ wsrc, u16* __restrict__ wdst) {
  __shared__ __attribute__((aligned(16))) float sm[2048];
  const int t = threadIdx.x;
  if (blockIdx.x >= 256 + NPF) {
    const int r0 = (blockIdx.x - 256 - NPF) * 16 + (t >> 7);
    u32x4 v[4];
#pragma unroll
    for (int i = 0; i < 4; ++i) v[i] = *(const u32x4*)(wsrc + (size_t)(r0 + 4 * i) * DM + (t & 127) * 8);
#pragma unroll
    for (int i = 0; i < 4; ++i) st16(wdst + (size_t)(r0 + 4 * i) * WDP + (t & 127) * 8, v[i]);
    return;
  }
  if (blockIdx.x >= 256) {
    touch16k(pf + (size_t)((blockIdx.x - 256) * 2 + (t >> 8)) * 16384, t & 255);
    return;
  }
  const int lane = t & 63, wv = t >> 6;
  const int row = blockIdx.x * 8 + wv;
  const u16* xr = x + (size_t)row * DM;
  u32x4 v0 = *(const u32x4*)(xr + lane * 8);
  u32x4 v1 = *(const u32x4*)(xr + 512 + lane * 8);
  {
    f32x4 pv[NS];
#pragma unroll
    for (int i = 0; i < NS; ++i) pv[i] = *(const f32x4*)(ss + i * (6 * DM) + t * 4);
    f32x4 a = pv[0];
#pragma unroll
    for (int i = 1; i < NS; ++i) a += pv[i];
#pragma unroll
    for (int j = 0; j < 4; ++j) {
      float v = rbf(a[j]);
      a[j] = t < 256 ? v : rbf(1.f + v);
    }
    *(f32x4*)(sm + t * 4) = a;
  }
  float f[16];
#pragma unroll
  for (int j = 0; j < 4; ++j) {
    f[2 * j] = bflo(v0[j]); f[2 * j + 1] = bfhi(v0[j]);
    f[8 + 2 * j] = bflo(v1[j]); f[8 + 2 * j + 1] = bfhi(v1[j]);
  }
  float s = 0.f;
#pragma unroll
  for (int j = 0; j < 16; ++j) s += f[j];
#pragma unroll
  for (int o = 1; o < 64; o <<= 1) s += __shfl_xor(s, o);
  const float mean = s * (1.f / DM);
  float q = 0.f;
#pragma unroll
  for (int j = 0; j < 16; ++j) { float d = f[j] - mean; q += d * d; }
#pragma unroll
  for (int o = 1; o < 64; o <<= 1) q += __shfl_xor(q, o);
  const float mb = rbf(mean);
  const float vb = rbf(q * (1.f / DM));
  const float rden = 1.f / rbf(sqrtf(rbf(vb + 1e-5f)));
  __syncthreads();
#pragma unroll
  for (int half = 0; half < 2; ++half) {
    const int k0 = half * 512 + lane * 8;
    f32x4 sh0 = *(const f32x4*)(sm + k0), sh1 = *(const f32x4*)(sm + k0 + 4);
    f32x4 sc0 = *(const f32x4*)(sm + 1024 + k0), sc1 = *(const f32x4*)(sm + 1024 + k0 + 4);
    float y[8];
#pragma unroll
    for (int j = 0; j < 8; ++j) {
      float g = j < 4 ? sc0[j] : sc1[j - 4];
      float sh = j < 4 ? sh0[j] : sh1[j - 4];
      float d = rbf(f[half * 8 + j] - mb);
      float n = rbf(d * rden);
      y[j] = rbf(n * g) + sh;
    }
    u32x4 o;
#pragma unroll
    for (int j = 0; j < 4; ++j) o[j] = pack2(y[2 * j], y[2 * j + 1]);
    *(u32x4*)(h16 + ((size_t)(k0 >> 4) * SEQ + row) * 16 + (k0 & 15)) = o;
  }
}

DI u32x4 pair16(u32x2 a, u32x2 b) {
  auto s0 = __builtin_amdgcn_permlane16_swap(a[0], b[0], false, false);
  auto s1 = __builtin_amdgcn_permlane16_swap(a[1], b[1], false, false);
  return u32x4{s0[0], s1[0], s0[1], s1[1]};
}

template <int BM, int NGRP, int KS, int NST, int NLW_ = 8>
struct GCfg {
  static constexpr int NLW = NLW_;
  static constexpr int A_SUB = BM * 64;
  static constexpr int W_SUB = NGRP * 4096;
  static constexpr int STAGE = KS * (A_SUB + W_SUB);
  static constexpr int APW = KS * BM / 16 / NLW;
  static constexpr int WPW = KS * NGRP * 4 / NLW;
  static constexpr int PPW = APW + WPW;
  static constexpr int LDS = STAGE * NST;
};

template <class C>
struct Loader {
  const char* abase;
  const char* wbase[C::WPW];
  unsigned av[C::APW];
  unsigned wv[C::WPW];
  int aoff[C::APW];
  int woff[C::WPW];
};

template <int BM, int NGRP, int KS, int NST, int MT, int NT, int KSPLIT, int NKT, int KID, int IOFS, int NLW = 8, bool LOADS = (NLW == 8)>
struct Engine {
  typedef GCfg<BM, NGRP, KS, NST, NLW> C;
  static constexpr bool DED = !LOADS;
  static constexpr int SPW = KS / KSPLIT;
  static constexpr int P = C::PPW;
  static constexpr int G = NT * SPW;
  f32x4 (&acc)[NT][MT];
  lds_ptr smem;
  Loader<C>& ld;
  long a_adv, w_adv;
  const int (&waddr)[NT];
  int a_sub0, w_sub0;
  int cur, nxt, fre;
  bf16x8 wf[NT], af[2][MT];

  DI Engine(f32x4 (&acc_)[NT][MT], lds_ptr smem_, Loader<C>& ld_, long a_adv_, long w_adv_, int aaddr, const int (&waddr_)[NT], int kg)
      : acc(acc_), smem(smem_), ld(ld_), a_adv(a_adv_), w_adv(w_adv_), waddr(waddr_) {
    a_sub0 = kg * C::A_SUB + aaddr;
    w_sub0 = KS * C::A_SUB + kg * C::W_SUB;
  }
  template <int I> DI void piece(int stage_off) {
    if constexpr (I < C::APW) {
      glds16(ld.abase, ld.av[I], lds_u32(smem) + stage_off + ld.aoff[I]);
      if constexpr (I == C::APW - 1) ld.abase += a_adv * 2;
    } else {
      constexpr int J = I - C::APW;
      glds16(ld.wbase[J], ld.wv[J], lds_u32(smem) + stage_off + ld.woff[J]);
      ld.wbase[J] += w_adv * 2;
    }
  }
  template <int I> DI void issue_all(int stage_off) {
    if constexpr (I < P) { piece<I>(stage_off); issue_all<I + 1>(stage_off); }
  }
  template <int g, int I> DI void issue_at(int stage_off) {
    if constexpr (I < P) {
      constexpr int span = (G - IOFS) > P ? (G - IOFS) : P;
      constexpr int pos = IOFS + (I * span) / P < G ? IOFS + (I * span) / P : G - 1;
      if constexpr (pos == g) piece<I>(stage_off);
      issue_at<g, I + 1>(stage_off);
    }
  }
  template <int PAR, int S, bool PF, bool DOISSUE, int n>
  DI void groups(lds_cptr ab, lds_cptr wb) {
    if constexpr (n < NT) {
#pragma unroll
      for (int m = 0; m < MT; ++m) acc[n][m] = MFMA16(wf[n], af[PAR][m], acc[n][m]);
      if constexpr (PF) {
        wf[n] = lds_tr8(wb + waddr[n], wb + waddr[n] + 512);
#pragma unroll
        for (int m = 0; m < MT; ++m)
          if ((m * NT) / MT == n) af[PAR ^ 1][m] = lds_read128(ab + m * 512);
      }
      if constexpr (DOISSUE && !DED) issue_at<S * NT + n, 0>(fre);
      __builtin_amdgcn_sched_barrier(0);
      groups<PAR, S, PF, DOISSUE, n + 1>(ab, wb);
    }
  }
  template <int PAR, bool DOISSUE, bool LAST, int S> DI void steps() {
    if constexpr (S < SPW) {
      if constexpr (S + 1 < SPW)
        groups<PAR, S, true, DOISSUE, 0>(smem + cur + a_sub0 + (S + 1) * KSPLIT * C::A_SUB, smem + cur + w_sub0 + (S + 1) * KSPLIT * C::W_SUB);
      else if constexpr (!LAST)
        groups<PAR, S, true, DOISSUE, 0>(smem + nxt + a_sub0, smem + nxt + w_sub0);
      else
        groups<PAR, S, false, false, 0>(smem, smem);
      steps<PAR ^ 1, DOISSUE, LAST, S + 1>();
    }
  }
  template <int PAR, bool DOISSUE, bool LAST, int WAITN> DI void iter() {
    if constexpr (!LAST) { if constexpr (!DED) wait_vm<WAITN>(); bar(); }
    steps<PAR, DOISSUE, LAST, 0>();
    fre = cur; cur = nxt; nxt = (nxt + C::STAGE == NST * C::STAGE) ? 0 : nxt + C::STAGE;
  }
  template <int KT> DI void tail() {
    if constexpr (KT < NKT) {
      constexpr bool DOISSUE = KT + NST - 1 < NKT;
      constexpr bool LAST = KT == NKT - 1;
      constexpr int newest = (KT + NST - 2) < (NKT - 1) ? (KT + NST - 2) : (NKT - 1);
      constexpr int younger = newest - (KT + 1) > 0 ? newest - (KT + 1) : 0;
      iter<(KT * SPW) & 1, DOISSUE, LAST, P * younger>();
      tail<KT + 1>();
    }
  }
  DI void start() {
    static_assert(NKT >= NST + 1, "too few k tiles");
    if constexpr (!DED) {
#pragma unroll
      for (int s = 0; s < NST - 1; ++s) issue_all<0>(s * C::STAGE);
    }
  }
  template <int KT, class Hook> DI void ltail(Hook& hook) {
    if constexpr (KT < NKT - 1) {
      constexpr int newest = (KT + NST - 2) < (NKT - 1) ? (KT + NST - 2) : (NKT - 1);
      constexpr int younger = newest - (KT + 1) > 0 ? newest - (KT + 1) : 0;
      wait_vm<P * younger>();
      if constexpr (KT == NKT - 2) { hook(1); asm volatile("s_waitcnt lgkmcnt(0)" ::: "memory"); }
      bar();
      if constexpr (KT == NKT - NST + 1) hook(0);
      ltail<KT + 1>(hook);
    }
  }
  static constexpr int FREE_OFF = ((NKT - NST) % NST) * C::STAGE;
  DI void loader_run() { loader_run([](int) {}); }
  template <class Hook> DI void loader_run(Hook hook) {
    __builtin_amdgcn_s_setprio(1);
#pragma unroll
    for (int s = 0; s < NST - 1; ++s) issue_all<0>(s * C::STAGE);
    wait_vm<P*(NST - 2)>();
    bar();
    fre = (NST - 1) * C::STAGE;
    for (int kt = 0; kt < NKT - NST + 1; ++kt) {
      wait_vm<P*(NST - 3)>();
      bar();
      issue_all<0>(fre);
      fre = (fre + C::STAGE == NST * C::STAGE) ? 0 : fre + C::STAGE;
    }
    ltail<NKT - NST + 1>(hook);
  }
  DI void run() { run([] {}); }
  template <class Hook> DI void run(Hook hook) {
    if constexpr (!DED) wait_vm<P*(NST - 2)>();
    bar();
    {
      lds_cptr ab = smem + a_sub0;
      lds_cptr wb = smem + w_sub0;
#pragma unroll
      for (int m = 0; m < MT; ++m) af[0][m] = lds_read128(ab + m * 512);
#pragma unroll
      for (int n = 0; n < NT; ++n) wf[n] = lds_tr8(wb + waddr[n], wb + waddr[n] + 512);
    }
    hook();
    cur = 0; nxt = C::STAGE; fre = (NST - 1) * C::STAGE;
    constexpr int STEADY = NKT - NST + 1;
    constexpr int LOOPN = (STEADY / 2) * 2;
    for (int kt = 0; kt < LOOPN; kt += 2) {
      iter<0, true, false, P*(NST - 3)>();
      iter<SPW & 1, true, false, P*(NST - 3)>();
    }
    tail<LOOPN>();
  }
};

template <class C, int BM, int NGRP, int KS>
DI void setup_loader_A(Loader<C>& ld, const u16* Ablk, int m0, int w, int lane) {
#pragma unroll
  for (int i = 0; i < C::APW; ++i) {
    int a = w + C::NLW * i;
    int blk = a / (BM / 32), j = a % (BM / 32);
    ld.aoff[i] = blk * (BM * 32) + j * 1024;
    ld.av[i] = ((blk * SEQ + m0 + j * 32) * 16) * 2 + lane * 16;
  }
  ld.abase = (const char*)Ablk;
}

DI int wfrag_addr(int t, int lane) {
  int g = lane >> 4, q = (lane & 15) >> 2, p = lane & 3;
  int f = ((q >> 1) & 1) | ((g & 1) << 1);
  return (t >> 2) * 4096 + (8 * g + q) * 128 + (((t & 3) ^ f) * 32) + p * 8;
}
template <int BM>
DI int afrag_addr(int mrow0, int lane) {
  int r = lane & 15, g = lane >> 4;
  return (g >> 1) * (BM * 32) + (mrow0 + r) * 32 + (g & 1) * 16;
}

constexpr float QSCALE = 0.17677669529663687f * 1.4426950408889634f;
typedef GCfg<128, 3, 2, 4, 4> CQ;
__global__ __launch_bounds__(768, 3) void k_qkv(const u16* __restrict__ Ablk, const u16* __restrict__ W,
                                                 u16* __restrict__ out) {
  extern __shared__ __attribute__((aligned(16))) char smem_[];
  lds_ptr smem = (lds_ptr)smem_;
  const int lane = threadIdx.x & 63;
  const int w = __builtin_amdgcn_readfirstlane(threadIdx.x >> 6);
  const int xcd = blockIdx.x & 7, idx = blockIdx.x >> 3;
  const int tm = (xcd & 1) * 8 + (idx & 7), tn = (xcd >> 1) * 4 + (idx >> 3);
  const int m0 = tm * 128, n0 = tn * 192;
  constexpr int LDW = 3 * DM;
  Loader<CQ> ld;
  const int lw = w & 3;
  setup_loader_A<CQ, 128, 3, 2>(ld, Ablk, m0, lw, lane);
#pragma unroll
  for (int i = 0; i < CQ::WPW; ++i) {
    int b = lw + 4 * i;
    int sub = b / 12, r = b % 12, grp = r >> 2, o = r & 3;
    ld.woff[i] = 2 * CQ::A_SUB + sub * CQ::W_SUB + grp * 4096 + o * 1024;
    int k = sub * 32 + 8 * o + (lane >> 3);
    int f = ((lane >> 4) & 1) | ((o & 1) << 1);
    int col = n0 + grp * 64 + ((((lane & 7) >> 1) ^ f) * 16) + (lane & 1) * 8;
    ld.wv[i] = (k * LDW + col) * 2;
    ld.wbase[i] = (const char*)W;
  }
  const int wm = w & 1, wn = (w >> 1) & 3;
  int waddr[3];
#pragma unroll
  for (int n = 0; n < 3; ++n) waddr[n] = wfrag_addr(wn * 3 + n, lane);
  const int aaddr = afrag_addr<128>(wm * 64, lane);
  f32x4 acc[3][4];
#pragma unroll
  for (int n = 0; n < 3; ++n)
#pragma unroll
    for (int m = 0; m < 4; ++m) acc[n][m] = f32x4{0.f, 0.f, 0.f, 0.f};
  Engine<128, 3, 2, 4, 4, 3, 1, DM / 64, 1, 0, 4> eng(acc, smem, ld, (long)2 * SEQ * 32, (long)64 * LDW, aaddr, waddr, 0);
  if (w >= 8) { eng.loader_run(); return; }
  eng.start();
  eng.run();
  const int g = lane >> 4;
#pragma unroll
  for (int n = 0; n < 3; ++n) {
    const int cb = (n0 >> 4) + wn * 3 + n;
    const float sc = cb < DM / 16 ? QSCALE : 1.f;
#pragma unroll
    for (int m = 0; m < 4; m += 2) {
      u32x2 a, b;
      a[0] = pack2(acc[n][m][0] * sc, acc[n][m][1] * sc);
      a[1] = pack2(acc[n][m][2] * sc, acc[n][m][3] * sc);
      b[0] = pack2(acc[n][m + 1][0] * sc, acc[n][m + 1][1] * sc);
      b[1] = pack2(acc[n][m + 1][2] * sc, acc[n][m + 1][3] * sc);
      const int row = m0 + wm * 64 + (m + (g & 1)) * 16 + (lane & 15);
      st16(out + ((size_t)cb * SEQ + row) * 16 + (g >> 1) * 8, pair16(a, b));
    }
  }
}

typedef GCfg<128, 1, 2, 6, 4> CP;
template <int KDIM, int LDW>
__global__ __launch_bounds__(768, 3) void k_proj(const u16* __restrict__ Ablk, const u16* __restrict__ W,
                                                  const u16* __restrict__ res, const float* __restrict__ gate,
                                                  u16* __restrict__ out) {
  extern __shared__ __attribute__((aligned(16))) char smem_[];
  lds_ptr smem = (lds_ptr)smem_;
  const int lane = threadIdx.x & 63;
  const int w = __builtin_amdgcn_readfirstlane(threadIdx.x >> 6);
  const int xcd = blockIdx.x & 7, idx = blockIdx.x >> 3;
  const int tm = (xcd & 3) * 4 + (idx & 3), tn = (xcd >> 2) * 8 + (idx >> 2);
  const int m0 = tm * 128, n0 = tn * 64;
  Loader<CP> ld;
  const int lw = w & 3;
  setup_loader_A<CP, 128, 1, 2>(ld, Ablk, m0, lw, lane);
#pragma unroll
  for (int i = 0; i < CP::WPW; ++i) {
    int b = lw + 4 * i;
    int sub = b >> 2, o = b & 3;
    ld.woff[i] = 2 * CP::A_SUB + sub * CP::W_SUB + o * 1024;
    int k = sub * 32 + 8 * o + (lane >> 3);
    int f = ((lane >> 4) & 1) | ((o & 1) << 1);
    int col = n0 + ((((lane & 7) >> 1) ^ f) * 16) + (lane & 1) * 8;
    ld.wv[i] = (k * LDW + col) * 2;
    ld.wbase[i] = (const char*)W;
  }
  const int wm = w & 1, wn = (w >> 1) & 1, kg = (w >> 2) & 1;
  int waddr[2];
#pragma unroll
  for (int n = 0; n < 2; ++n) waddr[n] = wfrag_addr(wn * 2 + n, lane);
  const int aaddr = afrag_addr<128>(wm * 64, lane);
  f32x4 acc[2][4];
#pragma unroll
  for (int n = 0; n < 2; ++n)
#pragma unroll
    for (int m = 0; m < 4; ++m) acc[n][m] = f32x4{0.f, 0.f, 0.f, 0.f};
  Engine<128, 1, 2, 6, 4, 2, 2, KDIM / 64, (KDIM == DM ? 3 : 5), 0, 4> eng(acc, smem, ld, (long)2 * SEQ * 32, (long)64 * LDW, aaddr, waddr, kg);
  constexpr int EOFF = decltype(eng)::FREE_OFF;
  static_assert(EOFF >= 32768, "overlaps the K-group exchange area");
  if (w >= 8) {
    float gp[MNS];
    eng.loader_run([&](int phase) {
      if (phase == 0) {
#pragma unroll
        for (int i = 0; i < 4; ++i) {
          const int pc = lw + 4 * i;
          glds16(res + (size_t)(m0 + pc * 8) * DM + n0, ((lane >> 3) * DM + (lane & 7) * 8) * 2, lds_u32(smem) + EOFF + pc * 1024);
        }
#pragma unroll
        for (int i = 0; i < MNS; ++i) gp[i] = gate[i * (6 * DM) + n0 + lane];
      } else if (w == 8) {
        float v = 0.f;
#pragma unroll
        for (int i = 0; i < MNS; ++i) v += gp[i];
        *(__attribute__((address_space(3))) float*)(smem + EOFF + 16384 + lane * 4) = rbf(v);
      }
    });
    return;
  }
  const int g = lane >> 4;
  const int colw = n0 + (wn * 2 + kg) * 16;
  eng.start();
  eng.run();
  __builtin_amdgcn_s_barrier();
  asm volatile("" ::: "memory");
  __attribute__((address_space(3))) f32x4* red = (__attribute__((address_space(3))) f32x4*)smem;
  f32x4 kp[4];
  if (kg == 0) {
#pragma unroll
    for (int m = 0; m < 4; ++m) { red[(w * 4 + m) * 64 + lane] = acc[1][m]; kp[m] = acc[0][m]; }
  } else {
#pragma unroll
    for (int m = 0; m < 4; ++m) { red[(w * 4 + m) * 64 + lane] = acc[0][m]; kp[m] = acc[1][m]; }
  }
  asm volatile("s_waitcnt lgkmcnt(0)" ::: "memory");
  __builtin_amdgcn_s_barrier();
  asm volatile("" ::: "memory");
  {
    const f32x4 gt = *(const __attribute__((address_space(3))) f32x4*)(smem + EOFF + 16384 + ((wn * 2 + kg) * 16 + 4 * g) * 4);
#pragma unroll
    for (int m = 0; m < 4; m += 2) {
      u32x2 o[2];
#pragma unroll
      for (int i = 0; i < 2; ++i) {
        f32x4 a = kp[m + i] + red[((w ^ 4) * 4 + m + i) * 64 + lane];
        const u32x2 r = *(const __attribute__((address_space(3))) u32x2*)(smem + EOFF + (wm * 64 + (m + i) * 16 + (lane & 15)) * 128 + ((wn * 2 + kg) * 16 + 4 * g) * 2);
        o[i][0] = pack2(bflo(r[0]) + rbf(gt[0] * rbf(a[0])), bfhi(r[0]) + rbf(gt[1] * rbf(a[1])));
        o[i][1] = pack2(bflo(r[1]) + rbf(gt[2] * rbf(a[2])), bfhi(r[1]) + rbf(gt[3] * rbf(a[3])));
      }
      const int row = m0 + wm * 64 + (m + (g & 1)) * 16 + (lane & 15);
      *(u32x4*)(out + (size_t)row * DM + colw + (g >> 1) * 8) = pair16(o[0], o[1]);
    }
  }
}

typedef GCfg<256, 4, 1, 4, 4> CG;
template <int NH_>
DI void glu_wave(lds_ptr smem, Loader<CG>& ld, int lane, int wm, int t0, int m0, int f0, u16* __restrict__ out) {
  constexpr int NT = 2 * NH_;
  int waddr[NT];
#pragma unroll
  for (int n = 0; n < NH_; ++n) {
    waddr[n] = wfrag_addr(t0 + n, lane);
    waddr[NH_ + n] = waddr[n] + 8192;
  }
  const int aaddr = afrag_addr<256>(wm * 64, lane);
  f32x4 acc[NT][4];
#pragma unroll
  for (int n = 0; n < NT; ++n)
#pragma unroll
    for (int m = 0; m < 4; ++m) acc[n][m] = f32x4{0.f, 0.f, 0.f, 0.f};
  Engine<256, 4, 1, 4, 4, NT, 1, DM / 32, 4, 0, 4, (NH_ == 3)> eng(acc, smem, ld, (long)SEQ * 32, (long)32 * FF, aaddr, waddr, 0);
  eng.start();
  eng.run();
  const int g = lane >> 4;
#pragma unroll
  for (int n = 0; n < NH_; ++n) {
    const int cb = (f0 >> 4) + t0 + n;
#pragma unroll
    for (int m = 0; m < 4; m += 2) {
      u32x2 o[2];
#pragma unroll
      for (int i = 0; i < 2; ++i) {
        f32x4 a = acc[n][m + i], b = acc[NH_ + n][m + i];
        o[i][0] = pack2(rbf(a[0]) * rbf(b[0]), rbf(a[1]) * rbf(b[1]));
        o[i][1] = pack2(rbf(a[2]) * rbf(b[2]), rbf(a[3]) * rbf(b[3]));
      }
      const int row = m0 + wm * 64 + (m + (g & 1)) * 16 + (lane & 15);
      st16(out + ((size_t)cb * SEQ + row) * 16 + (g >> 1) * 8, pair16(o[0], o[1]));
    }
  }
}

__global__ __launch_bounds__(512, 2) void k_glu(const u16* __restrict__ Ablk, const u16* __restrict__ Wg,
                                                 const u16* __restrict__ Wu, u16* __restrict__ out) {
  extern __shared__ __attribute__((aligned(16))) char smem_[];
  lds_ptr smem = (lds_ptr)smem_;
  const int lane = threadIdx.x & 63;
  const int w = __builtin_amdgcn_readfirstlane(threadIdx.x >> 6);
  const int xcd = blockIdx.x & 7, idx = blockIdx.x >> 3;
  const int tm = idx & 7, tn = xcd * 4 + (idx >> 3);
  const int m0 = tm * 256, f0 = tn * 112;
  Loader<CG> ld;
  const int lw = w & 3;
  setup_loader_A<CG, 256, 4, 1>(ld, Ablk, m0, lw, lane);
#pragma unroll
  for (int i = 0; i < CG::WPW; ++i) {
    int b = lw + 4 * i;
    int grp = b >> 2, o = b & 3;
    ld.woff[i] = CG::A_SUB + grp * 4096 + o * 1024;
    int k = 8 * o + (lane >> 3);
    int f = ((lane >> 4) & 1) | ((o & 1) << 1);
    int pr = ((lane & 7) >> 1) ^ f;
    if ((grp & 1) && pr == 3) pr = 0;
    int col = f0 + (grp & 1) * 64 + pr * 16 + (lane & 1) * 8;
    ld.wv[i] = (k * FF + col) * 2;
    ld.wbase[i] = (const char*)(grp >> 1 ? Wu : Wg);
  }
  if (w >= 4) __builtin_amdgcn_s_setprio(1);
  const int wm = w & 3;
  if (w < 4) glu_wave<4>(smem, ld, lane, wm, 0, m0, f0, out);
  else glu_wave<3>(smem, ld, lane, wm, 4, m0, f0, out);
}

constexpr int ACH = 128, ANST = 4, ASTAGE = ACH * 64 * 2 + 256;
constexpr int ALDS = ANST * ASTAGE + 1024 + 16;

DI float rowmax16(const f32x16& v) {
  float mx = fmaxf(fmaxf(v[0], v[1]), v[2]);
#pragma unroll
  for (int r = 3; r < 15; r += 2) mx = fmaxf(fmaxf(mx, v[r]), v[r + 1]);
  mx = fmaxf(mx, v[15]);
  auto sw = __builtin_amdgcn_permlane32_swap(__float_as_uint(mx), __float_as_uint(mx), false, false);
  return fmaxf(__uint_as_float(sw[0]), __uint_as_float(sw[1]));
}

template <bool SAFE>
DI void attn_pass(lds_ptr smem, int lane, int w, const char* Kp, const char* Vp, const char* pf_wm, const char* pf_wo, const char* pf_wg,
                  const char* pf_wu, const char* pf_wd, const bf16x8& qa0, const bf16x8& qa1, const bf16x8& qb0,
                  const bf16x8& qb1, f32x16& oa, f32x16& ob, float& refa, float& refb, float& la_out, float& lb_out,
                  ModTask& mt, const u16* c) {
  const int r32 = lane & 31, hh = lane >> 5, kh = w >> 2;
  const unsigned kvo = ((32 * (w & 3) + (lane >> 1)) * 16 + (((lane & 1) ^ ((lane >> 4) & 1)) * 8)) * 2;
  const unsigned vvo = ((32 * (w & 3)) * 16) * 2 + lane * 16;
  const char* ksrc = Kp;
  const char* vsrc = Vp;
  auto issue = [&](int buf) {
    if (w < 4) {
      const unsigned l0 = lds_u32(smem) + buf * ASTAGE + w * 1024;
      glds16(ksrc, kvo, l0);
      glds16(ksrc + SEQ * 32, kvo, l0 + 4096);
      glds16(vsrc, vvo, l0 + 8192);
      glds16(vsrc + SEQ * 32, vvo, l0 + 8192 + 4096 + 128);
    }
    ksrc += ACH * 32; vsrc += ACH * 32;
  };
  const unsigned pfo = lane * 16;
  auto prefetch = [&](int ch) {
    if (w >= 4) {
#pragma unroll
      for (int i = 0; i < 3; ++i) {
        int pc = (ch * 3 + i) * 4 + (w - 4);
        const char* base;
        if (pc < 32) base = pf_wm + (size_t)(blockIdx.x * 4 + (pc >> 3)) * (12 * DM) + 4 * DM + (pc & 7) * 1024;
        else if (pc < 40) base = pf_wo + (size_t)blockIdx.x * 8192 + (pc - 32) * 1024;
        else if (pc < 68) base = pf_wd + (size_t)blockIdx.x * 28672 + (pc - 40) * 1024;
        else if (pc < 96) base = pf_wg + (size_t)blockIdx.x * 28672 + (pc - 68) * 1024;
        else if (pc < 124) base = pf_wu + (size_t)blockIdx.x * 28672 + (pc - 96) * 1024;
        else break;
        glds16(base, pfo, lds_u32(smem) + ANST * ASTAGE);
      }
    }
  };
  issue(0); issue(1); issue(2);
  const int kaddr0 = kh * 2048 + r32 * 32 + ((hh ^ ((r32 >> 3) & 1)) * 16);
  const int kaddr1 = kaddr0 + 4096;
  const int vaddr = 8192 + kh * 2048 + ((lane >> 4) & 1) * (4096 + 128) + (4 * hh + ((lane & 15) >> 2)) * 32 + (lane & 3) * 8;
  bf16x8 sel;
  {
    const short one = ((lane & 15) == ((lane >> 4) & 1)) ? (short)0x3F80 : (short)0;
#pragma unroll
    for (int j = 0; j < 8; ++j) sel[j] = one;
  }
  f32x16 nma, nmb, sa, sb;
#pragma unroll
  for (int r = 0; r < 16; ++r) { oa[r] = 0.f; ob[r] = 0.f; nma[r] = 0.f; nmb[r] = 0.f; }
  f32x4 lsa = {0.f, 0.f, 0.f, 0.f}, lsb = {0.f, 0.f, 0.f, 0.f};
  float la = 0.f, lb = 0.f;
  constexpr int NCH = SEQ / ACH;
  if (w < 4) wait_vm<8>();
  bar();
  const bf16x8 kf0_ = lds_read128(smem + kaddr0);
  const bf16x8 kf1_ = lds_read128(smem + kaddr1);
  {
    sa = MFMA32(kf0_, qa0, nma); sa = MFMA32(kf1_, qa1, sa);
    sb = MFMA32(kf0_, qb0, nmb); sb = MFMA32(kf1_, qb1, sb);
    const float ma = rowmax16(sa), mb = rowmax16(sb);
#pragma unroll
    for (int r = 0; r < 16; ++r) { nma[r] = -ma; sa[r] -= ma; nmb[r] = -mb; sb[r] -= mb; }
  }
  if constexpr (SAFE) {
    auto half = [&](f32x16& s, f32x16& o, f32x16& nm, float& l, const bf16x8& q0f, const bf16x8& q1f,
                    const bf16x8& kn0, const bf16x8& kn1, const bf16x8& vf0, const bf16x8& vf1, bool has_next) {
      f32x16 p;
      const float dl = fmaxf(rowmax16(s), 0.f);
      const float f = __builtin_amdgcn_exp2f(-dl);
      float t = 0.f;
#pragma unroll
      for (int r = 0; r < 16; ++r) { nm[r] -= dl; o[r] *= f; p[r] = __builtin_amdgcn_exp2f(s[r] - dl); t += p[r]; }
      l = l * f + t;
      if (has_next) { s = MFMA32(kn0, q0f, nm); s = MFMA32(kn1, q1f, s); }
      u32x4 pa, pb;
#pragma unroll
      for (int j = 0; j < 4; ++j) { pa[j] = pack2(p[2 * j], p[2 * j + 1]); pb[j] = pack2(p[8 + 2 * j], p[8 + 2 * j + 1]); }
      o = MFMA32(vf0, __builtin_bit_cast(bf16x8, pa), o);
      o = MFMA32(vf1, __builtin_bit_cast(bf16x8, pb), o);
    };
    auto step = [&](lds_cptr vb, lds_cptr knext, bool has_next) {
      bf16x8 kn0, kn1;
      if (has_next) { kn0 = lds_read128(knext + kaddr0); kn1 = lds_read128(knext + kaddr1); }
      bf16x8 vf0 = lds_tr8(vb, vb + 256);
      bf16x8 vf1 = lds_tr8(vb + 512, vb + 768);
      half(sa, oa, nma, la, qa0, qa1, kn0, kn1, vf0, vf1, has_next);
      half(sb, ob, nmb, lb, qb0, qb1, kn0, kn1, vf0, vf1, has_next);
    };
    for (int ch = 0; ch < NCH; ++ch) {
      if (w < 4) { if (ch + 2 < NCH) wait_vm<4>(); else wait_vm<0>(); }
      bar();
      if (ch + 3 < NCH) issue((ch + 3) & 3);
      lds_cptr sbuf = smem + (ch & 3) * ASTAGE;
      lds_cptr nbuf = smem + ((ch + 1) & 3) * ASTAGE;
      step(sbuf + vaddr, sbuf + 1024, true);
      step(sbuf + 1024 + vaddr, nbuf, ch + 1 < NCH);
    }
  } else {
    u32x4 pa0, pa1, pb0, pb1;
    bf16x8 kc0 = kf0_, kc1 = kf1_;
    bf16x8 vp0, vp1;
#pragma unroll
    for (int j = 0; j < 4; ++j) { pb0[j] = 0u; pb1[j] = 0u; }
#pragma unroll
    for (int j = 0; j < 8; ++j) { vp0[j] = 0; vp1[j] = 0; }
#define SB_ __builtin_amdgcn_sched_barrier(0)
#define EX(i) e[i] = __builtin_amdgcn_exp2f(S[i])
#define PHASE(S, P0, P1, M0, M1, M2, M3, M4, M5, R0, R1) do { float e[16]; \
      M0; SB_; R0; EX(0); EX(1); EX(2); EX(3); SB_; \
      M1; SB_; R1; EX(4); EX(5); EX(6); EX(7); SB_; \
      M2; SB_; EX(8); EX(9); P0[0] = pack2(e[0], e[1]); P0[1] = pack2(e[2], e[3]); SB_; \
      M3; SB_; EX(10); EX(11); P0[2] = pack2(e[4], e[5]); P0[3] = pack2(e[6], e[7]); SB_; \
      M4; SB_; EX(12); EX(13); EX(14); EX(15); SB_; \
      M5; SB_; P1[0] = pack2(e[8], e[9]); P1[1] = pack2(e[10], e[11]); P1[2] = pack2(e[12], e[13]); P1[3] = pack2(e[14], e[15]); SB_; \
    } while (0)
#define BF(x) __builtin_bit_cast(bf16x8, x)
    auto step = [&](lds_cptr vb, lds_cptr knext, auto has_next_t) {
      constexpr bool HN = decltype(has_next_t)::value;
      bf16x8 kn0, kn1, vc0, vc1;
      { const f32x16& S = sa;
        PHASE(S, pa0, pa1,
              sb = MFMA32(kc0, qb0, nmb), sb = MFMA32(kc1, qb1, sb),
              ob = MFMA32(vp0, BF(pb0), ob), ob = MFMA32(vp1, BF(pb1), ob),
              lsb = MFMA16(sel, BF(pb0), lsb), lsb = MFMA16(sel, BF(pb1), lsb),
              if (HN) { kn0 = lds_read128(knext + kaddr0); kn1 = lds_read128(knext + kaddr1); },
              { vc0 = lds_tr8(vb, vb + 256); vc1 = lds_tr8(vb + 512, vb + 768); }); }
      { const f32x16& S = sb;
        PHASE(S, pb0, pb1,
              if (HN) sa = MFMA32(kn0, qa0, nma), if (HN) sa = MFMA32(kn1, qa1, sa),
              oa = MFMA32(vc0, BF(pa0), oa), oa = MFMA32(vc1, BF(pa1), oa),
              lsa = MFMA16(sel, BF(pa0), lsa), lsa = MFMA16(sel, BF(pa1), lsa),
              , ); }
      if (HN) { kc0 = kn0; kc1 = kn1; }
      vp0 = vc0; vp1 = vc1;
    };
    typedef std::integral_constant<bool, true> T_;
    typedef std::integral_constant<bool, false> F_;
    for (int ch = 0; ch < NCH - 1; ++ch) {
      if (w < 4) { if (ch + 2 < NCH) wait_vm<4>(); else wait_vm<0>(); }
      bar();
      if (ch + 3 < NCH) issue((ch + 3) & 3);
      prefetch(ch);
      lds_cptr sbuf = smem + (ch & 3) * ASTAGE;
      lds_cptr nbuf = smem + ((ch + 1) & 3) * ASTAGE;
      step(sbuf + vaddr, sbuf + 1024, T_{});
      step(sbuf + 1024 + vaddr, nbuf, T_{});
    }
    {
      const int task = blockIdx.x * 2 + (threadIdx.x >> 8);
      mod_load(mt, c, (const u16*)pf_wm, 32 + (task >> 3), task & 7, threadIdx.x & 255);
      lds_cptr sbuf = smem + ((NCH - 1) & 3) * ASTAGE;
      step(sbuf + vaddr, sbuf + 1024, T_{});
      step(sbuf + 1024 + vaddr, sbuf, F_{});
    }
    ob = MFMA32(vp0, BF(pb0), ob); ob = MFMA32(vp1, BF(pb1), ob);
    lsb = MFMA16(sel, BF(pb0), lsb); lsb = MFMA16(sel, BF(pb1), lsb);
#undef PHASE
#undef EX
#undef BF
  }
  refa = nma[0]; refb = nmb[0];
  if constexpr (SAFE) {
    auto s1 = __builtin_amdgcn_permlane32_swap(__float_as_uint(la), __float_as_uint(la), false, false);
    la_out = __uint_as_float(s1[0]) + __uint_as_float(s1[1]);
    auto s2 = __builtin_amdgcn_permlane32_swap(__float_as_uint(lb), __float_as_uint(lb), false, false);
    lb_out = __uint_as_float(s2[0]) + __uint_as_float(s2[1]);
  } else {
    const float a0 = __shfl(lsa[0], lane & 15), a1 = __shfl(lsa[1], lane & 15);
    const float b0 = __shfl(lsb[0], lane & 15), b1 = __shfl(lsb[1], lane & 15);
    la_out = (lane & 16) ? a1 : a0;
    lb_out = (lane & 16) ? b1 : b0;
  }
}

__global__ __launch_bounds__(512, 2) void k_attn(const u16* __restrict__ qkv, u16* __restrict__ oblk,
                                                  const u16* __restrict__ c, const u16* __restrict__ wmod, float* __restrict__ part,
                                                  const char* __restrict__ pf_wo, const char* __restrict__ pf_wg,
                                                  const char* __restrict__ pf_wu, const char* __restrict__ pf_wd) {
  extern __shared__ __attribute__((aligned(16))) char smem_[];
  lds_ptr smem = (lds_ptr)smem_;
  const int lane = threadIdx.x & 63;
  const int w = __builtin_amdgcn_readfirstlane(threadIdx.x >> 6);
  const int xcd = blockIdx.x & 7, idx = blockIdx.x >> 3;
  const int head = xcd * 4 + (idx >> 3), qb = idx & 7;
  const int r32 = lane & 31, hh = lane >> 5;
  const int qg = w & 3, kh = w >> 2;
  const u16* Qp = qkv + (size_t)head * SEQ * 32;
  const char* Kp = (const char*)(qkv + (size_t)(32 + head) * SEQ * 32);
  const char* Vp = (const char*)(qkv + (size_t)(64 + head) * SEQ * 32);
  const int q0 = qb * 256 + qg * 64;
  __attribute__((address_space(3))) int* flag = (__attribute__((address_space(3))) int*)(smem + ANST * ASTAGE + 1024);
  if (threadIdx.x == 0) *flag = 0;
  bf16x8 qa0 = *(const bf16x8*)(Qp + (q0 + r32) * 16 + hh * 8);
  bf16x8 qa1 = *(const bf16x8*)(Qp + (SEQ + q0 + r32) * 16 + hh * 8);
  bf16x8 qb0 = *(const bf16x8*)(Qp + (q0 + 32 + r32) * 16 + hh * 8);
  bf16x8 qb1 = *(const bf16x8*)(Qp + (SEQ + q0 + 32 + r32) * 16 + hh * 8);
  f32x16 oa, ob;
  float nma, nmb, la, lb;
  ModTask mt;
  attn_pass<false>(smem, lane, w, Kp, Vp, (const char*)wmod, pf_wo, pf_wg, pf_wu, pf_wd, qa0, qa1, qb0, qb1, oa, ob, nma, nmb, la, lb, mt, c);
  {
    float big = fmaxf(fabsf(la), fabsf(lb));
#pragma unroll
    for (int r = 0; r < 16; ++r) big = fmaxf(big, fmaxf(fabsf(oa[r]), fabsf(ob[r])));
    bool bad = !(big < 1e30f);
#pragma unroll
    for (int r = 0; r < 16; ++r) bad = bad || (oa[r] != oa[r]) || (ob[r] != ob[r]);
    bad = bad || (la != la) || (lb != lb);
    if (__any(bad) && lane == 0) *flag = 1;
  }
  __syncthreads();
  if (__builtin_amdgcn_readfirstlane(*flag) != 0) {
    attn_pass<true>(smem, lane, w, Kp, Vp, (const char*)wmod, pf_wo, pf_wg, pf_wu, pf_wd, qa0, qa1, qb0, qb1, oa, ob, nma, nmb, la, lb, mt, c);
    __syncthreads();
  }
  const int task = blockIdx.x * 2 + (threadIdx.x >> 8);
  {
    __attribute__((address_space(3))) float* xb = (__attribute__((address_space(3))) float*)smem;
    const f32x16& og = kh == 0 ? ob : oa;
    __attribute__((address_space(3))) float* dst = xb + w * (18 * 64) + lane;
#pragma unroll
    for (int r = 0; r < 16; ++r) dst[r * 64] = og[r];
    dst[16 * 64] = kh == 0 ? lb : la;
    dst[17 * 64] = kh == 0 ? nmb : nma;
    __syncthreads();
    const __attribute__((address_space(3))) float* src = xb + (w ^ 4) * (18 * 64) + lane;
    f32x16 om = kh == 0 ? oa : ob;
    const float nm_me = kh == 0 ? nma : nmb;
    const float nm_ot = src[17 * 64];
    const float nmin = fminf(nm_me, nm_ot);
    const float f_me = __builtin_amdgcn_exp2f(nmin - nm_me), f_ot = __builtin_amdgcn_exp2f(nmin - nm_ot);
    const float rl = 1.f / ((kh == 0 ? la : lb) * f_me + src[16 * 64] * f_ot);
    const float g_me = f_me * rl, g_ot = f_ot * rl;
    u16* op = oblk + ((size_t)head * 2 * SEQ + q0 + kh * 32 + r32) * 16 + 4 * hh;
#pragma unroll
    for (int g = 0; g < 4; ++g) {
      float v0 = om[4 * g] * g_me + src[(4 * g) * 64] * g_ot;
      float v1 = om[4 * g + 1] * g_me + src[(4 * g + 1) * 64] * g_ot;
      float v2 = om[4 * g + 2] * g_me + src[(4 * g + 2) * 64] * g_ot;
      float v3 = om[4 * g + 3] * g_me + src[(4 * g + 3) * 64] * g_ot;
      u32x2 v;
      v[0] = pack2(v0, v1);
      v[1] = pack2(v2, v3);
      *(u32x2*)(op + (g >> 1) * (SEQ * 16) + (g & 1) * 8) = v;
    }
  }
  mod_finish(mt, part, 32 + (task >> 3), task & 7, threadIdx.x & 255, (float*)(smem_ + 40960) + (threadIdx.x >> 8) * 256);
  wait_vm<0>();
}

extern "C" void kernel_launch(void* const* d_in, const int* in_sizes, int n_in, void* d_out, int out_size,
                              void* d_ws, size_t ws_size, hipStream_t stream) {
  const u16* x = (const u16*)d_in[0];
  const u16* wqkv = (const u16*)d_in[1];
  const u16* wo = (const u16*)d_in[2];
  const u16* wg = (const u16*)d_in[4];
  const u16* wu = (const u16*)d_in[5];
  const u16* wd = (const u16*)d_in[6];
  const u16* c = (const u16*)d_in[7];
  const u16* wmod = (const u16*)d_in[8];
  u16* out = (u16*)d_out;
  char* ws = (char*)d_ws;
  float* part = (float*)ws;
  u16* h1 = (u16*)(ws + (1 << 20));
  u16* qkv = (u16*)(ws + (8 << 20));
  u16* ob = (u16*)(ws + (24 << 20));
  u16* x2 = (u16*)(ws + (32 << 20));
  u16* h2 = (u16*)(ws + (40 << 20));
  u16* hh = (u16*)(ws + (48 << 20));
  u16* wdp = (u16*)(ws + (64 << 20));

  hipFuncSetAttribute((const void*)k_qkv, hipFuncAttributeMaxDynamicSharedMemorySize, CQ::LDS);
  hipFuncSetAttribute((const void*)k_proj<DM, DM>, hipFuncAttributeMaxDynamicSharedMemorySize, CP::LDS);
  hipFuncSetAttribute((const void*)k_proj<FF, WDP>, hipFuncAttributeMaxDynamicSharedMemorySize, CP::LDS);
  hipFuncSetAttribute((const void*)k_glu, hipFuncAttributeMaxDynamicSharedMemorySize, CG::LDS);
  hipFuncSetAttribute((const void*)k_attn, hipFuncAttributeMaxDynamicSharedMemorySize, ALDS);

  k_pre<<<256 + 256, 256, 0, stream>>>(c, wmod, part, (const char*)x);
  k_ln<6, MNS, 192><<<256 + 192, 512, 0, stream>>>(x, part, h1, (const char*)wqkv, wd, wdp);
  k_qkv<<<256, 768, CQ::LDS, stream>>>(h1, wqkv, qkv);
  k_attn<<<256, 512, ALDS, stream>>>(qkv, ob, c, wmod, part, (const char*)wo, (const char*)wg, (const char*)wu, (const char*)wd);
  k_proj<DM, DM><<<256, 768, CP::LDS, stream>>>(ob, wo, x, part + 2 * DM, x2);
  k_ln<7, MNS, 0><<<256 + 224, 512, 0, stream>>>(x2, part + 3 * DM, h2, (const char*)wqkv, wd, wdp);
  k_glu<<<256, 512, CG::LDS, stream>>>(h2, wg, wu, hh);
  k_proj<FF, WDP><<<256, 768, CP::LDS, stream>>>(hh, wdp, x2, part + 5 * DM, out);
}
```
